```python
import torch

def _softmax(x, axis=-1):
    return torch.softmax(x, dim=axis)
def _gelu(x):
    return torch.nn.functional.gelu(x, approximate="tanh")
def _silu(x):
    return torch.nn.functional.silu(x)
def _sigmoid(x):
    return torch.sigmoid(x)
def _layernorm(x, axis=-1, eps=1e-5):
    m = x.mean(dim=axis, keepdim=True)
    v = x.var(dim=axis, keepdim=True, unbiased=False)
    return (x - m) / torch.sqrt(v + eps)
def _scatter_add(tgt, idx, src, axis=0):
    return tgt.clone().index_add_(axis, idx, src)
def _segment_sum(src, seg, n):
    out = torch.zeros((n,) + src.shape[1:], dtype=src.dtype)
    return out.index_add_(0, seg, src)
def _pad(x, pw):
    flat = tuple(v for pair in reversed(pw) for v in pair)
    return torch.nn.functional.pad(x, flat)

kernel_name = 'recurrent_scan__spectral_gfnet__glu_gated_geglu__prompt'

def setup_inputs():

    _w = (lambda a: (lambda t: t.to(torch.bfloat16) if t.is_floating_point() else t)(torch.as_tensor(a)))
    def _A_setup():
        B, S, D = 1, 2048, 512
        Df = D // 2 + 1
        return {
            "x": _w((lambda *s: torch.randn(*s, dtype=torch.bfloat16) * 0.1)(B, S, D)),
            "w_r": _w((lambda *s: torch.randn(*s, dtype=torch.bfloat16) * 3.2)(S, Df) * 0.02),
            "w_i": _w((lambda *s: torch.randn(*s, dtype=torch.bfloat16) * 3.2)(S, Df) * 0.02),
        }
    _A_in = _A_setup()

    def _B_setup():
        B, S, Dm, Df = 1, 2048, 512, 1376
        return {
            "x": _w((lambda *s: torch.randn(*s, dtype=torch.bfloat16) * 0.1)(B, S, Dm)),
            "w_gate": _w((lambda *s: torch.randn(*s, dtype=torch.bfloat16) * 0.8)(Dm, Df) * (Dm ** -0.5)),
            "w_up": _w((lambda *s: torch.randn(*s, dtype=torch.bfloat16) * 0.8)(Dm, Df) * (Dm ** -0.5)),
            "w_down": _w((lambda *s: torch.randn(*s, dtype=torch.bfloat16) * 0.8)(Df, Dm) * (Df ** -0.5)),
        }
    _B_in = _B_setup()

    return {
        "A_x": _A_in["x"],
        "A_w_r": _A_in["w_r"],
        "A_w_i": _A_in["w_i"],
        "B_x": _B_in["x"],
        "B_w_gate": _B_in["w_gate"],
        "B_w_up": _B_in["w_up"],
        "B_w_down": _B_in["w_down"],
    }

def reference(A_x, A_w_r, A_w_i, B_x, B_w_gate, B_w_up, B_w_down):
    def _A_ref(x, w_r, w_i):
        B, S, D = x.shape
        X = (lambda x, axes=(-2,-1): torch.fft.rfft2(x.float(), dim=axes))(x, axes=(-2, -1))
        W = w_r + 1j * w_i
        X = X * W
        y = (lambda x, s, axes=(-2,-1): torch.fft.irfft2(x, s=s, dim=axes).to(torch.bfloat16))(X, (S, D), axes=(-2, -1))
        return y

    def _B_ref(x, w_gate, w_up, w_down):
        g = torch.einsum("bsd,df->bsf", x, w_gate)
        u = torch.einsum("bsd,df->bsf", x, w_up)
        h = _gelu(g) * u
        return torch.einsum("bsf,fd->bsd", h, w_down)

    x = A_x
    xn = _layernorm(x, axis=-1)
    s = _A_ref(xn, A_w_r, A_w_i)
    g = _silu(_B_ref(xn, B_w_gate, B_w_up, B_w_down))
    return x + s * g
```

```cpp
#include <hip/hip_runtime.h>
#include <hip/hip_bf16.h>
#include <stdint.h>

typedef unsigned short u16;
__device__ __forceinline__ float bf2f(u16 b) { return __uint_as_float(((unsigned)b) << 16); }
__device__ __forceinline__ u16 f2bf(float f) { __hip_bfloat16 h = __float2bfloat16(f); return *reinterpret_cast<u16*>(&h); }
__device__ __forceinline__ float rbf(float f) { return bf2f(f2bf(f)); }

constexpr int S = 2048, D = 512, DF = 257, F = 1376;

__global__ void k_ln(const u16* x, u16* xn) {
    int s = blockIdx.x, t = threadIdx.x;
    float v[8]; float sum = 0;
    for (int i = 0; i < 8; i++) { v[i] = bf2f(x[s * D + t + 64 * i]); sum += v[i]; }
    for (int o = 32; o; o >>= 1) sum += __shfl_xor(sum, o);
    float mean = sum / D; float q = 0;
    for (int i = 0; i < 8; i++) { float d = v[i] - mean; q += d * d; }
    for (int o = 32; o; o >>= 1) q += __shfl_xor(q, o);
    float var = rbf(q / D), m = rbf(mean);
    float den = rbf(sqrtf(rbf(var + 1e-5f)));
    for (int i = 0; i < 8; i++) xn[s * D + t + 64 * i] = f2bf(rbf(v[i] - m) / den);
}
__global__ void k_rowdft(const u16* xn, float2* R) {
    __shared__ float xs[D]; __shared__ float2 tw[D];
    int s = blockIdx.x, k = threadIdx.x;
    for (int i = k; i < D; i += blockDim.x) { xs[i] = bf2f(xn[s * D + i]); float sn, cs; sincospif(-2.0f * i / D, &sn, &cs); tw[i] = make_float2(cs, sn); }
    __syncthreads();
    if (k >= DF) return;
    float re = 0, im = 0;
    for (int d = 0; d < D; d++) { float2 w = tw[(k * d) & (D - 1)]; re += xs[d] * w.x; im += xs[d] * w.y; }
    R[s * DF + k] = make_float2(re, im);
}
template <int INV>
__global__ void k_coldft(const float2* R, float2* X, const u16* wr, const u16* wi) {
    __shared__ float2 tw[S];
    int u = blockIdx.x, k = threadIdx.x;
    for (int i = k; i < S; i += blockDim.x) { float sn, cs; sincospif((INV ? 2.0f : -2.0f) * i / S, &sn, &cs); tw[i] = make_float2(cs, sn); }
    __syncthreads();
    if (k >= DF) return;
    float re = 0, im = 0;
    for (int s = 0; s < S; s++) { float2 w = tw[(u * s) & (S - 1)]; float2 r = R[s * DF + k]; re += r.x * w.x - r.y * w.y; im += r.x * w.y + r.y * w.x; }
    if (!INV) { float a = bf2f(wr[u * DF + k]), b = bf2f(wi[u * DF + k]); float nr = re * a - im * b, ni = re * b + im * a; re = nr; im = ni; }
    else { re *= 1.0f / S; im *= 1.0f / S; }
    X[u * DF + k] = make_float2(re, im);
}
__global__ void k_h(const u16* xn, const u16* wg, const u16* wu, u16* h) {
    __shared__ float xs[D];
    int s = blockIdx.x, f = blockIdx.y * 256 + threadIdx.x;
    for (int i = threadIdx.x; i < D; i += 256) xs[i] = bf2f(xn[s * D + i]);
    __syncthreads();
    if (f >= F) return;
    float g = 0, u = 0;
    for (int d = 0; d < D; d++) { g += xs[d] * bf2f(wg[d * F + f]); u += xs[d] * bf2f(wu[d * F + f]); }
    g = rbf(g); u = rbf(u);
    float ge = rbf(0.5f * g * (1.0f + tanhf(0.7978845608f * (g + 0.044715f * g * g * g))));
    h[s * F + f] = f2bf(ge * u);
}
__global__ void k_out(const u16* x, const float2* Z, const u16* h, const u16* wd, u16* out) {
    __shared__ float hs[F]; __shared__ float2 zs[DF]; __shared__ float2 tw[D];
    int s = blockIdx.x, d = threadIdx.x;
    for (int i = d; i < F; i += 512) hs[i] = bf2f(h[s * F + i]);
    if (d < DF) zs[d] = Z[s * DF + d];
    { float sn, cs; sincospif(2.0f * d / D, &sn, &cs); tw[d] = make_float2(cs, sn); }
    __syncthreads();
    float o = 0;
    for (int f = 0; f < F; f++) o += hs[f] * bf2f(wd[f * D + d]);
    o = rbf(o);
    float g = rbf(o / (1.0f + expf(-o)));
    float y = zs[0].x + ((d & 1) ? -zs[256].x : zs[256].x);
    float acc = 0;
    for (int k = 1; k < 256; k++) { float2 w = tw[(k * d) & (D - 1)]; acc += zs[k].x * w.x - zs[k].y * w.y; }
    y = rbf((y + 2 * acc) / D);
    out[s * D + d] = f2bf(bf2f(x[s * D + d]) + rbf(y * g));
}

extern "C" void kernel_launch(void* const* d_in, const int* in_sizes, int n_in,
                              void* d_out, int out_size, void* d_ws, size_t ws_size,
                              hipStream_t stream) {
    const u16* x = (const u16*)d_in[0]; const u16* wr = (const u16*)d_in[1]; const u16* wi = (const u16*)d_in[2];
    const u16* wg = (const u16*)d_in[4]; const u16* wu = (const u16*)d_in[5]; const u16* wd = (const u16*)d_in[6];
    char* ws = (char*)d_ws;
    u16* xn = (u16*)ws; float2* R = (float2*)(ws + (4 << 20)); float2* X = (float2*)(ws + (16 << 20)); float2* Z = (float2*)(ws + (32 << 20)); u16* h = (u16*)(ws + (48 << 20));
    k_ln<<<S, 64, 0, stream>>>(x, xn);
    k_rowdft<<<S, 320, 0, stream>>>(xn, R);
    k_coldft<0><<<S, 320, 0, stream>>>(R, X, wr, wi);
    k_coldft<1><<<S, 320, 0, stream>>>(X, Z, wr, wi);
    k_h<<<dim3(S, 6), 256, 0, stream>>>(xn, wg, wu, h);
    k_out<<<S, 512, 0, stream>>>(x, Z, h, wd, (u16*)d_out);
}
```

```cpp
#include <hip/hip_runtime.h>
#include <stdint.h>

typedef unsigned short u16;
typedef unsigned u32;
typedef unsigned long long u64;
typedef float f2 __attribute__((ext_vector_type(2)));
typedef float f4 __attribute__((ext_vector_type(4)));
typedef u32 u4 __attribute__((ext_vector_type(4)));
typedef u32 u2 __attribute__((ext_vector_type(2)));
typedef short s4 __attribute__((ext_vector_type(4)));
typedef __bf16 bf8 __attribute__((ext_vector_type(8)));
typedef __bf16 bf2 __attribute__((ext_vector_type(2)));
#define LAS __attribute__((address_space(3)))
#define GAS __attribute__((address_space(1)))
#define DEV __device__ __forceinline__

constexpr int NFB = 43;
constexpr int WS_FLAG0 = 4096, WS_FLAG1 = 8192;
constexpr int WS_F1 = 1 << 20, WS_F2 = WS_F1 + (4 << 20), WS_H = WS_F2 + (4 << 20);
constexpr int WS_WDP = WS_H + 32 * 4 * NFB * 1024;
constexpr int WS_STAMP = WS_WDP + 8 * NFB * 4 * 1024;
constexpr int WS_BYTES = WS_STAMP + (1 << 20);
constexpr int L_RING = 0;
constexpr int L_FFT = 131072;
constexpr int L_RAW = L_FFT + 18688;
constexpr int L_STAT = L_RAW + 9216;
constexpr int L_CS = L_STAT + 2048;
constexpr int L_END = L_CS + 1024;
static_assert(L_END <= 163840, "lds");
constexpr u32 SENT = 0xFFFFFFFFu;
constexpr int SPIN_CAP = 1 << 16;

struct Args { const u16* x; const u16* wr; const u16* wi; const u16* wg; const u16* wu; const u16* wd; u16* out; unsigned char* ws; };

#define BAR() asm volatile("s_waitcnt lgkmcnt(0)\n\ts_barrier" ::: "memory")
#define WAITVM(n) asm volatile("s_waitcnt vmcnt(" #n ")" ::: "memory")
#define STAMP(i)

DEV f2 pkfma(f2 a, f2 b, f2 c) { return __builtin_elementwise_fma(a, b, c); }
struct KC { f2 pm, rpm; };
DEV f2 addmi(f2 a, f2 b, const KC& k) { return pkfma(b.yx, k.pm, a); }
DEV f2 addpi(f2 a, f2 b, const KC& k) { return pkfma(b.yx, k.pm.yx, a); }
template <bool INV> DEV void dft4(f2& a0, f2& a1, f2& a2, f2& a3, const KC& k) {
    f2 t0 = a0 + a2, t1 = a0 - a2, t2 = a1 + a3, t3 = a1 - a3;
    a0 = t0 + t2; a2 = t0 - t2;
    if (INV) { a1 = addpi(t1, t3, k); a3 = addmi(t1, t3, k); } else { a1 = addmi(t1, t3, k); a3 = addpi(t1, t3, k); }
}
template <bool INV> DEV void dft8(f2 (&v)[8], const KC& k) {
    f2 a0 = v[0] + v[4], a4 = v[0] - v[4], a1 = v[1] + v[5], a5 = v[1] - v[5];
    f2 a2 = v[2] + v[6], a6 = v[2] - v[6], a3 = v[3] + v[7], a7 = v[3] - v[7];
    dft4<INV>(a0, a1, a2, a3, k);
    v[0] = a0; v[2] = a1; v[4] = a2; v[6] = a3;
    f2 b1, b3, t0, t1;
    if (INV) { b1 = addpi(a5, a5, k); b3 = addpi(-a7, a7, k); t0 = addpi(a4, a6, k); t1 = addmi(a4, a6, k); }
    else { b1 = addmi(a5, a5, k); b3 = addmi(-a7, a7, k); t0 = addmi(a4, a6, k); t1 = addpi(a4, a6, k); }
    f2 t2 = b1 + b3, t3 = b1 - b3;
    v[1] = pkfma(t2, k.rpm.xx, t0); v[5] = pkfma(t2, k.rpm.yy, t0);
    if (INV) { v[3] = pkfma(t3.yx, k.rpm.yx, t1); v[7] = pkfma(t3.yx, k.rpm, t1); }
    else { v[3] = pkfma(t3.yx, k.rpm, t1); v[7] = pkfma(t3.yx, k.rpm.yx, t1); }
}
template <bool CONJ> DEV f2 cmul(f2 a, f2 w) {
    f2 r;
    if (CONJ) asm("v_pk_mul_f32 %0, %1, %2 op_sel:[1,1] op_sel_hi:[0,1]\n\tv_pk_fma_f32 %0, %1, %2, %0 op_sel_hi:[1,0,1] neg_hi:[0,0,1]" : "=&v"(r) : "v"(a), "v"(w));
    else asm("v_pk_mul_f32 %0, %1, %2 op_sel:[1,1] op_sel_hi:[0,1]\n\tv_pk_fma_f32 %0, %1, %2, %0 op_sel_hi:[1,0,1] neg_lo:[0,0,1]" : "=&v"(r) : "v"(a), "v"(w));
    return r;
}
DEV f2 cfmac_conj(f2 t, f2 beta, f2 B) {
    asm("v_pk_fma_f32 %0, %2, %1, %0 op_sel_hi:[0,1,1]\n\tv_pk_fma_f32 %0, %2, %1, %0 op_sel:[1,1,0] op_sel_hi:[1,0,1] neg_hi:[0,1,0]" : "+v"(t) : "v"(beta), "v"(B));
    return t;
}
DEV f2 twid(int idx, int mask, float invN) {
    float r = (float)(idx & mask) * invN;
    f2 o; o.x = __builtin_amdgcn_cosf(r); o.y = -__builtin_amdgcn_sinf(r);
    return o;
}
DEV float bflo(u32 v) { return __uint_as_float(v << 16); }
DEV float bfhi(u32 v) { return __uint_as_float(v & 0xFFFF0000u); }
DEV u32 pkbf(float lo, float hi) { bf2 r; r.x = (__bf16)lo; r.y = (__bf16)hi; return __builtin_bit_cast(u32, r); }
DEV float rbf(float f) { return __uint_as_float(((u32)__builtin_bit_cast(u16, (__bf16)f)) << 16); }
DEV f4 mfma(u4 a, u4 b, f4 c) { return __builtin_amdgcn_mfma_f32_16x16x32_bf16(__builtin_bit_cast(bf8, a), __builtin_bit_cast(bf8, b), c, 0, 0, 0); }
DEV u4 trfrag(LAS char* p) {
    s4 r0 = __builtin_amdgcn_ds_read_tr16_b64_v4i16((LAS s4*)p);
    s4 r1 = __builtin_amdgcn_ds_read_tr16_b64_v4i16((LAS s4*)(p + 256));
    u2 x0 = __builtin_bit_cast(u2, r0), x1 = __builtin_bit_cast(u2, r1);
    return u4{x0.x, x0.y, x1.x, x1.y};
}
DEV u4 sanitize(u4 v) { return u4{min(v.x, 0xFFFFFFFEu), min(v.y, 0xFFFFFFFEu), min(v.z, 0xFFFFFFFEu), min(v.w, 0xFFFFFFFEu)}; }
DEV u32 max4(u4 v) { return max(max(v.x, v.y), max(v.z, v.w)); }
DEV void dma16(u32 voff, const void* sbase, u32 ldsaddr) {
    asm volatile("s_mov_b32 m0, %2\n\ts_nop 0\n\tglobal_load_lds_dwordx4 %0, %1" :: "v"(voff), "s"(sbase), "s"(ldsaddr) : "memory");
}
#define LDG128(dst, voff, sbase, imm) asm volatile("global_load_dwordx4 %0, %1, %2 offset:" #imm : "=v"(dst) : "v"(voff), "s"(sbase) : "memory")

DEV f2 row_stats(LAS char* lds, int tile, int s1, const u16* xrow) {
    f2 p0 = *(LAS f2*)(lds + L_STAT + (tile * 64 + s1) * 8);
    f2 p1 = *(LAS f2*)(lds + L_STAT + 1024 + (tile * 64 + s1) * 8);
    float mean = (p0.x + p1.x) * (1.0f / 512.0f);
    float var = (p0.y + p1.y) * (1.0f / 512.0f) - mean * mean;
    if (!(mean * mean <= 1024.0f * var)) {
        float q = 0.f;
        for (int i = 0; i < 256; i++) { u32 v = ((const u32*)xrow)[i]; float d0 = bflo(v) - mean, d1 = bfhi(v) - mean; q += d0 * d0; q += d1 * d1; }
        var = q * (1.0f / 512.0f);
    }
    float den = rbf(sqrtf(rbf(rbf(var) + 1e-5f)));
    return f2{rbf(mean), 1.0f / den};
}

__global__ void __launch_bounds__(512, 2) fused(Args a) {
    extern __shared__ __attribute__((aligned(16))) char smem[];
    LAS char* lds = (LAS char*)smem;
    const int tid = threadIdx.x, lane = tid & 63, w = __builtin_amdgcn_readfirstlane(tid >> 6);
    const int b = blockIdx.x, xcd = b & 7, r = b >> 3, t = r & 7, par = xcd & 1, rg = (xcd >> 1) * 4 + (r >> 3);
    const int s2 = 2 * rg + par, cg = par * 8 + t, sig = xcd * 32 + r, pid = s2 * 8 + t;
    const int rq = w & 3, kh = w >> 2;
    const int li = lane & 15, lg = lane >> 4;
    const __amdgpu_buffer_rsrc_t rs = __builtin_amdgcn_make_buffer_rsrc(a.ws, 0, WS_BYTES, 0x00020000);
    KC kc; kc.pm = f2{1.f, -1.f}; kc.rpm = f2{0.70710678118654752f, -0.70710678118654752f};
    asm volatile("" : "+s"(kc.pm), "+s"(kc.rpm));
    STAMP(0);
    u4 xa[8], xb[8];
    u2 ev;
    { u32 zero = 0; asm volatile("global_load_dwordx2 %0, %1, %2 sc1" : "=v"(ev) : "v"(zero), "s"(a.ws) : "memory"); }
    {
        const u32 voff = (u32)((s2 + 32 * (16 * rq + li)) * 1024 + kh * 512 + lg * 16);
        const char* xA = (const char*)a.x; const char* xB = xA + (par ? -1024 : 1024);
        LDG128(xa[0], voff, xA, 0); LDG128(xa[1], voff, xA, 64); LDG128(xa[2], voff, xA, 128); LDG128(xa[3], voff, xA, 192);
        LDG128(xa[4], voff, xA, 256); LDG128(xa[5], voff, xA, 320); LDG128(xa[6], voff, xA, 384); LDG128(xa[7], voff, xA, 448);
        LDG128(xb[0], voff, xB, 0); LDG128(xb[1], voff, xB, 64); LDG128(xb[2], voff, xB, 128); LDG128(xb[3], voff, xB, 192);
        LDG128(xb[4], voff, xB, 256); LDG128(xb[5], voff, xB, 320); LDG128(xb[6], voff, xB, 384); LDG128(xb[7], voff, xB, 448);
    }
    const u4 sent4 = {SENT, SENT, SENT, SENT};
    if (w < 4) {
#pragma unroll
        for (int i = 0; i < 4; i++) __builtin_amdgcn_raw_buffer_store_b128(sent4, rs, WS_F1 + pid * 16384 + w * 4096 + i * 1024 + lane * 16, 0, 16);
    }
    const int nblk = (t < 6 - par) ? 3 : 2;
    const u32 wvoff = (u32)((lane >> 2) * 2752 + (((lane & 3) ^ (((lane >> 5) & 1) << 1)) * 16));
    auto dma_quarter = [&](int fb, int qd, u32 ldsbase) {
        const int kh_ = w >> 2, ks2 = (w >> 1) & 1, mat = w & 1;
        const int krow = (kh_ * 8 + qd * 2 + ks2) * 32;
        const char* src = (const char*)(mat ? a.wu : a.wg) + (size_t)krow * 2752 + fb * 64;
        const u32 dst = ldsbase + w * 2048;
        dma16(wvoff, src, dst);
        dma16(wvoff, src + 16 * 2752, dst + 1024);
    };
    {
#pragma unroll
        for (int qd = 0; qd < 4; qd++) dma_quarter(cg, qd, L_RING + qd * 16384);
#pragma unroll
        for (int qd = 0; qd < 4; qd++) dma_quarter(16 + cg, qd, L_RING + 65536 + qd * 16384);
    }
    const int c2p = lane & 3, l3 = (lane >> 2) & 7, qb = lane >> 5;
    f2 tw64[8], twT[8];
    int x3a[4];
    {
        const int wq = rq, q1 = qb ? (wq == 0 ? 4 : 8 - wq) : wq, c2 = 4 * t + c2p;
#pragma unroll
        for (int k = 1; k < 8; k++) tw64[k] = twid(l3 * k, 63, 1.0f / 64.0f);
#pragma unroll
        for (int k = 0; k < 8; k++) twT[k] = twid(8 * c2 * q1 + s2 * (l3 + 8 * k), 2047, 1.0f / 2048.0f);
#pragma unroll
        for (int k = 0; k < 4; k++) {
            const int ua = (k >> 1) * 4 + (k & 1), u1 = l3 + 8 * ua;
            const int cls = (u1 < 32) ? u1 : ((64 - u1) & 31), hi = u1 >> 5;
            int mbit, sub;
            if (wq == 0) { mbit = qb; sub = hi; } else if (cls != 0) { sub = hi; mbit = qb ^ hi; } else { mbit = hi; sub = qb ^ hi; }
            x3a[k] = L_FFT + wq * 4608 + (cls * 18 + mbit * 8 + sub * 4 + c2p) * 8;
        }
    }
    const u4 ones = {0x3F803F80u, 0x3F803F80u, 0x3F803F80u, 0x3F803F80u};
    {
        asm volatile("s_waitcnt vmcnt(16)" : "+v"(xa[0]), "+v"(xa[1]), "+v"(xa[2]), "+v"(xa[3]), "+v"(xa[4]), "+v"(xa[5]), "+v"(xa[6]), "+v"(xa[7]),
                     "+v"(xb[0]), "+v"(xb[1]), "+v"(xb[2]), "+v"(xb[3]), "+v"(xb[4]), "+v"(xb[5]), "+v"(xb[6]), "+v"(xb[7]), "+v"(ev) :: "memory");
        STAMP(1);
        f4 s1a = {0, 0, 0, 0}, s2a = s1a, s1b = s1a, s2b = s1a;
#pragma unroll
        for (int k = 0; k < 8; k++) { s1a = mfma(ones, xa[k], s1a); s2a = mfma(xa[k], xa[k], s2a); s1b = mfma(ones, xb[k], s1b); s2b = mfma(xb[k], xb[k], s2b); }
        const int rr = li & 3;
        float da = rr == 0 ? s2a.x : rr == 1 ? s2a.y : rr == 2 ? s2a.z : s2a.w;
        float db = rr == 0 ? s2b.x : rr == 1 ? s2b.y : rr == 2 ? s2b.z : s2b.w;
        if (lg == (li >> 2)) {
            *(LAS f2*)(lds + L_STAT + kh * 1024 + (16 * rq + li) * 8) = f2{s1a.x, da};
            *(LAS f2*)(lds + L_STAT + kh * 1024 + (64 + 16 * rq + li) * 8) = f2{s1b.x, db};
        }
        if (lg == (t & 3)) {
            LAS char* p = lds + L_RAW + (16 * rq + li) * 144 + kh * 64;
            if (t >> 2) { *(LAS u4*)(p) = xa[1]; *(LAS u4*)(p + 16) = xa[3]; *(LAS u4*)(p + 32) = xa[5]; *(LAS u4*)(p + 48) = xa[7]; }
            else { *(LAS u4*)(p) = xa[0]; *(LAS u4*)(p + 16) = xa[2]; *(LAS u4*)(p + 32) = xa[4]; *(LAS u4*)(p + 48) = xa[6]; }
        }
    }
    const u64 epoch = ((u64)(u32)__builtin_amdgcn_readfirstlane(ev.y) << 32) | (u32)__builtin_amdgcn_readfirstlane(ev.x);
    u64 tag = (epoch + 1) * 0x9E3779B97F4A7C15ull; tag ^= tag >> 29; tag *= 0xBF58476D1CE4E5B9ull; tag ^= tag >> 32;
    BAR();
    STAMP(2);
    const f2 stg = row_stats(lds, kh, 16 * rq + li, a.x + (size_t)((s2 ^ kh) + 32 * (16 * rq + li)) * 512);
    if (w < 4) {
        const int s1 = 16 * w + (lane >> 2);
        const f2 st = row_stats(lds, 0, s1, a.x + (size_t)(s2 + 32 * s1) * 512);
        f2 v[8];
#pragma unroll
        for (int c1 = 0; c1 < 8; c1++) {
            u32 d = *(LAS u32*)(lds + L_RAW + s1 * 144 + c1 * 16 + c2p * 4);
            u32 e = pkbf(bflo(d) - st.x, bfhi(d) - st.x);
            u32 f = pkbf(bflo(e) * st.y, bfhi(e) * st.y);
            v[c1] = f2{bflo(f), bfhi(f)};
        }
        dft8<false>(v, kc);
        LAS f2* xb1 = (LAS f2*)(lds + L_FFT) + w * 64 + lane;
#pragma unroll
        for (int q1 = 0; q1 < 8; q1++) { const int reg = (q1 < 4) ? q1 * 2 : (((8 - q1) & 3) * 2 + 1); xb1[reg * 288] = v[q1]; }
    } else {
        if (sig < 4 * NFB) {
            const int fb = sig >> 2, j = sig & 3;
#pragma unroll
            for (int tt = 0; tt < 2; tt++) {
                const int tcol = 2 * rq + tt;
                const u16* src = a.wd + (size_t)(fb * 32 + 4 * lg) * 512 + 128 * j + 64 * (li >> 3) + 8 * tcol + (li & 7);
                u32 e[8];
#pragma unroll
                for (int k = 0; k < 8; k++) e[k] = src[((k & 3) + 16 * (k >> 2)) * 512];
                u4 o = {e[0] | (e[1] << 16), e[2] | (e[3] << 16), e[4] | (e[5] << 16), e[6] | (e[7] << 16)};
                __builtin_amdgcn_raw_buffer_store_b128(o, rs, WS_WDP + ((tcol * NFB + fb) * 4 + j) * 1024 + lane * 16, 0, 16);
            }
        }
    }
    WAITVM(0);
    BAR();
    if (tid == 0) __hip_atomic_store((GAS u64*)(a.ws + WS_FLAG0) + pid, tag, __ATOMIC_RELAXED, __HIP_MEMORY_SCOPE_AGENT);
    u32 wA[8];
    u32 wS[4];
    int p3a1 = 0, p3a2 = 0, rowB = 0; bool rot = false, self = false;
    float snq[2][4], csq[2][4];
    const int cls2 = sig >> 3, m2 = sig & 7;
    if (w < 4) {
        LAS f2* reg = (LAS f2*)(lds + L_FFT) + (w * 2 + qb) * 288;
        f2 v[8];
#pragma unroll
        for (int k = 0; k < 8; k++) v[k] = reg[(lane & 31) + 32 * k];
        dft8<false>(v, kc);
#pragma unroll
        for (int k = 1; k < 8; k++) v[k] = cmul<false>(v[k], tw64[k]);
#pragma unroll
        for (int k = 0; k < 8; k++) reg[36 * k + (lane & 31)] = v[k];
        asm volatile("" ::: "memory");
#pragma unroll
        for (int k = 0; k < 8; k++) v[k] = reg[36 * l3 + 4 * k + c2p];
        dft8<false>(v, kc);
#pragma unroll
        for (int k = 0; k < 8; k++) v[k] = cmul<false>(v[k], twT[k]);
        asm volatile("" ::: "memory");
        *(LAS f2*)(lds + x3a[0]) = v[0];
        *(LAS f2*)(lds + x3a[1]) = v[1]; *(LAS f2*)(lds + x3a[1] + 1152) = v[2]; *(LAS f2*)(lds + x3a[1] + 2304) = v[3];
        *(LAS f2*)(lds + x3a[2]) = v[4];
        *(LAS f2*)(lds + x3a[3]) = v[5]; *(LAS f2*)(lds + x3a[3] - 1152) = v[6]; *(LAS f2*)(lds + x3a[3] - 2304) = v[7];
        asm volatile("" ::: "memory");
#pragma unroll
        for (int i = 0; i < 4; i++) {
            u4 o = *(LAS u4*)(lds + L_FFT + w * 4608 + (i * 8 + (lane >> 3)) * 144 + (lane & 7) * 16);
            __builtin_amdgcn_raw_buffer_store_b128(sanitize(o), rs, WS_F1 + pid * 16384 + w * 4096 + i * 1024 + lane * 16, 0, 16);
        }
        STAMP(3);
#pragma unroll
        for (int H = 0; H < 4; H++) __builtin_amdgcn_raw_buffer_store_b128(sent4, rs, WS_F2 + sig * 16384 + (2 * w + 8 * H) * 512 + lane * 16, 0, 16);
        const bool special = (cls2 == 0) && ((m2 & 3) == 0);
        int u1a, q1a, u1b, q1b;
        if (cls2 != 0) { u1a = cls2; q1a = m2; u1b = 64 - cls2; q1b = (8 - m2) & 7; }
        else if (special) { u1a = 0; u1b = 32; q1a = q1b = m2; }
        else { u1a = u1b = (m2 < 4) ? 0 : 32; q1a = m2; q1b = 8 - m2; }
        int sub0, vH0, sub1, vH1;
        if (!special) { sub0 = 0; vH0 = w; sub1 = 1; vH1 = (u1a == 0) ? ((4 - w) & 3) : 3 - w; }
        else { sub0 = sub1 = w >> 1; vH0 = w & 1; vH1 = (w == 0) ? 2 : (w == 1) ? 3 : (w == 2) ? 3 : 2; }
        rowB = L_FFT + ((qb ? (vH1 * 2 + sub1) : (vH0 * 2 + sub0)) * 36 + (lane & 31)) * 8;
        int y1 = lane >> 3, t1 = lane & 7, y2, t2, sA = sub0, hA = vH0, sB = sub1, hB = vH1;
        const bool q0 = (q1a == 0);
        if (special && w == 0) {
            const int k = lane & 31;
            if (qb) { hA = hB = 2; y1 = k >> 3; t1 = k & 7; y2 = 7 - y1; t2 = q0 ? ((8 - t1) & 7) : 7 - t1; }
            else {
                hA = hB = 0; const int vx = k & 3, y = k >> 2;
                if (!q0) { y1 = y; t1 = vx; y2 = (8 - y) & 7; t2 = 7 - vx; }
                else if (vx) { y1 = y; t1 = vx; y2 = (8 - y) & 7; t2 = 8 - vx; }
                else { t1 = t2 = (y >= 4) ? 4 : 0; const int yy = y & 3; if (yy) { y1 = yy; y2 = 8 - yy; } else { y1 = 0; y2 = 4; self = true; } }
            }
        } else {
            const int u1A = sA ? u1b : u1a;
            y2 = (u1A == 0 && hA == 0) ? ((8 - y1) & 7) : 7 - y1;
            t2 = q0 ? ((8 - t1) & 7) : 7 - t1;
        }
        rot = q0 && (t1 == 0);
        p3a1 = L_FFT + (y1 * 290 + (hA * 2 + sA) * 36 + t1 * 4) * 8;
        p3a2 = L_FFT + (y2 * 290 + (hB * 2 + sB) * 36 + t2 * 4) * 8;
        const int uA = (sA ? u1b : u1a) + 64 * (hA + 4 * y1), qA = (sA ? q1b : q1a) + 8 * t1;
        const int uB = (sB ? u1b : u1a) + 64 * (hB + 4 * y2), qB = (sB ? q1b : q1a) + 8 * t2;
#pragma unroll
        for (int k = 0; k < 4; k++) {
            wA[k] = (u32)a.wr[uA * 257 + qA + 64 * k] | ((u32)a.wi[uA * 257 + qA + 64 * k] << 16);
            wA[4 + k] = (u32)a.wr[uB * 257 + qB + 64 * k] | ((u32)a.wi[uB * 257 + qB + 64 * k] << 16);
        }
        wS[0] = (u32)a.wr[uA * 257 + 256] | ((u32)a.wi[uA * 257 + 256] << 16);
        wS[1] = (u32)a.wr[uB * 257 + 256] | ((u32)a.wi[uB * 257 + 256] << 16);
#pragma unroll
        for (int k = 0; k < 4; k++) {
            snq[0][k] = __builtin_amdgcn_sinf((float)(qA + 64 * k) * (1.0f / 512.0f)); csq[0][k] = __builtin_amdgcn_cosf((float)(qA + 64 * k) * (1.0f / 512.0f));
            snq[1][k] = __builtin_amdgcn_sinf((float)(qB + 64 * k) * (1.0f / 512.0f)); csq[1][k] = __builtin_amdgcn_cosf((float)(qB + 64 * k) * (1.0f / 512.0f));
        }
    }
    const int hbase = WS_H + (((s2 ^ kh) * 4 + rq) * NFB) * 1024 + lane * 16;
    const int fb2 = 32 + par * 6 + t;
    __builtin_amdgcn_raw_buffer_store_b128(sent4, rs, hbase + cg * 1024, 0, 16);
    __builtin_amdgcn_raw_buffer_store_b128(sent4, rs, hbase + (16 + cg) * 1024, 0, 16);
    if (nblk == 3) __builtin_amdgcn_raw_buffer_store_b128(sent4, rs, hbase + fb2 * 1024, 0, 16);

    const u32 trb = (u32)((8 * lg + (li >> 2)) * 64 + (lg & 1) * 32 + 8 * (li & 3) + kh * 8192);
    auto gemm1_block = [&](int blk, int fb, u32 q01, u32 q23) {
        f4 acc[2][4]; f4 acs = {0, 0, 0, 0};
#pragma unroll
        for (int i = 0; i < 2; i++)
#pragma unroll
            for (int j = 0; j < 4; j++) acc[i][j] = f4{0, 0, 0, 0};
#pragma unroll
        for (int half = 0; half < 2; half++) {
            LAS char* p0 = lds + (half ? q23 : q01) + trb;
            LAS char* p1 = lds + (half ? q23 : q01) + (trb ^ 32);
            LAS char* pc = lds + (half ? q23 : q01) + (trb ^ ((rq & 1) * 32)) + (rq >> 1) * 2048;
#pragma unroll
            for (int k4 = 0; k4 < 4; k4++) {
                const int ks = half * 4 + k4, off = (k4 >> 1) * 16384 + (k4 & 1) * 4096;
                u4 g0 = trfrag(p0 + off), g1 = trfrag(p1 + off), u0 = trfrag(p0 + off + 2048), u1 = trfrag(p1 + off + 2048), cf = trfrag(pc + off);
                acc[0][0] = mfma(g0, xa[ks], acc[0][0]); acc[1][0] = mfma(g0, xb[ks], acc[1][0]);
                acc[0][1] = mfma(g1, xa[ks], acc[0][1]); acc[1][1] = mfma(g1, xb[ks], acc[1][1]);
                acc[0][2] = mfma(u0, xa[ks], acc[0][2]); acc[1][2] = mfma(u0, xb[ks], acc[1][2]);
                acc[0][3] = mfma(u1, xa[ks], acc[0][3]); acc[1][3] = mfma(u1, xb[ks], acc[1][3]);
                acs = mfma(cf, ones, acs);
            }
            if (half == 0) BAR();
        }
        LAS f4* xs = (LAS f4*)(lds + q01 + w * 4096) + lane;
        LAS f4* xr = (LAS f4*)(lds + q01 + (w ^ 4) * 4096) + lane;
        f4 mine[4];
#pragma unroll
        for (int j = 0; j < 4; j++) { xs[j * 64] = kh ? acc[0][j] : acc[1][j]; mine[j] = kh ? acc[1][j] : acc[0][j]; }
        if (li == 0) *(LAS f4*)(lds + L_CS + (blk & 1) * 512 + (kh * 4 + rq) * 64 + lg * 16) = acs;
        BAR();
        u32 hw[4];
#pragma unroll
        for (int c = 0; c < 2; c++) {
            f4 G = mine[c] + xr[c * 64], U = mine[2 + c] + xr[(2 + c) * 64];
            LAS char* cs = lds + L_CS + (blk & 1) * 512 + lg * 16;
            f4 cg_ = *(LAS f4*)(cs + c * 64) + *(LAS f4*)(cs + 256 + c * 64);
            f4 cu_ = *(LAS f4*)(cs + (2 + c) * 64) + *(LAS f4*)(cs + 256 + (2 + c) * 64);
            float hv[4];
#pragma unroll
            for (int e = 0; e < 4; e++) {
                float g = (G[e] - stg.x * cg_[e]) * stg.y, u = (U[e] - stg.x * cu_[e]) * stg.y;
                float z = g * (-2.3022082f - 0.10294324f * g * g);
                float ge = g * __builtin_amdgcn_rcpf(1.0f + __builtin_amdgcn_exp2f(z));
                hv[e] = ge * u;
            }
            hw[2 * c] = pkbf(hv[0], hv[1]); hw[2 * c + 1] = pkbf(hv[2], hv[3]);
        }
        __builtin_amdgcn_raw_buffer_store_b128(sanitize(u4{hw[0], hw[1], hw[2], hw[3]}), rs, hbase + fb * 1024, 0, 16);
    };
    STAMP(4);
    gemm1_block(0, cg, L_RING, L_RING + 32768);
    STAMP(5);
    WAITVM(1);
    BAR();
    if (tid == 0) __hip_atomic_store((GAS u64*)(a.ws + WS_FLAG1) + b, tag, __ATOMIC_RELAXED, __HIP_MEMORY_SCOPE_AGENT);
    if (nblk == 3) {
#pragma unroll
        for (int qd = 0; qd < 4; qd++) dma_quarter(fb2, qd, L_RING + qd * 16384);
    }
    gemm1_block(1, 16 + cg, L_RING + 65536, L_RING + 65536 + 32768);
    STAMP(6);
    {
        f2 v[8], tw0[4], tw1[8], al[8], be[8];
        const int rb = qb, r3 = 2 * rq + rb;
        if (w < 4) {
            {
                const int pp = ((lane >> 4) * 8 + 2 * w + ((lane >> 3) & 1)) * 8 + (lane & 7);
                for (int spins = 0; spins < SPIN_CAP; spins++) {
                    u64 f = __hip_atomic_load((GAS u64*)(a.ws + WS_FLAG0) + pp, __ATOMIC_RELAXED, __HIP_MEMORY_SCOPE_AGENT);
                    if (__all(f == tag)) break;
                }
                __builtin_amdgcn_fence(__ATOMIC_ACQUIRE, "wavefront");
            }
            const int wq2 = (m2 < 4) ? m2 : ((8 - m2) & 3), pos1 = wq2 * 64 + cls2 * 2 + (m2 >> 2);
            const int f1off = WS_F1 + (r3 * 8 + l3) * 16384 + pos1 * 64 + c2p * 16;
            u4 d[4];
#pragma unroll
            for (int H = 0; H < 4; H++) d[H] = __builtin_amdgcn_raw_buffer_load_b128(rs, f1off + H * (64 * 16384), 0, 16);
#pragma unroll
            for (int k = 1; k < 4; k++) tw0[k] = twid(r3 * k, 31, 1.0f / 32.0f);
#pragma unroll
            for (int k = 1; k < 8; k++) tw1[k] = twid(c2p * k, 31, 1.0f / 32.0f);
            {
                u32 wB[8];
#pragma unroll
                for (int k = 0; k < 4; k++) { wB[k] = wA[4 + 3 - k]; wB[4 + k] = wA[3 - k]; }
                if (m2 == 0) {
#pragma unroll
                    for (int k = 0; k < 4; k++) {
                        u32 n1 = rot ? wA[(4 - k) & 3] : wA[3 - k], n2 = rot ? wA[4 + ((4 - k) & 3)] : wA[4 + 3 - k];
                        wB[k] = self ? n1 : n2; wB[4 + k] = self ? n2 : n1;
                    }
                }
                const float sc = 0.5f / 524288.0f;
#pragma unroll
                for (int e = 0; e < 8; e++) {
                    f2 W1 = f2{bflo(wA[e]), bfhi(wA[e])}, W2 = f2{bflo(wB[e]), -bfhi(wB[e])};
                    if ((e & 3) == 0 && m2 == 0) {
                        u32 own = wS[e >> 2], oth = self ? own : wS[1 - (e >> 2)];
                        f2 S0 = (W1 + W2) * 0.5f, S1 = f2{bflo(own) + bflo(oth), bfhi(own) - bfhi(oth)} * 0.5f;
                        W1 = rot ? S0 : W1; W2 = rot ? S1 : W2;
                    }
                    f2 P = (W1 + W2) * sc, M = (W1 - W2) * sc;
                    al[e] = P - M * snq[e >> 2][e & 3];
                    be[e] = f2{-M.y, M.x} * csq[e >> 2][e & 3];
                }
            }
            for (int spins = 0; spins < SPIN_CAP; spins++) {
                u32 mx = max(max(max4(d[0]), max4(d[1])), max(max4(d[2]), max4(d[3])));
                if (!__any(mx == SENT)) break;
#pragma unroll
                for (int H = 0; H < 4; H++) d[H] = __builtin_amdgcn_raw_buffer_load_b128(rs, f1off + H * (64 * 16384), 0, 16);
                asm volatile("" ::: "memory");
            }
            STAMP(7);
            f2 e0[4], e1[4];
#pragma unroll
            for (int H = 0; H < 4; H++) { e0[H] = f2{__uint_as_float(d[H].x), __uint_as_float(d[H].y)}; e1[H] = f2{__uint_as_float(d[H].z), __uint_as_float(d[H].w)}; }
            dft4<false>(e0[0], e0[1], e0[2], e0[3], kc); dft4<false>(e1[0], e1[1], e1[2], e1[3], kc);
            LAS char* area = lds + L_FFT + w * 4640;
#pragma unroll
            for (int k = 0; k < 4; k++) {
                if (k) { e0[k] = cmul<false>(e0[k], tw0[k]); e1[k] = cmul<false>(e1[k], tw0[k]); }
                *(LAS f4*)(area + (l3 * 72 + 2 * c2p + 8 * k + 32 * rb) * 8) = f4{e0[k].x, e0[k].y, e1[k].x, e1[k].y};
            }
            asm volatile("" ::: "memory");
#pragma unroll
            for (int k = 0; k < 8; k++) v[k] = *(LAS f2*)(area + (k * 72 + lane) * 8);
            dft8<false>(v, kc);
#pragma unroll
            for (int k = 1; k < 8; k++) v[k] = cmul<false>(v[k], tw1[k]);
            asm volatile("" ::: "memory");
            LAS char* pb = lds + L_FFT + (r3 * 290 + l3 * 36 + c2p) * 8;
#pragma unroll
            for (int k = 0; k < 8; k++) *(LAS f2*)(pb + k * 32) = v[k];
        }
        BAR();
        if (w < 4) {
#pragma unroll
            for (int k = 0; k < 8; k++) v[k] = *(LAS f2*)(lds + rowB + k * 2320);
            dft8<false>(v, kc);
#pragma unroll
            for (int k = 0; k < 8; k++) *(LAS f2*)(lds + rowB + k * 2320) = v[k];
            asm volatile("" ::: "memory");
            f4 a0 = *(LAS f4*)(lds + p3a1), a1 = *(LAS f4*)(lds + p3a1 + 16), b0 = *(LAS f4*)(lds + p3a2), b1 = *(LAS f4*)(lds + p3a2 + 16);
            f2 X[8] = {a0.xy, a0.zw, a1.xy, a1.zw, b0.xy, b0.zw, b1.xy, b1.zw};
            dft4<false>(X[0], X[1], X[2], X[3], kc); dft4<false>(X[4], X[5], X[6], X[7], kc);
            f2 Y[8];
#pragma unroll
            for (int k = 0; k < 4; k++) { Y[k] = X[4 + 3 - k]; Y[4 + k] = X[3 - k]; }
            if (m2 == 0) {
#pragma unroll
                for (int k = 0; k < 4; k++) {
                    f2 n1 = rot ? X[(4 - k) & 3] : X[3 - k], n2 = rot ? X[4 + ((4 - k) & 3)] : X[4 + 3 - k];
                    Y[k] = self ? n1 : n2; Y[4 + k] = self ? n2 : n1;
                }
            }
            f2 O[8];
#pragma unroll
            for (int e = 0; e < 8; e++) O[e] = cfmac_conj(cmul<false>(X[e], al[e]), be[e], Y[e]);
            dft4<true>(O[0], O[1], O[2], O[3], kc); dft4<true>(O[4], O[5], O[6], O[7], kc);
            *(LAS f4*)(lds + p3a1) = f4{O[0].x, O[0].y, O[1].x, O[1].y}; *(LAS f4*)(lds + p3a1 + 16) = f4{O[2].x, O[2].y, O[3].x, O[3].y};
            *(LAS f4*)(lds + p3a2) = f4{O[4].x, O[4].y, O[5].x, O[5].y}; *(LAS f4*)(lds + p3a2 + 16) = f4{O[6].x, O[6].y, O[7].x, O[7].y};
            asm volatile("" ::: "memory");
#pragma unroll
            for (int k = 0; k < 8; k++) v[k] = *(LAS f2*)(lds + rowB + k * 2320);
            dft8<true>(v, kc);
#pragma unroll
            for (int k = 0; k < 8; k++) *(LAS f2*)(lds + rowB + k * 2320) = v[k];
        } else if (w == 4) {
            for (int spins = 0; spins < SPIN_CAP; spins++) {
                bool ok = true;
#pragma unroll
                for (int k = 0; k < 4; k++) ok &= __hip_atomic_load((GAS u64*)(a.ws + WS_FLAG1) + lane + 64 * k, __ATOMIC_RELAXED, __HIP_MEMORY_SCOPE_AGENT) == tag;
                if (__all(ok)) break;
            }
            __builtin_amdgcn_fence(__ATOMIC_ACQUIRE, "agent");
            if (b == 0 && lane == 0) __hip_atomic_store((GAS u64*)a.ws, epoch + 1, __ATOMIC_RELAXED, __HIP_MEMORY_SCOPE_AGENT);
        }
        WAITVM(0);
        BAR();
        if (w < 4) {
            LAS char* pb = lds + L_FFT + (r3 * 290 + l3 * 36 + c2p) * 8;
#pragma unroll
            for (int k = 0; k < 8; k++) v[k] = *(LAS f2*)(pb + k * 32);
#pragma unroll
            for (int k = 1; k < 8; k++) v[k] = cmul<true>(v[k], tw1[k]);
            dft8<true>(v, kc);
            asm volatile("" ::: "memory");
            LAS char* area = lds + L_FFT + w * 4640;
#pragma unroll
            for (int k = 0; k < 8; k++) *(LAS f2*)(area + (k * 72 + lane) * 8) = v[k];
            asm volatile("" ::: "memory");
            f2 e0[4], e1[4];
#pragma unroll
            for (int k = 0; k < 4; k++) {
                f4 q = *(LAS f4*)(area + (l3 * 72 + 2 * c2p + 8 * k + 32 * rb) * 8);
                e0[k] = q.xy; e1[k] = q.zw;
                if (k) { e0[k] = cmul<true>(e0[k], tw0[k]); e1[k] = cmul<true>(e1[k], tw0[k]); }
            }
            dft4<true>(e0[0], e0[1], e0[2], e0[3], kc); dft4<true>(e1[0], e1[1], e1[2], e1[3], kc);
#pragma unroll
            for (int H = 0; H < 4; H++) {
                u4 o = {__float_as_uint(e0[H].x), __float_as_uint(e0[H].y), __float_as_uint(e1[H].x), __float_as_uint(e1[H].y)};
                __builtin_amdgcn_raw_buffer_store_b128(sanitize(o), rs, WS_F2 + sig * 16384 + (2 * w + 8 * H) * 512 + lane * 16, 0, 16);
            }
        }
    }
    STAMP(8);
    const char* wdp = (const char*)a.ws + WS_WDP + (size_t)t * NFB * 4096;
    auto dma_wd = [&](int p0, int n, u32 ldsbase) {
        for (int i = w; i < 4 * n; i += 8) dma16((u32)lane * 16, wdp + (size_t)p0 * 4096 + i * 1024, ldsbase + i * 1024);
    };
    dma_wd(0, 16, L_RING + 65536);
    if (nblk == 3) gemm1_block(2, fb2, L_RING, L_RING + 32768);
    STAMP(9);
    BAR();
    dma_wd(16, 16, L_RING);
    f4 acc2[4];
#pragma unroll
    for (int j = 0; j < 4; j++) acc2[j] = f4{0, 0, 0, 0};
    const int hsrc = WS_H + ((s2 * 4 + rq) * NFB + kh) * 1024 + lane * 16;
    auto gemm2_part = [&](auto NC, int p0, int nvalid, u32 ldsbase) {
        constexpr int N = decltype(NC)::value;
        u4 hf[N];
        for (int spins = 0; spins < SPIN_CAP; spins++) {
            u32 mx = 0;
#pragma unroll
            for (int i = 0; i < N; i++) hf[i] = __builtin_amdgcn_raw_buffer_load_b128(rs, hsrc + (p0 + 2 * (i < nvalid ? i : 0)) * 1024, 0, 16);
#pragma unroll
            for (int i = 0; i < N; i++) mx = max(mx, max4(hf[i]));
            if (!__any(mx == SENT)) break;
            asm volatile("" ::: "memory");
        }
        WAITVM(0);
        BAR();
#pragma unroll
        for (int i = 0; i < N; i++) {
            if (i < nvalid) {
                LAS u4* p = (LAS u4*)(lds + ldsbase + (kh + 2 * i) * 4096) + lane;
#pragma unroll
                for (int j = 0; j < 4; j++) acc2[j] = mfma(p[j * 64], hf[i], acc2[j]);
            }
        }
    };
    gemm2_part(std::integral_constant<int, 8>{}, 0, 8, L_RING + 65536);
    gemm2_part(std::integral_constant<int, 8>{}, 16, 8, L_RING);
    dma_wd(32, 11, L_RING + 65536);
    STAMP(10);
    u2 xo[2];
    const size_t orow = (size_t)(s2 + 32 * (16 * rq + li)) * 512 + 8 * t + 4 * (lg & 1);
#pragma unroll
    for (int jj = 0; jj < 2; jj++) xo[jj] = *(const u2*)(a.x + orow + 64 * (2 * (2 * kh + jj) + (lg >> 1)));
    {
        f2 v[8];
        if (w < 4) {
            u4 d[4]; int off[4];
#pragma unroll
            for (int i = 0; i < 4; i++) {
                const int lp = i * 16 + (lane >> 2), cls = lp >> 1, mbit = lp & 1;
                const int m = mbit ? (w == 0 ? 4 : 8 - w) : w;
                off[i] = WS_F2 + (cls * 8 + m) * 16384 + pid * 64 + c2p * 16;
            }
            for (int spins = 0; spins < SPIN_CAP; spins++) {
#pragma unroll
                for (int i = 0; i < 4; i++) d[i] = __builtin_amdgcn_raw_buffer_load_b128(rs, off[i], 0, 16);
                u32 mx = max(max(max4(d[0]), max4(d[1])), max(max4(d[2]), max4(d[3])));
                if (!__any(mx == SENT)) break;
                asm volatile("" ::: "memory");
            }
            STAMP(11);
#pragma unroll
            for (int i = 0; i < 4; i++) *(LAS u4*)(lds + L_FFT + w * 4608 + (i * 8 + (lane >> 3)) * 144 + (lane & 7) * 16) = d[i];
            asm volatile("" ::: "memory");
            v[0] = *(LAS f2*)(lds + x3a[0]);
            v[1] = *(LAS f2*)(lds + x3a[1]); v[2] = *(LAS f2*)(lds + x3a[1] + 1152); v[3] = *(LAS f2*)(lds + x3a[1] + 2304);
            v[4] = *(LAS f2*)(lds + x3a[2]);
            v[5] = *(LAS f2*)(lds + x3a[3]); v[6] = *(LAS f2*)(lds + x3a[3] - 1152); v[7] = *(LAS f2*)(lds + x3a[3] - 2304);
#pragma unroll
            for (int k = 0; k < 8; k++) v[k] = cmul<true>(v[k], twT[k]);
            dft8<true>(v, kc);
#pragma unroll
            for (int k = 1; k < 8; k++) v[k] = cmul<true>(v[k], tw64[k]);
            asm volatile("" ::: "memory");
            LAS f2* reg = (LAS f2*)(lds + L_FFT) + (w * 2 + qb) * 288;
#pragma unroll
            for (int k = 0; k < 8; k++) reg[36 * l3 + 4 * k + c2p] = v[k];
            asm volatile("" ::: "memory");
#pragma unroll
            for (int k = 0; k < 8; k++) v[k] = reg[36 * k + (lane & 31)];
            dft8<true>(v, kc);
            asm volatile("" ::: "memory");
#pragma unroll
            for (int k = 0; k < 8; k++) reg[(lane & 31) + 32 * k] = v[k];
        }
        BAR();
        if (w < 4) {
            LAS f2* xb1 = (LAS f2*)(lds + L_FFT) + w * 64 + lane;
#pragma unroll
            for (int q1 = 0; q1 < 8; q1++) { const int reg = (q1 < 4) ? q1 * 2 : (((8 - q1) & 3) * 2 + 1); v[q1] = xb1[reg * 288]; }
            dft8<true>(v, kc);
            const int s1 = 16 * w + (lane >> 2);
#pragma unroll
            for (int c1 = 0; c1 < 8; c1++) *(LAS u32*)(lds + L_RAW + s1 * 144 + c1 * 16 + c2p * 4) = pkbf(v[c1].x, v[c1].y);
        }
    }
    STAMP(12);
    gemm2_part(std::integral_constant<int, 6>{}, 32, 6 - kh, L_RING + 65536);
    STAMP(13);
    {
        LAS f4* xs = (LAS f4*)(lds + L_RING + w * 2048) + lane;
        LAS f4* xr = (LAS f4*)(lds + L_RING + (w ^ 4) * 2048) + lane;
        f4 mine[2];
#pragma unroll
        for (int jj = 0; jj < 2; jj++) { xs[jj * 64] = kh ? acc2[jj] : acc2[2 + jj]; mine[jj] = kh ? acc2[2 + jj] : acc2[jj]; }
        BAR();
#pragma unroll
        for (int jj = 0; jj < 2; jj++) {
            const int c1 = 2 * (2 * kh + jj) + (lg >> 1);
            f4 o = mine[jj] + xr[jj * 64];
            u2 sv = *(LAS u2*)(lds + L_RAW + (16 * rq + li) * 144 + c1 * 16 + (lg & 1) * 8);
            float s[4] = {bflo(sv.x), bfhi(sv.x), bflo(sv.y), bfhi(sv.y)};
            float xv[4] = {bflo(xo[jj].x), bfhi(xo[jj].x), bflo(xo[jj].y), bfhi(xo[jj].y)};
            float res[4];
#pragma unroll
            for (int e = 0; e < 4; e++) {
                float ov = rbf(o[e]);
                float g = rbf(ov * __builtin_amdgcn_rcpf(1.0f + __builtin_amdgcn_exp2f(-1.44269504f * ov)));
                res[e] = xv[e] + rbf(s[e] * g);
            }
            *(u2*)(a.out + orow + 64 * c1) = u2{pkbf(res[0], res[1]), pkbf(res[2], res[3])};
        }
    }
    STAMP(14);
}

extern "C" void kernel_launch(void* const* d_in, const int* in_sizes, int n_in,
                              void* d_out, int out_size, void* d_ws, size_t ws_size,
                              hipStream_t stream) {
    Args a;
    a.x = (const u16*)d_in[0]; a.wr = (const u16*)d_in[1]; a.wi = (const u16*)d_in[2];
    a.wg = (const u16*)d_in[4]; a.wu = (const u16*)d_in[5]; a.wd = (const u16*)d_in[6];
    a.out = (u16*)d_out; a.ws = (unsigned char*)d_ws;
    (void)hipFuncSetAttribute((const void*)fused, hipFuncAttributeMaxDynamicSharedMemorySize, L_END);
    fused<<<256, 512, L_END, stream>>>(a);
}
```

```cpp
#include <hip/hip_runtime.h>
#include <stdint.h>
#include <type_traits>

typedef unsigned short u16;
typedef unsigned u32;
typedef unsigned long long u64;
typedef float f2 __attribute__((ext_vector_type(2)));
typedef float f4 __attribute__((ext_vector_type(4)));
typedef u32 u4 __attribute__((ext_vector_type(4)));
typedef u32 u2 __attribute__((ext_vector_type(2)));
typedef short s4 __attribute__((ext_vector_type(4)));
typedef __bf16 bf8 __attribute__((ext_vector_type(8)));
typedef __bf16 bf2 __attribute__((ext_vector_type(2)));
#define LAS __attribute__((address_space(3)))
#define GAS __attribute__((address_space(1)))
#define DEV __device__ __forceinline__
template <int N> using IC = std::integral_constant<int, N>;

constexpr int NFB = 43;
constexpr int WS_FLAG0 = 4096, WS_FLAG1 = 8192;
constexpr int WS_F1 = 1 << 20, WS_F2 = WS_F1 + (4 << 20), WS_H = WS_F2 + (4 << 20);
constexpr int WS_WDP = WS_H + 32 * 4 * NFB * 1024;
constexpr int WS_WP = WS_WDP + 8 * NFB * 4 * 1024;
constexpr int WS_STAMP = WS_WP + (2 << 20);
constexpr int WS_BYTES = WS_STAMP + (1 << 20);
constexpr int PG = 8192;
constexpr int L_SET1 = 8 * PG;
constexpr int L_FFT = 16 * PG;
constexpr int L_SMALL = 19 * PG;
constexpr int L_PROG = L_SMALL;
constexpr int L_STAT = L_SMALL + 64;
constexpr int L_CS = L_SMALL + 2048;
constexpr int L_X0 = L_FFT + 18688, L_X1 = L_SMALL + 3072;
constexpr int L_S = 14 * PG;
constexpr int L_END = 20 * PG;
constexpr u32 SENT = 0xFFFFFFFFu;
constexpr int SPIN_CAP = 1 << 16;

struct Args { const u16* x; const u16* wr; const u16* wi; const u16* wg; const u16* wu; const u16* wd; u16* out; unsigned char* ws; };

#define BAR() asm volatile("s_waitcnt lgkmcnt(0)\n\ts_barrier" ::: "memory")
#define WAITVM(n) asm volatile("s_waitcnt vmcnt(" #n ")" ::: "memory")
#define FENCE() asm volatile("" ::: "memory")
#define SCHED() __builtin_amdgcn_sched_barrier(0)
#define SGB(mask, n) __builtin_amdgcn_sched_group_barrier(mask, n, 0)
#define STAMP(i)

DEV f2 pkfma(f2 a, f2 b, f2 c) { return __builtin_elementwise_fma(a, b, c); }
struct KC { f2 pm, rpm; };
DEV f2 addmi(f2 a, f2 b, const KC& k) { return pkfma(b.yx, k.pm, a); }
DEV f2 addpi(f2 a, f2 b, const KC& k) { return pkfma(b.yx, k.pm.yx, a); }
template <bool INV> DEV void dft4(f2& a0, f2& a1, f2& a2, f2& a3, const KC& k) {
    f2 t0 = a0 + a2, t1 = a0 - a2, t2 = a1 + a3, t3 = a1 - a3;
    a0 = t0 + t2; a2 = t0 - t2;
    if (INV) { a1 = addpi(t1, t3, k); a3 = addmi(t1, t3, k); } else { a1 = addmi(t1, t3, k); a3 = addpi(t1, t3, k); }
}
template <bool INV> DEV void dft8(f2 (&v)[8], const KC& k) {
    f2 a0 = v[0] + v[4], a4 = v[0] - v[4], a1 = v[1] + v[5], a5 = v[1] - v[5];
    f2 a2 = v[2] + v[6], a6 = v[2] - v[6], a3 = v[3] + v[7], a7 = v[3] - v[7];
    dft4<INV>(a0, a1, a2, a3, k);
    v[0] = a0; v[2] = a1; v[4] = a2; v[6] = a3;
    f2 b1, b3, t0, t1;
    if (INV) { b1 = addpi(a5, a5, k); b3 = addpi(-a7, a7, k); t0 = addpi(a4, a6, k); t1 = addmi(a4, a6, k); }
    else { b1 = addmi(a5, a5, k); b3 = addmi(-a7, a7, k); t0 = addmi(a4, a6, k); t1 = addpi(a4, a6, k); }
    f2 t2 = b1 + b3, t3 = b1 - b3;
    v[1] = pkfma(t2, k.rpm.xx, t0); v[5] = pkfma(t2, k.rpm.yy, t0);
    if (INV) { v[3] = pkfma(t3.yx, k.rpm.yx, t1); v[7] = pkfma(t3.yx, k.rpm, t1); }
    else { v[3] = pkfma(t3.yx, k.rpm, t1); v[7] = pkfma(t3.yx, k.rpm.yx, t1); }
}
template <bool CONJ> DEV f2 cmul(f2 a, f2 w) {
    f2 r;
    if (CONJ) asm("v_pk_mul_f32 %0, %1, %2 op_sel:[1,1] op_sel_hi:[0,1]\n\tv_pk_fma_f32 %0, %1, %2, %0 op_sel_hi:[1,0,1] neg_hi:[0,0,1]" : "=&v"(r) : "v"(a), "v"(w));
    else asm("v_pk_mul_f32 %0, %1, %2 op_sel:[1,1] op_sel_hi:[0,1]\n\tv_pk_fma_f32 %0, %1, %2, %0 op_sel_hi:[1,0,1] neg_lo:[0,0,1]" : "=&v"(r) : "v"(a), "v"(w));
    return r;
}
DEV f2 cfmac_conj(f2 t, f2 beta, f2 B) {
    asm("v_pk_fma_f32 %0, %2, %1, %0 op_sel_hi:[0,1,1]\n\tv_pk_fma_f32 %0, %2, %1, %0 op_sel:[1,1,0] op_sel_hi:[1,0,1] neg_hi:[0,1,0]" : "+v"(t) : "v"(beta), "v"(B));
    return t;
}
DEV f2 cis(float r) { return f2{__builtin_amdgcn_cosf(r), __builtin_amdgcn_sinf(r)}; }
DEV float bflo(u32 v) { return __uint_as_float(v << 16); }
DEV float bfhi(u32 v) { return __uint_as_float(v & 0xFFFF0000u); }
DEV f2 bfup(u32 v) { return f2{bflo(v), bfhi(v)}; }
DEV u32 pkbf(f2 v) { bf2 r; r.x = (__bf16)v.x; r.y = (__bf16)v.y; return __builtin_bit_cast(u32, r); }
DEV float rbf(float f) { return __uint_as_float(((u32)__builtin_bit_cast(u16, (__bf16)f)) << 16); }
DEV f4 mfma(u4 a, u4 b, f4 c) { return __builtin_amdgcn_mfma_f32_16x16x32_bf16(__builtin_bit_cast(bf8, a), __builtin_bit_cast(bf8, b), c, 0, 0, 0); }
DEV u4 sanitize(u4 v) { return u4{min(v.x, 0xFFFFFFFEu), min(v.y, 0xFFFFFFFEu), min(v.z, 0xFFFFFFFEu), min(v.w, 0xFFFFFFFEu)}; }
DEV u32 max4(u4 v) { return max(max(v.x, v.y), max(v.z, v.w)); }
DEV void st128(u4 v, __amdgpu_buffer_rsrc_t rs, u32 voff, int soff) {
    __builtin_amdgcn_raw_buffer_store_b128(v, rs, voff, soff, 16);
    asm volatile("s_nop 1" :: "v"(v) : "memory");
}
#define CLR128(rs, voff, soff) st128(sent4, rs, voff, soff)
template <int OFF2> DEV u4 trfrag2(LAS char* p) {
    s4 r0 = __builtin_amdgcn_ds_read_tr16_b64_v4i16((LAS s4*)p);
    s4 r1 = __builtin_amdgcn_ds_read_tr16_b64_v4i16((LAS s4*)(p + OFF2));
    u2 x0 = __builtin_bit_cast(u2, r0), x1 = __builtin_bit_cast(u2, r1);
    return u4{x0.x, x0.y, x1.x, x1.y};
}
template <int LI, int GI> DEV void dma_add(u32 vb, const void* sbase, u32 sl) {
    u32 tmp;
    asm volatile("s_add_u32 m0, %3, %4\n\tv_add_u32 %0, %5, %1\n\tglobal_load_lds_dwordx4 %0, %2" : "=&v"(tmp) : "v"(vb), "s"(sbase), "s"(sl), "i"(LI), "i"(GI) : "memory", "scc");
}
template <int LI, int GI> DEV void dma_xor(u32 vb, const void* sbase, u32 sl) {
    u32 tmp;
    asm volatile("s_add_u32 m0, %3, %4\n\tv_xor_b32 %0, %5, %1\n\tglobal_load_lds_dwordx4 %0, %2" : "=&v"(tmp) : "v"(vb), "s"(sbase), "s"(sl), "i"(LI), "i"(GI) : "memory", "scc");
}
template <int LI, int GI> DEV void dma4k_c(u32 vb, const void* sbase, u32 sl) {
    u32 tmp;
    asm volatile("s_add_u32 m0, %3, %4\n\tv_add_u32 %0, %5, %1\n\tglobal_load_lds_dwordx4 %0, %2 sc1\n\tglobal_load_lds_dwordx4 %0, %2 offset:1024 sc1\n\t"
                 "global_load_lds_dwordx4 %0, %2 offset:2048 sc1\n\tglobal_load_lds_dwordx4 %0, %2 offset:3072 sc1"
                 : "=&v"(tmp) : "v"(vb), "s"(sbase), "s"(sl), "i"(LI), "i"(GI) : "memory", "scc");
}
template <int OFF> DEV void ldg_u16(u32& d, u32 vo, const void* sb) { asm volatile("global_load_ushort %0, %1, %2 offset:%3" : "=v"(d) : "v"(vo), "s"(sb), "i"(OFF) : "memory"); }
template <int OFF> DEV void ldg_u32(u32& d, u32 vo, const void* sb) { asm volatile("global_load_dword %0, %1, %2 offset:%3" : "=v"(d) : "v"(vo), "s"(sb), "i"(OFF) : "memory"); }
DEV void lds_wait(u32 addr, u32 v) {
    u32 tmp, cnt;
    asm volatile("s_mov_b32 %1, 0x80000\n"
                 "1:\n\tds_read_b32 %0, %2\n\ts_sub_u32 %1, %1, 1\n\ts_waitcnt lgkmcnt(0)\n\ts_cbranch_scc1 2f\n\tv_cmp_gt_u32 vcc, %3, %0\n\ts_cbranch_vccnz 1b\n2:"
                 : "=&v"(tmp), "=&s"(cnt) : "v"(addr), "s"(v) : "memory", "vcc", "scc");
}
template <int N, typename F> DEV void sfor(F&& f) { if constexpr (N > 0) { sfor<N - 1>(f); f(IC<N - 1>{}); } }

DEV void pin9(f4 (&x)[4], f4 (&y)[4], f4& z) {
    asm volatile("" : "+v"(x[0]), "+v"(x[1]), "+v"(x[2]), "+v"(x[3]), "+v"(y[0]), "+v"(y[1]), "+v"(y[2]), "+v"(y[3]), "+v"(z));
}
DEV u4 geglu8(const f4 (&acc)[4], LAS char* cs, f2 st) {
    u32 hw[4];
    const f2 nm = f2{-st.x, -st.x}, ri = f2{st.y, st.y};
#pragma unroll
    for (int c = 0; c < 2; c++) {
        f4 cg_ = *(LAS f4*)(cs + c * 64), cu_ = *(LAS f4*)(cs + (2 + c) * 64);
#pragma unroll
        for (int e = 0; e < 2; e++) {
            f2 g = pkfma(e ? cg_.zw : cg_.xy, nm, e ? acc[c].zw : acc[c].xy) * ri;
            f2 u = pkfma(e ? cu_.zw : cu_.xy, nm, e ? acc[2 + c].zw : acc[2 + c].xy) * ri;
            f2 z = g * pkfma(g * g, f2{-0.10294324f, -0.10294324f}, f2{-2.3022082f, -2.3022082f});
            f2 d = f2{__builtin_amdgcn_exp2f(z.x), __builtin_amdgcn_exp2f(z.y)} + f2{1.f, 1.f};
            f2 r = f2{__builtin_amdgcn_rcpf(d.x), __builtin_amdgcn_rcpf(d.y)};
            hw[2 * c + e] = pkbf(g * r * u);
        }
    }
    return sanitize(u4{hw[0], hw[1], hw[2], hw[3]});
}

enum { A_XL = 1, A_XC, A_P1, A_XB, A_S0, A_CS, A_WB, A_PB, A_D1, A_G1, A_D2, A_F3, A_D3 };
enum { B_XL = 1, B_W0, B_W1, B_W2, B_S0, B_W3, B_CS, B_FL, B_E1, B_E2, B_PB, B_G1 };

__global__ void __launch_bounds__(512, 2) fused(Args a) {
    extern __shared__ __attribute__((aligned(16))) char smem[];
    LAS char* lds = (LAS char*)smem;
    const int tid = threadIdx.x, lane = tid & 63, w = __builtin_amdgcn_readfirstlane(tid >> 6);
    const int b = blockIdx.x, xcd = b & 7, r = b >> 3, t = r & 7, par = xcd & 1, rg = (xcd >> 1) * 4 + (r >> 3);
    const int s2 = 2 * rg + par, cg = par * 8 + t, sig = xcd * 32 + r, pid = s2 * 8 + t;
    const int rq = w & 3, hf = w >> 2;
    const u32 l16 = lane * 16;
    {
        const char* xs = (const char*)a.x + (size_t)((s2 ^ hf) + 512 * rq) * 1024;
        const u32 sl = (u32)(w * 16384);
        sfor<16>([&](auto I) { constexpr int i = I; dma_xor<i * 1024, i * 32768 + i * 16>(l16, xs, sl); });
    }
    const int li = lane & 15, lg = lane >> 4;
    const int c2p = lane & 3, l3 = (lane >> 2) & 7, qb = lane >> 5;
    const __amdgpu_buffer_rsrc_t rs = __builtin_amdgcn_make_buffer_rsrc(a.ws, 0, WS_BYTES, 0x00020000);
    KC kc; kc.pm = f2{1.f, -1.f}; kc.rpm = f2{0.70710678118654752f, -0.70710678118654752f};
    asm volatile("" : "+s"(kc.pm), "+s"(kc.rpm));
    const u4 sent4 = {SENT, SENT, SENT, SENT};
    const u4 ones = {0x3F803F80u, 0x3F803F80u, 0x3F803F80u, 0x3F803F80u};
    *(volatile LAS u32*)(lds + L_PROG + w * 4) = 0;
    BAR();
    STAMP(0);
    auto post = [&](u32 v) { asm volatile("s_waitcnt lgkmcnt(0)" ::: "memory"); *(volatile LAS u32*)(lds + L_PROG + w * 4) = v; FENCE(); };
    const u32 vpg = (u32)(L_PROG + (lane & 3) * 4);
    auto wait_grp = [&](int g, u32 v) { lds_wait(vpg + g * 16, v); };
    auto wait_pair = [&](u32 v) { lds_wait((u32)(L_PROG + (w ^ 4) * 4), v); };
    u4 xa[16], xb[16];
    auto read_tile = [&](u4 (&x)[16], int tile) {
        LAS char* p = lds + tile * 65536 + (16 * rq + li) * 1024;
#pragma unroll
        for (int k = 0; k < 16; k++) x[k] = *(LAS u4*)(p + (((4 * k + lg) ^ li) * 16));
    };
    auto stats = [&]() {
        f4 s1 = {0, 0, 0, 0}, sq = s1;
#pragma unroll
        for (int k = 0; k < 16; k++) { s1 = mfma(ones, xa[k], s1); sq = mfma(xa[k], xa[k], sq); }
        const int rr = li & 3;
        const float d = rr == 0 ? sq.x : rr == 1 ? sq.y : rr == 2 ? sq.z : sq.w;
        if (lg == (li >> 2)) {
            const float mean = s1.x * (1.0f / 512.0f);
            float var = d * (1.0f / 512.0f) - mean * mean;
            if (!(mean * mean <= 1024.0f * var)) {
                const u32* xrow = (const u32*)(a.x + (size_t)((s2 ^ hf) + 32 * (16 * rq + li)) * 512);
                float q = 0.f;
                _Pragma("unroll 1") for (int i = 0; i < 256; i++) { u32 v = xrow[i]; float d0 = bflo(v) - mean, d1 = bfhi(v) - mean; q += d0 * d0; q += d1 * d1; }
                var = q * (1.0f / 512.0f);
            }
            const float den = rbf(__builtin_amdgcn_sqrtf(rbf(rbf(var) + 1e-5f)));
            *(LAS f2*)(lds + L_STAT + (hf * 64 + 16 * rq + li) * 8) = f2{rbf(mean), __builtin_amdgcn_rcpf(den)};
        }
        FENCE();
    };
    const int hoffA = WS_H + (((s2 ^ hf) * 4 + rq) * NFB) * 1024, hoffB = WS_H + (((s2 ^ hf ^ 1) * 4 + rq) * NFB) * 1024;
    const int nblk = (t < 6 - par) ? 3 : 2, fb2 = 32 + par * 6 + t;
    auto h_clears = [&]() {
        CLR128(rs, l16, hoffA + (2 * cg + hf) * 1024);
        CLR128(rs, l16, hoffB + (2 * cg + hf) * 1024);
        CLR128(rs, l16, nblk == 3 ? hoffA + fb2 * 1024 : WS_BYTES);
    };
    f2 stP;
    auto phaseA = [&](auto&& hook, u32 step_cs) {
        const u32 fz = (u32)((li >> 3) | ((lg & 1) << 1));
        const u32 tb = (u32)((8 * lg + (li >> 2)) * 128 + 8 * (li & 3));
        LAS char* p0 = lds + tb + (((2 * hf) ^ fz) << 5);
        LAS char* p1 = lds + tb + (((2 * hf + 1) ^ fz) << 5);
        LAS char* pc = lds + tb + (((2 * hf + (rq & 1)) ^ fz) << 5) + (rq >> 1) * 4096;
        f4 accA[4], accB[4], acs = {0, 0, 0, 0};
#pragma unroll
        for (int j = 0; j < 4; j++) { accA[j] = f4{0, 0, 0, 0}; accB[j] = f4{0, 0, 0, 0}; }
        u4 F[2][5];
        auto ldf = [&](int ks, u4 (&f)[5]) {
            const int off = ks * PG;
            f[0] = trfrag2<512>(p0 + off); f[1] = trfrag2<512>(p1 + off); f[2] = trfrag2<512>(p0 + off + 4096); f[3] = trfrag2<512>(p1 + off + 4096); f[4] = trfrag2<512>(pc + off);
        };
        STAMP(13);
        hook(IC<-1>{});
        STAMP(14);
        ldf(0, F[0]);
        sfor<16>([&](auto S) {
            constexpr int s = S, ks = (s & 8) | ((s & 3) << 1) | ((s >> 2) & 1), sn = s + 1, kn = (sn & 8) | ((sn & 3) << 1) | ((sn >> 2) & 1);
            if (s == 1 || s == 10) pin9(accA, accB, acs);
            if (s == 1) STAMP(15); if (s == 3) STAMP(17); if (s == 7) STAMP(19); if (s == 8) STAMP(21); if (s == 11) STAMP(23);
            hook(S);
            if (s == 1) STAMP(16); if (s == 3) STAMP(18); if (s == 7) STAMP(20); if (s == 8) STAMP(22); if (s == 11) STAMP(24);
            SCHED();
            if (s + 1 < 16) ldf(kn, F[(s + 1) & 1]);
            u4 (&f)[5] = F[s & 1];
#pragma unroll
            for (int j = 0; j < 4; j++) { accA[j] = mfma(f[j], xa[ks], accA[j]); accB[j] = mfma(f[j], xb[ks], accB[j]); }
            acs = mfma(f[4], ones, acs);
            if (s + 1 < 16) {
                SGB(0x100, 2);
#pragma unroll
                for (int i = 0; i < 8; i++) { SGB(0x8, 1); SGB(0x100, 1); }
                SGB(0x8, 1);
            }
            SCHED();
        });
        if (li == 0) *(LAS f4*)(lds + L_CS + (hf * 4 + rq) * 64 + lg * 16) = acs;
        post(step_cs);
        STAMP(25);
        wait_grp(hf, step_cs);
        STAMP(26);
        LAS char* cs = lds + L_CS + hf * 256 + lg * 16;
        stP = *(LAS f2*)(lds + L_STAT + (hf * 64 + 16 * rq + li) * 8);
        const f2 stB = *(LAS f2*)(lds + L_STAT + ((1 - hf) * 64 + 16 * rq + li) * 8);
        st128(geglu8(accA, cs, stP), rs, l16, hoffA + (2 * cg + hf) * 1024);
        st128(geglu8(accB, cs, stB), rs, l16, hoffB + (2 * cg + hf) * 1024);
    };
    auto phaseB = [&](u32 step_cs) {
        const u32 tb = (u32)((8 * lg + (li >> 2)) * 64 + (lg & 1) * 32 + 8 * (li & 3));
        LAS char* p0 = lds + tb; LAS char* p1 = lds + (tb ^ 32);
        LAS char* pc = lds + (tb ^ ((rq & 1) * 32)) + (rq >> 1) * 2048;
        f4 acc[4], acs = {0, 0, 0, 0};
#pragma unroll
        for (int j = 0; j < 4; j++) acc[j] = f4{0, 0, 0, 0};
        u4 F[2][5];
        auto ldf = [&](int ks, u4 (&f)[5]) {
            const int off = ks * 4096;
            f[0] = trfrag2<256>(p0 + off); f[1] = trfrag2<256>(p1 + off); f[2] = trfrag2<256>(p0 + off + 2048); f[3] = trfrag2<256>(p1 + off + 2048); f[4] = trfrag2<256>(pc + off);
        };
        ldf(0, F[0]);
        sfor<16>([&](auto S) {
            constexpr int ks = S;
            SCHED();
            if (ks + 1 < 16) ldf(ks + 1, F[(ks + 1) & 1]);
            u4 (&f)[5] = F[ks & 1];
#pragma unroll
            for (int j = 0; j < 4; j++) acc[j] = mfma(f[j], xa[ks], acc[j]);
            acs = mfma(f[4], ones, acs);
            if (ks + 1 < 16) {
#pragma unroll
                for (int i = 0; i < 5; i++) { SGB(0x100, 2); SGB(0x8, 1); }
            }
            SCHED();
        });
        if (li == 0) *(LAS f4*)(lds + L_CS + 512 + (hf * 4 + rq) * 64 + lg * 16) = acs;
        post(step_cs);
        wait_grp(hf, step_cs);
        st128(geglu8(acc, lds + L_CS + 512 + hf * 256 + lg * 16, stP), rs, l16, hoffA + fb2 * 1024);
    };
    f4 acc2[4];
#pragma unroll
    for (int j = 0; j < 4; j++) acc2[j] = f4{0, 0, 0, 0};
    const int hsrc = WS_H + ((s2 * 4 + rq) * NFB + hf) * 1024;
    auto h_load = [&](auto NC, u4* hfr, int p0, int nvalid) {
        constexpr int N = decltype(NC)::value;
#pragma unroll
        for (int i = 0; i < N; i++) hfr[i] = __builtin_amdgcn_raw_buffer_load_b128(rs, l16, hsrc + (p0 + 2 * (i < nvalid ? i : 0)) * 1024, 16);
    };
    auto h_check = [&](auto NC, u4* hfr, int p0, int nvalid) {
        constexpr int N = decltype(NC)::value;
        _Pragma("unroll 1") for (int spins = 0; spins < SPIN_CAP; spins++) {
            u32 mx = 0;
#pragma unroll
            for (int i = 0; i < N; i++) mx = max(mx, max4(hfr[i]));
            if (!__any(mx == SENT)) break;
            h_load(NC, hfr, p0, nvalid);
            FENCE();
        }
    };
    auto g2_mma = [&](auto NC, const u4* hfr, int ldsbase) {
        constexpr int N = decltype(NC)::value;
        LAS u4* p = (LAS u4*)(lds + ldsbase + hf * 4096) + lane;
        u4 F[2][4];
#pragma unroll
        for (int j = 0; j < 4; j++) F[0][j] = p[j * 64];
        sfor<N>([&](auto I) {
            constexpr int i = I;
            SCHED();
            if (i + 1 < N) {
#pragma unroll
                for (int j = 0; j < 4; j++) F[(i + 1) & 1][j] = p[(i + 1) * 512 + j * 64];
            }
#pragma unroll
            for (int j = 0; j < 4; j++) acc2[j] = mfma(F[i & 1][j], hfr[i], acc2[j]);
            if (i + 1 < N) {
#pragma unroll
                for (int j = 0; j < 4; j++) { SGB(0x100, 1); SGB(0x8, 1); }
            }
            SCHED();
        });
    };
    const char* wdp = (const char*)a.ws + WS_WDP + (size_t)(t * NFB + rq) * 4096;

    if (hf == 0) {
        __builtin_amdgcn_s_setprio(2);
        const int f1st = WS_F1 + ((lane >> 3) * 8 + (((lane >> 2) & 1) ? (rq == 0 ? 4 : 8 - rq) : rq)) * 16384 + pid * 64 + c2p * 16;
#pragma unroll
        for (int i = 0; i < 4; i++) CLR128(rs, f1st, i * (64 * 16384));
        f2 tw64[8], twT[8];
        int x3a[4];
        auto fft13_consts = [&](float scale) {
            int lane_ = lane; asm volatile("" : "+v"(lane_));
            const int c2p = lane_ & 3, l3 = (lane_ >> 2) & 7, qb = lane_ >> 5;
            const int wq = rq, q1 = qb ? (wq == 0 ? 4 : 8 - wq) : wq, c2 = 4 * t + c2p;
            const float f64 = (float)l3 * (1.0f / 64.0f);
#pragma unroll
            for (int k = 1; k < 8; k++) tw64[k] = cis(f64 * (float)k);
            const float fT = (float)(8 * c2 * q1 + s2 * l3) * (1.0f / 2048.0f), dT = (float)s2 * (1.0f / 256.0f);
#pragma unroll
            for (int k = 0; k < 8; k++) twT[k] = cis(fT + dT * (float)k) * scale;
#pragma unroll
            for (int k = 0; k < 4; k++) {
                const int ua = (k >> 1) * 4 + (k & 1), u1 = l3 + 8 * ua;
                const int cls = (u1 < 32) ? u1 : ((64 - u1) & 31), hi = u1 >> 5;
                int mbit, sub;
                if (wq == 0) { mbit = qb; sub = hi; } else if (cls != 0) { sub = hi; mbit = qb ^ hi; } else { mbit = hi; sub = qb ^ hi; }
                x3a[k] = L_FFT + wq * 4608 + (cls * 18 + mbit * 8 + sub * 4 + c2p) * 8 - (k == 3 ? 2304 : 0);
            }
            asm volatile("" : "+v"(tw64[1]), "+v"(tw64[2]), "+v"(tw64[3]), "+v"(tw64[4]), "+v"(tw64[5]), "+v"(tw64[6]), "+v"(tw64[7]),
                         "+v"(twT[0]), "+v"(twT[1]), "+v"(twT[2]), "+v"(twT[3]), "+v"(twT[4]), "+v"(twT[5]), "+v"(twT[6]), "+v"(twT[7]),
                         "+v"(x3a[0]), "+v"(x3a[1]), "+v"(x3a[2]), "+v"(x3a[3]));
        };
        fft13_consts(1.0f);
        WAITVM(4);
        post(A_XL);
        STAMP(1);
        u32 raw[8];
        const int rw = lane >> 2;
        read_tile(xa, 0);
#pragma unroll
        for (int c1 = 0; c1 < 8; c1++) raw[c1] = *(LAS u32*)(lds + (16 * rq + rw) * 1024 + (((8 * c1 + t) ^ rw) * 16) + c2p * 4);
        post(A_XC);
        stats();
        STAMP(2);
        f2 v[8];
        {
            const f2 st = *(LAS f2*)(lds + L_STAT + (16 * rq + rw) * 8);
#pragma unroll
            for (int c1 = 0; c1 < 8; c1++) {
                *(LAS u32*)(lds + ((rq >> 1) ? L_X1 : L_X0) + ((rq & 1) * 16 + rw) * 128 + ((c1 ^ (rw & 7)) * 16) + c2p * 4) = raw[c1];
                v[c1] = bfup(pkbf(bfup(pkbf(bfup(raw[c1]) - st.xx)) * st.yy));
            }
            dft8<false>(v, kc);
            LAS f2* xb1 = (LAS f2*)(lds + L_FFT) + w * 64 + lane;
#pragma unroll
            for (int q1 = 0; q1 < 8; q1++) { const int reg = (q1 < 4) ? q1 * 2 : (((8 - q1) & 3) * 2 + 1); xb1[reg * 288] = v[q1]; }
        }
        post(A_P1);
        wait_grp(0, A_P1);
        {
            LAS f2* reg = (LAS f2*)(lds + L_FFT) + (w * 2 + qb) * 288;
#pragma unroll
            for (int k = 0; k < 8; k++) v[k] = reg[(lane & 31) + 32 * k];
            dft8<false>(v, kc);
#pragma unroll
            for (int k = 1; k < 8; k++) v[k] = cmul<true>(v[k], tw64[k]);
#pragma unroll
            for (int k = 0; k < 8; k++) reg[36 * k + (lane & 31)] = v[k];
            FENCE();
#pragma unroll
            for (int k = 0; k < 8; k++) v[k] = reg[36 * l3 + 4 * k + c2p];
            dft8<false>(v, kc);
#pragma unroll
            for (int k = 0; k < 8; k++) v[k] = cmul<true>(v[k], twT[k]);
            FENCE();
            *(LAS f2*)(lds + x3a[0]) = v[0];
            *(LAS f2*)(lds + x3a[1]) = v[1]; *(LAS f2*)(lds + x3a[1] + 1152) = v[2]; *(LAS f2*)(lds + x3a[1] + 2304) = v[3];
            *(LAS f2*)(lds + x3a[2]) = v[4];
            *(LAS f2*)(lds + x3a[3] + 2304) = v[5]; *(LAS f2*)(lds + x3a[3] + 1152) = v[6]; *(LAS f2*)(lds + x3a[3]) = v[7];
            FENCE();
#pragma unroll
            for (int i = 0; i < 4; i++) {
                u4 o = *(LAS u4*)(lds + L_FFT + w * 4608 + (i * 8 + (lane >> 3)) * 144 + (lane & 7) * 16);
                st128(sanitize(o), rs, f1st, i * (64 * 16384));
            }
        }
        STAMP(3);
        __builtin_amdgcn_s_setprio(0);
        wait_pair(B_XL);
        read_tile(xb, 1);
        h_clears();
        WAITVM(7);
        post(A_XB);
        const u32 wvB = (u32)((lane >> 2) * 2752 + (((lane & 3) ^ (((lane >> 5) & 1) << 1)) * 16));
        phaseA([&](auto S) {
            constexpr int s = S;
            if (s == -1) wait_grp(1, B_W0);
            if (s == 3) wait_grp(1, B_W1);
            if (s == 7) wait_grp(1, B_W2);
            if (s == 8) {
                WAITVM(0);
                post(A_S0);
            }
            if (s == 10 && nblk == 3) {
                wait_grp(0, A_S0); wait_grp(1, B_S0);
                const char* src = (const char*)((rq >> 1) ? a.wu : a.wg) + (size_t)((rq & 1) * 16) * 2752 + fb2 * 64;
                sfor<16>([&](auto K) { constexpr int ks = K; dma_add<ks * 4096, ks * 32 * 2752>(wvB, src, (u32)(rq * 1024)); });
            }
            if (s == 11) wait_grp(1, B_W3);
        }, A_CS);
        STAMP(4);
        WAITVM(2);
        post(A_WB);
        wait_grp(1, B_FL);
        sfor<4>([&](auto I) { constexpr int i = I; dma4k_c<L_SET1 + i * 16384, i * 16384>(l16, wdp, (u32)(rq * 4096)); });
        STAMP(5);
        if (nblk == 3) { wait_grp(0, A_WB); phaseB(A_PB); } else post(A_PB);
        STAMP(6);
        u4 hfr[16];
        h_load(IC<16>{}, hfr, 0, 16);
        WAITVM(16);
        post(A_D1);
        h_check(IC<16>{}, hfr, 0, 16);
        STAMP(7);
        wait_grp(0, A_D1);
        g2_mma(IC<8>{}, hfr, L_SET1);
        wait_grp(1, B_PB);
        sfor<4>([&](auto I) { constexpr int i = I; dma4k_c<i * 16384, (16 + 4 * i) * 4096>(l16, wdp, (u32)(rq * 4096)); });
        WAITVM(0);
        post(A_D2);
        wait_grp(0, A_D2);
        g2_mma(IC<8>{}, hfr + 8, 0);
        wait_grp(1, B_G1);
        sfor<3>([&](auto I) { constexpr int i = I; if (i < 2 || rq < 3) dma4k_c<L_SET1 + i * 16384, (32 + 4 * i) * 4096>(l16, wdp, (u32)(rq * 4096)); });
        STAMP(8);
        {
            u4 d[4];
            const int off = WS_F2 + pid * 16384 + w * 4096;
#pragma unroll
            for (int i = 0; i < 4; i++) d[i] = __builtin_amdgcn_raw_buffer_load_b128(rs, l16, off + i * 1024, 16);
            fft13_consts(0.5f / 524288.0f);
            _Pragma("unroll 1") for (int spins = 0; spins < SPIN_CAP; spins++) {
                u32 mx = max(max(max4(d[0]), max4(d[1])), max(max4(d[2]), max4(d[3])));
                if (!__any(mx == SENT)) break;
#pragma unroll
                for (int i = 0; i < 4; i++) d[i] = __builtin_amdgcn_raw_buffer_load_b128(rs, l16, off + i * 1024, 16);
                FENCE();
            }
            STAMP(9);
            __builtin_amdgcn_s_setprio(2);
            h_load(IC<6>{}, hfr, 32, 6);
#pragma unroll
            for (int i = 0; i < 4; i++) *(LAS u4*)(lds + L_FFT + w * 4608 + (i * 8 + (lane >> 3)) * 144 + (lane & 7) * 16) = d[i];
            FENCE();
            v[0] = *(LAS f2*)(lds + x3a[0]);
            v[1] = *(LAS f2*)(lds + x3a[1]); v[2] = *(LAS f2*)(lds + x3a[1] + 1152); v[3] = *(LAS f2*)(lds + x3a[1] + 2304);
            v[4] = *(LAS f2*)(lds + x3a[2]);
            v[5] = *(LAS f2*)(lds + x3a[3] + 2304); v[6] = *(LAS f2*)(lds + x3a[3] + 1152); v[7] = *(LAS f2*)(lds + x3a[3]);
#pragma unroll
            for (int k = 0; k < 8; k++) v[k] = cmul<false>(v[k], twT[k]);
            dft8<true>(v, kc);
#pragma unroll
            for (int k = 1; k < 8; k++) v[k] = cmul<false>(v[k], tw64[k]);
            FENCE();
            LAS f2* reg = (LAS f2*)(lds + L_FFT) + (w * 2 + qb) * 288;
#pragma unroll
            for (int k = 0; k < 8; k++) reg[36 * l3 + 4 * k + c2p] = v[k];
            FENCE();
#pragma unroll
            for (int k = 0; k < 8; k++) v[k] = reg[36 * k + (lane & 31)];
            dft8<true>(v, kc);
            FENCE();
#pragma unroll
            for (int k = 0; k < 8; k++) reg[(lane & 31) + 32 * k] = v[k];
            post(A_D3);
            wait_grp(0, A_D3);
            LAS f2* xb1 = (LAS f2*)(lds + L_FFT) + w * 64 + lane;
#pragma unroll
            for (int q1 = 0; q1 < 8; q1++) { const int rg_ = (q1 < 4) ? q1 * 2 : (((8 - q1) & 3) * 2 + 1); v[q1] = xb1[rg_ * 288]; }
            dft8<true>(v, kc);
#pragma unroll
            for (int c1 = 0; c1 < 8; c1++) *(LAS u32*)(lds + L_S + (16 * w + rw) * 144 + c1 * 16 + c2p * 4) = pkbf(v[c1]);
        }
        STAMP(10);
        h_check(IC<6>{}, hfr, 32, 6);
        g2_mma(IC<6>{}, hfr, L_SET1);
        STAMP(11);
    } else {
        u2 ev;
        { u32 zero = 0; asm volatile("global_load_dwordx2 %0, %1, %2 sc1" : "=v"(ev) : "v"(zero), "s"(a.ws) : "memory"); }
        u32 pk[24];
        {
            const u32 vo = (u32)(((8 * sig + 2 * rq) * 257 + lane) * 2);
            sfor<8>([&](auto I) {
                constexpr int rr = I >> 2, k = I & 3;
                ldg_u16<rr * 514 + k * 128>(pk[I], vo, a.wr);
                ldg_u16<rr * 514 + k * 128>(pk[8 + I], vo, a.wi);
            });
            const int un = sig < 4 * NFB ? sig : 0, fb = un >> 2, jj = (un >> 1) & 1, tcol = (un & 1) * 4 + rq;
            const u32 vd = (u32)(((fb * 32 + 4 * lg) * 512 + 64 * (4 * jj + (li >> 2)) + 8 * tcol + 2 * (li & 3)) * 2);
            sfor<4>([&](auto I) {
                ldg_u32<I * 1024>(pk[16 + I], vd, a.wd);
                ldg_u32<I * 1024>(pk[20 + I], vd, (const char*)a.wd + 16384);
            });
        }
        const u32 wv0 = (u32)((lane >> 3) * 2752 + (((lane & 7) ^ (((lane >> 4) & 1) << 1)) * 16)), wv1 = (u32)((lane >> 3) * 2752 + (((lane & 7) ^ (((lane >> 4) & 1) << 1) ^ 4) * 16));
        const char* wgs = (const char*)a.wg + (size_t)(64 * rq) * 2752 + cg * 128;
        const char* wus = (const char*)a.wu + (size_t)(64 * rq) * 2752 + cg * 128;
        auto dma_wa = [&](auto KS) {
            constexpr int ks = KS;
            sfor<4>([&](auto G) { constexpr int g = G;
                dma_add<ks * PG + g * 1024, (ks * 32 + g * 8) * 2752>((g & 1) ? wv1 : wv0, wgs, (u32)(rq * 2 * PG));
                dma_add<ks * PG + 4096 + g * 1024, (ks * 32 + g * 8) * 2752>((g & 1) ? wv1 : wv0, wus, (u32)(rq * 2 * PG)); });
        };
        wait_pair(A_XL);
        read_tile(xb, 0);
        asm volatile("s_waitcnt lgkmcnt(0)" ::: "memory");
        wait_pair(A_XC);
        dma_wa(IC<0>{}); dma_wa(IC<1>{});
        asm volatile("s_waitcnt vmcnt(40)" : "+v"(ev) :: "memory");
        post(B_XL);
        STAMP(1);
        read_tile(xa, 1);
        stats();
        const u64 epoch = ((u64)(u32)__builtin_amdgcn_readfirstlane(ev.y) << 32) | (u32)__builtin_amdgcn_readfirstlane(ev.x);
        u64 tag = (epoch + 1) * 0x9E3779B97F4A7C15ull; tag ^= tag >> 29; tag *= 0xBF58476D1CE4E5B9ull; tag ^= tag >> 32;
        STAMP(2);
        const int cls2 = sig >> 3, m2 = sig & 7;
        const int pos1 = ((m2 < 4) ? m2 : ((8 - m2) & 3)) * 64 + cls2 * 2 + (m2 >> 2);
        const int f2st = WS_F2 + ((2 * rq + qb) * 8 + l3) * 16384 + pos1 * 64 + c2p * 16;
        {
            asm volatile("s_waitcnt vmcnt(16)" : "+v"(pk[0]), "+v"(pk[1]), "+v"(pk[2]), "+v"(pk[3]), "+v"(pk[4]), "+v"(pk[5]), "+v"(pk[6]), "+v"(pk[7]),
                         "+v"(pk[8]), "+v"(pk[9]), "+v"(pk[10]), "+v"(pk[11]), "+v"(pk[12]), "+v"(pk[13]), "+v"(pk[14]), "+v"(pk[15]),
                         "+v"(pk[16]), "+v"(pk[17]), "+v"(pk[18]), "+v"(pk[19]), "+v"(pk[20]), "+v"(pk[21]), "+v"(pk[22]), "+v"(pk[23]) :: "memory");
            const int q1 = lane >> 3, vt = lane & 7, srcl = (q1 + 8 * vt) * 4;
#pragma unroll
            for (int rr = 0; rr < 2; rr++) {
                const int u = 8 * sig + 2 * rq + rr, u1 = u & 63, u2 = u >> 6;
                int cls, m, sub;
                if ((u1 & 31) != 0) { sub = u1 >> 5; cls = sub ? 64 - u1 : u1; m = sub ? ((8 - q1) & 7) : q1; }
                else { cls = 0; const int hi = u1 >> 5; if ((q1 & 3) == 0) { m = q1; sub = hi; } else { sub = (q1 >> 2) ^ hi; m = sub ? 8 - q1 : q1; } }
                u4 o;
                o.x = __builtin_amdgcn_ds_bpermute(srcl, pk[rr * 4 + 0] | (pk[8 + rr * 4 + 0] << 16)); o.y = __builtin_amdgcn_ds_bpermute(srcl, pk[rr * 4 + 1] | (pk[8 + rr * 4 + 1] << 16));
                o.z = __builtin_amdgcn_ds_bpermute(srcl, pk[rr * 4 + 2] | (pk[8 + rr * 4 + 2] << 16)); o.w = __builtin_amdgcn_ds_bpermute(srcl, pk[rr * 4 + 3] | (pk[8 + rr * 4 + 3] << 16));
                st128(o, rs, WS_WP + (cls * 8 + m) * 8192 + sub * 4096 + u2 * 128 + vt * 16, 0);
            }
            const int fb = sig >> 2, jj = (sig >> 1) & 1, tcol = (sig & 1) * 4 + rq;
            const u32* e = pk + 16;
            const int so = sig < 4 * NFB ? WS_WDP + ((tcol * NFB + fb) * 4 + 2 * jj) * 1024 : WS_BYTES;
            u4 oe = {__builtin_amdgcn_perm(e[1], e[0], 0x05040100), __builtin_amdgcn_perm(e[3], e[2], 0x05040100), __builtin_amdgcn_perm(e[5], e[4], 0x05040100), __builtin_amdgcn_perm(e[7], e[6], 0x05040100)};
            u4 oo = {__builtin_amdgcn_perm(e[1], e[0], 0x07060302), __builtin_amdgcn_perm(e[3], e[2], 0x07060302), __builtin_amdgcn_perm(e[5], e[4], 0x07060302), __builtin_amdgcn_perm(e[7], e[6], 0x07060302)};
            st128(oe, rs, l16, so);
            st128(oo, rs, l16, so + 1024);
#pragma unroll
            for (int H = 0; H < 4; H++) CLR128(rs, f2st, H * (64 * 16384));
            h_clears();
        }
        STAMP(3);
        phaseA([&](auto S) {
            constexpr int s = S;
            if (s == -1) { WAITVM(19); post(B_W0); wait_grp(1, B_W0); }
            if (s == 1) {
                wait_pair(A_XB);
                dma_wa(IC<8>{}); dma_wa(IC<9>{});
                if (w == 4) { wait_grp(0, A_XB); if (lane == 0) __hip_atomic_store((GAS u64*)(a.ws + WS_FLAG0) + pid, tag, __ATOMIC_RELAXED, __HIP_MEMORY_SCOPE_AGENT); }
            }
            if (s == 3) { WAITVM(27); post(B_W1); wait_grp(1, B_W1); }
            if (s == 7) { WAITVM(8); post(B_W2); wait_grp(1, B_W2); }
            if (s == 8) post(B_S0);
            if (s == 11) { WAITVM(0); post(B_W3); wait_grp(1, B_W3); }
        }, B_CS);
        STAMP(4);
        if (w == 4) { wait_grp(0, A_S0); if (lane == 0) __hip_atomic_store((GAS u64*)(a.ws + WS_FLAG1) + b, tag, __ATOMIC_RELAXED, __HIP_MEMORY_SCOPE_AGENT); }
        {
            f2 v[8], tw0[4], tw1[8], al[8], be[8];
            const int rb = qb, r3 = 2 * rq + rb;
            _Pragma("unroll 1") for (int spins = 0; spins < SPIN_CAP; spins++) {
                bool ok = true;
#pragma unroll
                for (int k = 0; k < 4; k++) ok &= __hip_atomic_load((GAS u64*)(a.ws + WS_FLAG1) + lane + 64 * k, __ATOMIC_RELAXED, __HIP_MEMORY_SCOPE_AGENT) == tag;
                if (__all(ok)) break;
            }
            __builtin_amdgcn_fence(__ATOMIC_ACQUIRE, "wavefront");
            post(B_FL);
            STAMP(5);
            if (w == 4 && b == 0 && lane == 0) __hip_atomic_store((GAS u64*)a.ws, epoch + 1, __ATOMIC_RELAXED, __HIP_MEMORY_SCOPE_AGENT);
            const int f1off = WS_F1 + sig * 16384 + 2 * rq * 512;
            u4 d[4];
#pragma unroll
            for (int H = 0; H < 4; H++) d[H] = __builtin_amdgcn_raw_buffer_load_b128(rs, l16, f1off + H * 4096, 16);
            const bool special = (cls2 == 0) && ((m2 & 3) == 0);
            int u1a, q1a, u1b, q1b;
            if (cls2 != 0) { u1a = cls2; q1a = m2; u1b = 64 - cls2; q1b = (8 - m2) & 7; }
            else if (special) { u1a = 0; u1b = 32; q1a = q1b = m2; }
            else { u1a = u1b = (m2 < 4) ? 0 : 32; q1a = m2; q1b = 8 - m2; }
            int sA, vH0, sB, vH1;
            if (!special) { sA = 0; vH0 = rq; sB = 1; vH1 = (u1a == 0) ? ((4 - rq) & 3) : 3 - rq; }
            else { sA = sB = rq >> 1; vH0 = rq & 1; vH1 = (rq == 0) ? 2 : (rq == 1) ? 3 : (rq == 2) ? 3 : 2; }
            const int rowB = L_FFT + ((qb ? (vH1 * 2 + sB) : (vH0 * 2 + sA)) * 36 + (lane & 31)) * 8;
            const bool q0 = (m2 == 0);
            const int dy = ((sA ? u1b : u1a) == 0 && vH0 == 0) ? 8 : 7, dq = q0 ? 8 : 7;
            int y1 = lane >> 3, t1 = lane & 7, y2 = (dy - y1) & 7, t2 = (dq - t1) & 7, hA = vH0, hB = vH1;
            bool self = false;
            if (special && rq == 0) {
                const int k = lane & 31;
                if (qb) { hA = hB = 2; y1 = k >> 3; t1 = k & 7; y2 = 7 - y1; t2 = (dq - t1) & 7; }
                else {
                    hA = hB = 0; const int vx = k & 3, y = k >> 2;
                    if (!q0 || vx) { y1 = y; t1 = vx; y2 = (8 - y) & 7; t2 = dq - vx; }
                    else { t1 = t2 = (y >= 4) ? 4 : 0; const int yy = y & 3; if (yy) { y1 = yy; y2 = 8 - yy; } else { y1 = 0; y2 = 4; self = true; } }
                }
            }
            const bool rot = q0 && (t1 == 0);
            const int p3a1 = L_FFT + (y1 * 290 + (hA * 2 + sA) * 36 + t1 * 4) * 8;
            const int p3a2 = L_FFT + (y2 * 290 + (hB * 2 + sB) * 36 + t2 * 4) * 8;
            const u4 wa4 = __builtin_amdgcn_raw_buffer_load_b128(rs, (hA + 4 * y1) * 128 + t1 * 16, WS_WP + sig * 8192 + sA * 4096, 16);
            const u4 wb4 = __builtin_amdgcn_raw_buffer_load_b128(rs, (hB + 4 * y2) * 128 + t2 * 16, WS_WP + sig * 8192 + sB * 4096, 16);
            u32 wS[2] = {0, 0};
            if (q0) {
                const int uA = (sA ? u1b : u1a) + 64 * (hA + 4 * y1), uB = (sB ? u1b : u1a) + 64 * (hB + 4 * y2);
                wS[0] = (u32)a.wr[uA * 257 + 256] | ((u32)a.wi[uA * 257 + 256] << 16);
                wS[1] = (u32)a.wr[uB * 257 + 256] | ((u32)a.wi[uB * 257 + 256] << 16);
            }
            const float thA = (float)((sA ? q1b : q1a) + 8 * t1) * (1.0f / 512.0f), thB = (float)((sB ? q1b : q1a) + 8 * t2) * (1.0f / 512.0f);
#pragma unroll
            for (int k = 1; k < 4; k++) tw0[k] = cis((float)r3 * ((float)k / 32.0f));
#pragma unroll
            for (int k = 1; k < 8; k++) tw1[k] = cis((float)c2p * ((float)k / 32.0f));
            {
                const u32 wA[8] = {wa4.x, wa4.y, wa4.z, wa4.w, wb4.x, wb4.y, wb4.z, wb4.w};
                f2 W[8], WB[8];
#pragma unroll
                for (int e = 0; e < 8; e++) W[e] = bfup(wA[e]);
#pragma unroll
                for (int k = 0; k < 4; k++) { WB[k] = W[4 + 3 - k]; WB[4 + k] = W[3 - k]; }
                if (q0) {
#pragma unroll
                    for (int k = 0; k < 4; k++) {
                        f2 n1 = rot ? W[(4 - k) & 3] : W[3 - k], n2 = rot ? W[4 + ((4 - k) & 3)] : W[4 + 3 - k];
                        WB[k] = self ? n1 : n2; WB[4 + k] = self ? n2 : n1;
                    }
#pragma unroll
                    for (int e = 0; e < 8; e += 4) {
                        f2 own = bfup(wS[e >> 2]), oth = self ? own : bfup(wS[1 - (e >> 2)]);
                        f2 S0 = pkfma(WB[e], kc.pm, W[e]) * 0.5f, S1c = pkfma(own, kc.pm, oth) * 0.5f;
                        WB[e] = rot ? S1c : WB[e];
                        al[e] = rot ? S0 : W[e];
                    }
                    W[0] = al[0]; W[4] = al[4];
                }
#pragma unroll
                for (int e = 0; e < 8; e++) {
                    const f2 cs = cis(((e >> 2) ? thB : thA) + 0.125f * (float)(e & 3));
                    f2 P = pkfma(WB[e], kc.pm, W[e]), M = pkfma(WB[e], kc.pm.yx, W[e]);
                    al[e] = pkfma(M, -cs.yy, P);
                    be[e] = M.yx * (kc.pm.yx * cs.xx);
                }
            }
            _Pragma("unroll 1") for (int spins = 0; spins < SPIN_CAP; spins++) {
                u32 mx = max(max(max4(d[0]), max4(d[1])), max(max4(d[2]), max4(d[3])));
                if (!__any(mx == SENT)) break;
#pragma unroll
                for (int H = 0; H < 4; H++) d[H] = __builtin_amdgcn_raw_buffer_load_b128(rs, l16, f1off + H * 4096, 16);
                FENCE();
            }
            STAMP(6);
            __builtin_amdgcn_s_setprio(2);
            f2 e0[4], e1[4];
#pragma unroll
            for (int H = 0; H < 4; H++) { e0[H] = f2{__uint_as_float(d[H].x), __uint_as_float(d[H].y)}; e1[H] = f2{__uint_as_float(d[H].z), __uint_as_float(d[H].w)}; }
            dft4<false>(e0[0], e0[1], e0[2], e0[3], kc); dft4<false>(e1[0], e1[1], e1[2], e1[3], kc);
            LAS char* area = lds + L_FFT + rq * 4640;
#pragma unroll
            for (int k = 0; k < 4; k++) {
                if (k) { e0[k] = cmul<true>(e0[k], tw0[k]); e1[k] = cmul<true>(e1[k], tw0[k]); }
                *(LAS f4*)(area + (l3 * 72 + 2 * c2p + 8 * k + 32 * rb) * 8) = f4{e0[k].x, e0[k].y, e1[k].x, e1[k].y};
            }
            FENCE();
#pragma unroll
            for (int k = 0; k < 8; k++) v[k] = *(LAS f2*)(area + (k * 72 + lane) * 8);
            dft8<false>(v, kc);
#pragma unroll
            for (int k = 1; k < 8; k++) v[k] = cmul<true>(v[k], tw1[k]);
            FENCE();
            LAS char* pb = lds + L_FFT + (r3 * 290 + l3 * 36 + c2p) * 8;
#pragma unroll
            for (int k = 0; k < 8; k++) *(LAS f2*)(pb + k * 32) = v[k];
            post(B_E1);
            wait_grp(1, B_E1);
#pragma unroll
            for (int k = 0; k < 8; k++) v[k] = *(LAS f2*)(lds + rowB + k * 2320);
            dft8<false>(v, kc);
#pragma unroll
            for (int k = 0; k < 8; k++) *(LAS f2*)(lds + rowB + k * 2320) = v[k];
            FENCE();
            f4 a0 = *(LAS f4*)(lds + p3a1), a1 = *(LAS f4*)(lds + p3a1 + 16), b0 = *(LAS f4*)(lds + p3a2), b1 = *(LAS f4*)(lds + p3a2 + 16);
            f2 X[8] = {a0.xy, a0.zw, a1.xy, a1.zw, b0.xy, b0.zw, b1.xy, b1.zw};
            dft4<false>(X[0], X[1], X[2], X[3], kc); dft4<false>(X[4], X[5], X[6], X[7], kc);
            f2 Y[8];
#pragma unroll
            for (int k = 0; k < 4; k++) { Y[k] = X[4 + 3 - k]; Y[4 + k] = X[3 - k]; }
            if (q0) {
#pragma unroll
                for (int k = 0; k < 4; k++) {
                    f2 n1 = rot ? X[(4 - k) & 3] : X[3 - k], n2 = rot ? X[4 + ((4 - k) & 3)] : X[4 + 3 - k];
                    Y[k] = self ? n1 : n2; Y[4 + k] = self ? n2 : n1;
                }
            }
            f2 O[8];
#pragma unroll
            for (int e = 0; e < 8; e++) O[e] = cfmac_conj(cmul<false>(X[e], al[e]), be[e], Y[e]);
            dft4<true>(O[0], O[1], O[2], O[3], kc); dft4<true>(O[4], O[5], O[6], O[7], kc);
            *(LAS f4*)(lds + p3a1) = f4{O[0].x, O[0].y, O[1].x, O[1].y}; *(LAS f4*)(lds + p3a1 + 16) = f4{O[2].x, O[2].y, O[3].x, O[3].y};
            *(LAS f4*)(lds + p3a2) = f4{O[4].x, O[4].y, O[5].x, O[5].y}; *(LAS f4*)(lds + p3a2 + 16) = f4{O[6].x, O[6].y, O[7].x, O[7].y};
            FENCE();
#pragma unroll
            for (int k = 0; k < 8; k++) v[k] = *(LAS f2*)(lds + rowB + k * 2320);
            dft8<true>(v, kc);
#pragma unroll
            for (int k = 0; k < 8; k++) *(LAS f2*)(lds + rowB + k * 2320) = v[k];
            post(B_E2);
            wait_grp(1, B_E2);
#pragma unroll
            for (int k = 0; k < 8; k++) v[k] = *(LAS f2*)(pb + k * 32);
#pragma unroll
            for (int k = 1; k < 8; k++) v[k] = cmul<false>(v[k], tw1[k]);
            dft8<true>(v, kc);
            FENCE();
#pragma unroll
            for (int k = 0; k < 8; k++) *(LAS f2*)(area + (k * 72 + lane) * 8) = v[k];
            FENCE();
#pragma unroll
            for (int k = 0; k < 4; k++) {
                f4 q = *(LAS f4*)(area + (l3 * 72 + 2 * c2p + 8 * k + 32 * rb) * 8);
                e0[k] = q.xy; e1[k] = q.zw;
                if (k) { e0[k] = cmul<false>(e0[k], tw0[k]); e1[k] = cmul<false>(e1[k], tw0[k]); }
            }
            dft4<true>(e0[0], e0[1], e0[2], e0[3], kc); dft4<true>(e1[0], e1[1], e1[2], e1[3], kc);
#pragma unroll
            for (int H = 0; H < 4; H++) {
                u4 o = {__float_as_uint(e0[H].x), __float_as_uint(e0[H].y), __float_as_uint(e1[H].x), __float_as_uint(e1[H].y)};
                st128(sanitize(o), rs, f2st, H * (64 * 16384));
            }
            __builtin_amdgcn_s_setprio(0);
        }
        STAMP(7);
        if (nblk == 3) { wait_grp(0, A_WB); phaseB(B_PB); } else post(B_PB);
        STAMP(8);
        u4 hfr[16];
        h_load(IC<16>{}, hfr, 0, 16);
        h_check(IC<16>{}, hfr, 0, 16);
        STAMP(9);
        wait_grp(0, A_D1);
        g2_mma(IC<8>{}, hfr, L_SET1);
        post(B_G1);
        wait_grp(0, A_D2);
        g2_mma(IC<8>{}, hfr + 8, 0);
        STAMP(10);
        h_load(IC<5>{}, hfr, 32, 5);
        h_check(IC<5>{}, hfr, 32, 5);
        wait_grp(0, A_D3);
        g2_mma(IC<5>{}, hfr, L_SET1);
        STAMP(11);
    }
    BAR();
    {
        LAS f4* xs = (LAS f4*)(lds + w * 2048) + lane;
        LAS f4* xr = (LAS f4*)(lds + (w ^ 4) * 2048) + lane;
        f4 me, mo;
        if (hf) { xs[0] = acc2[0]; xs[64] = acc2[1]; me = acc2[2]; mo = acc2[3]; } else { xs[0] = acc2[2]; xs[64] = acc2[3]; me = acc2[0]; mo = acc2[1]; }
        BAR();
        const int c1 = 4 * hf + lg;
        me += xr[0]; mo += xr[64];
        const u4 sv = *(LAS u4*)(lds + L_S + (16 * rq + li) * 144 + c1 * 16);
        const u4 xv = *(LAS u4*)(lds + ((rq >> 1) ? L_X1 : L_X0) + ((rq & 1) * 16 + li) * 128 + ((c1 ^ (li & 7)) * 16));
        u32 res[4];
#pragma unroll
        for (int e = 0; e < 4; e++) {
            const f2 ov = bfup(pkbf(f2{me[e], mo[e]}));
            const f2 z = ov * -1.44269504f;
            const f2 d = f2{__builtin_amdgcn_exp2f(z.x), __builtin_amdgcn_exp2f(z.y)} + f2{1.f, 1.f};
            const f2 g = bfup(pkbf(ov * f2{__builtin_amdgcn_rcpf(d.x), __builtin_amdgcn_rcpf(d.y)}));
            res[e] = pkbf(bfup(xv[e]) + bfup(pkbf(bfup(sv[e]) * g)));
        }
        *(u4*)(a.out + (size_t)(s2 + 32 * (16 * rq + li)) * 512 + 64 * c1 + 8 * t) = u4{res[0], res[1], res[2], res[3]};
    }
    STAMP(12);
}

extern "C" void kernel_launch(void* const* d_in, const int* in_sizes, int n_in,
                              void* d_out, int out_size, void* d_ws, size_t ws_size,
                              hipStream_t stream) {
    Args a;
    a.x = (const u16*)d_in[0]; a.wr = (const u16*)d_in[1]; a.wi = (const u16*)d_in[2];
    a.wg = (const u16*)d_in[4]; a.wu = (const u16*)d_in[5]; a.wd = (const u16*)d_in[6];
    a.out = (u16*)d_out; a.ws = (unsigned char*)d_ws;
    (void)hipFuncSetAttribute((const void*)fused, hipFuncAttributeMaxDynamicSharedMemorySize, L_END);
    fused<<<256, 512, L_END, stream>>>(a);
}
```

```cpp
#include <hip/hip_runtime.h>
#include <stdint.h>
#include <type_traits>

typedef unsigned short u16;
typedef unsigned u32;
typedef unsigned long long u64;
typedef float f2 __attribute__((ext_vector_type(2)));
typedef float f4 __attribute__((ext_vector_type(4)));
typedef u32 u4 __attribute__((ext_vector_type(4)));
typedef u32 u2 __attribute__((ext_vector_type(2)));
typedef short s4 __attribute__((ext_vector_type(4)));
typedef __bf16 bf8 __attribute__((ext_vector_type(8)));
typedef __bf16 bf2 __attribute__((ext_vector_type(2)));
#define LAS __attribute__((address_space(3)))
#define GAS __attribute__((address_space(1)))
#define DEV __device__ __forceinline__
template <int N> using IC = std::integral_constant<int, N>;

constexpr int NFB = 43;
constexpr int WS_FF1 = 4096;
constexpr int WS_FF2 = WS_FF1 + 8192;
constexpr int WS_FHA = WS_FF2 + 8192;
constexpr int WS_FHB = WS_FHA + 16384;
constexpr int WS_FP = WS_FHB + 16384;
constexpr int WS_F1 = 1 << 20, WS_F2 = WS_F1 + (4 << 20), WS_H = WS_F2 + (4 << 20);
constexpr int WS_WDP = WS_H + 32 * 4 * NFB * 1024;
constexpr int WS_WP = WS_WDP + 8 * NFB * 4 * 1024;
constexpr int WS_STAMP = WS_WP + (2 << 20);
constexpr int WS_BYTES = WS_STAMP + (1 << 20);
constexpr int PG = 8192;
constexpr int L_SET1 = 8 * PG;
constexpr int L_FFT = 16 * PG;
constexpr int L_SMALL = 19 * PG;
constexpr int L_PROG = L_SMALL;
constexpr int L_TAG = L_SMALL + 32;
constexpr int L_STAT = L_SMALL + 64;
constexpr int L_CS = L_SMALL + 2048;
constexpr int L_X0 = L_FFT + 18688, L_X1 = L_SMALL + 3072;
constexpr int L_S = 14 * PG;
constexpr int L_END = 20 * PG;
constexpr u32 SENT = 0xFFFFFFFFu;
constexpr int SPIN_CAP = 1 << 16;

struct Args { const u16* x; const u16* wr; const u16* wi; const u16* wg; const u16* wu; const u16* wd; u16* out; unsigned char* ws; };

#define BAR() asm volatile("s_waitcnt lgkmcnt(0)\n\ts_barrier" ::: "memory")
#define WAITVM(n) asm volatile("s_waitcnt vmcnt(" #n ")" ::: "memory")
#define FENCE() asm volatile("" ::: "memory")
#define SCHED() __builtin_amdgcn_sched_barrier(0)
#define SGB(mask, n) __builtin_amdgcn_sched_group_barrier(mask, n, 0)
#define STAMP(i)
#define CSTAMP(i)

DEV f2 pkfma(f2 a, f2 b, f2 c) { return __builtin_elementwise_fma(a, b, c); }
struct KC { f2 pm, rpm; };
DEV f2 addmi(f2 a, f2 b, const KC& k) { return pkfma(b.yx, k.pm, a); }
DEV f2 addpi(f2 a, f2 b, const KC& k) { return pkfma(b.yx, k.pm.yx, a); }
template <bool INV> DEV void dft4(f2& a0, f2& a1, f2& a2, f2& a3, const KC& k) {
    f2 t0 = a0 + a2, t1 = a0 - a2, t2 = a1 + a3, t3 = a1 - a3;
    a0 = t0 + t2; a2 = t0 - t2;
    if (INV) { a1 = addpi(t1, t3, k); a3 = addmi(t1, t3, k); } else { a1 = addmi(t1, t3, k); a3 = addpi(t1, t3, k); }
}
template <bool INV> DEV void dft8(f2 (&v)[8], const KC& k) {
    f2 a0 = v[0] + v[4], a4 = v[0] - v[4], a1 = v[1] + v[5], a5 = v[1] - v[5];
    f2 a2 = v[2] + v[6], a6 = v[2] - v[6], a3 = v[3] + v[7], a7 = v[3] - v[7];
    dft4<INV>(a0, a1, a2, a3, k);
    v[0] = a0; v[2] = a1; v[4] = a2; v[6] = a3;
    f2 b1, b3, t0, t1;
    if (INV) { b1 = addpi(a5, a5, k); b3 = addpi(-a7, a7, k); t0 = addpi(a4, a6, k); t1 = addmi(a4, a6, k); }
    else { b1 = addmi(a5, a5, k); b3 = addmi(-a7, a7, k); t0 = addmi(a4, a6, k); t1 = addpi(a4, a6, k); }
    f2 t2 = b1 + b3, t3 = b1 - b3;
    v[1] = pkfma(t2, k.rpm.xx, t0); v[5] = pkfma(t2, k.rpm.yy, t0);
    if (INV) { v[3] = pkfma(t3.yx, k.rpm.yx, t1); v[7] = pkfma(t3.yx, k.rpm, t1); }
    else { v[3] = pkfma(t3.yx, k.rpm, t1); v[7] = pkfma(t3.yx, k.rpm.yx, t1); }
}
template <bool CONJ> DEV f2 cmul(f2 a, f2 w) {
    f2 r;
    if (CONJ) asm("v_pk_mul_f32 %0, %1, %2 op_sel:[1,1] op_sel_hi:[0,1]\n\tv_pk_fma_f32 %0, %1, %2, %0 op_sel_hi:[1,0,1] neg_hi:[0,0,1]" : "=&v"(r) : "v"(a), "v"(w));
    else asm("v_pk_mul_f32 %0, %1, %2 op_sel:[1,1] op_sel_hi:[0,1]\n\tv_pk_fma_f32 %0, %1, %2, %0 op_sel_hi:[1,0,1] neg_lo:[0,0,1]" : "=&v"(r) : "v"(a), "v"(w));
    return r;
}
DEV f2 cfmac_conj(f2 t, f2 beta, f2 B) {
    asm("v_pk_fma_f32 %0, %2, %1, %0 op_sel_hi:[0,1,1]\n\tv_pk_fma_f32 %0, %2, %1, %0 op_sel:[1,1,0] op_sel_hi:[1,0,1] neg_hi:[0,1,0]" : "+v"(t) : "v"(beta), "v"(B));
    return t;
}
DEV f2 cis(float r) { return f2{__builtin_amdgcn_cosf(r), __builtin_amdgcn_sinf(r)}; }
DEV float bflo(u32 v) { return __uint_as_float(v << 16); }
DEV float bfhi(u32 v) { return __uint_as_float(v & 0xFFFF0000u); }
DEV f2 bfup(u32 v) { return f2{bflo(v), bfhi(v)}; }
DEV u32 pkbf(f2 v) { bf2 r; r.x = (__bf16)v.x; r.y = (__bf16)v.y; return __builtin_bit_cast(u32, r); }
DEV float rbf(float f) { return __uint_as_float(((u32)__builtin_bit_cast(u16, (__bf16)f)) << 16); }
DEV f4 mfma(u4 a, u4 b, f4 c) { return __builtin_amdgcn_mfma_f32_16x16x32_bf16(__builtin_bit_cast(bf8, a), __builtin_bit_cast(bf8, b), c, 0, 0, 0); }
DEV u4 sanitize(u4 v) { return u4{min(v.x, 0xFFFFFFFEu), min(v.y, 0xFFFFFFFEu), min(v.z, 0xFFFFFFFEu), min(v.w, 0xFFFFFFFEu)}; }
DEV u32 max4(u4 v) { return max(max(v.x, v.y), max(v.z, v.w)); }
DEV void st128(u4 v, __amdgpu_buffer_rsrc_t rs, u32 voff, int soff) {
    __builtin_amdgcn_raw_buffer_store_b128(v, rs, voff, soff, 16);
    asm volatile("s_nop 1" :: "v"(v) : "memory");
}
template <int OFF2> DEV u4 trfrag2(LAS char* p) {
    s4 r0 = __builtin_amdgcn_ds_read_tr16_b64_v4i16((LAS s4*)p);
    s4 r1 = __builtin_amdgcn_ds_read_tr16_b64_v4i16((LAS s4*)(p + OFF2));
    u2 x0 = __builtin_bit_cast(u2, r0), x1 = __builtin_bit_cast(u2, r1);
    return u4{x0.x, x0.y, x1.x, x1.y};
}
template <int LI, int GI> DEV void dma_add(u32 vb, const void* sbase, u32 sl) {
    const u32 va = vb + (u32)GI;
    asm volatile("s_add_u32 m0, %2, %3\n\ts_nop 0\n\tglobal_load_lds_dwordx4 %0, %1" :: "v"(va), "s"(sbase), "s"(sl), "i"(LI) : "memory", "scc");
}
template <int LI, int GI> DEV void dma4k_c(u32 vb, const void* sbase, u32 sl) {
    const u32 va = vb + (u32)GI;
    asm volatile("s_add_u32 m0, %2, %3\n\ts_nop 0\n\tglobal_load_lds_dwordx4 %0, %1 sc1\n\tglobal_load_lds_dwordx4 %0, %1 offset:1024 sc1\n\t"
                 "global_load_lds_dwordx4 %0, %1 offset:2048 sc1\n\tglobal_load_lds_dwordx4 %0, %1 offset:3072 sc1"
                 :: "v"(va), "s"(sbase), "s"(sl), "i"(LI) : "memory", "scc");
}
template <int OFF> DEV void ldg_u16(u32& d, u32 vo, const void* sb) { asm volatile("global_load_ushort %0, %1, %2 offset:%3" : "=v"(d) : "v"(vo), "s"(sb), "i"(OFF) : "memory"); }
template <int OFF> DEV void ldg_u32(u32& d, u32 vo, const void* sb) { asm volatile("global_load_dword %0, %1, %2 offset:%3" : "=v"(d) : "v"(vo), "s"(sb), "i"(OFF) : "memory"); }
DEV void lds_wait(u32 addr, u32 v) {
    u32 tmp, cnt;
    asm volatile("s_mov_b32 %1, 0x80000\n"
                 "1:\n\tds_read_b32 %0, %2\n\ts_sub_u32 %1, %1, 1\n\ts_waitcnt lgkmcnt(0)\n\ts_cbranch_scc1 2f\n\tv_cmp_gt_u32 vcc, %3, %0\n\ts_cbranch_vccnz 1b\n2:"
                 : "=&v"(tmp), "=&s"(cnt) : "v"(addr), "s"(v) : "memory", "vcc", "scc");
}
template <int N, typename F> DEV void sfor(F&& f) { if constexpr (N > 0) { sfor<N - 1>(f); f(IC<N - 1>{}); } }

DEV void pin4(f4 (&x)[4]) { asm volatile("" : "+v"(x[0]), "+v"(x[1]), "+v"(x[2]), "+v"(x[3])); }
DEV void dma_rt(u32 voff, const void* sbase, u32 sl) {
    asm volatile("s_mov_b32 m0, %2\n\ts_nop 0\n\tglobal_load_lds_dwordx4 %0, %1" :: "v"(voff), "s"(sbase), "s"(sl) : "memory");
}
DEV u4 geglu8(const f4 (&acc)[4], LAS char* cs, f2 st) {
    u32 hw[4];
    const f2 nm = f2{-st.x, -st.x}, ri = f2{st.y, st.y};
#pragma unroll
    for (int c = 0; c < 2; c++) {
        f4 cg_ = *(LAS f4*)(cs + c * 64), cu_ = *(LAS f4*)(cs + (2 + c) * 64);
#pragma unroll
        for (int e = 0; e < 2; e++) {
            f2 g = pkfma(e ? cg_.zw : cg_.xy, nm, e ? acc[c].zw : acc[c].xy) * ri;
            f2 u = pkfma(e ? cu_.zw : cu_.xy, nm, e ? acc[2 + c].zw : acc[2 + c].xy) * ri;
            f2 z = g * pkfma(g * g, f2{-0.10294324f, -0.10294324f}, f2{-2.3022082f, -2.3022082f});
            f2 d = f2{__builtin_amdgcn_exp2f(z.x), __builtin_amdgcn_exp2f(z.y)} + f2{1.f, 1.f};
            f2 r = f2{__builtin_amdgcn_rcpf(d.x), __builtin_amdgcn_rcpf(d.y)};
            hw[2 * c + e] = pkbf(g * r * u);
        }
    }
    return u4{hw[0], hw[1], hw[2], hw[3]};
}

enum { F_XL = 1, F_XC, F_P1, F_C0, F_CS, F_WL, F_WB, F_D1, F_E1, F_E2, F_E3, F_F3, F_S };
enum { G_TAG = 1, G_W0, G_W1, G_W2, G_S0, G_W3, G_CS, G_PB, G_D2, G_D3 };

__global__ void __launch_bounds__(512, 2) fused(Args a) {
    extern __shared__ __attribute__((aligned(16))) char smem[];
    LAS char* lds = (LAS char*)smem;
    const int tid = threadIdx.x, lane = tid & 63, w = __builtin_amdgcn_readfirstlane(tid >> 6);
    const int b = blockIdx.x, xcd = b & 7, r = b >> 3, t = r & 7, par = xcd & 1, rg = (xcd >> 1) * 4 + (r >> 3);
    const int s2 = 2 * rg + par, cg = par * 8 + t, sig = xcd * 32 + r, pid = s2 * 8 + t;
    const int rq = w & 3, hf = w >> 2;
    const u32 l16 = lane * 16;
    *(volatile LAS u32*)(lds + L_PROG + w * 4) = 0;
    BAR();
    STAMP(0);
    {
        const char* xs = (const char*)a.x + (size_t)((s2 ^ hf) + 512 * rq) * 1024;
        if (hf) __builtin_amdgcn_s_sleep(4);
#pragma unroll
        for (int i = 0; i < 16; i++) { const int row = (i + 2 * t) & 15; dma_rt(l16 ^ (u32)(row * (32768 + 16)), xs, (u32)(w * 16384 + row * 1024)); }
    }
    const int li = lane & 15, lg = lane >> 4;
    const int c2p = lane & 3, l3 = (lane >> 2) & 7, qb = lane >> 5;
    const __amdgpu_buffer_rsrc_t rs = __builtin_amdgcn_make_buffer_rsrc(a.ws, 0, WS_BYTES, 0x00020000);
    KC kc; kc.pm = f2{1.f, -1.f}; kc.rpm = f2{0.70710678118654752f, -0.70710678118654752f};
    asm volatile("" : "+s"(kc.pm), "+s"(kc.rpm));
    const u4 ones = {0x3F803F80u, 0x3F803F80u, 0x3F803F80u, 0x3F803F80u};
    auto post = [&](u32 v) { asm volatile("s_waitcnt lgkmcnt(0)" ::: "memory"); *(volatile LAS u32*)(lds + L_PROG + w * 4) = v; FENCE(); };
    const u32 vpg = (u32)(L_PROG + (lane & 3) * 4);
    auto wait_grp = [&](int g, u32 v) { lds_wait(vpg + g * 16, v); };
    auto wait_pair = [&](u32 v) { lds_wait((u32)(L_PROG + (w ^ 4) * 4), v); };
    u2 tag;
    auto get_tag = [&]() { u2 tg = *(LAS u2*)(lds + L_TAG); tag.x = __builtin_amdgcn_readfirstlane(tg.x); tag.y = __builtin_amdgcn_readfirstlane(tg.y); };
    auto flag_store = [&](int off) { __builtin_amdgcn_raw_buffer_store_b64(tag, rs, lane ? WS_BYTES : 0, off, 16); };
    auto flag_wait = [&](u32 voff, int soff) {
        _Pragma("unroll 1") for (int spins = 0; spins < SPIN_CAP; spins++) {
            u2 f = __builtin_amdgcn_raw_buffer_load_b64(rs, voff, soff, 16);
            if (__all(f.x == tag.x && f.y == tag.y)) break;
            __builtin_amdgcn_s_sleep(4);
            FENCE();
        }
        __builtin_amdgcn_fence(__ATOMIC_ACQUIRE, "wavefront");
        FENCE();
    };
    auto read_tile = [&](u4 (&x)[16], int tile) {
        LAS char* p = lds + tile * 65536 + (16 * rq + li) * 1024;
#pragma unroll
        for (int k = 0; k < 16; k++) x[k] = *(LAS u4*)(p + (((4 * k + lg) ^ li) * 16));
    };
    auto stats = [&](const u4 (&x)[16]) {
        f4 s1 = {0, 0, 0, 0}, sq = s1, s1b = s1, sqb = s1;
#pragma unroll
        for (int k = 0; k < 16; k += 2) { s1 = mfma(ones, x[k], s1); sq = mfma(x[k], x[k], sq); s1b = mfma(ones, x[k + 1], s1b); sqb = mfma(x[k + 1], x[k + 1], sqb); }
        sq += sqb;
        const int rr = li & 3;
        const float d = rr == 0 ? sq.x : rr == 1 ? sq.y : rr == 2 ? sq.z : sq.w;
        if (lg == (li >> 2)) {
            const float mean = (s1.x + s1b.x) * (1.0f / 512.0f);
            float var = d * (1.0f / 512.0f) - mean * mean;
            if (!(mean * mean <= 1024.0f * var)) {
                const u32* xrow = (const u32*)(a.x + (size_t)((s2 ^ hf) + 32 * (16 * rq + li)) * 512);
                float q = 0.f;
                _Pragma("unroll 1") for (int i = 0; i < 256; i++) { u32 v = xrow[i]; float d0 = bflo(v) - mean, d1 = bfhi(v) - mean; q += d0 * d0; q += d1 * d1; }
                var = q * (1.0f / 512.0f);
            }
            const float den = rbf(__builtin_amdgcn_sqrtf(rbf(rbf(var) + 1e-5f)));
            *(LAS f2*)(lds + L_STAT + (hf * 64 + 16 * rq + li) * 8) = f2{rbf(mean), __builtin_amdgcn_rcpf(den)};
        }
        FENCE();
    };
    const int nblk = (t < 6 - par) ? 3 : 2, fb2 = 32 + par * 6 + t;
    const char* wdp = (const char*)a.ws + WS_WDP + (size_t)(t * NFB + rq) * 4096;

    if (hf == 0) {
        __builtin_amdgcn_s_setprio(2);
        f2 tw64[8], twT[8];
        int x3a[4];
        auto fft13_consts = [&](float scale) {
            int lane_ = lane; asm volatile("" : "+v"(lane_));
            const int c2p = lane_ & 3, l3 = (lane_ >> 2) & 7, qb = lane_ >> 5;
            const int wq = rq, q1 = qb ? (wq == 0 ? 4 : 8 - wq) : wq, c2 = 4 * t + c2p;
            const float f64 = (float)l3 * (1.0f / 64.0f);
#pragma unroll
            for (int k = 1; k < 8; k++) tw64[k] = cis(f64 * (float)k);
            const float fT = (float)(8 * c2 * q1 + s2 * l3) * (1.0f / 2048.0f), dT = (float)s2 * (1.0f / 256.0f);
#pragma unroll
            for (int k = 0; k < 8; k++) twT[k] = cis(fT + dT * (float)k) * scale;
#pragma unroll
            for (int k = 0; k < 4; k++) {
                const int ua = (k >> 1) * 4 + (k & 1), u1 = l3 + 8 * ua;
                const int cls = (u1 < 32) ? u1 : ((64 - u1) & 31), hi = u1 >> 5;
                int mbit, sub;
                if (wq == 0) { mbit = qb; sub = hi; } else if (cls != 0) { sub = hi; mbit = qb ^ hi; } else { mbit = hi; sub = qb ^ hi; }
                x3a[k] = L_FFT + wq * 4608 + (cls * 18 + mbit * 8 + sub * 4 + c2p) * 8 - (k == 3 ? 2304 : 0);
            }
            asm volatile("" : "+v"(tw64[1]), "+v"(tw64[2]), "+v"(tw64[3]), "+v"(tw64[4]), "+v"(tw64[5]), "+v"(tw64[6]), "+v"(tw64[7]),
                         "+v"(twT[0]), "+v"(twT[1]), "+v"(twT[2]), "+v"(twT[3]), "+v"(twT[4]), "+v"(twT[5]), "+v"(twT[6]), "+v"(twT[7]),
                         "+v"(x3a[0]), "+v"(x3a[1]), "+v"(x3a[2]), "+v"(x3a[3]));
        };

        const int f1st = WS_F1 + ((lane >> 3) * 8 + (((lane >> 2) & 1) ? (rq == 0 ? 4 : 8 - rq) : rq)) * 16384 + pid * 64 + c2p * 16;
        fft13_consts(1.0f);
        WAITVM(0);
        post(F_XL);
        STAMP(1);
        u32 raw[8];
        const int rw = lane >> 2;
        {
            u4 xa[16];
            read_tile(xa, 0);
#pragma unroll
            for (int c1 = 0; c1 < 8; c1++) raw[c1] = *(LAS u32*)(lds + (16 * rq + rw) * 1024 + (((8 * c1 + t) ^ rw) * 16) + c2p * 4);
            post(F_XC);
            stats(xa);
        }
        STAMP(2);
        f2 v[8];
        {
            const f2 st = *(LAS f2*)(lds + L_STAT + (16 * rq + rw) * 8);
#pragma unroll
            for (int c1 = 0; c1 < 8; c1++) v[c1] = bfup(pkbf(bfup(pkbf(bfup(raw[c1]) - st.xx)) * st.yy));
            dft8<false>(v, kc);
            LAS f2* xb1 = (LAS f2*)(lds + L_FFT) + w * 64 + lane;
#pragma unroll
            for (int q1 = 0; q1 < 8; q1++) { const int reg = (q1 < 4) ? q1 * 2 : (((8 - q1) & 3) * 2 + 1); xb1[reg * 288] = v[q1]; }
        }
        post(F_P1);
#pragma unroll
        for (int c1 = 0; c1 < 8; c1++) *(LAS u32*)(lds + ((rq >> 1) ? L_X1 : L_X0) + ((rq & 1) * 16 + rw) * 128 + ((c1 ^ (rw & 7)) * 16) + c2p * 4) = raw[c1];
        wait_grp(0, F_P1);
        {
            LAS f2* reg = (LAS f2*)(lds + L_FFT) + (w * 2 + qb) * 288;
#pragma unroll
            for (int k = 0; k < 8; k++) v[k] = reg[(lane & 31) + 32 * k];
            dft8<false>(v, kc);
#pragma unroll
            for (int k = 1; k < 8; k++) v[k] = cmul<true>(v[k], tw64[k]);
#pragma unroll
            for (int k = 0; k < 8; k++) reg[36 * k + (lane & 31)] = v[k];
            FENCE();
#pragma unroll
            for (int k = 0; k < 8; k++) v[k] = reg[36 * l3 + 4 * k + c2p];
            dft8<false>(v, kc);
#pragma unroll
            for (int k = 0; k < 8; k++) v[k] = cmul<true>(v[k], twT[k]);
            FENCE();
            *(LAS f2*)(lds + x3a[0]) = v[0];
            *(LAS f2*)(lds + x3a[1]) = v[1]; *(LAS f2*)(lds + x3a[1] + 1152) = v[2]; *(LAS f2*)(lds + x3a[1] + 2304) = v[3];
            *(LAS f2*)(lds + x3a[2]) = v[4];
            *(LAS f2*)(lds + x3a[3] + 2304) = v[5]; *(LAS f2*)(lds + x3a[3] + 1152) = v[6]; *(LAS f2*)(lds + x3a[3]) = v[7];
            FENCE();
#pragma unroll
            for (int i = 0; i < 4; i++) {
                u4 o = *(LAS u4*)(lds + L_FFT + w * 4608 + (i * 8 + (lane >> 3)) * 144 + (lane & 7) * 16);
                st128(o, rs, f1st, i * (64 * 16384));
            }
        }
        STAMP(3);
        f2 tw0[4], tw1[8], al[8], be[8];
        const int rb = qb, r3 = 2 * rq + rb;
        const int cls2 = sig >> 3, m2 = sig & 7;
        const int wq2 = (m2 < 4) ? m2 : ((8 - m2) & 3), pos1 = wq2 * 64 + cls2 * 2 + (m2 >> 2);
        const bool special = (cls2 == 0) && ((m2 & 3) == 0);
        int u1a, q1a, u1b, q1b;
        if (cls2 != 0) { u1a = cls2; q1a = m2; u1b = 64 - cls2; q1b = (8 - m2) & 7; }
        else if (special) { u1a = 0; u1b = 32; q1a = q1b = m2; }
        else { u1a = u1b = (m2 < 4) ? 0 : 32; q1a = m2; q1b = 8 - m2; }
        int sA, vH0, sB, vH1;
        if (!special) { sA = 0; vH0 = rq; sB = 1; vH1 = (u1a == 0) ? ((4 - rq) & 3) : 3 - rq; }
        else { sA = sB = rq >> 1; vH0 = rq & 1; vH1 = (rq == 0) ? 2 : (rq == 1) ? 3 : (rq == 2) ? 3 : 2; }
        const bool q0 = (m2 == 0);
        const int dy = ((sA ? u1b : u1a) == 0 && vH0 == 0) ? 8 : 7, dq = q0 ? 8 : 7;
        int y1 = lane >> 3, t1 = lane & 7, y2 = (dy - y1) & 7, t2 = (dq - t1) & 7, hA = vH0, hB = vH1;
        bool self = false;
        if (special && rq == 0) {
            const int k = lane & 31;
            if (qb) { hA = hB = 2; y1 = k >> 3; t1 = k & 7; y2 = 7 - y1; t2 = (dq - t1) & 7; }
            else {
                hA = hB = 0; const int vx = k & 3, y = k >> 2;
                if (!q0 || vx) { y1 = y; t1 = vx; y2 = (8 - y) & 7; t2 = dq - vx; }
                else { t1 = t2 = (y >= 4) ? 4 : 0; const int yy = y & 3; if (yy) { y1 = yy; y2 = 8 - yy; } else { y1 = 0; y2 = 4; self = true; } }
            }
        }
        const int uA = (sA ? u1b : u1a) + 64 * (hA + 4 * y1), uB = (sB ? u1b : u1a) + 64 * (hB + 4 * y2);
        const int qA = (sA ? q1b : q1a) + 8 * t1, qB = (sB ? q1b : q1a) + 8 * t2;
        lds_wait((u32)(L_PROG + 16), G_TAG);
        get_tag();
        WAITVM(0);
        flag_store(WS_FF1 + ((rq * 4 + ((s2 & 7) >> 1)) * 64 + (s2 >> 3) * 16 + (s2 & 1) * 8 + t) * 8);
        STAMP(4);
        __builtin_amdgcn_s_setprio(0);
        {
            const u32 fz = (u32)((li >> 3) | ((lg & 1) << 1));
            const u32 tb = (u32)((8 * lg + (li >> 2)) * 128 + 8 * (li & 3)) + (rq >> 1) * 4096;
            u32 pc0 = tb + (((rq & 1) ^ fz) << 5), pc1 = tb + (((2 + (rq & 1)) ^ fz) << 5);
            f4 c0 = {0, 0, 0, 0}, c1 = c0;
            wait_grp(1, G_W1);
#pragma unroll
            for (int ks = 0; ks < 8; ks++) { c0 = mfma(trfrag2<512>(lds + pc0 + ks * PG), ones, c0); c1 = mfma(trfrag2<512>(lds + pc1 + ks * PG), ones, c1); }
            post(F_C0);
            if (nblk == 3) {
                wait_grp(0, F_C0); wait_grp(1, G_S0);
                const u32 wvB = (u32)((lane >> 2) * 2752 + (((lane & 3) ^ (((lane >> 5) & 1) << 1)) * 16));
                const char* src = (const char*)((rq >> 1) ? a.wu : a.wg) + (size_t)((rq & 1) * 16) * 2752 + fb2 * 64;
                sfor<16>([&](auto K) { constexpr int k = K; dma_add<k * 4096, k * 32 * 2752>(wvB, src, (u32)(rq * 1024)); });
            }
            pc0 += 65536; pc1 += 65536; asm volatile("" : "+v"(pc0), "+v"(pc1));
            wait_grp(1, G_W3);
#pragma unroll
            for (int ks = 0; ks < 8; ks++) { c0 = mfma(trfrag2<512>(lds + pc0 + ks * PG), ones, c0); c1 = mfma(trfrag2<512>(lds + pc1 + ks * PG), ones, c1); }
            if (li == 0) { *(LAS f4*)(lds + L_CS + rq * 64 + lg * 16) = c0; *(LAS f4*)(lds + L_CS + 256 + rq * 64 + lg * 16) = c1; }
            post(F_CS);
        }
        WAITVM(0);
        post(F_WL);
        u32 wl[20];
        {
            const u32 vA = (u32)((uA * 257 + qA) * 2), vB = (u32)((uB * 257 + qB) * 2);
            sfor<4>([&](auto K) { ldg_u16<K * 128>(wl[K], vA, a.wr); ldg_u16<K * 128>(wl[8 + K], vA, a.wi); ldg_u16<K * 128>(wl[4 + K], vB, a.wr); ldg_u16<K * 128>(wl[12 + K], vB, a.wi); });
            const u32 vA2 = (u32)(uA * 514 + 512), vB2 = (u32)(uB * 514 + 512);
            ldg_u16<0>(wl[16], vA2, a.wr); ldg_u16<0>(wl[17], vA2, a.wi); ldg_u16<0>(wl[18], vB2, a.wr); ldg_u16<0>(wl[19], vB2, a.wi);
        }
        const int rowB = L_FFT + ((qb ? (vH1 * 2 + sB) : (vH0 * 2 + sA)) * 36 + (lane & 31)) * 8;
        const bool rot = q0 && (t1 == 0);
        const int p3a1 = L_FFT + (y1 * 290 + (hA * 2 + sA) * 36 + t1 * 4) * 8;
        const int p3a2 = L_FFT + (y2 * 290 + (hB * 2 + sB) * 36 + t2 * 4) * 8;
        const float thA = (float)qA * (1.0f / 512.0f), thB = (float)qB * (1.0f / 512.0f);
#pragma unroll
        for (int k = 1; k < 4; k++) tw0[k] = cis((float)r3 * ((float)k / 32.0f));
#pragma unroll
        for (int k = 1; k < 8; k++) tw1[k] = cis((float)c2p * ((float)k / 32.0f));
        flag_wait(lane * 8, WS_FP + t * 1024);
        flag_wait(min(lane + 64, 85) * 8, WS_FP + t * 1024);
        wait_grp(0, F_WL);
        wait_grp(1, G_CS);
        sfor<4>([&](auto I) { constexpr int i = I; dma4k_c<L_SET1 + i * 16384, i * 16384>(l16, wdp, (u32)(rq * 4096)); });
        if (nblk == 3) {
            const u32 tb = (u32)((8 * lg + (li >> 2)) * 64 + (lg & 1) * 32 + 8 * (li & 3));
            LAS char* pc = lds + (tb ^ ((rq & 1) * 32)) + (rq >> 1) * 2048;
            f4 c0 = {0, 0, 0, 0}, c1 = c0;
#pragma unroll
            for (int ks = 0; ks < 16; ks += 2) { c0 = mfma(trfrag2<256>(pc + ks * 4096), ones, c0); c1 = mfma(trfrag2<256>(pc + (ks + 1) * 4096), ones, c1); }
            if (li == 0) *(LAS f4*)(lds + L_CS + 512 + rq * 64 + lg * 16) = c0 + c1;
        }
        post(F_WB);
        __builtin_amdgcn_s_setprio(2);
        STAMP(13);
        flag_wait(lane * 8, WS_FF1 + (wq2 * 4 + rq) * 512);
        post(F_D1);
        STAMP(5);
        const int f1off = WS_F1 + sig * 16384 + 2 * rq * 512;
        u4 d[4];
#pragma unroll
        for (int H = 0; H < 4; H++) d[H] = __builtin_amdgcn_raw_buffer_load_b128(rs, l16, f1off + H * 4096, 16);
        asm volatile("" : "+v"(wl[0]), "+v"(wl[1]), "+v"(wl[2]), "+v"(wl[3]), "+v"(wl[4]), "+v"(wl[5]), "+v"(wl[6]), "+v"(wl[7]), "+v"(wl[8]), "+v"(wl[9]),
                     "+v"(wl[10]), "+v"(wl[11]), "+v"(wl[12]), "+v"(wl[13]), "+v"(wl[14]), "+v"(wl[15]), "+v"(wl[16]), "+v"(wl[17]), "+v"(wl[18]), "+v"(wl[19]) :: "memory");
        {
            f2 W[8], WB[8];
#pragma unroll
            for (int e = 0; e < 8; e++) W[e] = f2{bflo(wl[e]), bflo(wl[8 + e])};
#pragma unroll
            for (int k = 0; k < 4; k++) { WB[k] = W[4 + 3 - k]; WB[4 + k] = W[3 - k]; }
            if (q0) {
#pragma unroll
                for (int k = 0; k < 4; k++) {
                    f2 n1 = rot ? W[(4 - k) & 3] : W[3 - k], n2 = rot ? W[4 + ((4 - k) & 3)] : W[4 + 3 - k];
                    WB[k] = self ? n1 : n2; WB[4 + k] = self ? n2 : n1;
                }
                const f2 wS[2] = {f2{bflo(wl[16]), bflo(wl[17])}, f2{bflo(wl[18]), bflo(wl[19])}};
#pragma unroll
                for (int e = 0; e < 8; e += 4) {
                    f2 own = wS[e >> 2], oth = self ? own : wS[1 - (e >> 2)];
                    f2 S0 = pkfma(WB[e], kc.pm, W[e]) * 0.5f, S1c = pkfma(own, kc.pm, oth) * 0.5f;
                    WB[e] = rot ? S1c : WB[e];
                    al[e] = rot ? S0 : W[e];
                }
                W[0] = al[0]; W[4] = al[4];
            }
#pragma unroll
            for (int e = 0; e < 8; e++) {
                const f2 cs = cis(((e >> 2) ? thB : thA) + 0.125f * (float)(e & 3));
                f2 P = pkfma(WB[e], kc.pm, W[e]), M = pkfma(WB[e], kc.pm.yx, W[e]);
                al[e] = pkfma(M, -cs.yy, P);
                be[e] = M.yx * (kc.pm.yx * cs.xx);
            }
            asm volatile("" : "+v"(al[0]), "+v"(al[1]), "+v"(al[2]), "+v"(al[3]), "+v"(al[4]), "+v"(al[5]), "+v"(al[6]), "+v"(al[7]),
                         "+v"(be[0]), "+v"(be[1]), "+v"(be[2]), "+v"(be[3]), "+v"(be[4]), "+v"(be[5]), "+v"(be[6]), "+v"(be[7]),
                         "+v"(tw0[1]), "+v"(tw0[2]), "+v"(tw0[3]), "+v"(tw1[1]), "+v"(tw1[2]), "+v"(tw1[3]), "+v"(tw1[4]), "+v"(tw1[5]), "+v"(tw1[6]), "+v"(tw1[7]));
        }
        f2 e0[4], e1[4];
#pragma unroll
        for (int H = 0; H < 4; H++) { e0[H] = f2{__uint_as_float(d[H].x), __uint_as_float(d[H].y)}; e1[H] = f2{__uint_as_float(d[H].z), __uint_as_float(d[H].w)}; }
        dft4<false>(e0[0], e0[1], e0[2], e0[3], kc); dft4<false>(e1[0], e1[1], e1[2], e1[3], kc);
        STAMP(6);
        LAS char* area = lds + L_FFT + rq * 4640;
#pragma unroll
        for (int k = 0; k < 4; k++) {
            if (k) { e0[k] = cmul<true>(e0[k], tw0[k]); e1[k] = cmul<true>(e1[k], tw0[k]); }
            *(LAS f4*)(area + (l3 * 72 + 2 * c2p + 8 * k + 32 * rb) * 8) = f4{e0[k].x, e0[k].y, e1[k].x, e1[k].y};
        }
        FENCE();
#pragma unroll
        for (int k = 0; k < 8; k++) v[k] = *(LAS f2*)(area + (k * 72 + lane) * 8);
        dft8<false>(v, kc);
#pragma unroll
        for (int k = 1; k < 8; k++) v[k] = cmul<true>(v[k], tw1[k]);
        FENCE();
        LAS char* pb = lds + L_FFT + (r3 * 290 + l3 * 36 + c2p) * 8;
#pragma unroll
        for (int k = 0; k < 8; k++) *(LAS f2*)(pb + k * 32) = v[k];
        post(F_E1);
        wait_grp(0, F_E1);
#pragma unroll
        for (int k = 0; k < 8; k++) v[k] = *(LAS f2*)(lds + rowB + k * 2320);
        dft8<false>(v, kc);
#pragma unroll
        for (int k = 0; k < 8; k++) *(LAS f2*)(lds + rowB + k * 2320) = v[k];
        FENCE();
        f4 a0 = *(LAS f4*)(lds + p3a1), a1 = *(LAS f4*)(lds + p3a1 + 16), b0 = *(LAS f4*)(lds + p3a2), b1 = *(LAS f4*)(lds + p3a2 + 16);
        f2 X[8] = {a0.xy, a0.zw, a1.xy, a1.zw, b0.xy, b0.zw, b1.xy, b1.zw};
        dft4<false>(X[0], X[1], X[2], X[3], kc); dft4<false>(X[4], X[5], X[6], X[7], kc);
        f2 Y[8];
#pragma unroll
        for (int k = 0; k < 4; k++) { Y[k] = X[4 + 3 - k]; Y[4 + k] = X[3 - k]; }
        if (q0) {
#pragma unroll
            for (int k = 0; k < 4; k++) {
                f2 n1 = rot ? X[(4 - k) & 3] : X[3 - k], n2 = rot ? X[4 + ((4 - k) & 3)] : X[4 + 3 - k];
                Y[k] = self ? n1 : n2; Y[4 + k] = self ? n2 : n1;
            }
        }
        f2 O[8];
#pragma unroll
        for (int e = 0; e < 8; e++) O[e] = cfmac_conj(cmul<false>(X[e], al[e]), be[e], Y[e]);
        dft4<true>(O[0], O[1], O[2], O[3], kc); dft4<true>(O[4], O[5], O[6], O[7], kc);
        *(LAS f4*)(lds + p3a1) = f4{O[0].x, O[0].y, O[1].x, O[1].y}; *(LAS f4*)(lds + p3a1 + 16) = f4{O[2].x, O[2].y, O[3].x, O[3].y};
        *(LAS f4*)(lds + p3a2) = f4{O[4].x, O[4].y, O[5].x, O[5].y}; *(LAS f4*)(lds + p3a2 + 16) = f4{O[6].x, O[6].y, O[7].x, O[7].y};
        FENCE();
#pragma unroll
        for (int k = 0; k < 8; k++) v[k] = *(LAS f2*)(lds + rowB + k * 2320);
        dft8<true>(v, kc);
#pragma unroll
        for (int k = 0; k < 8; k++) *(LAS f2*)(lds + rowB + k * 2320) = v[k];
        post(F_E2);
        wait_grp(0, F_E2);
#pragma unroll
        for (int k = 0; k < 8; k++) v[k] = *(LAS f2*)(pb + k * 32);
#pragma unroll
        for (int k = 1; k < 8; k++) v[k] = cmul<false>(v[k], tw1[k]);
        dft8<true>(v, kc);
        FENCE();
#pragma unroll
        for (int k = 0; k < 8; k++) *(LAS f2*)(area + (k * 72 + lane) * 8) = v[k];
        FENCE();
#pragma unroll
        for (int k = 0; k < 4; k++) {
            f4 q = *(LAS f4*)(area + (l3 * 72 + 2 * c2p + 8 * k + 32 * rb) * 8);
            e0[k] = q.xy; e1[k] = q.zw;
            if (k) { e0[k] = cmul<false>(e0[k], tw0[k]); e1[k] = cmul<false>(e1[k], tw0[k]); }
        }
        dft4<true>(e0[0], e0[1], e0[2], e0[3], kc); dft4<true>(e1[0], e1[1], e1[2], e1[3], kc);
        const int f2st = WS_F2 + (r3 * 8 + l3) * 16384 + pos1 * 64 + c2p * 16;
#pragma unroll
        for (int H = 0; H < 4; H++) {
            u4 o = {__float_as_uint(e0[H].x), __float_as_uint(e0[H].y), __float_as_uint(e1[H].x), __float_as_uint(e1[H].y)};
            st128(o, rs, f2st, H * (64 * 16384));
        }
        STAMP(7);
        post(F_E3);
        WAITVM(0);
        flag_store(WS_FF2 + (rq * 256 + pos1) * 8);
        STAMP(8);
        fft13_consts(0.5f / 524288.0f);
        wait_grp(0, F_E3);
        flag_wait(lane * 8, WS_FF2 + (((s2 & 7) >> 1) * 256 + w * 64) * 8);
        const int off = WS_F2 + pid * 16384 + w * 4096;
#pragma unroll
        for (int i = 0; i < 4; i++) d[i] = __builtin_amdgcn_raw_buffer_load_b128(rs, l16, off + i * 1024, 16);
        STAMP(9);
#pragma unroll
        for (int i = 0; i < 4; i++) *(LAS u4*)(lds + L_FFT + w * 4608 + (i * 8 + (lane >> 3)) * 144 + (lane & 7) * 16) = d[i];
        FENCE();
        v[0] = *(LAS f2*)(lds + x3a[0]);
        v[1] = *(LAS f2*)(lds + x3a[1]); v[2] = *(LAS f2*)(lds + x3a[1] + 1152); v[3] = *(LAS f2*)(lds + x3a[1] + 2304);
        v[4] = *(LAS f2*)(lds + x3a[2]);
        v[5] = *(LAS f2*)(lds + x3a[3] + 2304); v[6] = *(LAS f2*)(lds + x3a[3] + 1152); v[7] = *(LAS f2*)(lds + x3a[3]);
#pragma unroll
        for (int k = 0; k < 8; k++) v[k] = cmul<false>(v[k], twT[k]);
        dft8<true>(v, kc);
#pragma unroll
        for (int k = 1; k < 8; k++) v[k] = cmul<false>(v[k], tw64[k]);
        FENCE();
        LAS f2* reg = (LAS f2*)(lds + L_FFT) + (w * 2 + qb) * 288;
#pragma unroll
        for (int k = 0; k < 8; k++) reg[36 * l3 + 4 * k + c2p] = v[k];
        FENCE();
#pragma unroll
        for (int k = 0; k < 8; k++) v[k] = reg[36 * k + (lane & 31)];
        dft8<true>(v, kc);
        FENCE();
#pragma unroll
        for (int k = 0; k < 8; k++) reg[(lane & 31) + 32 * k] = v[k];
        post(F_F3);
        wait_grp(1, G_D2);
        wait_grp(0, F_F3);
        if (b == 0 && w == 0) __builtin_amdgcn_raw_buffer_store_b64(*(LAS u2*)(lds + L_TAG + 8), rs, lane ? WS_BYTES : 0, 0, 16);
        LAS f2* xb1 = (LAS f2*)(lds + L_FFT) + w * 64 + lane;
#pragma unroll
        for (int q1 = 0; q1 < 8; q1++) { const int rg_ = (q1 < 4) ? q1 * 2 : (((8 - q1) & 3) * 2 + 1); v[q1] = xb1[rg_ * 288]; }
        dft8<true>(v, kc);
#pragma unroll
        for (int c1 = 0; c1 < 8; c1++) *(LAS u32*)(lds + L_S + (16 * w + (lane >> 2)) * 144 + c1 * 16 + c2p * 4) = pkbf(v[c1]);
        post(F_S);
        STAMP(10);
    } else {
        u2 ev;
        { u32 zero = 0; asm volatile("global_load_dwordx2 %0, %1, %2 sc1" : "=v"(ev) : "v"(zero), "s"(a.ws) : "memory"); }
        u32 pk[8];
        const int un = sig < 4 * NFB ? sig : 0, fb = un >> 2, jj = (un >> 1) & 1, tcol = (un & 1) * 4 + rq;
        {
            const u32 vd = (u32)(((fb * 32 + 4 * lg) * 512 + 64 * (4 * jj + (li >> 2)) + 8 * tcol + 2 * (li & 3)) * 2);
            sfor<4>([&](auto I) {
                ldg_u32<I * 1024>(pk[I], vd, a.wd);
                ldg_u32<I * 1024>(pk[4 + I], vd, (const char*)a.wd + 16384);
            });
        }
        const char* wgs = (const char*)a.wg + (size_t)(64 * rq) * 2752 + cg * 128;
        const char* wus = (const char*)a.wu + (size_t)(64 * rq) * 2752 + cg * 128;
        const int j4 = r >> 3;
        const u32 wvE = (u32)((lane >> 3) * 2752 + (((lane & 7) ^ (((lane >> 4) & 1) << 1) ^ ((j4 & 1) << 2)) * 16)), wvO = (u32)((lane >> 3) * 2752 + (((lane & 7) ^ (((lane >> 4) & 1) << 1) ^ ((~j4 & 1) << 2)) * 16));
        auto dma_w1 = [&](auto KK, auto I) {
            constexpr int kk = KK, g = I >> 1, mat = I & 1;
            const int gr = (g + j4) & 3;
            dma_add<kk * PG + mat * 4096, kk * 32 * 2752>(((g & 1) ? wvO : wvE) + (u32)(gr * 8 * 2752), mat ? wus : wgs, (u32)(rq * 2 * PG + gr * 1024));
        };
        u4 xa[16], xb[16];
        wait_pair(F_XL);
        read_tile(xa, 0);
        asm volatile("s_waitcnt lgkmcnt(0)" ::: "memory");
        wait_pair(F_XC);
        sfor<8>([&](auto I) { dma_w1(IC<0>{}, I); });
        sfor<8>([&](auto I) { dma_w1(IC<1>{}, I); });
        asm volatile("s_waitcnt vmcnt(16)" : "+v"(ev), "+v"(pk[0]), "+v"(pk[1]), "+v"(pk[2]), "+v"(pk[3]), "+v"(pk[4]), "+v"(pk[5]), "+v"(pk[6]), "+v"(pk[7]) :: "memory");
        STAMP(1);
        read_tile(xb, 1);
        {
            const u64 epoch = ((u64)(u32)__builtin_amdgcn_readfirstlane(ev.y) << 32) | (u32)__builtin_amdgcn_readfirstlane(ev.x);
            u64 tg = (epoch + 1) * 0x9E3779B97F4A7C15ull; tg ^= tg >> 29; tg *= 0xBF58476D1CE4E5B9ull; tg ^= tg >> 32;
            if (w == 4) *(LAS u4*)(lds + L_TAG) = u4{(u32)tg, (u32)(tg >> 32), (u32)(epoch + 1), (u32)((epoch + 1) >> 32)};
        }
        post(G_TAG);
        {
            const int so = sig < 4 * NFB ? WS_WDP + ((tcol * NFB + fb) * 4 + 2 * jj) * 1024 : WS_BYTES;
            u4 oe = {__builtin_amdgcn_perm(pk[1], pk[0], 0x05040100), __builtin_amdgcn_perm(pk[3], pk[2], 0x05040100), __builtin_amdgcn_perm(pk[5], pk[4], 0x05040100), __builtin_amdgcn_perm(pk[7], pk[6], 0x05040100)};
            u4 oo = {__builtin_amdgcn_perm(pk[1], pk[0], 0x07060302), __builtin_amdgcn_perm(pk[3], pk[2], 0x07060302), __builtin_amdgcn_perm(pk[5], pk[4], 0x07060302), __builtin_amdgcn_perm(pk[7], pk[6], 0x07060302)};
            st128(oe, rs, l16, so);
            st128(oo, rs, l16, so + 1024);
        }
        stats(xb);
        STAMP(2);
        const int hoff0 = WS_H + ((s2 * 4 + rq) * NFB) * 1024, hoff1 = WS_H + (((s2 ^ 1) * 4 + rq) * NFB) * 1024;
        f2 st0, st1;
        {
            const u32 fz = (u32)((li >> 3) | ((lg & 1) << 1));
            const u32 tb = (u32)((8 * lg + (li >> 2)) * 128 + 8 * (li & 3));
            u32 pt[4];
#pragma unroll
            for (int c = 0; c < 4; c++) pt[c] = tb + ((c ^ fz) << 5);
            f4 acc[2][2][4];
#pragma unroll
            for (int k = 0; k < 2; k++) {
#pragma unroll
                for (int j = 0; j < 4; j++) { acc[k][0][j] = f4{0, 0, 0, 0}; acc[k][1][j] = f4{0, 0, 0, 0}; }
            }
            u4 F[2][4];
            auto ldf = [&](int ks, int k, u4 (&f)[4]) {
                const int off = (ks & 7) * PG;
                f[0] = trfrag2<512>(lds + pt[2 * k] + off); f[1] = trfrag2<512>(lds + pt[2 * k + 1] + off);
                f[2] = trfrag2<512>(lds + pt[2 * k] + off + 4096); f[3] = trfrag2<512>(lds + pt[2 * k + 1] + off + 4096);
            };
            WAITVM(10); post(G_W0); wait_grp(1, G_W0);
            get_tag();
            STAMP(3);
            ldf(0, 0, F[0]);
            sfor<32>([&](auto S) {
                constexpr int hs = S, s = hs >> 1, k = hs & 1, ks = (s & 8) | ((s & 3) << 1) | ((s >> 2) & 1);
                constexpr int hn = hs + 1, sn = hn >> 1, kn = (sn & 8) | ((sn & 3) << 1) | ((sn >> 2) & 1);
                if (hs == 7) { WAITVM(7); post(G_W1); flag_store(sig < 4 * NFB ? WS_FP + (((sig & 1) * 4 + rq) * 128 + (sig >> 1)) * 8 : WS_BYTES); wait_grp(1, G_W1); STAMP(14); }
                if (hs == 15) {
                    WAITVM(7); post(G_W2); wait_grp(1, G_W2); STAMP(15);
#pragma unroll
                    for (int c = 0; c < 4; c++) pt[c] += 65536;
                    asm volatile("" : "+v"(pt[0]), "+v"(pt[1]), "+v"(pt[2]), "+v"(pt[3]));
                }
                if (hs == 16) post(G_S0);
                if (hs == 23) {
                    WAITVM(0); post(G_W3);
                    wait_grp(1, G_W3); STAMP(16); CSTAMP(17);
                }
                if (hs < 16) dma_w1(IC<8 + (hs >> 3)>{}, IC<hs & 7>{});
                SCHED();
                if (hs + 1 < 32) ldf(kn, hn & 1, F[hn & 1]);
                u4 (&f)[4] = F[hs & 1];
#pragma unroll
                for (int j = 0; j < 4; j++) { acc[k][0][j] = mfma(f[j], xa[ks], acc[k][0][j]); acc[k][1][j] = mfma(f[j], xb[ks], acc[k][1][j]); }
                if (hs + 1 < 32) {
#pragma unroll
                    for (int i = 0; i < 8; i++) { SGB(0x100, 1); SGB(0x8, 1); }
                }
                SCHED();
            });
            post(G_CS);
            STAMP(4); CSTAMP(18);
            wait_grp(0, F_CS);
            st0 = *(LAS f2*)(lds + L_STAT + (16 * rq + li) * 8);
            st1 = *(LAS f2*)(lds + L_STAT + (64 + 16 * rq + li) * 8);
#pragma unroll
            for (int k = 0; k < 2; k++) {
                st128(geglu8(acc[k][0], lds + L_CS + k * 256 + lg * 16, st0), rs, l16, hoff0 + (2 * cg + k) * 1024);
                st128(geglu8(acc[k][1], lds + L_CS + k * 256 + lg * 16, st1), rs, l16, hoff1 + (2 * cg + k) * 1024);
            }
        }
        STAMP(5);
        wait_grp(0, F_WL);
        if (nblk == 3) {
            const u32 tb = (u32)((8 * lg + (li >> 2)) * 64 + (lg & 1) * 32 + 8 * (li & 3));
            LAS char* p0 = lds + tb; LAS char* p1 = lds + (tb ^ 32);
            f4 acc[2][4];
#pragma unroll
            for (int j = 0; j < 4; j++) { acc[0][j] = f4{0, 0, 0, 0}; acc[1][j] = f4{0, 0, 0, 0}; }
            u4 F[2][4];
            auto ldf = [&](int ks, u4 (&f)[4]) {
                const int off = ks * 4096;
                f[0] = trfrag2<256>(p0 + off); f[1] = trfrag2<256>(p1 + off); f[2] = trfrag2<256>(p0 + off + 2048); f[3] = trfrag2<256>(p1 + off + 2048);
            };
            ldf(0, F[0]);
            sfor<16>([&](auto S) {
                constexpr int ks = S;
                if (ks == 10) { WAITVM(0); flag_store(WS_FHA + ((rg * 4 + rq) * 16 + cg) * 8); }
                SCHED();
                if (ks + 1 < 16) ldf(ks + 1, F[(ks + 1) & 1]);
                u4 (&f)[4] = F[ks & 1];
#pragma unroll
                for (int j = 0; j < 4; j++) { acc[0][j] = mfma(f[j], xa[ks], acc[0][j]); acc[1][j] = mfma(f[j], xb[ks], acc[1][j]); }
                if (ks + 1 < 16) {
#pragma unroll
                    for (int i = 0; i < 8; i++) { SGB(0x100, 1); SGB(0x8, 1); }
                }
                SCHED();
            });
            post(G_PB);
            wait_grp(0, F_WB);
            st128(geglu8(acc[0], lds + L_CS + 512 + lg * 16, st0), rs, l16, hoff0 + fb2 * 1024);
            st128(geglu8(acc[1], lds + L_CS + 512 + lg * 16, st1), rs, l16, hoff1 + fb2 * 1024);
        } else { WAITVM(0); flag_store(WS_FHA + ((rg * 4 + rq) * 16 + cg) * 8); }
        STAMP(6);
        if (nblk == 3) wait_grp(1, G_PB); else wait_grp(0, F_WB);
        sfor<4>([&](auto I) { constexpr int i = I; dma4k_c<i * 16384, (16 + 4 * i) * 4096>(l16, wdp, (u32)(rq * 4096)); });
        if (nblk == 3) { WAITVM(16); flag_store(WS_FHB + ((rg * 4 + rq) * 16 + fb2 - 32) * 8); }
        f4 acc2[4];
#pragma unroll
        for (int j = 0; j < 4; j++) acc2[j] = f4{0, 0, 0, 0};
        auto g2_mma = [&](auto NC, const u4* hfr, int ldsbase) {
            constexpr int N = decltype(NC)::value;
            u32 pa = (u32)ldsbase + l16; asm volatile("" : "+v"(pa));
            LAS u4* p = (LAS u4*)(lds + pa);
            u4 F[2][4];
#pragma unroll
            for (int j = 0; j < 4; j++) F[0][j] = p[j * 64];
            sfor<N>([&](auto I) {
                constexpr int i = I;
                SCHED();
                if (i + 1 < N) {
#pragma unroll
                    for (int j = 0; j < 4; j++) F[(i + 1) & 1][j] = p[(i + 1) * 256 + j * 64];
                }
#pragma unroll
                for (int j = 0; j < 4; j++) acc2[j] = mfma(F[i & 1][j], hfr[i], acc2[j]);
                if (i + 1 < N) {
#pragma unroll
                    for (int j = 0; j < 4; j++) { SGB(0x100, 1); SGB(0x8, 1); }
                }
                SCHED();
            });
        };
        u4 hfr[32];
        flag_wait(li * 8, WS_FHA + (rg * 4 + rq) * 128);
        STAMP(7);
#pragma unroll
        for (int i = 0; i < 32; i++) hfr[i] = __builtin_amdgcn_raw_buffer_load_b128(rs, l16, hoff0 + i * 1024, 16);
        wait_grp(0, F_D1);
        g2_mma(IC<16>{}, hfr, L_SET1);
        post(G_D2);
        wait_grp(1, G_D2);
        sfor<3>([&](auto I) { constexpr int i = I; if (i < 2 || rq < 3) dma4k_c<L_SET1 + i * 16384, (32 + 4 * i) * 4096>(l16, wdp, (u32)(rq * 4096)); });
        pin4(acc2);
        g2_mma(IC<16>{}, hfr + 16, 0);
        STAMP(8);
        flag_wait(min(li, 10) * 8, WS_FHB + (rg * 4 + rq) * 128);
        post(G_D3);
#pragma unroll
        for (int i = 0; i < 11; i++) hfr[i] = __builtin_amdgcn_raw_buffer_load_b128(rs, l16, hoff0 + (32 + i) * 1024, 16);
        STAMP(9);
        wait_grp(1, G_D3);
        g2_mma(IC<11>{}, hfr, L_SET1);
        STAMP(10);
        f2 gv[2][4];
#pragma unroll
        for (int jj2 = 0; jj2 < 2; jj2++)
#pragma unroll
            for (int e = 0; e < 4; e++) {
                const f2 ov = bfup(pkbf(f2{acc2[2 * jj2][e], acc2[2 * jj2 + 1][e]}));
                const f2 z = ov * -1.44269504f;
                const f2 d = f2{__builtin_amdgcn_exp2f(z.x), __builtin_amdgcn_exp2f(z.y)} + f2{1.f, 1.f};
                gv[jj2][e] = bfup(pkbf(ov * f2{__builtin_amdgcn_rcpf(d.x), __builtin_amdgcn_rcpf(d.y)}));
            }
        asm volatile("" : "+v"(gv[0][0]), "+v"(gv[0][1]), "+v"(gv[0][2]), "+v"(gv[0][3]), "+v"(gv[1][0]), "+v"(gv[1][1]), "+v"(gv[1][2]), "+v"(gv[1][3]));
        STAMP(11);
        wait_pair(F_S);
#pragma unroll
        for (int jj2 = 0; jj2 < 2; jj2++) {
            const int c1 = 4 * jj2 + lg;
            const u4 sv = *(LAS u4*)(lds + L_S + (16 * rq + li) * 144 + c1 * 16);
            const u4 xv = *(LAS u4*)(lds + ((rq >> 1) ? L_X1 : L_X0) + ((rq & 1) * 16 + li) * 128 + ((c1 ^ (li & 7)) * 16));
            u32 res[4];
#pragma unroll
            for (int e = 0; e < 4; e++) res[e] = pkbf(bfup(xv[e]) + bfup(pkbf(bfup(sv[e]) * gv[jj2][e])));
            *(u4*)(a.out + (size_t)(s2 + 32 * (16 * rq + li)) * 512 + 64 * c1 + 8 * t) = u4{res[0], res[1], res[2], res[3]};
        }
        STAMP(12);
    }
}

extern "C" void kernel_launch(void* const* d_in, const int* in_sizes, int n_in,
                              void* d_out, int out_size, void* d_ws, size_t ws_size,
                              hipStream_t stream) {
    Args a;
    a.x = (const u16*)d_in[0]; a.wr = (const u16*)d_in[1]; a.wi = (const u16*)d_in[2];
    a.wg = (const u16*)d_in[4]; a.wu = (const u16*)d_in[5]; a.wd = (const u16*)d_in[6];
    a.out = (u16*)d_out; a.ws = (unsigned char*)d_ws;
    (void)hipFuncSetAttribute((const void*)fused, hipFuncAttributeMaxDynamicSharedMemorySize, L_END);
    fused<<<256, 512, L_END, stream>>>(a);
}
```

```cpp
#include <hip/hip_runtime.h>
#include <stdint.h>
#include <type_traits>

typedef unsigned short u16;
typedef unsigned u32;
typedef unsigned long long u64;
typedef float f2 __attribute__((ext_vector_type(2)));
typedef float f4 __attribute__((ext_vector_type(4)));
typedef u32 u4 __attribute__((ext_vector_type(4)));
typedef u32 u2 __attribute__((ext_vector_type(2)));
typedef short s4 __attribute__((ext_vector_type(4)));
typedef __bf16 bf8 __attribute__((ext_vector_type(8)));
typedef __bf16 bf2 __attribute__((ext_vector_type(2)));
#define LAS __attribute__((address_space(3)))
#define GAS __attribute__((address_space(1)))
#define DEV __device__ __forceinline__
template <int N> using IC = std::integral_constant<int, N>;

constexpr int NFB = 43;
constexpr int WS_FF1 = 4096;
constexpr int WS_FF2 = WS_FF1 + 8192;
constexpr int WS_FHA = WS_FF2 + 8192;
constexpr int WS_FHB = WS_FHA + 16384;
constexpr int WS_FP = WS_FHB + 16384;
constexpr int WS_F1 = 1 << 20, WS_F2 = WS_F1 + (4 << 20), WS_H = WS_F2 + (4 << 20);
constexpr int WS_WDP = WS_H + 32 * 4 * NFB * 1024;
constexpr int WS_WP = WS_WDP + 8 * NFB * 4 * 1024;
constexpr int WS_STAMP = WS_WP + (2 << 20);
constexpr int WS_BYTES = WS_STAMP + (1 << 20);
constexpr int PG = 8192;
constexpr int L_SET1 = 8 * PG;
constexpr int L_FFT = 16 * PG;
constexpr int L_SMALL = 19 * PG;
constexpr int L_PROG = L_SMALL;
constexpr int L_TAG = L_SMALL + 32;
constexpr int L_STAT = L_SMALL + 64;
constexpr int L_CS = L_SMALL + 2048;
constexpr int L_X0 = L_FFT + 18688, L_X1 = L_SMALL + 3072;
constexpr int L_S = 14 * PG;
constexpr int L_END = 20 * PG;
constexpr u32 SENT = 0xFFFFFFFFu;
constexpr int CODE_BYTES = 44 * 1024;
constexpr int SPIN_CAP = 1 << 16;

struct Args { const u16* x; const u16* wr; const u16* wi; const u16* wg; const u16* wu; const u16* wd; u16* out; unsigned char* ws; };

#define BAR() asm volatile("s_waitcnt lgkmcnt(0)\n\ts_barrier" ::: "memory")
#define WAITVM(n) asm volatile("s_waitcnt vmcnt(" #n ")" ::: "memory")
#define FENCE() asm volatile("" ::: "memory")
#define SCHED() __builtin_amdgcn_sched_barrier(0)
#define SGB(mask, n) __builtin_amdgcn_sched_group_barrier(mask, n, 0)
#define STAMP(i)
#define CSTAMP(i)

DEV f2 pkfma(f2 a, f2 b, f2 c) { return __builtin_elementwise_fma(a, b, c); }
struct KC { f2 pm, rpm; };
DEV f2 addmi(f2 a, f2 b, const KC& k) { return pkfma(b.yx, k.pm, a); }
DEV f2 addpi(f2 a, f2 b, const KC& k) { return pkfma(b.yx, k.pm.yx, a); }
template <bool INV> DEV void dft4(f2& a0, f2& a1, f2& a2, f2& a3, const KC& k) {
    f2 t0 = a0 + a2, t1 = a0 - a2, t2 = a1 + a3, t3 = a1 - a3;
    a0 = t0 + t2; a2 = t0 - t2;
    if (INV) { a1 = addpi(t1, t3, k); a3 = addmi(t1, t3, k); } else { a1 = addmi(t1, t3, k); a3 = addpi(t1, t3, k); }
}
template <bool INV> DEV void dft8(f2 (&v)[8], const KC& k) {
    f2 a0 = v[0] + v[4], a4 = v[0] - v[4], a1 = v[1] + v[5], a5 = v[1] - v[5];
    f2 a2 = v[2] + v[6], a6 = v[2] - v[6], a3 = v[3] + v[7], a7 = v[3] - v[7];
    dft4<INV>(a0, a1, a2, a3, k);
    v[0] = a0; v[2] = a1; v[4] = a2; v[6] = a3;
    f2 b1, b3, t0, t1;
    if (INV) { b1 = addpi(a5, a5, k); b3 = addpi(-a7, a7, k); t0 = addpi(a4, a6, k); t1 = addmi(a4, a6, k); }
    else { b1 = addmi(a5, a5, k); b3 = addmi(-a7, a7, k); t0 = addmi(a4, a6, k); t1 = addpi(a4, a6, k); }
    f2 t2 = b1 + b3, t3 = b1 - b3;
    v[1] = pkfma(t2, k.rpm.xx, t0); v[5] = pkfma(t2, k.rpm.yy, t0);
    if (INV) { v[3] = pkfma(t3.yx, k.rpm.yx, t1); v[7] = pkfma(t3.yx, k.rpm, t1); }
    else { v[3] = pkfma(t3.yx, k.rpm, t1); v[7] = pkfma(t3.yx, k.rpm.yx, t1); }
}
template <bool CONJ> DEV f2 cmul(f2 a, f2 w) {
    f2 r;
    if (CONJ) asm("v_pk_mul_f32 %0, %1, %2 op_sel:[1,1] op_sel_hi:[0,1]\n\tv_pk_fma_f32 %0, %1, %2, %0 op_sel_hi:[1,0,1] neg_hi:[0,0,1]" : "=&v"(r) : "v"(a), "v"(w));
    else asm("v_pk_mul_f32 %0, %1, %2 op_sel:[1,1] op_sel_hi:[0,1]\n\tv_pk_fma_f32 %0, %1, %2, %0 op_sel_hi:[1,0,1] neg_lo:[0,0,1]" : "=&v"(r) : "v"(a), "v"(w));
    return r;
}
DEV f2 cfmac_conj(f2 t, f2 beta, f2 B) {
    asm("v_pk_fma_f32 %0, %2, %1, %0 op_sel_hi:[0,1,1]\n\tv_pk_fma_f32 %0, %2, %1, %0 op_sel:[1,1,0] op_sel_hi:[1,0,1] neg_hi:[0,1,0]" : "+v"(t) : "v"(beta), "v"(B));
    return t;
}
DEV f2 cis(float r) { return f2{__builtin_amdgcn_cosf(r), __builtin_amdgcn_sinf(r)}; }
DEV float bflo(u32 v) { return __uint_as_float(v << 16); }
DEV float bfhi(u32 v) { return __uint_as_float(v & 0xFFFF0000u); }
DEV f2 bfup(u32 v) { return f2{bflo(v), bfhi(v)}; }
DEV u32 pkbf(f2 v) { bf2 r; r.x = (__bf16)v.x; r.y = (__bf16)v.y; return __builtin_bit_cast(u32, r); }
DEV float rbf(float f) { return __uint_as_float(((u32)__builtin_bit_cast(u16, (__bf16)f)) << 16); }
DEV f4 mfma(u4 a, u4 b, f4 c) { return __builtin_amdgcn_mfma_f32_16x16x32_bf16(__builtin_bit_cast(bf8, a), __builtin_bit_cast(bf8, b), c, 0, 0, 0); }
DEV u4 sanitize(u4 v) { return u4{min(v.x, 0xFFFFFFFEu), min(v.y, 0xFFFFFFFEu), min(v.z, 0xFFFFFFFEu), min(v.w, 0xFFFFFFFEu)}; }
DEV u32 max4(u4 v) { return max(max(v.x, v.y), max(v.z, v.w)); }
DEV void st128(u4 v, __amdgpu_buffer_rsrc_t rs, u32 voff, int soff) {
    __builtin_amdgcn_raw_buffer_store_b128(v, rs, voff, soff, 16);
    asm volatile("s_nop 1" :: "v"(v) : "memory");
}
template <int OFF2> DEV u4 trfrag2(LAS char* p) {
    s4 r0 = __builtin_amdgcn_ds_read_tr16_b64_v4i16((LAS s4*)p);
    s4 r1 = __builtin_amdgcn_ds_read_tr16_b64_v4i16((LAS s4*)(p + OFF2));
    u2 x0 = __builtin_bit_cast(u2, r0), x1 = __builtin_bit_cast(u2, r1);
    return u4{x0.x, x0.y, x1.x, x1.y};
}
template <int LI, int GI> DEV void dma_add(u32 vb, const void* sbase, u32 sl) {
    const u32 va = vb + (u32)GI;
    asm volatile("s_add_u32 m0, %2, %3\n\ts_nop 0\n\tglobal_load_lds_dwordx4 %0, %1" :: "v"(va), "s"(sbase), "s"(sl), "i"(LI) : "memory", "scc");
}
template <int LI, int GI> DEV void dma4k_c(u32 vb, const void* sbase, u32 sl) {
    const u32 va = vb + (u32)GI;
    asm volatile("s_add_u32 m0, %2, %3\n\ts_nop 0\n\tglobal_load_lds_dwordx4 %0, %1 sc1\n\tglobal_load_lds_dwordx4 %0, %1 offset:1024 sc1\n\t"
                 "global_load_lds_dwordx4 %0, %1 offset:2048 sc1\n\tglobal_load_lds_dwordx4 %0, %1 offset:3072 sc1"
                 :: "v"(va), "s"(sbase), "s"(sl), "i"(LI) : "memory", "scc");
}
template <int OFF> DEV void ldg_u16(u32& d, u32 vo, const void* sb) { asm volatile("global_load_ushort %0, %1, %2 offset:%3" : "=v"(d) : "v"(vo), "s"(sb), "i"(OFF) : "memory"); }
template <int OFF> DEV void ldg_u32(u32& d, u32 vo, const void* sb) { asm volatile("global_load_dword %0, %1, %2 offset:%3" : "=v"(d) : "v"(vo), "s"(sb), "i"(OFF) : "memory"); }
DEV void lds_wait(u32 addr, u32 v) {
    u32 tmp, cnt;
    asm volatile("s_mov_b32 %1, 0x80000\n"
                 "1:\n\tds_read_b32 %0, %2\n\ts_sub_u32 %1, %1, 1\n\ts_waitcnt lgkmcnt(0)\n\ts_cbranch_scc1 2f\n\tv_cmp_gt_u32 vcc, %3, %0\n\ts_cbranch_vccnz 1b\n2:"
                 : "=&v"(tmp), "=&s"(cnt) : "v"(addr), "s"(v) : "memory", "vcc", "scc");
}
template <int N, typename F> DEV void sfor(F&& f) { if constexpr (N > 0) { sfor<N - 1>(f); f(IC<N - 1>{}); } }

DEV void pin4(f4 (&x)[4]) { asm volatile("" : "+v"(x[0]), "+v"(x[1]), "+v"(x[2]), "+v"(x[3])); }
DEV void dma_rt(u32 voff, const void* sbase, u32 sl) {
    asm volatile("s_mov_b32 m0, %2\n\ts_nop 0\n\tglobal_load_lds_dwordx4 %0, %1" :: "v"(voff), "s"(sbase), "s"(sl) : "memory");
}
DEV u4 geglu8(const f4 (&acc)[4], LAS char* cs, f2 st) {
    u32 hw[4];
    const f2 nm = f2{-st.x, -st.x}, ri = f2{st.y, st.y};
#pragma unroll
    for (int c = 0; c < 2; c++) {
        f4 cg_ = *(LAS f4*)(cs + c * 64), cu_ = *(LAS f4*)(cs + (2 + c) * 64);
#pragma unroll
        for (int e = 0; e < 2; e++) {
            f2 g = pkfma(e ? cg_.zw : cg_.xy, nm, e ? acc[c].zw : acc[c].xy) * ri;
            f2 u = pkfma(e ? cu_.zw : cu_.xy, nm, e ? acc[2 + c].zw : acc[2 + c].xy) * ri;
            f2 z = g * pkfma(g * g, f2{-0.10294324f, -0.10294324f}, f2{-2.3022082f, -2.3022082f});
            f2 d = f2{__builtin_amdgcn_exp2f(z.x), __builtin_amdgcn_exp2f(z.y)} + f2{1.f, 1.f};
            f2 r = f2{__builtin_amdgcn_rcpf(d.x), __builtin_amdgcn_rcpf(d.y)};
            hw[2 * c + e] = pkbf(g * r * u);
        }
    }
    return u4{hw[0], hw[1], hw[2], hw[3]};
}

enum { F_XL = 1, F_XC, F_P1, F_C0, F_CS, F_WL, F_WB, F_D1, F_E1, F_E2, F_E3, F_F3, F_S };
enum { G_TAG = 1, G_W0, G_W1, G_W2, G_S0, G_W3, G_CS, G_PB, G_D2, G_D3 };

__global__ void __launch_bounds__(512, 2) fused(Args a) {
    extern __shared__ __attribute__((aligned(16))) char smem[];
    LAS char* lds = (LAS char*)smem;
    const int tid = threadIdx.x, lane = tid & 63, w = __builtin_amdgcn_readfirstlane(tid >> 6);
    const int b = blockIdx.x, xcd = b & 7, r = b >> 3, t = r & 7, par = xcd & 1, rg = (xcd >> 1) * 4 + (r >> 3);
    const int s2 = 2 * rg + par, cg = par * 8 + t, sig = xcd * 32 + r, pid = s2 * 8 + t;
    const int rq = w & 3, hf = w >> 2;
    const u32 l16 = lane * 16;
    u32 cpf = 0;
    if (w == 7) {
        const u64 pc = __builtin_amdgcn_s_getpc() & ~(u64)255;
        const u32 off = min((u32)((t * 64 + lane) * 128), (u32)(CODE_BYTES - 128));
        asm volatile("global_load_dword %0, %1, %2" : "=v"(cpf) : "v"(off), "s"(pc) : "memory");
    }
    *(volatile LAS u32*)(lds + L_PROG + w * 4) = 0;
    BAR();
    STAMP(0);
    {
        const char* xs = (const char*)a.x + (size_t)((s2 ^ hf) + 512 * rq) * 1024;
        if (hf) __builtin_amdgcn_s_sleep(4);
#pragma unroll
        for (int i = 0; i < 16; i++) { const int row = (i + 2 * t) & 15; dma_rt(l16 ^ (u32)(row * (32768 + 16)), xs, (u32)(w * 16384 + row * 1024)); }
    }
    const int li = lane & 15, lg = lane >> 4;
    const int c2p = lane & 3, l3 = (lane >> 2) & 7, qb = lane >> 5;
    const __amdgpu_buffer_rsrc_t rs = __builtin_amdgcn_make_buffer_rsrc(a.ws, 0, WS_BYTES, 0x00020000);
    KC kc; kc.pm = f2{1.f, -1.f}; kc.rpm = f2{0.70710678118654752f, -0.70710678118654752f};
    asm volatile("" : "+s"(kc.pm), "+s"(kc.rpm));
    const u4 ones = {0x3F803F80u, 0x3F803F80u, 0x3F803F80u, 0x3F803F80u};
    auto post = [&](u32 v) { asm volatile("s_waitcnt lgkmcnt(0)" ::: "memory"); *(volatile LAS u32*)(lds + L_PROG + w * 4) = v; FENCE(); };
    const u32 vpg = (u32)(L_PROG + (lane & 3) * 4);
    auto wait_grp = [&](int g, u32 v) { lds_wait(vpg + g * 16, v); };
    auto wait_pair = [&](u32 v) { lds_wait((u32)(L_PROG + (w ^ 4) * 4), v); };
    u2 tag;
    auto get_tag = [&]() { u2 tg = *(LAS u2*)(lds + L_TAG); tag.x = __builtin_amdgcn_readfirstlane(tg.x); tag.y = __builtin_amdgcn_readfirstlane(tg.y); };
    auto flag_store = [&](int off) { __builtin_amdgcn_raw_buffer_store_b64(tag, rs, lane ? WS_BYTES : 0, off, 16); };
    auto flag_wait = [&](u32 voff, int soff) {
        _Pragma("unroll 1") for (int spins = 0; spins < SPIN_CAP; spins++) {
            u2 f = __builtin_amdgcn_raw_buffer_load_b64(rs, voff, soff, 16);
            if (__all(f.x == tag.x && f.y == tag.y)) break;
            __builtin_amdgcn_s_sleep(4);
            FENCE();
        }
        __builtin_amdgcn_fence(__ATOMIC_ACQUIRE, "wavefront");
        FENCE();
    };
    auto read_tile = [&](u4 (&x)[16], int tile) {
        LAS char* p = lds + tile * 65536 + (16 * rq + li) * 1024;
#pragma unroll
        for (int k = 0; k < 16; k++) x[k] = *(LAS u4*)(p + (((4 * k + lg) ^ li) * 16));
    };
    auto stats = [&](const u4 (&x)[16]) {
        f4 s1 = {0, 0, 0, 0}, sq = s1, s1b = s1, sqb = s1;
#pragma unroll
        for (int k = 0; k < 16; k += 2) { s1 = mfma(ones, x[k], s1); sq = mfma(x[k], x[k], sq); s1b = mfma(ones, x[k + 1], s1b); sqb = mfma(x[k + 1], x[k + 1], sqb); }
        sq += sqb;
        const int rr = li & 3;
        const float d = rr == 0 ? sq.x : rr == 1 ? sq.y : rr == 2 ? sq.z : sq.w;
        if (lg == (li >> 2)) {
            const float mean = (s1.x + s1b.x) * (1.0f / 512.0f);
            float var = d * (1.0f / 512.0f) - mean * mean;
            if (!(mean * mean <= 1024.0f * var)) {
                const u32* xrow = (const u32*)(a.x + (size_t)((s2 ^ hf) + 32 * (16 * rq + li)) * 512);
                float q = 0.f;
                _Pragma("unroll 1") for (int i = 0; i < 256; i++) { u32 v = xrow[i]; float d0 = bflo(v) - mean, d1 = bfhi(v) - mean; q += d0 * d0; q += d1 * d1; }
                var = q * (1.0f / 512.0f);
            }
            const float den = rbf(__builtin_amdgcn_sqrtf(rbf(rbf(var) + 1e-5f)));
            *(LAS f2*)(lds + L_STAT + (hf * 64 + 16 * rq + li) * 8) = f2{rbf(mean), __builtin_amdgcn_rcpf(den)};
        }
        FENCE();
    };
    const int nblk = (t < 6 - par) ? 3 : 2, fb2 = 32 + par * 6 + t;
    const char* wdp = (const char*)a.ws + WS_WDP + (size_t)(t * NFB + rq) * 4096;

    if (hf == 0) {
        __builtin_amdgcn_s_setprio(2);
        f2 tw64[8], twT[8];
        int x3a[4];
        auto fft13_consts = [&](float scale) {
            int lane_ = lane; asm volatile("" : "+v"(lane_));
            const int c2p = lane_ & 3, l3 = (lane_ >> 2) & 7, qb = lane_ >> 5;
            const int wq = rq, q1 = qb ? (wq == 0 ? 4 : 8 - wq) : wq, c2 = 4 * t + c2p;
            const float f64 = (float)l3 * (1.0f / 64.0f);
#pragma unroll
            for (int k = 1; k < 8; k++) tw64[k] = cis(f64 * (float)k);
            const float fT = (float)(8 * c2 * q1 + s2 * l3) * (1.0f / 2048.0f), dT = (float)s2 * (1.0f / 256.0f);
#pragma unroll
            for (int k = 0; k < 8; k++) twT[k] = cis(fT + dT * (float)k) * scale;
#pragma unroll
            for (int k = 0; k < 4; k++) {
                const int ua = (k >> 1) * 4 + (k & 1), u1 = l3 + 8 * ua;
                const int cls = (u1 < 32) ? u1 : ((64 - u1) & 31), hi = u1 >> 5;
                int mbit, sub;
                if (wq == 0) { mbit = qb; sub = hi; } else if (cls != 0) { sub = hi; mbit = qb ^ hi; } else { mbit = hi; sub = qb ^ hi; }
                x3a[k] = L_FFT + wq * 4608 + (cls * 18 + mbit * 8 + sub * 4 + c2p) * 8 - (k == 3 ? 2304 : 0);
            }
            asm volatile("" : "+v"(tw64[1]), "+v"(tw64[2]), "+v"(tw64[3]), "+v"(tw64[4]), "+v"(tw64[5]), "+v"(tw64[6]), "+v"(tw64[7]),
                         "+v"(twT[0]), "+v"(twT[1]), "+v"(twT[2]), "+v"(twT[3]), "+v"(twT[4]), "+v"(twT[5]), "+v"(twT[6]), "+v"(twT[7]),
                         "+v"(x3a[0]), "+v"(x3a[1]), "+v"(x3a[2]), "+v"(x3a[3]));
        };

        const int f1st = WS_F1 + ((lane >> 3) * 8 + (((lane >> 2) & 1) ? (rq == 0 ? 4 : 8 - rq) : rq)) * 16384 + pid * 64 + c2p * 16;
        fft13_consts(1.0f);
        WAITVM(0);
        post(F_XL);
        STAMP(1);
        u32 raw[8];
        const int rw = lane >> 2;
        {
            u4 xa[16];
            read_tile(xa, 0);
#pragma unroll
            for (int c1 = 0; c1 < 8; c1++) raw[c1] = *(LAS u32*)(lds + (16 * rq + rw) * 1024 + (((8 * c1 + t) ^ rw) * 16) + c2p * 4);
            post(F_XC);
            stats(xa);
        }
        STAMP(2);
        f2 v[8];
        {
            const f2 st = *(LAS f2*)(lds + L_STAT + (16 * rq + rw) * 8);
#pragma unroll
            for (int c1 = 0; c1 < 8; c1++) v[c1] = bfup(pkbf(bfup(pkbf(bfup(raw[c1]) - st.xx)) * st.yy));
            dft8<false>(v, kc);
            LAS f2* xb1 = (LAS f2*)(lds + L_FFT) + w * 64 + lane;
#pragma unroll
            for (int q1 = 0; q1 < 8; q1++) { const int reg = (q1 < 4) ? q1 * 2 : (((8 - q1) & 3) * 2 + 1); xb1[reg * 288] = v[q1]; }
        }
        post(F_P1);
#pragma unroll
        for (int c1 = 0; c1 < 8; c1++) *(LAS u32*)(lds + ((rq >> 1) ? L_X1 : L_X0) + ((rq & 1) * 16 + rw) * 128 + ((c1 ^ (rw & 7)) * 16) + c2p * 4) = raw[c1];
        wait_grp(0, F_P1);
        {
            LAS f2* reg = (LAS f2*)(lds + L_FFT) + (w * 2 + qb) * 288;
#pragma unroll
            for (int k = 0; k < 8; k++) v[k] = reg[(lane & 31) + 32 * k];
            dft8<false>(v, kc);
#pragma unroll
            for (int k = 1; k < 8; k++) v[k] = cmul<true>(v[k], tw64[k]);
#pragma unroll
            for (int k = 0; k < 8; k++) reg[36 * k + (lane & 31)] = v[k];
            FENCE();
#pragma unroll
            for (int k = 0; k < 8; k++) v[k] = reg[36 * l3 + 4 * k + c2p];
            dft8<false>(v, kc);
#pragma unroll
            for (int k = 0; k < 8; k++) v[k] = cmul<true>(v[k], twT[k]);
            FENCE();
            *(LAS f2*)(lds + x3a[0]) = v[0];
            *(LAS f2*)(lds + x3a[1]) = v[1]; *(LAS f2*)(lds + x3a[1] + 1152) = v[2]; *(LAS f2*)(lds + x3a[1] + 2304) = v[3];
            *(LAS f2*)(lds + x3a[2]) = v[4];
            *(LAS f2*)(lds + x3a[3] + 2304) = v[5]; *(LAS f2*)(lds + x3a[3] + 1152) = v[6]; *(LAS f2*)(lds + x3a[3]) = v[7];
            FENCE();
#pragma unroll
            for (int i = 0; i < 4; i++) {
                u4 o = *(LAS u4*)(lds + L_FFT + w * 4608 + (i * 8 + (lane >> 3)) * 144 + (lane & 7) * 16);
                st128(o, rs, f1st, i * (64 * 16384));
            }
        }
        STAMP(3);
        f2 tw0[4], tw1[8], al[8], be[8];
        const int rb = qb, r3 = 2 * rq + rb;
        const int cls2 = sig >> 3, m2 = sig & 7;
        const int wq2 = (m2 < 4) ? m2 : ((8 - m2) & 3), pos1 = wq2 * 64 + cls2 * 2 + (m2 >> 2);
        const bool special = (cls2 == 0) && ((m2 & 3) == 0);
        int u1a, q1a, u1b, q1b;
        if (cls2 != 0) { u1a = cls2; q1a = m2; u1b = 64 - cls2; q1b = (8 - m2) & 7; }
        else if (special) { u1a = 0; u1b = 32; q1a = q1b = m2; }
        else { u1a = u1b = (m2 < 4) ? 0 : 32; q1a = m2; q1b = 8 - m2; }
        int sA, vH0, sB, vH1;
        if (!special) { sA = 0; vH0 = rq; sB = 1; vH1 = (u1a == 0) ? ((4 - rq) & 3) : 3 - rq; }
        else { sA = sB = rq >> 1; vH0 = rq & 1; vH1 = (rq == 0) ? 2 : (rq == 1) ? 3 : (rq == 2) ? 3 : 2; }
        const bool q0 = (m2 == 0);
        const int dy = ((sA ? u1b : u1a) == 0 && vH0 == 0) ? 8 : 7, dq = q0 ? 8 : 7;
        int y1 = lane >> 3, t1 = lane & 7, y2 = (dy - y1) & 7, t2 = (dq - t1) & 7, hA = vH0, hB = vH1;
        bool self = false;
        if (special && rq == 0) {
            const int k = lane & 31;
            if (qb) { hA = hB = 2; y1 = k >> 3; t1 = k & 7; y2 = 7 - y1; t2 = (dq - t1) & 7; }
            else {
                hA = hB = 0; const int vx = k & 3, y = k >> 2;
                if (!q0 || vx) { y1 = y; t1 = vx; y2 = (8 - y) & 7; t2 = dq - vx; }
                else { t1 = t2 = (y >= 4) ? 4 : 0; const int yy = y & 3; if (yy) { y1 = yy; y2 = 8 - yy; } else { y1 = 0; y2 = 4; self = true; } }
            }
        }
        const int uA = (sA ? u1b : u1a) + 64 * (hA + 4 * y1), uB = (sB ? u1b : u1a) + 64 * (hB + 4 * y2);
        const int qA = (sA ? q1b : q1a) + 8 * t1, qB = (sB ? q1b : q1a) + 8 * t2;
        lds_wait((u32)(L_PROG + 16), G_TAG);
        get_tag();
        WAITVM(0);
        flag_store(WS_FF1 + ((rq * 4 + ((s2 & 7) >> 1)) * 64 + (s2 >> 3) * 16 + (s2 & 1) * 8 + t) * 8);
        STAMP(4);
        __builtin_amdgcn_s_setprio(0);
        {
            const u32 fz = (u32)((li >> 3) | ((lg & 1) << 1));
            const u32 tb = (u32)((8 * lg + (li >> 2)) * 128 + 8 * (li & 3)) + (rq >> 1) * 4096;
            u32 pc0 = tb + (((rq & 1) ^ fz) << 5), pc1 = tb + (((2 + (rq & 1)) ^ fz) << 5);
            f4 c0 = {0, 0, 0, 0}, c1 = c0;
            wait_grp(1, G_W1);
#pragma unroll
            for (int ks = 0; ks < 8; ks++) { c0 = mfma(trfrag2<512>(lds + pc0 + ks * PG), ones, c0); c1 = mfma(trfrag2<512>(lds + pc1 + ks * PG), ones, c1); }
            post(F_C0);
            if (nblk == 3) {
                wait_grp(0, F_C0); wait_grp(1, G_S0);
                const u32 wvB = (u32)((lane >> 2) * 2752 + (((lane & 3) ^ (((lane >> 5) & 1) << 1)) * 16));
                const char* src = (const char*)((rq >> 1) ? a.wu : a.wg) + (size_t)((rq & 1) * 16) * 2752 + fb2 * 64;
                sfor<16>([&](auto K) { constexpr int k = K; dma_add<k * 4096, k * 32 * 2752>(wvB, src, (u32)(rq * 1024)); });
            }
            pc0 += 65536; pc1 += 65536; asm volatile("" : "+v"(pc0), "+v"(pc1));
            wait_grp(1, G_W3);
#pragma unroll
            for (int ks = 0; ks < 8; ks++) { c0 = mfma(trfrag2<512>(lds + pc0 + ks * PG), ones, c0); c1 = mfma(trfrag2<512>(lds + pc1 + ks * PG), ones, c1); }
            if (li == 0) { *(LAS f4*)(lds + L_CS + rq * 64 + lg * 16) = c0; *(LAS f4*)(lds + L_CS + 256 + rq * 64 + lg * 16) = c1; }
            post(F_CS);
        }
        WAITVM(0);
        post(F_WL);
        u32 wl[20];
        {
            const u32 vA = (u32)((uA * 257 + qA) * 2), vB = (u32)((uB * 257 + qB) * 2);
            sfor<4>([&](auto K) { ldg_u16<K * 128>(wl[K], vA, a.wr); ldg_u16<K * 128>(wl[8 + K], vA, a.wi); ldg_u16<K * 128>(wl[4 + K], vB, a.wr); ldg_u16<K * 128>(wl[12 + K], vB, a.wi); });
            const u32 vA2 = (u32)(uA * 514 + 512), vB2 = (u32)(uB * 514 + 512);
            ldg_u16<0>(wl[16], vA2, a.wr); ldg_u16<0>(wl[17], vA2, a.wi); ldg_u16<0>(wl[18], vB2, a.wr); ldg_u16<0>(wl[19], vB2, a.wi);
        }
        const int rowB = L_FFT + ((qb ? (vH1 * 2 + sB) : (vH0 * 2 + sA)) * 36 + (lane & 31)) * 8;
        const bool rot = q0 && (t1 == 0);
        const int p3a1 = L_FFT + (y1 * 290 + (hA * 2 + sA) * 36 + t1 * 4) * 8;
        const int p3a2 = L_FFT + (y2 * 290 + (hB * 2 + sB) * 36 + t2 * 4) * 8;
        const float thA = (float)qA * (1.0f / 512.0f), thB = (float)qB * (1.0f / 512.0f);
#pragma unroll
        for (int k = 1; k < 4; k++) tw0[k] = cis((float)r3 * ((float)k / 32.0f));
#pragma unroll
        for (int k = 1; k < 8; k++) tw1[k] = cis((float)c2p * ((float)k / 32.0f));
        flag_wait(lane * 8, WS_FP + t * 1024);
        flag_wait(min(lane + 64, 85) * 8, WS_FP + t * 1024);
        wait_grp(0, F_WL);
        wait_grp(1, G_CS);
        sfor<4>([&](auto I) { constexpr int i = I; dma4k_c<L_SET1 + i * 16384, i * 16384>(l16, wdp, (u32)(rq * 4096)); });
        if (nblk == 3) {
            const u32 tb = (u32)((8 * lg + (li >> 2)) * 64 + (lg & 1) * 32 + 8 * (li & 3));
            LAS char* pc = lds + (tb ^ ((rq & 1) * 32)) + (rq >> 1) * 2048;
            f4 c0 = {0, 0, 0, 0}, c1 = c0;
#pragma unroll
            for (int ks = 0; ks < 16; ks += 2) { c0 = mfma(trfrag2<256>(pc + ks * 4096), ones, c0); c1 = mfma(trfrag2<256>(pc + (ks + 1) * 4096), ones, c1); }
            if (li == 0) *(LAS f4*)(lds + L_CS + 512 + rq * 64 + lg * 16) = c0 + c1;
        }
        post(F_WB);
        __builtin_amdgcn_s_setprio(2);
        STAMP(13);
        flag_wait(lane * 8, WS_FF1 + (wq2 * 4 + rq) * 512);
        post(F_D1);
        STAMP(5);
        const int f1off = WS_F1 + sig * 16384 + 2 * rq * 512;
        u4 d[4];
#pragma unroll
        for (int H = 0; H < 4; H++) d[H] = __builtin_amdgcn_raw_buffer_load_b128(rs, l16, f1off + H * 4096, 16);
        asm volatile("" : "+v"(wl[0]), "+v"(wl[1]), "+v"(wl[2]), "+v"(wl[3]), "+v"(wl[4]), "+v"(wl[5]), "+v"(wl[6]), "+v"(wl[7]), "+v"(wl[8]), "+v"(wl[9]),
                     "+v"(wl[10]), "+v"(wl[11]), "+v"(wl[12]), "+v"(wl[13]), "+v"(wl[14]), "+v"(wl[15]), "+v"(wl[16]), "+v"(wl[17]), "+v"(wl[18]), "+v"(wl[19]) :: "memory");
        {
            f2 W[8], WB[8];
#pragma unroll
            for (int e = 0; e < 8; e++) W[e] = f2{bflo(wl[e]), bflo(wl[8 + e])};
#pragma unroll
            for (int k = 0; k < 4; k++) { WB[k] = W[4 + 3 - k]; WB[4 + k] = W[3 - k]; }
            if (q0) {
#pragma unroll
                for (int k = 0; k < 4; k++) {
                    f2 n1 = rot ? W[(4 - k) & 3] : W[3 - k], n2 = rot ? W[4 + ((4 - k) & 3)] : W[4 + 3 - k];
                    WB[k] = self ? n1 : n2; WB[4 + k] = self ? n2 : n1;
                }
                const f2 wS[2] = {f2{bflo(wl[16]), bflo(wl[17])}, f2{bflo(wl[18]), bflo(wl[19])}};
#pragma unroll
                for (int e = 0; e < 8; e += 4) {
                    f2 own = wS[e >> 2], oth = self ? own : wS[1 - (e >> 2)];
                    f2 S0 = pkfma(WB[e], kc.pm, W[e]) * 0.5f, S1c = pkfma(own, kc.pm, oth) * 0.5f;
                    WB[e] = rot ? S1c : WB[e];
                    al[e] = rot ? S0 : W[e];
                }
                W[0] = al[0]; W[4] = al[4];
            }
#pragma unroll
            for (int e = 0; e < 8; e++) {
                const f2 cs = cis(((e >> 2) ? thB : thA) + 0.125f * (float)(e & 3));
                f2 P = pkfma(WB[e], kc.pm, W[e]), M = pkfma(WB[e], kc.pm.yx, W[e]);
                al[e] = pkfma(M, -cs.yy, P);
                be[e] = M.yx * (kc.pm.yx * cs.xx);
            }
            asm volatile("" : "+v"(al[0]), "+v"(al[1]), "+v"(al[2]), "+v"(al[3]), "+v"(al[4]), "+v"(al[5]), "+v"(al[6]), "+v"(al[7]),
                         "+v"(be[0]), "+v"(be[1]), "+v"(be[2]), "+v"(be[3]), "+v"(be[4]), "+v"(be[5]), "+v"(be[6]), "+v"(be[7]),
                         "+v"(tw0[1]), "+v"(tw0[2]), "+v"(tw0[3]), "+v"(tw1[1]), "+v"(tw1[2]), "+v"(tw1[3]), "+v"(tw1[4]), "+v"(tw1[5]), "+v"(tw1[6]), "+v"(tw1[7]));
        }
        f2 e0[4], e1[4];
#pragma unroll
        for (int H = 0; H < 4; H++) { e0[H] = f2{__uint_as_float(d[H].x), __uint_as_float(d[H].y)}; e1[H] = f2{__uint_as_float(d[H].z), __uint_as_float(d[H].w)}; }
        dft4<false>(e0[0], e0[1], e0[2], e0[3], kc); dft4<false>(e1[0], e1[1], e1[2], e1[3], kc);
        STAMP(6);
        LAS char* area = lds + L_FFT + rq * 4640;
#pragma unroll
        for (int k = 0; k < 4; k++) {
            if (k) { e0[k] = cmul<true>(e0[k], tw0[k]); e1[k] = cmul<true>(e1[k], tw0[k]); }
            *(LAS f4*)(area + (l3 * 72 + 2 * c2p + 8 * k + 32 * rb) * 8) = f4{e0[k].x, e0[k].y, e1[k].x, e1[k].y};
        }
        FENCE();
#pragma unroll
        for (int k = 0; k < 8; k++) v[k] = *(LAS f2*)(area + (k * 72 + lane) * 8);
        dft8<false>(v, kc);
#pragma unroll
        for (int k = 1; k < 8; k++) v[k] = cmul<true>(v[k], tw1[k]);
        FENCE();
        LAS char* pb = lds + L_FFT + (r3 * 290 + l3 * 36 + c2p) * 8;
#pragma unroll
        for (int k = 0; k < 8; k++) *(LAS f2*)(pb + k * 32) = v[k];
        post(F_E1);
        wait_grp(0, F_E1);
#pragma unroll
        for (int k = 0; k < 8; k++) v[k] = *(LAS f2*)(lds + rowB + k * 2320);
        dft8<false>(v, kc);
#pragma unroll
        for (int k = 0; k < 8; k++) *(LAS f2*)(lds + rowB + k * 2320) = v[k];
        FENCE();
        f4 a0 = *(LAS f4*)(lds + p3a1), a1 = *(LAS f4*)(lds + p3a1 + 16), b0 = *(LAS f4*)(lds + p3a2), b1 = *(LAS f4*)(lds + p3a2 + 16);
        f2 X[8] = {a0.xy, a0.zw, a1.xy, a1.zw, b0.xy, b0.zw, b1.xy, b1.zw};
        dft4<false>(X[0], X[1], X[2], X[3], kc); dft4<false>(X[4], X[5], X[6], X[7], kc);
        f2 Y[8];
#pragma unroll
        for (int k = 0; k < 4; k++) { Y[k] = X[4 + 3 - k]; Y[4 + k] = X[3 - k]; }
        if (q0) {
#pragma unroll
            for (int k = 0; k < 4; k++) {
                f2 n1 = rot ? X[(4 - k) & 3] : X[3 - k], n2 = rot ? X[4 + ((4 - k) & 3)] : X[4 + 3 - k];
                Y[k] = self ? n1 : n2; Y[4 + k] = self ? n2 : n1;
            }
        }
        f2 O[8];
#pragma unroll
        for (int e = 0; e < 8; e++) O[e] = cfmac_conj(cmul<false>(X[e], al[e]), be[e], Y[e]);
        dft4<true>(O[0], O[1], O[2], O[3], kc); dft4<true>(O[4], O[5], O[6], O[7], kc);
        *(LAS f4*)(lds + p3a1) = f4{O[0].x, O[0].y, O[1].x, O[1].y}; *(LAS f4*)(lds + p3a1 + 16) = f4{O[2].x, O[2].y, O[3].x, O[3].y};
        *(LAS f4*)(lds + p3a2) = f4{O[4].x, O[4].y, O[5].x, O[5].y}; *(LAS f4*)(lds + p3a2 + 16) = f4{O[6].x, O[6].y, O[7].x, O[7].y};
        FENCE();
#pragma unroll
        for (int k = 0; k < 8; k++) v[k] = *(LAS f2*)(lds + rowB + k * 2320);
        dft8<true>(v, kc);
#pragma unroll
        for (int k = 0; k < 8; k++) *(LAS f2*)(lds + rowB + k * 2320) = v[k];
        post(F_E2);
        wait_grp(0, F_E2);
#pragma unroll
        for (int k = 0; k < 8; k++) v[k] = *(LAS f2*)(pb + k * 32);
#pragma unroll
        for (int k = 1; k < 8; k++) v[k] = cmul<false>(v[k], tw1[k]);
        dft8<true>(v, kc);
        FENCE();
#pragma unroll
        for (int k = 0; k < 8; k++) *(LAS f2*)(area + (k * 72 + lane) * 8) = v[k];
        FENCE();
#pragma unroll
        for (int k = 0; k < 4; k++) {
            f4 q = *(LAS f4*)(area + (l3 * 72 + 2 * c2p + 8 * k + 32 * rb) * 8);
            e0[k] = q.xy; e1[k] = q.zw;
            if (k) { e0[k] = cmul<false>(e0[k], tw0[k]); e1[k] = cmul<false>(e1[k], tw0[k]); }
        }
        dft4<true>(e0[0], e0[1], e0[2], e0[3], kc); dft4<true>(e1[0], e1[1], e1[2], e1[3], kc);
        const int f2st = WS_F2 + (r3 * 8 + l3) * 16384 + pos1 * 64 + c2p * 16;
#pragma unroll
        for (int H = 0; H < 4; H++) {
            u4 o = {__float_as_uint(e0[H].x), __float_as_uint(e0[H].y), __float_as_uint(e1[H].x), __float_as_uint(e1[H].y)};
            st128(o, rs, f2st, H * (64 * 16384));
        }
        STAMP(7);
        post(F_E3);
        WAITVM(0);
        flag_store(WS_FF2 + (rq * 256 + pos1) * 8);
        STAMP(8);
        fft13_consts(0.5f / 524288.0f);
        wait_grp(0, F_E3);
        flag_wait(lane * 8, WS_FF2 + (((s2 & 7) >> 1) * 256 + w * 64) * 8);
        const int off = WS_F2 + pid * 16384 + w * 4096;
#pragma unroll
        for (int i = 0; i < 4; i++) d[i] = __builtin_amdgcn_raw_buffer_load_b128(rs, l16, off + i * 1024, 16);
        STAMP(9);
#pragma unroll
        for (int i = 0; i < 4; i++) *(LAS u4*)(lds + L_FFT + w * 4608 + (i * 8 + (lane >> 3)) * 144 + (lane & 7) * 16) = d[i];
        FENCE();
        v[0] = *(LAS f2*)(lds + x3a[0]);
        v[1] = *(LAS f2*)(lds + x3a[1]); v[2] = *(LAS f2*)(lds + x3a[1] + 1152); v[3] = *(LAS f2*)(lds + x3a[1] + 2304);
        v[4] = *(LAS f2*)(lds + x3a[2]);
        v[5] = *(LAS f2*)(lds + x3a[3] + 2304); v[6] = *(LAS f2*)(lds + x3a[3] + 1152); v[7] = *(LAS f2*)(lds + x3a[3]);
#pragma unroll
        for (int k = 0; k < 8; k++) v[k] = cmul<false>(v[k], twT[k]);
        dft8<true>(v, kc);
#pragma unroll
        for (int k = 1; k < 8; k++) v[k] = cmul<false>(v[k], tw64[k]);
        FENCE();
        LAS f2* reg = (LAS f2*)(lds + L_FFT) + (w * 2 + qb) * 288;
#pragma unroll
        for (int k = 0; k < 8; k++) reg[36 * l3 + 4 * k + c2p] = v[k];
        FENCE();
#pragma unroll
        for (int k = 0; k < 8; k++) v[k] = reg[36 * k + (lane & 31)];
        dft8<true>(v, kc);
        FENCE();
#pragma unroll
        for (int k = 0; k < 8; k++) reg[(lane & 31) + 32 * k] = v[k];
        post(F_F3);
        wait_grp(1, G_D2);
        wait_grp(0, F_F3);
        if (b == 0 && w == 0) __builtin_amdgcn_raw_buffer_store_b64(*(LAS u2*)(lds + L_TAG + 8), rs, lane ? WS_BYTES : 0, 0, 16);
        LAS f2* xb1 = (LAS f2*)(lds + L_FFT) + w * 64 + lane;
#pragma unroll
        for (int q1 = 0; q1 < 8; q1++) { const int rg_ = (q1 < 4) ? q1 * 2 : (((8 - q1) & 3) * 2 + 1); v[q1] = xb1[rg_ * 288]; }
        dft8<true>(v, kc);
#pragma unroll
        for (int c1 = 0; c1 < 8; c1++) *(LAS u32*)(lds + L_S + (16 * w + (lane >> 2)) * 144 + c1 * 16 + c2p * 4) = pkbf(v[c1]);
        post(F_S);
        STAMP(10);
    } else {
        u2 ev;
        { u32 zero = 0; asm volatile("global_load_dwordx2 %0, %1, %2 sc1" : "=v"(ev) : "v"(zero), "s"(a.ws) : "memory"); }
        u32 pk[8];
        const int un = sig < 4 * NFB ? sig : 0, fb = un >> 2, jj = (un >> 1) & 1, tcol = (un & 1) * 4 + rq;
        {
            const u32 vd = (u32)(((fb * 32 + 4 * lg) * 512 + 64 * (4 * jj + (li >> 2)) + 8 * tcol + 2 * (li & 3)) * 2);
            sfor<4>([&](auto I) {
                ldg_u32<I * 1024>(pk[I], vd, a.wd);
                ldg_u32<I * 1024>(pk[4 + I], vd, (const char*)a.wd + 16384);
            });
        }
        const char* wgs = (const char*)a.wg + (size_t)(64 * rq) * 2752 + cg * 128;
        const char* wus = (const char*)a.wu + (size_t)(64 * rq) * 2752 + cg * 128;
        const int j4 = r >> 3;
        const u32 wvE = (u32)((lane >> 3) * 2752 + (((lane & 7) ^ (((lane >> 4) & 1) << 1) ^ ((j4 & 1) << 2)) * 16)), wvO = (u32)((lane >> 3) * 2752 + (((lane & 7) ^ (((lane >> 4) & 1) << 1) ^ ((~j4 & 1) << 2)) * 16));
        auto dma_w1 = [&](auto KK, auto I) {
            constexpr int kk = KK, g = I >> 1, mat = I & 1;
            const int gr = (g + j4) & 3;
            dma_add<kk * PG + mat * 4096, kk * 32 * 2752>(((g & 1) ? wvO : wvE) + (u32)(gr * 8 * 2752), mat ? wus : wgs, (u32)(rq * 2 * PG + gr * 1024));
        };
        u4 xa[16], xb[16];
        wait_pair(F_XL);
        read_tile(xa, 0);
        asm volatile("s_waitcnt lgkmcnt(0)" ::: "memory");
        wait_pair(F_XC);
        sfor<8>([&](auto I) { dma_w1(IC<0>{}, I); });
        sfor<8>([&](auto I) { dma_w1(IC<1>{}, I); });
        asm volatile("s_waitcnt vmcnt(16)" : "+v"(cpf), "+v"(ev), "+v"(pk[0]), "+v"(pk[1]), "+v"(pk[2]), "+v"(pk[3]), "+v"(pk[4]), "+v"(pk[5]), "+v"(pk[6]), "+v"(pk[7]) :: "memory");
        STAMP(1);
        read_tile(xb, 1);
        {
            const u64 epoch = ((u64)(u32)__builtin_amdgcn_readfirstlane(ev.y) << 32) | (u32)__builtin_amdgcn_readfirstlane(ev.x);
            u64 tg = (epoch + 1) * 0x9E3779B97F4A7C15ull; tg ^= tg >> 29; tg *= 0xBF58476D1CE4E5B9ull; tg ^= tg >> 32;
            if (w == 4) *(LAS u4*)(lds + L_TAG) = u4{(u32)tg, (u32)(tg >> 32), (u32)(epoch + 1), (u32)((epoch + 1) >> 32)};
        }
        post(G_TAG);
        {
            const int so = sig < 4 * NFB ? WS_WDP + ((tcol * NFB + fb) * 4 + 2 * jj) * 1024 : WS_BYTES;
            u4 oe = {__builtin_amdgcn_perm(pk[1], pk[0], 0x05040100), __builtin_amdgcn_perm(pk[3], pk[2], 0x05040100), __builtin_amdgcn_perm(pk[5], pk[4], 0x05040100), __builtin_amdgcn_perm(pk[7], pk[6], 0x05040100)};
            u4 oo = {__builtin_amdgcn_perm(pk[1], pk[0], 0x07060302), __builtin_amdgcn_perm(pk[3], pk[2], 0x07060302), __builtin_amdgcn_perm(pk[5], pk[4], 0x07060302), __builtin_amdgcn_perm(pk[7], pk[6], 0x07060302)};
            st128(oe, rs, l16, so);
            st128(oo, rs, l16, so + 1024);
        }
        stats(xb);
        STAMP(2);
        const int hoff0 = WS_H + ((s2 * 4 + rq) * NFB) * 1024, hoff1 = WS_H + (((s2 ^ 1) * 4 + rq) * NFB) * 1024;
        f2 st0, st1;
        {
            const u32 fz = (u32)((li >> 3) | ((lg & 1) << 1));
            const u32 tb = (u32)((8 * lg + (li >> 2)) * 128 + 8 * (li & 3));
            u32 pt[4];
#pragma unroll
            for (int c = 0; c < 4; c++) pt[c] = tb + ((c ^ fz) << 5);
            f4 acc[2][2][4];
#pragma unroll
            for (int k = 0; k < 2; k++) {
#pragma unroll
                for (int j = 0; j < 4; j++) { acc[k][0][j] = f4{0, 0, 0, 0}; acc[k][1][j] = f4{0, 0, 0, 0}; }
            }
            u4 F[2][4];
            auto ldf = [&](int ks, int k, u4 (&f)[4]) {
                const int off = (ks & 7) * PG;
                f[0] = trfrag2<512>(lds + pt[2 * k] + off); f[1] = trfrag2<512>(lds + pt[2 * k + 1] + off);
                f[2] = trfrag2<512>(lds + pt[2 * k] + off + 4096); f[3] = trfrag2<512>(lds + pt[2 * k + 1] + off + 4096);
            };
            WAITVM(10); post(G_W0); wait_grp(1, G_W0);
            get_tag();
            STAMP(3);
            ldf(0, 0, F[0]);
            sfor<32>([&](auto S) {
                constexpr int hs = S, s = hs >> 1, k = hs & 1, ks = (s & 8) | ((s & 3) << 1) | ((s >> 2) & 1);
                constexpr int hn = hs + 1, sn = hn >> 1, kn = (sn & 8) | ((sn & 3) << 1) | ((sn >> 2) & 1);
                if (hs == 7) { WAITVM(7); post(G_W1); flag_store(sig < 4 * NFB ? WS_FP + (((sig & 1) * 4 + rq) * 128 + (sig >> 1)) * 8 : WS_BYTES); wait_grp(1, G_W1); STAMP(14); }
                if (hs == 15) {
                    WAITVM(7); post(G_W2); wait_grp(1, G_W2); STAMP(15);
#pragma unroll
                    for (int c = 0; c < 4; c++) pt[c] += 65536;
                    asm volatile("" : "+v"(pt[0]), "+v"(pt[1]), "+v"(pt[2]), "+v"(pt[3]));
                }
                if (hs == 16) post(G_S0);
                if (hs == 23) {
                    WAITVM(0); post(G_W3);
                    wait_grp(1, G_W3); STAMP(16); CSTAMP(17);
                }
                if (hs < 16) dma_w1(IC<8 + (hs >> 3)>{}, IC<hs & 7>{});
                SCHED();
                if (hs + 1 < 32) ldf(kn, hn & 1, F[hn & 1]);
                u4 (&f)[4] = F[hs & 1];
#pragma unroll
                for (int j = 0; j < 4; j++) { acc[k][0][j] = mfma(f[j], xa[ks], acc[k][0][j]); acc[k][1][j] = mfma(f[j], xb[ks], acc[k][1][j]); }
                if (hs + 1 < 32) {
#pragma unroll
                    for (int i = 0; i < 8; i++) { SGB(0x100, 1); SGB(0x8, 1); }
                }
                SCHED();
            });
            post(G_CS);
            STAMP(4); CSTAMP(18);
            wait_grp(0, F_CS);
            st0 = *(LAS f2*)(lds + L_STAT + (16 * rq + li) * 8);
            st1 = *(LAS f2*)(lds + L_STAT + (64 + 16 * rq + li) * 8);
#pragma unroll
            for (int k = 0; k < 2; k++) {
                st128(geglu8(acc[k][0], lds + L_CS + k * 256 + lg * 16, st0), rs, l16, hoff0 + (2 * cg + k) * 1024);
                st128(geglu8(acc[k][1], lds + L_CS + k * 256 + lg * 16, st1), rs, l16, hoff1 + (2 * cg + k) * 1024);
            }
        }
        STAMP(5);
        wait_grp(0, F_WL);
        if (nblk == 3) {
            const u32 tb = (u32)((8 * lg + (li >> 2)) * 64 + (lg & 1) * 32 + 8 * (li & 3));
            LAS char* p0 = lds + tb; LAS char* p1 = lds + (tb ^ 32);
            f4 acc[2][4];
#pragma unroll
            for (int j = 0; j < 4; j++) { acc[0][j] = f4{0, 0, 0, 0}; acc[1][j] = f4{0, 0, 0, 0}; }
            u4 F[2][4];
            auto ldf = [&](int ks, u4 (&f)[4]) {
                const int off = ks * 4096;
                f[0] = trfrag2<256>(p0 + off); f[1] = trfrag2<256>(p1 + off); f[2] = trfrag2<256>(p0 + off + 2048); f[3] = trfrag2<256>(p1 + off + 2048);
            };
            ldf(0, F[0]);
            sfor<16>([&](auto S) {
                constexpr int ks = S;
                if (ks == 10) { WAITVM(0); flag_store(WS_FHA + ((rg * 4 + rq) * 16 + cg) * 8); }
                SCHED();
                if (ks + 1 < 16) ldf(ks + 1, F[(ks + 1) & 1]);
                u4 (&f)[4] = F[ks & 1];
#pragma unroll
                for (int j = 0; j < 4; j++) { acc[0][j] = mfma(f[j], xa[ks], acc[0][j]); acc[1][j] = mfma(f[j], xb[ks], acc[1][j]); }
                if (ks + 1 < 16) {
#pragma unroll
                    for (int i = 0; i < 8; i++) { SGB(0x100, 1); SGB(0x8, 1); }
                }
                SCHED();
            });
            post(G_PB);
            wait_grp(0, F_WB);
            st128(geglu8(acc[0], lds + L_CS + 512 + lg * 16, st0), rs, l16, hoff0 + fb2 * 1024);
            st128(geglu8(acc[1], lds + L_CS + 512 + lg * 16, st1), rs, l16, hoff1 + fb2 * 1024);
        } else { WAITVM(0); flag_store(WS_FHA + ((rg * 4 + rq) * 16 + cg) * 8); }
        STAMP(6);
        if (nblk == 3) wait_grp(1, G_PB); else wait_grp(0, F_WB);
        sfor<4>([&](auto I) { constexpr int i = I; dma4k_c<i * 16384, (16 + 4 * i) * 4096>(l16, wdp, (u32)(rq * 4096)); });
        if (nblk == 3) { WAITVM(16); flag_store(WS_FHB + ((rg * 4 + rq) * 16 + fb2 - 32) * 8); }
        f4 acc2[4];
#pragma unroll
        for (int j = 0; j < 4; j++) acc2[j] = f4{0, 0, 0, 0};
        auto g2_mma = [&](auto NC, const u4* hfr, int ldsbase) {
            constexpr int N = decltype(NC)::value;
            u32 pa = (u32)ldsbase + l16; asm volatile("" : "+v"(pa));
            LAS u4* p = (LAS u4*)(lds + pa);
            u4 F[2][4];
#pragma unroll
            for (int j = 0; j < 4; j++) F[0][j] = p[j * 64];
            sfor<N>([&](auto I) {
                constexpr int i = I;
                SCHED();
                if (i + 1 < N) {
#pragma unroll
                    for (int j = 0; j < 4; j++) F[(i + 1) & 1][j] = p[(i + 1) * 256 + j * 64];
                }
#pragma unroll
                for (int j = 0; j < 4; j++) acc2[j] = mfma(F[i & 1][j], hfr[i], acc2[j]);
                if (i + 1 < N) {
#pragma unroll
                    for (int j = 0; j < 4; j++) { SGB(0x100, 1); SGB(0x8, 1); }
                }
                SCHED();
            });
        };
        u4 hfr[32];
        flag_wait(li * 8, WS_FHA + (rg * 4 + rq) * 128);
        STAMP(7);
#pragma unroll
        for (int i = 0; i < 32; i++) hfr[i] = __builtin_amdgcn_raw_buffer_load_b128(rs, l16, hoff0 + i * 1024, 16);
        wait_grp(0, F_D1);
        g2_mma(IC<16>{}, hfr, L_SET1);
        post(G_D2);
        wait_grp(1, G_D2);
        sfor<3>([&](auto I) { constexpr int i = I; if (i < 2 || rq < 3) dma4k_c<L_SET1 + i * 16384, (32 + 4 * i) * 4096>(l16, wdp, (u32)(rq * 4096)); });
        pin4(acc2);
        g2_mma(IC<16>{}, hfr + 16, 0);
        STAMP(8);
        flag_wait(min(li, 10) * 8, WS_FHB + (rg * 4 + rq) * 128);
        post(G_D3);
#pragma unroll
        for (int i = 0; i < 11; i++) hfr[i] = __builtin_amdgcn_raw_buffer_load_b128(rs, l16, hoff0 + (32 + i) * 1024, 16);
        STAMP(9);
        wait_grp(1, G_D3);
        g2_mma(IC<11>{}, hfr, L_SET1);
        STAMP(10);
        f2 gv[2][4];
#pragma unroll
        for (int jj2 = 0; jj2 < 2; jj2++)
#pragma unroll
            for (int e = 0; e < 4; e++) {
                const f2 ov = bfup(pkbf(f2{acc2[2 * jj2][e], acc2[2 * jj2 + 1][e]}));
                const f2 z = ov * -1.44269504f;
                const f2 d = f2{__builtin_amdgcn_exp2f(z.x), __builtin_amdgcn_exp2f(z.y)} + f2{1.f, 1.f};
                gv[jj2][e] = bfup(pkbf(ov * f2{__builtin_amdgcn_rcpf(d.x), __builtin_amdgcn_rcpf(d.y)}));
            }
        asm volatile("" : "+v"(gv[0][0]), "+v"(gv[0][1]), "+v"(gv[0][2]), "+v"(gv[0][3]), "+v"(gv[1][0]), "+v"(gv[1][1]), "+v"(gv[1][2]), "+v"(gv[1][3]));
        STAMP(11);
        wait_pair(F_S);
#pragma unroll
        for (int jj2 = 0; jj2 < 2; jj2++) {
            const int c1 = 4 * jj2 + lg;
            const u4 sv = *(LAS u4*)(lds + L_S + (16 * rq + li) * 144 + c1 * 16);
            const u4 xv = *(LAS u4*)(lds + ((rq >> 1) ? L_X1 : L_X0) + ((rq & 1) * 16 + li) * 128 + ((c1 ^ (li & 7)) * 16));
            u32 res[4];
#pragma unroll
            for (int e = 0; e < 4; e++) res[e] = pkbf(bfup(xv[e]) + bfup(pkbf(bfup(sv[e]) * gv[jj2][e])));
            *(u4*)(a.out + (size_t)(s2 + 32 * (16 * rq + li)) * 512 + 64 * c1 + 8 * t) = u4{res[0], res[1], res[2], res[3]};
        }
        STAMP(12);
    }
}

extern "C" void kernel_launch(void* const* d_in, const int* in_sizes, int n_in,
                              void* d_out, int out_size, void* d_ws, size_t ws_size,
                              hipStream_t stream) {
    Args a;
    a.x = (const u16*)d_in[0]; a.wr = (const u16*)d_in[1]; a.wi = (const u16*)d_in[2];
    a.wg = (const u16*)d_in[4]; a.wu = (const u16*)d_in[5]; a.wd = (const u16*)d_in[6];
    a.out = (u16*)d_out; a.ws = (unsigned char*)d_ws;
    (void)hipFuncSetAttribute((const void*)fused, hipFuncAttributeMaxDynamicSharedMemorySize, L_END);
    fused<<<256, 512, L_END, stream>>>(a);
}
```

```cpp
#include <hip/hip_runtime.h>
#include <stdint.h>
#include <type_traits>

typedef unsigned short u16;
typedef unsigned u32;
typedef unsigned long long u64;
typedef float f2 __attribute__((ext_vector_type(2)));
typedef float f4 __attribute__((ext_vector_type(4)));
typedef u32 u4 __attribute__((ext_vector_type(4)));
typedef u32 u2 __attribute__((ext_vector_type(2)));
typedef short s4 __attribute__((ext_vector_type(4)));
typedef __bf16 bf8 __attribute__((ext_vector_type(8)));
typedef __bf16 bf2 __attribute__((ext_vector_type(2)));
#define LAS __attribute__((address_space(3)))
#define GAS __attribute__((address_space(1)))
#define DEV __device__ __forceinline__
template <int N> using IC = std::integral_constant<int, N>;

constexpr int NFB = 43;
constexpr int WS_FF1 = 4096;
constexpr int WS_FF2 = WS_FF1 + 8192;
constexpr int WS_FHA = WS_FF2 + 8192;
constexpr int WS_FHB = WS_FHA + 16384;
constexpr int WS_FP = WS_FHB + 16384;
constexpr int WS_FW = WS_FP + 8192;
constexpr int WS_F1 = 1 << 20, WS_F2 = WS_F1 + (4 << 20), WS_H = WS_F2 + (4 << 20);
constexpr int WS_WDP = WS_H + 32 * 4 * NFB * 1024;
constexpr int WS_WP = WS_WDP + 8 * NFB * 4 * 1024;
constexpr int WS_STAMP = WS_WP + (2 << 20);
constexpr int WS_BYTES = WS_STAMP + (1 << 20);
constexpr int PG = 8192;
constexpr int L_SET1 = 8 * PG;
constexpr int L_FFT = 16 * PG;
constexpr int L_SMALL = 19 * PG;
constexpr int L_PROG = L_SMALL;
constexpr int L_TAG = L_SMALL + 32;
constexpr int L_STAT = L_SMALL + 64;
constexpr int L_CS = L_SMALL + 2048;
constexpr int L_X0 = L_FFT + 18688, L_X1 = L_SMALL + 3072;
constexpr int L_S = 14 * PG;
constexpr int L_END = 20 * PG;
constexpr u32 SENT = 0xFFFFFFFFu;
constexpr int CODE_BYTES = 36 * 1024;
constexpr int SPIN_CAP = 1 << 16;

struct Args { const u16* x; const u16* wr; const u16* wi; const u16* wg; const u16* wu; const u16* wd; u16* out; unsigned char* ws; };

#define BAR() asm volatile("s_waitcnt lgkmcnt(0)\n\ts_barrier" ::: "memory")
#define WAITVM(n) asm volatile("s_waitcnt vmcnt(" #n ")" ::: "memory")
#define NOF(x) (x)
#define FENCE() asm volatile("" ::: "memory")
#define SCHED() __builtin_amdgcn_sched_barrier(0)
#define SGB(mask, n) __builtin_amdgcn_sched_group_barrier(mask, n, 0)
#define STAMP(i)
#define CSTAMP(i)

DEV f2 pkfma(f2 a, f2 b, f2 c) { return __builtin_elementwise_fma(a, b, c); }
struct KC { f2 pm, rpm; };
DEV f2 addmi(f2 a, f2 b, const KC& k) { return pkfma(b.yx, k.pm, a); }
DEV f2 addpi(f2 a, f2 b, const KC& k) { return pkfma(b.yx, k.pm.yx, a); }
template <bool INV> DEV void dft4(f2& a0, f2& a1, f2& a2, f2& a3, const KC& k) {
    f2 t0 = a0 + a2, t1 = a0 - a2, t2 = a1 + a3, t3 = a1 - a3;
    a0 = t0 + t2; a2 = t0 - t2;
    if (INV) { a1 = addpi(t1, t3, k); a3 = addmi(t1, t3, k); } else { a1 = addmi(t1, t3, k); a3 = addpi(t1, t3, k); }
}
template <bool INV> DEV void dft8(f2 (&v)[8], const KC& k) {
    f2 a0 = v[0] + v[4], a4 = v[0] - v[4], a1 = v[1] + v[5], a5 = v[1] - v[5];
    f2 a2 = v[2] + v[6], a6 = v[2] - v[6], a3 = v[3] + v[7], a7 = v[3] - v[7];
    dft4<INV>(a0, a1, a2, a3, k);
    v[0] = a0; v[2] = a1; v[4] = a2; v[6] = a3;
    f2 b1, b3, t0, t1;
    if (INV) { b1 = addpi(a5, a5, k); b3 = addpi(-a7, a7, k); t0 = addpi(a4, a6, k); t1 = addmi(a4, a6, k); }
    else { b1 = addmi(a5, a5, k); b3 = addmi(-a7, a7, k); t0 = addmi(a4, a6, k); t1 = addpi(a4, a6, k); }
    f2 t2 = b1 + b3, t3 = b1 - b3;
    v[1] = pkfma(t2, k.rpm.xx, t0); v[5] = pkfma(t2, k.rpm.yy, t0);
    if (INV) { v[3] = pkfma(t3.yx, k.rpm.yx, t1); v[7] = pkfma(t3.yx, k.rpm, t1); }
    else { v[3] = pkfma(t3.yx, k.rpm, t1); v[7] = pkfma(t3.yx, k.rpm.yx, t1); }
}
template <bool CONJ> DEV f2 cmul(f2 a, f2 w) {
    f2 r;
    if (CONJ) asm("v_pk_mul_f32 %0, %1, %2 op_sel:[1,1] op_sel_hi:[0,1]\n\tv_pk_fma_f32 %0, %1, %2, %0 op_sel_hi:[1,0,1] neg_hi:[0,0,1]" : "=&v"(r) : "v"(a), "v"(w));
    else asm("v_pk_mul_f32 %0, %1, %2 op_sel:[1,1] op_sel_hi:[0,1]\n\tv_pk_fma_f32 %0, %1, %2, %0 op_sel_hi:[1,0,1] neg_lo:[0,0,1]" : "=&v"(r) : "v"(a), "v"(w));
    return r;
}
DEV f2 cfmac_conj(f2 t, f2 beta, f2 B) {
    asm("v_pk_fma_f32 %0, %2, %1, %0 op_sel_hi:[0,1,1]\n\tv_pk_fma_f32 %0, %2, %1, %0 op_sel:[1,1,0] op_sel_hi:[1,0,1] neg_hi:[0,1,0]" : "+v"(t) : "v"(beta), "v"(B));
    return t;
}
DEV f2 cis(float r) { return f2{__builtin_amdgcn_cosf(r), __builtin_amdgcn_sinf(r)}; }
DEV float bflo(u32 v) { return __uint_as_float(v << 16); }
DEV float bfhi(u32 v) { return __uint_as_float(v & 0xFFFF0000u); }
DEV f2 bfup(u32 v) { return f2{bflo(v), bfhi(v)}; }
DEV u32 pkbf(f2 v) { bf2 r; r.x = (__bf16)v.x; r.y = (__bf16)v.y; u32 o = __builtin_bit_cast(u32, r); asm("" : "+v"(o)); return o; }
DEV float rbf(float f) { return __uint_as_float(((u32)__builtin_bit_cast(u16, (__bf16)f)) << 16); }
DEV f4 mfma(u4 a, u4 b, f4 c) { return __builtin_amdgcn_mfma_f32_16x16x32_bf16(__builtin_bit_cast(bf8, a), __builtin_bit_cast(bf8, b), c, 0, 0, 0); }
DEV u4 sanitize(u4 v) { return u4{min(v.x, 0xFFFFFFFEu), min(v.y, 0xFFFFFFFEu), min(v.z, 0xFFFFFFFEu), min(v.w, 0xFFFFFFFEu)}; }
DEV u32 max4(u4 v) { return max(max(v.x, v.y), max(v.z, v.w)); }
DEV void st128(u4 v, __amdgpu_buffer_rsrc_t rs, u32 voff, int soff) {
    __builtin_amdgcn_raw_buffer_store_b128(v, rs, voff, soff, 16);
    asm volatile("s_nop 1" :: "v"(v) : "memory");
}
template <int OFF2> DEV u4 trfrag2(LAS char* p) {
    s4 r0 = __builtin_amdgcn_ds_read_tr16_b64_v4i16((LAS s4*)p);
    s4 r1 = __builtin_amdgcn_ds_read_tr16_b64_v4i16((LAS s4*)(p + OFF2));
    u2 x0 = __builtin_bit_cast(u2, r0), x1 = __builtin_bit_cast(u2, r1);
    return u4{x0.x, x0.y, x1.x, x1.y};
}
template <int LI, int GI> DEV void dma_add(u32 vb, const void* sbase, u32 sl) {
    const u32 va = vb + (u32)GI;
    asm volatile("s_add_u32 m0, %2, %3\n\ts_nop 0\n\tglobal_load_lds_dwordx4 %0, %1" :: "v"(va), "s"(sbase), "s"(sl), "i"(LI) : "memory", "scc");
}
template <int LI, int GI> DEV void dma4k_c(u32 vb, const void* sbase, u32 sl) {
    const u32 va = vb + (u32)GI;
    asm volatile("s_add_u32 m0, %2, %3\n\ts_nop 0\n\tglobal_load_lds_dwordx4 %0, %1 sc1\n\tglobal_load_lds_dwordx4 %0, %1 offset:1024 sc1\n\t"
                 "global_load_lds_dwordx4 %0, %1 offset:2048 sc1\n\tglobal_load_lds_dwordx4 %0, %1 offset:3072 sc1"
                 :: "v"(va), "s"(sbase), "s"(sl), "i"(LI) : "memory", "scc");
}
template <int OFF> DEV void ldg_u16(u32& d, u32 vo, const void* sb) { asm volatile("global_load_ushort %0, %1, %2 offset:%3" : "=v"(d) : "v"(vo), "s"(sb), "i"(OFF) : "memory"); }
template <int OFF> DEV void ldg_u32(u32& d, u32 vo, const void* sb) { asm volatile("global_load_dword %0, %1, %2 offset:%3" : "=v"(d) : "v"(vo), "s"(sb), "i"(OFF) : "memory"); }
DEV void lds_wait(u32 addr, u32 v) {
    u32 tmp, cnt;
    asm volatile("s_mov_b32 %1, 0x80000\n"
                 "1:\n\tds_read_b32 %0, %2\n\ts_sub_u32 %1, %1, 1\n\ts_waitcnt lgkmcnt(0)\n\ts_cbranch_scc1 2f\n\tv_cmp_gt_u32 vcc, %3, %0\n\ts_cbranch_vccnz 1b\n2:"
                 : "=&v"(tmp), "=&s"(cnt) : "v"(addr), "s"(v) : "memory", "vcc", "scc");
}
template <int N, typename F> DEV void sfor(F&& f) { if constexpr (N > 0) { sfor<N - 1>(f); f(IC<N - 1>{}); } }

DEV void pin9(f4 (&x)[4], f4 (&y)[4], f4& z) {
    asm volatile("" : "+v"(x[0]), "+v"(x[1]), "+v"(x[2]), "+v"(x[3]), "+v"(y[0]), "+v"(y[1]), "+v"(y[2]), "+v"(y[3]), "+v"(z));
}
DEV void pin4(f4 (&x)[4]) { asm volatile("" : "+v"(x[0]), "+v"(x[1]), "+v"(x[2]), "+v"(x[3])); }
DEV void dma_rt(u32 voff, const void* sbase, u32 sl) {
    asm volatile("s_mov_b32 m0, %2\n\ts_nop 0\n\tglobal_load_lds_dwordx4 %0, %1" :: "v"(voff), "s"(sbase), "s"(sl) : "memory");
}
DEV u4 geglu8(const f4 (&acc)[4], LAS char* cs, f2 st) {
    u32 hw[4];
    const f2 nm = f2{-st.x, -st.x}, ri = f2{st.y, st.y};
#pragma unroll
    for (int c = 0; c < 2; c++) {
        f4 cg_ = *(LAS f4*)(cs + c * 64), cu_ = *(LAS f4*)(cs + (2 + c) * 64);
#pragma unroll
        for (int e = 0; e < 2; e++) {
            f2 g = pkfma(e ? cg_.zw : cg_.xy, nm, e ? acc[c].zw : acc[c].xy) * ri;
            f2 u = pkfma(e ? cu_.zw : cu_.xy, nm, e ? acc[2 + c].zw : acc[2 + c].xy) * ri;
            f2 z = g * pkfma(g * g, f2{-0.10294324f, -0.10294324f}, f2{-2.3022082f, -2.3022082f});
            f2 d = f2{__builtin_amdgcn_exp2f(z.x), __builtin_amdgcn_exp2f(z.y)} + f2{1.f, 1.f};
            f2 r = f2{__builtin_amdgcn_rcpf(d.x), __builtin_amdgcn_rcpf(d.y)};
            hw[2 * c + e] = pkbf(g * r * u);
        }
    }
    return u4{hw[0], hw[1], hw[2], hw[3]};
}

enum { A_XL = 1, A_XC, A_P1, A_XB, A_S0, A_CS, A_WB, A_PB, A_D1, A_G1, A_D3 };
enum { B_XL = 1, B_W0, B_W1, B_W2, B_S0, B_W3, B_CS, B_FP, B_E1, B_E2, B_G1, B_F3 };

__global__ void __launch_bounds__(512, 2) fused(Args a) {
    extern __shared__ __attribute__((aligned(16))) char smem[];
    LAS char* lds = (LAS char*)smem;
    const int tid = threadIdx.x, w = __builtin_amdgcn_readfirstlane(tid >> 6);
    int lane = tid & 63;
    const int b = blockIdx.x, xcd = b & 7, r = b >> 3, t = r & 7, par = xcd & 1, rg = (xcd >> 1) * 4 + (r >> 3);
    const int s2 = 2 * rg + par, cg = par * 8 + t, sig = xcd * 32 + r, pid = s2 * 8 + t;
    const int rq = w & 3, hf = w >> 2;
    u32 l16 = lane * 16;
    u32 cpf = 0;
    if (w == 7) {
        const u64 pc = __builtin_amdgcn_s_getpc() & ~(u64)255;
        const u32 off = min((u32)((t * 64 + lane) * 128), (u32)(CODE_BYTES - 128));
        asm volatile("global_load_dword %0, %1, %2" : "=v"(cpf) : "v"(off), "s"(pc) : "memory");
    }
    *(volatile LAS u32*)(lds + L_PROG + w * 4) = 0;
    BAR();
    STAMP(0);
    {
        const char* xs = (const char*)a.x + (size_t)((s2 ^ hf) + 512 * rq) * 1024;
        if (hf) __builtin_amdgcn_s_sleep(4);
#pragma unroll
        for (int i = 0; i < 16; i++) { const int row = (i + 2 * t) & 15; dma_rt(l16 ^ (u32)(row * (32768 + 16)), xs, (u32)(w * 16384 + row * 1024)); }
    }
    int li, lg, c2p, l3, qb; u32 vpg;
#define RELANE() do { asm volatile("" : "+v"(lane)); l16 = lane * 16; li = lane & 15; lg = lane >> 4; c2p = lane & 3; l3 = (lane >> 2) & 7; qb = lane >> 5; vpg = (u32)(L_PROG + (lane & 3) * 4); } while (0)
    const __amdgpu_buffer_rsrc_t rs = __builtin_amdgcn_make_buffer_rsrc(a.ws, 0, WS_BYTES, 0x00020000);
    KC kc; kc.pm = f2{1.f, -1.f}; kc.rpm = f2{0.70710678118654752f, -0.70710678118654752f};
    asm volatile("" : "+s"(kc.pm), "+s"(kc.rpm));
    const u4 ones = {0x3F803F80u, 0x3F803F80u, 0x3F803F80u, 0x3F803F80u};
    auto post = [&](u32 v) { asm volatile("s_waitcnt lgkmcnt(0)" ::: "memory"); *(volatile LAS u32*)(lds + L_PROG + w * 4) = v; FENCE(); };
    auto wait_grp = [&](int g, u32 v) { lds_wait(vpg + g * 16, v); };
    auto wait_pair = [&](u32 v) { lds_wait((u32)(L_PROG + (w ^ 4) * 4), v); };
    u2 tag;
    auto get_tag = [&]() { u2 tg = *(LAS u2*)(lds + L_TAG); tag.x = __builtin_amdgcn_readfirstlane(tg.x); tag.y = __builtin_amdgcn_readfirstlane(tg.y); };
    auto flag_store = [&](int off) { __builtin_amdgcn_raw_buffer_store_b64(tag, rs, lane ? WS_BYTES : 0, off, 16); };
    auto flag_wait = [&](u32 voff, int soff) {
        _Pragma("unroll 1") for (int spins = 0; spins < SPIN_CAP; spins++) {
            u2 f = __builtin_amdgcn_raw_buffer_load_b64(rs, voff, soff, 16);
            if (__all(f.x == tag.x && f.y == tag.y)) break;
            __builtin_amdgcn_s_sleep(4);
            FENCE();
        }
        __builtin_amdgcn_fence(__ATOMIC_ACQUIRE, "wavefront");
        FENCE();
    };
    auto read_tile = [&](u4 (&x)[16], int tile) {
        LAS char* p = lds + tile * 65536 + (16 * rq + li) * 1024;
#pragma unroll
        for (int k = 0; k < 16; k++) x[k] = *(LAS u4*)(p + (((4 * k + lg) ^ li) * 16));
    };
    auto stats = [&](const u4 (&x)[16]) {
        f4 s1 = {0, 0, 0, 0}, sq = s1, s1b = s1, sqb = s1;
#pragma unroll
        for (int k = 0; k < 16; k += 2) { s1 = mfma(ones, x[k], s1); sq = mfma(x[k], x[k], sq); s1b = mfma(ones, x[k + 1], s1b); sqb = mfma(x[k + 1], x[k + 1], sqb); }
        sq += sqb;
        const int rr = li & 3;
        const float d = rr == 0 ? sq.x : rr == 1 ? sq.y : rr == 2 ? sq.z : sq.w;
        if (lg == (li >> 2)) {
            const float mean = (s1.x + s1b.x) * (1.0f / 512.0f);
            float var = d * (1.0f / 512.0f) - mean * mean;
            if (!(mean * mean <= 1024.0f * var)) {
                const u32* xrow = (const u32*)(a.x + (size_t)((s2 ^ hf) + 32 * (16 * rq + li)) * 512);
                float q = 0.f;
                _Pragma("unroll 1") for (int i = 0; i < 256; i++) { u32 v = xrow[i]; float d0 = bflo(v) - mean, d1 = bfhi(v) - mean; q += d0 * d0; q += d1 * d1; }
                var = q * (1.0f / 512.0f);
            }
            const float den = rbf(__builtin_amdgcn_sqrtf(rbf(rbf(var) + 1e-5f)));
            *(LAS f2*)(lds + L_STAT + (hf * 64 + 16 * rq + li) * 8) = f2{rbf(mean), __builtin_amdgcn_rcpf(den)};
        }
        FENCE();
    };
    const int nblk = (t < 6 - par) ? 3 : 2, fb2 = 32 + par * 6 + t;
    const char* wdp = (const char*)a.ws + WS_WDP + (size_t)(t * NFB + rq) * 4096;

    f2 tw64[8], twT[8];
    int x3a[4];
    auto fft13_consts = [&](float scale) {
        int lane_ = lane; asm volatile("" : "+v"(lane_));
        const int c2p = lane_ & 3, l3 = (lane_ >> 2) & 7, qb = lane_ >> 5;
        const int wq = rq, q1 = qb ? (wq == 0 ? 4 : 8 - wq) : wq, c2 = 4 * t + c2p;
        const float f64 = (float)l3 * (1.0f / 64.0f);
#pragma unroll
        for (int k = 1; k < 8; k++) tw64[k] = cis(f64 * (float)k);
        const float fT = (float)(8 * c2 * q1 + s2 * l3) * (1.0f / 2048.0f), dT = (float)s2 * (1.0f / 256.0f);
#pragma unroll
        for (int k = 0; k < 8; k++) twT[k] = cis(fT + dT * (float)k) * scale;
#pragma unroll
        for (int k = 0; k < 4; k++) {
            const int ua = (k >> 1) * 4 + (k & 1), u1 = l3 + 8 * ua;
            const int cls = (u1 < 32) ? u1 : ((64 - u1) & 31), hi = u1 >> 5;
            int mbit, sub;
            if (wq == 0) { mbit = qb; sub = hi; } else if (cls != 0) { sub = hi; mbit = qb ^ hi; } else { mbit = hi; sub = qb ^ hi; }
            x3a[k] = L_FFT + wq * 4608 + (cls * 18 + mbit * 8 + sub * 4 + c2p) * 8 - (k == 3 ? 2304 : 0);
        }
        asm volatile("" : "+v"(tw64[1]), "+v"(tw64[2]), "+v"(tw64[3]), "+v"(tw64[4]), "+v"(tw64[5]), "+v"(tw64[6]), "+v"(tw64[7]),
                     "+v"(twT[0]), "+v"(twT[1]), "+v"(twT[2]), "+v"(twT[3]), "+v"(twT[4]), "+v"(twT[5]), "+v"(twT[6]), "+v"(twT[7]),
                     "+v"(x3a[0]), "+v"(x3a[1]), "+v"(x3a[2]), "+v"(x3a[3]));
    };

    u4 xa[16], xb[16];
    const int hoff0 = WS_H + ((s2 * 4 + rq) * NFB) * 1024, hoff1 = WS_H + (((s2 ^ 1) * 4 + rq) * NFB) * 1024;
    RELANE();
    const char* wgs = (const char*)a.wg + (size_t)(64 * rq) * 2752 + cg * 128;
    const char* wus = (const char*)a.wu + (size_t)(64 * rq) * 2752 + cg * 128;
    const int j4 = r >> 3;
    const u32 wvE = (u32)((lane >> 3) * 2752 + (((lane & 7) ^ (((lane >> 4) & 1) << 1) ^ ((j4 & 1) << 2)) * 16)), wvO = (u32)((lane >> 3) * 2752 + (((lane & 7) ^ (((lane >> 4) & 1) << 1) ^ ((~j4 & 1) << 2)) * 16));
    auto dma_w1 = [&](auto KK, auto I) {
        constexpr int kk = KK, g = I >> 1, mat = I & 1;
        const int gr = (g + j4) & 3;
        dma_add<kk * PG + mat * 4096, kk * 32 * 2752>(((g & 1) ? wvO : wvE) + (u32)(gr * 8 * 2752), mat ? wus : wgs, (u32)(rq * 2 * PG + gr * 1024));
    };

    if (hf == 0) {
        __builtin_amdgcn_s_setprio(2);
        const int f1st = WS_F1 + ((lane >> 3) * 8 + (((lane >> 2) & 1) ? (rq == 0 ? 4 : 8 - rq) : rq)) * 16384 + pid * 64 + c2p * 16;
        u2 ev;
        { u32 zero = 0; asm volatile("global_load_dwordx2 %0, %1, %2 sc1" : "=v"(ev) : "v"(zero), "s"(a.ws) : "memory"); }
        fft13_consts(1.0f);
        WAITVM(1);
        post(A_XL);
        STAMP(1);
        u32 raw[8];
        const int rw = lane >> 2;
        read_tile(xa, 0);
#pragma unroll
        for (int c1 = 0; c1 < 8; c1++) raw[c1] = *(LAS u32*)(lds + (16 * rq + rw) * 1024 + (((8 * c1 + t) ^ rw) * 16) + c2p * 4);
        post(A_XC);
        stats(xa);
        STAMP(2);
        f2 v[8];
        {
            const f2 st = *(LAS f2*)(lds + L_STAT + (16 * rq + rw) * 8);
#pragma unroll
            for (int c1 = 0; c1 < 8; c1++) v[c1] = bfup(pkbf(bfup(pkbf(bfup(raw[c1]) - st.xx)) * st.yy));
            dft8<false>(v, kc);
            LAS f2* xb1 = (LAS f2*)(lds + L_FFT) + w * 64 + lane;
#pragma unroll
            for (int q1 = 0; q1 < 8; q1++) { const int reg = (q1 < 4) ? q1 * 2 : (((8 - q1) & 3) * 2 + 1); xb1[reg * 288] = v[q1]; }
        }
        asm volatile("s_waitcnt vmcnt(0)" : "+v"(ev) :: "memory");
        {
            const u64 epoch = ((u64)(u32)__builtin_amdgcn_readfirstlane(ev.y) << 32) | (u32)__builtin_amdgcn_readfirstlane(ev.x);
            u64 tg = (epoch + 1) * 0x9E3779B97F4A7C15ull; tg ^= tg >> 29; tg *= 0xBF58476D1CE4E5B9ull; tg ^= tg >> 32;
            tag = u2{(u32)tg, (u32)(tg >> 32)};
            *(LAS u4*)(lds + L_TAG) = u4{(u32)tg, (u32)(tg >> 32), (u32)(epoch + 1), (u32)((epoch + 1) >> 32)};
        }
        post(A_P1);
#pragma unroll
        for (int c1 = 0; c1 < 8; c1++) *(LAS u32*)(lds + ((rq >> 1) ? L_X1 : L_X0) + ((rq & 1) * 16 + rw) * 128 + ((c1 ^ (rw & 7)) * 16) + c2p * 4) = raw[c1];
        wait_pair(B_XL);
        read_tile(xb, 1);
        post(A_XB);
        u32 pk[8], fr[16];
        {
            const u32 vo = (u32)(((8 * sig + 2 * rq) * 257 + lane) * 2);
            sfor<8>([&](auto I) { constexpr int rr = I >> 2, k = I & 3; ldg_u16<rr * 514 + k * 128>(fr[I], vo, a.wr); ldg_u16<rr * 514 + k * 128>(fr[8 + I], vo, a.wi); });
        }
        const int un = sig < 4 * NFB ? sig : 0, fb = un >> 2, jj = (un >> 1) & 1, tcol = (un & 1) * 4 + rq;
        {
            const u32 vd = (u32)(((fb * 32 + 4 * lg) * 512 + 64 * (4 * jj + (li >> 2)) + 8 * tcol + 2 * (li & 3)) * 2);
            sfor<4>([&](auto I) {
                ldg_u32<I * 1024>(pk[I], vd, a.wd);
                ldg_u32<I * 1024>(pk[4 + I], vd, (const char*)a.wd + 16384);
            });
        }
        wait_grp(0, A_P1);
        {
            LAS f2* reg = (LAS f2*)(lds + L_FFT) + (w * 2 + qb) * 288;
#pragma unroll
            for (int k = 0; k < 8; k++) v[k] = reg[(lane & 31) + 32 * k];
            dft8<false>(v, kc);
#pragma unroll
            for (int k = 1; k < 8; k++) v[k] = cmul<true>(v[k], tw64[k]);
#pragma unroll
            for (int k = 0; k < 8; k++) reg[36 * k + (lane & 31)] = v[k];
            FENCE();
#pragma unroll
            for (int k = 0; k < 8; k++) v[k] = reg[36 * l3 + 4 * k + c2p];
            dft8<false>(v, kc);
#pragma unroll
            for (int k = 0; k < 8; k++) v[k] = cmul<true>(v[k], twT[k]);
            FENCE();
            *(LAS f2*)(lds + x3a[0]) = v[0];
            *(LAS f2*)(lds + x3a[1]) = v[1]; *(LAS f2*)(lds + x3a[1] + 1152) = v[2]; *(LAS f2*)(lds + x3a[1] + 2304) = v[3];
            *(LAS f2*)(lds + x3a[2]) = v[4];
            *(LAS f2*)(lds + x3a[3] + 2304) = v[5]; *(LAS f2*)(lds + x3a[3] + 1152) = v[6]; *(LAS f2*)(lds + x3a[3]) = v[7];
            FENCE();
#pragma unroll
            for (int i = 0; i < 4; i++) {
                u4 o = *(LAS u4*)(lds + L_FFT + w * 4608 + (i * 8 + (lane >> 3)) * 144 + (lane & 7) * 16);
                st128(o, rs, NOF(f1st), i * (64 * 16384));
            }
        }
        STAMP(3);
        __builtin_amdgcn_s_setprio(0);
        asm volatile("s_waitcnt vmcnt(4)" : "+v"(pk[0]), "+v"(pk[1]), "+v"(pk[2]), "+v"(pk[3]), "+v"(pk[4]), "+v"(pk[5]), "+v"(pk[6]), "+v"(pk[7]),
                     "+v"(fr[0]), "+v"(fr[1]), "+v"(fr[2]), "+v"(fr[3]), "+v"(fr[4]), "+v"(fr[5]), "+v"(fr[6]), "+v"(fr[7]),
                     "+v"(fr[8]), "+v"(fr[9]), "+v"(fr[10]), "+v"(fr[11]), "+v"(fr[12]), "+v"(fr[13]), "+v"(fr[14]), "+v"(fr[15]) :: "memory");
        {
            const int so = sig < 4 * NFB ? WS_WDP + ((tcol * NFB + fb) * 4 + 2 * jj) * 1024 : WS_BYTES;
            u4 oe = {__builtin_amdgcn_perm(pk[1], pk[0], 0x05040100), __builtin_amdgcn_perm(pk[3], pk[2], 0x05040100), __builtin_amdgcn_perm(pk[5], pk[4], 0x05040100), __builtin_amdgcn_perm(pk[7], pk[6], 0x05040100)};
            u4 oo = {__builtin_amdgcn_perm(pk[1], pk[0], 0x07060302), __builtin_amdgcn_perm(pk[3], pk[2], 0x07060302), __builtin_amdgcn_perm(pk[5], pk[4], 0x07060302), __builtin_amdgcn_perm(pk[7], pk[6], 0x07060302)};
            st128(oe, rs, l16, so);
            st128(oo, rs, l16, so + 1024);
        }
        {
            const int q1 = lane >> 3, vt = lane & 7, srcl = (q1 + 8 * vt) * 4;
#pragma unroll
            for (int rr = 0; rr < 2; rr++) {
                const int u = 8 * sig + 2 * rq + rr, u1 = u & 63, u2_ = u >> 6;
                int cls, m, sub;
                if ((u1 & 31) != 0) { sub = u1 >> 5; cls = sub ? 64 - u1 : u1; m = sub ? ((8 - q1) & 7) : q1; }
                else { cls = 0; const int hi = u1 >> 5; if ((q1 & 3) == 0) { m = q1; sub = hi; } else { sub = (q1 >> 2) ^ hi; m = sub ? 8 - q1 : q1; } }
                u4 o;
                o.x = __builtin_amdgcn_ds_bpermute(srcl, fr[rr * 4 + 0] | (fr[8 + rr * 4 + 0] << 16)); o.y = __builtin_amdgcn_ds_bpermute(srcl, fr[rr * 4 + 1] | (fr[8 + rr * 4 + 1] << 16));
                o.z = __builtin_amdgcn_ds_bpermute(srcl, fr[rr * 4 + 2] | (fr[8 + rr * 4 + 2] << 16)); o.w = __builtin_amdgcn_ds_bpermute(srcl, fr[rr * 4 + 3] | (fr[8 + rr * 4 + 3] << 16));
                st128(o, rs, NOF(WS_WP + (cls * 8 + m) * 8192 + sub * 4096 + u2_ * 128 + vt * 16), 0);
            }
        }
        STAMP(4);
    } else {
        wait_pair(A_XL);
        read_tile(xa, 0);
        asm volatile("s_waitcnt lgkmcnt(0)" ::: "memory");
        wait_pair(A_XC);
        sfor<8>([&](auto I) { dma_w1(IC<0>{}, I); });
        asm volatile("s_waitcnt vmcnt(8)" : "+v"(cpf) :: "memory");
        post(B_XL);
        STAMP(1);
        read_tile(xb, 1);
        sfor<8>([&](auto I) { dma_w1(IC<1>{}, I); });
        stats(xb);
        STAMP(2);
        wait_pair(A_XB);
        sfor<8>([&](auto I) { dma_w1(IC<8>{}, I); });
        sfor<8>([&](auto I) { dma_w1(IC<9>{}, I); });
    }
    f2 st0, st1;
    {
        const u32 fz = (u32)((li >> 3) | ((lg & 1) << 1));
        const u32 tb = (u32)((8 * lg + (li >> 2)) * 128 + 8 * (li & 3));
        u32 p0 = tb + (((2 * hf) ^ fz) << 5), p1 = tb + (((2 * hf + 1) ^ fz) << 5), pc = tb + (((2 * hf + (rq & 1)) ^ fz) << 5) + (rq >> 1) * 4096;
        const u32 wvB = (u32)((lane >> 2) * 2752 + (((lane & 3) ^ (((lane >> 5) & 1) << 1)) * 16));
        const char* wbs = (const char*)((rq >> 1) ? a.wu : a.wg) + (size_t)((rq & 1) * 16) * 2752 + fb2 * 64;
        f4 accA[4], accB[4], acs = {0, 0, 0, 0};
#pragma unroll
        for (int j = 0; j < 4; j++) { accA[j] = f4{0, 0, 0, 0}; accB[j] = f4{0, 0, 0, 0}; }
        u4 F[2][5];
        auto ldf = [&](int ks, u4 (&f)[5]) {
            const int off = (ks & 7) * PG;
            f[0] = trfrag2<512>(lds + p0 + off); f[1] = trfrag2<512>(lds + p1 + off); f[2] = trfrag2<512>(lds + p0 + off + 4096); f[3] = trfrag2<512>(lds + p1 + off + 4096); f[4] = trfrag2<512>(lds + pc + off);
        };
        if (hf) { WAITVM(24); post(B_W0); }
        wait_grp(1, B_W0);
        STAMP(14);
        ldf(0, F[0]);
        sfor<16>([&](auto S) {
            constexpr int s = S, ks = (s & 8) | ((s & 3) << 1) | ((s >> 2) & 1), sn = s + 1, kn = (sn & 8) | ((sn & 3) << 1) | ((sn >> 2) & 1);
            if (s == 2 || s == 3 || s == 7 || s >= 9) pin9(accA, accB, acs);
            if (s == 3) {
                if (hf) { WAITVM(16); post(B_W1); }
                wait_grp(1, B_W1); STAMP(15);
            }
            if (s == 2 && hf == 0) {
                WAITVM(4);
                flag_store(WS_FF1 + ((rq * 4 + ((s2 & 7) >> 1)) * 64 + (s2 >> 3) * 16 + (s2 & 1) * 8 + t) * 8);
            }
            if (s == 7) {
                if (hf) { WAITVM(8); post(B_W2); }
                else {
                    WAITVM(1);
                    flag_store(sig < 4 * NFB ? WS_FP + (((sig & 1) * 4 + rq) * 128 + (sig >> 1)) * 8 : WS_BYTES);
                    flag_store(WS_FW + (((sig & 7) * 4 + rq) * 32 + (sig >> 3)) * 8);
                }
                wait_grp(1, B_W2); STAMP(16);
                p0 += 65536; p1 += 65536; pc += 65536;
                asm volatile("" : "+v"(p0), "+v"(p1), "+v"(pc));
            }
            if (s == 8) post(hf ? B_S0 : A_S0);
            if (s == 11) {
                if (hf) { WAITVM(0); post(B_W3); }
                wait_grp(1, B_W3); STAMP(17);
            }
            if (s >= 9 && hf == 0 && nblk == 3) {
                if (s == 9) { wait_grp(0, A_S0); wait_grp(1, B_S0); sfor<2>([&](auto I) { dma_add<I * 4096, I * 32 * 2752>(wvB, wbs, (u32)(rq * 1024)); }); }
                sfor<2>([&](auto I) { constexpr int k = (s - 8) * 2 + I; dma_add<k * 4096, k * 32 * 2752>(wvB, wbs, (u32)(rq * 1024)); });
            }
            SCHED();
            if (s + 1 < 16) ldf(kn, F[(s + 1) & 1]);
            u4 (&f)[5] = F[s & 1];
#pragma unroll
            for (int j = 0; j < 4; j++) { accA[j] = mfma(f[j], xa[ks], accA[j]); accB[j] = mfma(f[j], xb[ks], accB[j]); }
            acs = mfma(f[4], ones, acs);
            if (s + 1 < 16) {
                SGB(0x100, 2);
#pragma unroll
                for (int i = 0; i < 8; i++) { SGB(0x8, 1); SGB(0x100, 1); }
                SGB(0x8, 1);
            }
            SCHED();
        });
        RELANE();
        if (li == 0) *(LAS f4*)(lds + L_CS + (hf * 4 + rq) * 64 + lg * 16) = acs;
        post(hf ? B_CS : A_CS);
        STAMP(18);
        wait_grp(hf, hf ? B_CS : A_CS);
        get_tag();
        LAS char* cs = lds + L_CS + hf * 256 + lg * 16;
        st0 = *(LAS f2*)(lds + L_STAT + (16 * rq + li) * 8);
        st1 = *(LAS f2*)(lds + L_STAT + (64 + 16 * rq + li) * 8);
        st128(geglu8(accA, cs, st0), rs, l16, hoff0 + (2 * cg + hf) * 1024);
        st128(geglu8(accB, cs, st1), rs, l16, hoff1 + (2 * cg + hf) * 1024);
    }
    STAMP(5);
    if (hf == 0) {
        WAITVM(2); post(A_WB);
        if (nblk == 3) {
            wait_grp(0, A_WB);
            const u32 tb = (u32)((8 * lg + (li >> 2)) * 64 + (lg & 1) * 32 + 8 * (li & 3));
            LAS char* p0 = lds + tb; LAS char* p1 = lds + (tb ^ 32);
            LAS char* pc = lds + (tb ^ ((rq & 1) * 32)) + (rq >> 1) * 2048;
            f4 acc[2][4], acs = {0, 0, 0, 0};
#pragma unroll
            for (int j = 0; j < 4; j++) { acc[0][j] = f4{0, 0, 0, 0}; acc[1][j] = f4{0, 0, 0, 0}; }
            u4 F[2][5];
            auto ldf = [&](int ks, u4 (&f)[5]) {
                const int off = ks * 4096;
                f[4] = trfrag2<256>(pc + off);
                f[0] = trfrag2<256>(p0 + off); f[1] = trfrag2<256>(p1 + off); f[2] = trfrag2<256>(p0 + off + 2048); f[3] = trfrag2<256>(p1 + off + 2048);
            };
            ldf(0, F[0]);
            sfor<16>([&](auto S) {
                constexpr int ks = S;
                if (ks == 4) { WAITVM(0); flag_store(WS_FHA + ((rg * 8 + w) * 16 + cg) * 8); wait_grp(1, B_FP); }
                if (ks >= 4 && ks % 3 == 1) dma4k_c<L_SET1 + ((ks - 4) / 3) * 16384, ((ks - 4) / 3) * 16384>(l16, wdp, (u32)(rq * 4096));
                SCHED();
                if (ks + 1 < 16) ldf(ks + 1, F[(ks + 1) & 1]);
                u4 (&f)[5] = F[ks & 1];
                acs = mfma(f[4], ones, acs);
#pragma unroll
                for (int j = 0; j < 4; j++) { acc[0][j] = mfma(f[j], xa[ks], acc[0][j]); acc[1][j] = mfma(f[j], xb[ks], acc[1][j]); }
                if (ks + 1 < 16) {
                    SGB(0x100, 2);
#pragma unroll
                    for (int i = 0; i < 8; i++) { SGB(0x8, 1); SGB(0x100, 1); }
                    SGB(0x8, 1);
                }
                SCHED();
            });
            if (li == 0) *(LAS f4*)(lds + L_CS + 512 + rq * 64 + lg * 16) = acs;
            post(A_PB);
            wait_grp(0, A_PB);
            st128(geglu8(acc[0], lds + L_CS + 512 + lg * 16, st0), rs, l16, hoff0 + fb2 * 1024);
            st128(geglu8(acc[1], lds + L_CS + 512 + lg * 16, st1), rs, l16, hoff1 + fb2 * 1024);
        } else {
            WAITVM(0); flag_store(WS_FHA + ((rg * 8 + w) * 16 + cg) * 8);
            wait_grp(1, B_FP);
            sfor<4>([&](auto I) { constexpr int i = I; dma4k_c<L_SET1 + i * 16384, i * 16384>(l16, wdp, (u32)(rq * 4096)); });
        }
        STAMP(6);
        sfor<4>([&](auto I) { constexpr int i = I; dma4k_c<i * 16384, (16 + 4 * i) * 4096>(l16, wdp, (u32)(rq * 4096)); });
        if (nblk == 3) { WAITVM(16); flag_store(WS_FHB + ((rg * 4 + rq) * 16 + fb2 - 32) * 8); }
    } else {
        f2 v[8], tw0[4], tw1[8], al[8], be[8];
        const int rb = qb, r3 = 2 * rq + rb;
        const int cls2 = sig >> 3, m2 = sig & 7;
        const int wq2 = (m2 < 4) ? m2 : ((8 - m2) & 3), pos1 = wq2 * 64 + cls2 * 2 + (m2 >> 2);
        const bool special = (cls2 == 0) && ((m2 & 3) == 0);
        int u1a, q1a, u1b, q1b;
        if (cls2 != 0) { u1a = cls2; q1a = m2; u1b = 64 - cls2; q1b = (8 - m2) & 7; }
        else if (special) { u1a = 0; u1b = 32; q1a = q1b = m2; }
        else { u1a = u1b = (m2 < 4) ? 0 : 32; q1a = m2; q1b = 8 - m2; }
        int sA, vH0, sB, vH1;
        if (!special) { sA = 0; vH0 = rq; sB = 1; vH1 = (u1a == 0) ? ((4 - rq) & 3) : 3 - rq; }
        else { sA = sB = rq >> 1; vH0 = rq & 1; vH1 = (rq == 0) ? 2 : (rq == 1) ? 3 : (rq == 2) ? 3 : 2; }
        const bool q0 = (m2 == 0);
        const int dy = ((sA ? u1b : u1a) == 0 && vH0 == 0) ? 8 : 7, dq = q0 ? 8 : 7;
        int y1 = lane >> 3, t1 = lane & 7, y2 = (dy - y1) & 7, t2 = (dq - t1) & 7, hA = vH0, hB = vH1;
        bool self = false;
        if (special && rq == 0) {
            const int k = lane & 31;
            if (qb) { hA = hB = 2; y1 = k >> 3; t1 = k & 7; y2 = 7 - y1; t2 = (dq - t1) & 7; }
            else {
                hA = hB = 0; const int vx = k & 3, y = k >> 2;
                if (!q0 || vx) { y1 = y; t1 = vx; y2 = (8 - y) & 7; t2 = dq - vx; }
                else { t1 = t2 = (y >= 4) ? 4 : 0; const int yy = y & 3; if (yy) { y1 = yy; y2 = 8 - yy; } else { y1 = 0; y2 = 4; self = true; } }
            }
        }
        const int uA = (sA ? u1b : u1a) + 64 * (hA + 4 * y1), uB = (sB ? u1b : u1a) + 64 * (hB + 4 * y2);
        const int qA = (sA ? q1b : q1a) + 8 * t1, qB = (sB ? q1b : q1a) + 8 * t2;
        const int rowB = L_FFT + ((qb ? (vH1 * 2 + sB) : (vH0 * 2 + sA)) * 36 + (lane & 31)) * 8;
        const bool rot = q0 && (t1 == 0);
        const int p3a1 = L_FFT + (y1 * 290 + (hA * 2 + sA) * 36 + t1 * 4) * 8;
        const int p3a2 = L_FFT + (y2 * 290 + (hB * 2 + sB) * 36 + t2 * 4) * 8;
        const float thA = (float)qA * (1.0f / 512.0f), thB = (float)qB * (1.0f / 512.0f);
#pragma unroll
        for (int k = 1; k < 4; k++) tw0[k] = cis((float)r3 * ((float)k / 32.0f));
#pragma unroll
        for (int k = 1; k < 8; k++) tw1[k] = cis((float)c2p * ((float)k / 32.0f));
        WAITVM(0);
        flag_store(WS_FHA + ((rg * 8 + w) * 16 + cg) * 8);
        flag_wait(min(rq * 22 + lane, 85) * 8, WS_FP + t * 1024);
        post(B_FP);
        flag_wait((((qb ? u1b : u1a) >> 1) * 32 + (lane & 31)) * 8, WS_FW);
        const u4 wa4 = __builtin_amdgcn_raw_buffer_load_b128(rs, (hA + 4 * y1) * 128 + t1 * 16, WS_WP + sig * 8192 + sA * 4096, 16);
        const u4 wb4 = __builtin_amdgcn_raw_buffer_load_b128(rs, (hB + 4 * y2) * 128 + t2 * 16, WS_WP + sig * 8192 + sB * 4096, 16);
        f2 wS[2] = {f2{0.f, 0.f}, f2{0.f, 0.f}};
        if (q0) {
            wS[0] = f2{bflo(a.wr[uA * 257 + 256]), bflo(a.wi[uA * 257 + 256])};
            wS[1] = f2{bflo(a.wr[uB * 257 + 256]), bflo(a.wi[uB * 257 + 256])};
        }
        {
            f2 W[8], WB[8];
#pragma unroll
            for (int e = 0; e < 4; e++) { W[e] = bfup(wa4[e]); W[4 + e] = bfup(wb4[e]); }
#pragma unroll
            for (int k = 0; k < 4; k++) { WB[k] = W[4 + 3 - k]; WB[4 + k] = W[3 - k]; }
            if (q0) {
#pragma unroll
                for (int k = 0; k < 4; k++) {
                    f2 n1 = rot ? W[(4 - k) & 3] : W[3 - k], n2 = rot ? W[4 + ((4 - k) & 3)] : W[4 + 3 - k];
                    WB[k] = self ? n1 : n2; WB[4 + k] = self ? n2 : n1;
                }
#pragma unroll
                for (int e = 0; e < 8; e += 4) {
                    f2 own = wS[e >> 2], oth = self ? own : wS[1 - (e >> 2)];
                    f2 S0 = pkfma(WB[e], kc.pm, W[e]) * 0.5f, S1c = pkfma(own, kc.pm, oth) * 0.5f;
                    WB[e] = rot ? S1c : WB[e];
                    al[e] = rot ? S0 : W[e];
                }
                W[0] = al[0]; W[4] = al[4];
            }
#pragma unroll
            for (int e = 0; e < 8; e++) {
                const f2 cs = cis(((e >> 2) ? thB : thA) + 0.125f * (float)(e & 3));
                f2 P = pkfma(WB[e], kc.pm, W[e]), M = pkfma(WB[e], kc.pm.yx, W[e]);
                al[e] = pkfma(M, -cs.yy, P);
                be[e] = M.yx * (kc.pm.yx * cs.xx);
            }
            asm volatile("" : "+v"(al[0]), "+v"(al[1]), "+v"(al[2]), "+v"(al[3]), "+v"(al[4]), "+v"(al[5]), "+v"(al[6]), "+v"(al[7]),
                         "+v"(be[0]), "+v"(be[1]), "+v"(be[2]), "+v"(be[3]), "+v"(be[4]), "+v"(be[5]), "+v"(be[6]), "+v"(be[7]),
                         "+v"(tw0[1]), "+v"(tw0[2]), "+v"(tw0[3]), "+v"(tw1[1]), "+v"(tw1[2]), "+v"(tw1[3]), "+v"(tw1[4]), "+v"(tw1[5]), "+v"(tw1[6]), "+v"(tw1[7]));
        }
        STAMP(3);
        flag_wait(lane * 8, WS_FF1 + (wq2 * 4 + rq) * 512);
        STAMP(4);
        wait_grp(0, A_S0);
        __builtin_amdgcn_s_setprio(2);
        const int f1off = WS_F1 + sig * 16384 + 2 * rq * 512;
        u4 d[4];
#pragma unroll
        for (int H = 0; H < 4; H++) d[H] = __builtin_amdgcn_raw_buffer_load_b128(rs, NOF(l16), f1off + H * 4096, 16);
        f2 e0[4], e1[4];
#pragma unroll
        for (int H = 0; H < 4; H++) { e0[H] = f2{__uint_as_float(d[H].x), __uint_as_float(d[H].y)}; e1[H] = f2{__uint_as_float(d[H].z), __uint_as_float(d[H].w)}; }
        dft4<false>(e0[0], e0[1], e0[2], e0[3], kc); dft4<false>(e1[0], e1[1], e1[2], e1[3], kc);
        STAMP(6);
        LAS char* area = lds + L_FFT + rq * 4640;
#pragma unroll
        for (int k = 0; k < 4; k++) {
            if (k) { e0[k] = cmul<true>(e0[k], tw0[k]); e1[k] = cmul<true>(e1[k], tw0[k]); }
            *(LAS f4*)(area + (l3 * 72 + 2 * c2p + 8 * k + 32 * rb) * 8) = f4{e0[k].x, e0[k].y, e1[k].x, e1[k].y};
        }
        FENCE();
#pragma unroll
        for (int k = 0; k < 8; k++) v[k] = *(LAS f2*)(area + (k * 72 + lane) * 8);
        dft8<false>(v, kc);
#pragma unroll
        for (int k = 1; k < 8; k++) v[k] = cmul<true>(v[k], tw1[k]);
        FENCE();
        LAS char* pb = lds + L_FFT + (r3 * 290 + l3 * 36 + c2p) * 8;
#pragma unroll
        for (int k = 0; k < 8; k++) *(LAS f2*)(pb + k * 32) = v[k];
        post(B_E1);
        wait_grp(1, B_E1);
#pragma unroll
        for (int k = 0; k < 8; k++) v[k] = *(LAS f2*)(lds + rowB + k * 2320);
        dft8<false>(v, kc);
#pragma unroll
        for (int k = 0; k < 8; k++) *(LAS f2*)(lds + rowB + k * 2320) = v[k];
        FENCE();
        f4 a0 = *(LAS f4*)(lds + p3a1), a1 = *(LAS f4*)(lds + p3a1 + 16), b0 = *(LAS f4*)(lds + p3a2), b1 = *(LAS f4*)(lds + p3a2 + 16);
        f2 X[8] = {a0.xy, a0.zw, a1.xy, a1.zw, b0.xy, b0.zw, b1.xy, b1.zw};
        dft4<false>(X[0], X[1], X[2], X[3], kc); dft4<false>(X[4], X[5], X[6], X[7], kc);
        f2 Y[8];
#pragma unroll
        for (int k = 0; k < 4; k++) { Y[k] = X[4 + 3 - k]; Y[4 + k] = X[3 - k]; }
        if (q0) {
#pragma unroll
            for (int k = 0; k < 4; k++) {
                f2 n1 = rot ? X[(4 - k) & 3] : X[3 - k], n2 = rot ? X[4 + ((4 - k) & 3)] : X[4 + 3 - k];
                Y[k] = self ? n1 : n2; Y[4 + k] = self ? n2 : n1;
            }
        }
        f2 O[8];
#pragma unroll
        for (int e = 0; e < 8; e++) O[e] = cfmac_conj(cmul<false>(X[e], al[e]), be[e], Y[e]);
        dft4<true>(O[0], O[1], O[2], O[3], kc); dft4<true>(O[4], O[5], O[6], O[7], kc);
        *(LAS f4*)(lds + p3a1) = f4{O[0].x, O[0].y, O[1].x, O[1].y}; *(LAS f4*)(lds + p3a1 + 16) = f4{O[2].x, O[2].y, O[3].x, O[3].y};
        *(LAS f4*)(lds + p3a2) = f4{O[4].x, O[4].y, O[5].x, O[5].y}; *(LAS f4*)(lds + p3a2 + 16) = f4{O[6].x, O[6].y, O[7].x, O[7].y};
        FENCE();
#pragma unroll
        for (int k = 0; k < 8; k++) v[k] = *(LAS f2*)(lds + rowB + k * 2320);
        dft8<true>(v, kc);
#pragma unroll
        for (int k = 0; k < 8; k++) *(LAS f2*)(lds + rowB + k * 2320) = v[k];
        post(B_E2);
        wait_grp(1, B_E2);
#pragma unroll
        for (int k = 0; k < 8; k++) v[k] = *(LAS f2*)(pb + k * 32);
#pragma unroll
        for (int k = 1; k < 8; k++) v[k] = cmul<false>(v[k], tw1[k]);
        dft8<true>(v, kc);
        FENCE();
#pragma unroll
        for (int k = 0; k < 8; k++) *(LAS f2*)(area + (k * 72 + lane) * 8) = v[k];
        FENCE();
#pragma unroll
        for (int k = 0; k < 4; k++) {
            f4 q = *(LAS f4*)(area + (l3 * 72 + 2 * c2p + 8 * k + 32 * rb) * 8);
            e0[k] = q.xy; e1[k] = q.zw;
            if (k) { e0[k] = cmul<false>(e0[k], tw0[k]); e1[k] = cmul<false>(e1[k], tw0[k]); }
        }
        dft4<true>(e0[0], e0[1], e0[2], e0[3], kc); dft4<true>(e1[0], e1[1], e1[2], e1[3], kc);
        const int f2st = WS_F2 + (r3 * 8 + l3) * 16384 + pos1 * 64 + c2p * 16;
#pragma unroll
        for (int H = 0; H < 4; H++) {
            u4 o = {__float_as_uint(e0[H].x), __float_as_uint(e0[H].y), __float_as_uint(e1[H].x), __float_as_uint(e1[H].y)};
            st128(o, rs, NOF(f2st), H * (64 * 16384));
        }
        STAMP(7);
        __builtin_amdgcn_s_setprio(0);
        WAITVM(0);
        flag_store(WS_FF2 + (rq * 256 + pos1) * 8);
    }
    STAMP(8);
    RELANE();
    f4 acc2[4];
#pragma unroll
    for (int j = 0; j < 4; j++) acc2[j] = f4{0, 0, 0, 0};
    const int hsrc = hoff0 + hf * 1024;
    auto g2_mma = [&](auto NC, const u4* hfr, int ldsbase) {
        constexpr int N = decltype(NC)::value;
        u32 pa = (u32)(ldsbase + hf * 4096) + l16; asm volatile("" : "+v"(pa));
        LAS u4* p = (LAS u4*)(lds + pa);
        u4 F[2][4];
#pragma unroll
        for (int j = 0; j < 4; j++) F[0][j] = p[j * 64];
        sfor<N>([&](auto I) {
            constexpr int i = I;
            SCHED();
            if (i + 1 < N) {
#pragma unroll
                for (int j = 0; j < 4; j++) F[(i + 1) & 1][j] = p[(i + 1) * 512 + j * 64];
            }
#pragma unroll
            for (int j = 0; j < 4; j++) acc2[j] = mfma(F[i & 1][j], hfr[i], acc2[j]);
            if (i + 1 < N) {
#pragma unroll
                for (int j = 0; j < 4; j++) { SGB(0x100, 1); SGB(0x8, 1); }
            }
            SCHED();
        });
    };
    u4 hfr[16];
    flag_wait(li * 8, WS_FHA + (rg * 8 + w) * 128);
    if (hf == 0) post(A_D1);
#pragma unroll
    for (int i = 0; i < 16; i++) { hfr[i] = __builtin_amdgcn_raw_buffer_load_b128(rs, l16, hsrc + 2 * i * 1024, 16); SCHED(); }
    STAMP(9);
    wait_grp(0, A_D1);
    g2_mma(IC<8>{}, hfr, L_SET1);
    post(hf ? B_G1 : A_G1);
    g2_mma(IC<8>{}, hfr + 8, 0);
    pin4(acc2);
    STAMP(10);
    if (hf == 0) {
        wait_grp(0, A_G1); wait_grp(1, B_G1);
        sfor<3>([&](auto I) { constexpr int i = I; if (i < 2 || rq < 3) dma4k_c<L_SET1 + i * 16384, (32 + 4 * i) * 4096>(l16, wdp, (u32)(rq * 4096)); });
        flag_wait(min(li, 10) * 8, WS_FHB + (rg * 4 + rq) * 128);
        post(A_D3);
#pragma unroll
        for (int i = 0; i < 11; i++) { hfr[i] = __builtin_amdgcn_raw_buffer_load_b128(rs, l16, hoff0 + (32 + (i < 6 ? 2 * i : 2 * (i - 6) + 1)) * 1024, 16); SCHED(); }
        STAMP(13);
        wait_grp(0, A_D3);
        g2_mma(IC<6>{}, hfr, L_SET1);
        g2_mma(IC<5>{}, hfr + 6, L_SET1 + 4096);
    } else {
        wait_grp(1, B_G1); wait_grp(0, A_G1);
        fft13_consts(0.5f / 524288.0f);
        flag_wait(lane * 8, WS_FF2 + (((s2 & 7) >> 1) * 256 + rq * 64) * 8);
        u4 d[4]; f2 v[8];
        const int off = WS_F2 + pid * 16384 + rq * 4096;
#pragma unroll
        for (int i = 0; i < 4; i++) d[i] = __builtin_amdgcn_raw_buffer_load_b128(rs, NOF(l16), off + i * 1024, 16);
        STAMP(13);
        __builtin_amdgcn_s_setprio(2);
#pragma unroll
        for (int i = 0; i < 4; i++) *(LAS u4*)(lds + L_FFT + rq * 4608 + (i * 8 + (lane >> 3)) * 144 + (lane & 7) * 16) = d[i];
        FENCE();
        v[0] = *(LAS f2*)(lds + x3a[0]);
        v[1] = *(LAS f2*)(lds + x3a[1]); v[2] = *(LAS f2*)(lds + x3a[1] + 1152); v[3] = *(LAS f2*)(lds + x3a[1] + 2304);
        v[4] = *(LAS f2*)(lds + x3a[2]);
        v[5] = *(LAS f2*)(lds + x3a[3] + 2304); v[6] = *(LAS f2*)(lds + x3a[3] + 1152); v[7] = *(LAS f2*)(lds + x3a[3]);
#pragma unroll
        for (int k = 0; k < 8; k++) v[k] = cmul<false>(v[k], twT[k]);
        dft8<true>(v, kc);
#pragma unroll
        for (int k = 1; k < 8; k++) v[k] = cmul<false>(v[k], tw64[k]);
        FENCE();
        LAS f2* reg = (LAS f2*)(lds + L_FFT) + (rq * 2 + qb) * 288;
#pragma unroll
        for (int k = 0; k < 8; k++) reg[36 * l3 + 4 * k + c2p] = v[k];
        FENCE();
#pragma unroll
        for (int k = 0; k < 8; k++) v[k] = reg[36 * k + (lane & 31)];
        dft8<true>(v, kc);
        FENCE();
#pragma unroll
        for (int k = 0; k < 8; k++) reg[(lane & 31) + 32 * k] = v[k];
        post(B_F3);
        wait_grp(1, B_F3);
        if (b == 0 && rq == 0) __builtin_amdgcn_raw_buffer_store_b64(*(LAS u2*)(lds + L_TAG + 8), rs, lane ? WS_BYTES : 0, 0, 16);
        LAS f2* xb1 = (LAS f2*)(lds + L_FFT) + rq * 64 + lane;
#pragma unroll
        for (int q1 = 0; q1 < 8; q1++) { const int rg_ = (q1 < 4) ? q1 * 2 : (((8 - q1) & 3) * 2 + 1); v[q1] = xb1[rg_ * 288]; }
        dft8<true>(v, kc);
#pragma unroll
        for (int c1 = 0; c1 < 8; c1++) *(LAS u32*)(lds + L_S + (16 * rq + (lane >> 2)) * 144 + c1 * 16 + c2p * 4) = pkbf(v[c1]);
    }
    STAMP(11);
    BAR();
    {
        LAS f4* xs = (LAS f4*)(lds + w * 2048) + lane;
        LAS f4* xr = (LAS f4*)(lds + (w ^ 4) * 2048) + lane;
        f4 me, mo;
        if (hf) { xs[0] = acc2[0]; xs[64] = acc2[1]; me = acc2[2]; mo = acc2[3]; } else { xs[0] = acc2[2]; xs[64] = acc2[3]; me = acc2[0]; mo = acc2[1]; }
        BAR();
        const int c1 = 4 * hf + lg;
        me += xr[0]; mo += xr[64];
        const u4 sv = *(LAS u4*)(lds + L_S + (16 * rq + li) * 144 + c1 * 16);
        const u4 xv = *(LAS u4*)(lds + ((rq >> 1) ? L_X1 : L_X0) + ((rq & 1) * 16 + li) * 128 + ((c1 ^ (li & 7)) * 16));
        u32 res[4];
#pragma unroll
        for (int e = 0; e < 4; e++) {
            const f2 ov = bfup(pkbf(f2{me[e], mo[e]}));
            const f2 z = ov * -1.44269504f;
            const f2 d = f2{__builtin_amdgcn_exp2f(z.x), __builtin_amdgcn_exp2f(z.y)} + f2{1.f, 1.f};
            const f2 g = bfup(pkbf(ov * f2{__builtin_amdgcn_rcpf(d.x), __builtin_amdgcn_rcpf(d.y)}));
            res[e] = pkbf(bfup(xv[e]) + bfup(pkbf(bfup(sv[e]) * g)));
        }
        *(u4*)(a.out + (size_t)(s2 + 32 * (16 * rq + li)) * 512 + 64 * c1 + 8 * t) = u4{res[0], res[1], res[2], res[3]};
    }
    STAMP(12);
}

extern "C" void kernel_launch(void* const* d_in, const int* in_sizes, int n_in,
                              void* d_out, int out_size, void* d_ws, size_t ws_size,
                              hipStream_t stream) {
    Args a;
    a.x = (const u16*)d_in[0]; a.wr = (const u16*)d_in[1]; a.wi = (const u16*)d_in[2];
    a.wg = (const u16*)d_in[4]; a.wu = (const u16*)d_in[5]; a.wd = (const u16*)d_in[6];
    a.out = (u16*)d_out; a.ws = (unsigned char*)d_ws + ((ws_size / 2) & ~(size_t)4095);
    (void)hipFuncSetAttribute((const void*)fused, hipFuncAttributeMaxDynamicSharedMemorySize, L_END);
    fused<<<256, 512, L_END, stream>>>(a);
}
```

```cpp
#include <hip/hip_runtime.h>
#include <stdint.h>
#include <type_traits>

typedef unsigned short u16;
typedef unsigned u32;
typedef unsigned long long u64;
typedef float f2 __attribute__((ext_vector_type(2)));
typedef float f4 __attribute__((ext_vector_type(4)));
typedef u32 u4 __attribute__((ext_vector_type(4)));
typedef u32 u2 __attribute__((ext_vector_type(2)));
typedef short s4 __attribute__((ext_vector_type(4)));
typedef __bf16 bf8 __attribute__((ext_vector_type(8)));
typedef __bf16 bf2 __attribute__((ext_vector_type(2)));
#define LAS __attribute__((address_space(3)))
#define GAS __attribute__((address_space(1)))
#define DEV __device__ __forceinline__
template <int N> using IC = std::integral_constant<int, N>;

constexpr int NFB = 43;
constexpr int WS_FF1 = 4096;
constexpr int WS_FF2 = WS_FF1 + 8192;
constexpr int WS_FHA = WS_FF2 + 8192;
constexpr int WS_FHB = WS_FHA + 16384;
constexpr int WS_FP = WS_FHB + 16384;
constexpr int WS_FW = WS_FP + 8192;
constexpr int WS_F1 = 1 << 20, WS_F2 = WS_F1 + (4 << 20), WS_H = WS_F2 + (4 << 20);
constexpr int WS_WDP = WS_H + 32 * 4 * NFB * 1024;
constexpr int WS_WP = WS_WDP + 8 * NFB * 4 * 1024;
constexpr int WS_STAMP = WS_WP + (2 << 20);
constexpr int WS_BYTES = WS_STAMP + (1 << 20);
constexpr int PG = 8192;
constexpr int L_SET1 = 8 * PG;
constexpr int L_FFT = 16 * PG;
constexpr int L_SMALL = 19 * PG;
constexpr int L_PROG = L_SMALL;
constexpr int L_TAG = L_SMALL + 32;
constexpr int L_STAT = L_SMALL + 64;
constexpr int L_CS = L_SMALL + 2048;
constexpr int L_X0 = L_FFT + 18688, L_X1 = L_SMALL + 3072;
constexpr int L_S = 4 * PG;
constexpr int L_END = 20 * PG;
constexpr u32 SENT = 0xFFFFFFFFu;
constexpr int CODE_BYTES = 36 * 1024;
constexpr int SPIN_CAP = 1 << 16;

struct Args { const u16* x; const u16* wr; const u16* wi; const u16* wg; const u16* wu; const u16* wd; u16* out; unsigned char* ws; };

#define BAR() asm volatile("s_waitcnt lgkmcnt(0)\n\ts_barrier" ::: "memory")
#define WAITVM(n) asm volatile("s_waitcnt vmcnt(" #n ")" ::: "memory")
#define NOF(x) (x)
#define NOPL(x) (x)
#define FENCE() asm volatile("" ::: "memory")
#define SCHED() __builtin_amdgcn_sched_barrier(0)
#define SGB(mask, n) __builtin_amdgcn_sched_group_barrier(mask, n, 0)
#define STAMP(i)
#define CSTAMP(i)

DEV f2 pkfma(f2 a, f2 b, f2 c) { return __builtin_elementwise_fma(a, b, c); }
struct KC { f2 pm, rpm; };
DEV f2 addmi(f2 a, f2 b, const KC& k) { return pkfma(b.yx, k.pm, a); }
DEV f2 addpi(f2 a, f2 b, const KC& k) { return pkfma(b.yx, k.pm.yx, a); }
template <bool INV> DEV void dft4(f2& a0, f2& a1, f2& a2, f2& a3, const KC& k) {
    f2 t0 = a0 + a2, t1 = a0 - a2, t2 = a1 + a3, t3 = a1 - a3;
    a0 = t0 + t2; a2 = t0 - t2;
    if (INV) { a1 = addpi(t1, t3, k); a3 = addmi(t1, t3, k); } else { a1 = addmi(t1, t3, k); a3 = addpi(t1, t3, k); }
}
template <bool INV> DEV void dft8(f2 (&v)[8], const KC& k) {
    f2 a0 = v[0] + v[4], a4 = v[0] - v[4], a1 = v[1] + v[5], a5 = v[1] - v[5];
    f2 a2 = v[2] + v[6], a6 = v[2] - v[6], a3 = v[3] + v[7], a7 = v[3] - v[7];
    dft4<INV>(a0, a1, a2, a3, k);
    v[0] = a0; v[2] = a1; v[4] = a2; v[6] = a3;
    f2 b1, b3, t0, t1;
    if (INV) { b1 = addpi(a5, a5, k); b3 = addpi(-a7, a7, k); t0 = addpi(a4, a6, k); t1 = addmi(a4, a6, k); }
    else { b1 = addmi(a5, a5, k); b3 = addmi(-a7, a7, k); t0 = addmi(a4, a6, k); t1 = addpi(a4, a6, k); }
    f2 t2 = b1 + b3, t3 = b1 - b3;
    v[1] = pkfma(t2, k.rpm.xx, t0); v[5] = pkfma(t2, k.rpm.yy, t0);
    if (INV) { v[3] = pkfma(t3.yx, k.rpm.yx, t1); v[7] = pkfma(t3.yx, k.rpm, t1); }
    else { v[3] = pkfma(t3.yx, k.rpm, t1); v[7] = pkfma(t3.yx, k.rpm.yx, t1); }
}
template <bool CONJ> DEV f2 cmul(f2 a, f2 w) {
    f2 r;
    if (CONJ) asm("v_pk_mul_f32 %0, %1, %2 op_sel:[1,1] op_sel_hi:[0,1]\n\tv_pk_fma_f32 %0, %1, %2, %0 op_sel_hi:[1,0,1] neg_hi:[0,0,1]" : "=&v"(r) : "v"(a), "v"(w));
    else asm("v_pk_mul_f32 %0, %1, %2 op_sel:[1,1] op_sel_hi:[0,1]\n\tv_pk_fma_f32 %0, %1, %2, %0 op_sel_hi:[1,0,1] neg_lo:[0,0,1]" : "=&v"(r) : "v"(a), "v"(w));
    return r;
}
DEV f2 cfmac_conj(f2 t, f2 beta, f2 B) {
    asm("v_pk_fma_f32 %0, %2, %1, %0 op_sel_hi:[0,1,1]\n\tv_pk_fma_f32 %0, %2, %1, %0 op_sel:[1,1,0] op_sel_hi:[1,0,1] neg_hi:[0,1,0]" : "+v"(t) : "v"(beta), "v"(B));
    return t;
}
DEV f2 cis(float r) { return f2{__builtin_amdgcn_cosf(r), __builtin_amdgcn_sinf(r)}; }
DEV float bflo(u32 v) { return __uint_as_float(v << 16); }
DEV float bfhi(u32 v) { return __uint_as_float(v & 0xFFFF0000u); }
DEV f2 bfup(u32 v) { return f2{bflo(v), bfhi(v)}; }
DEV u32 pkbf(f2 v) { bf2 r; r.x = (__bf16)v.x; r.y = (__bf16)v.y; u32 o = __builtin_bit_cast(u32, r); asm("" : "+v"(o)); return o; }
DEV float rbf(float f) { return __uint_as_float(((u32)__builtin_bit_cast(u16, (__bf16)f)) << 16); }
DEV f4 mfma(u4 a, u4 b, f4 c) { return __builtin_amdgcn_mfma_f32_16x16x32_bf16(__builtin_bit_cast(bf8, a), __builtin_bit_cast(bf8, b), c, 0, 0, 0); }
DEV u4 sanitize(u4 v) { return u4{min(v.x, 0xFFFFFFFEu), min(v.y, 0xFFFFFFFEu), min(v.z, 0xFFFFFFFEu), min(v.w, 0xFFFFFFFEu)}; }
DEV u32 max4(u4 v) { return max(max(v.x, v.y), max(v.z, v.w)); }
DEV void st128(u4 v, __amdgpu_buffer_rsrc_t rs, u32 voff, int soff) {
    __builtin_amdgcn_raw_buffer_store_b128(v, rs, voff, soff, 16);
    asm volatile("s_nop 1" :: "v"(v) : "memory");
}
template <int OFF2> DEV u4 trfrag2(LAS char* p) {
    s4 r0 = __builtin_amdgcn_ds_read_tr16_b64_v4i16((LAS s4*)p);
    s4 r1 = __builtin_amdgcn_ds_read_tr16_b64_v4i16((LAS s4*)(p + OFF2));
    u2 x0 = __builtin_bit_cast(u2, r0), x1 = __builtin_bit_cast(u2, r1);
    return u4{x0.x, x0.y, x1.x, x1.y};
}
template <int LI, int GI> DEV void dma_add(u32 vb, const void* sbase, u32 sl) {
    const u32 va = vb + (u32)GI;
    asm volatile("s_add_u32 m0, %2, %3\n\ts_nop 0\n\tglobal_load_lds_dwordx4 %0, %1" :: "v"(va), "s"(sbase), "s"(sl), "i"(LI) : "memory", "scc");
}
template <int LI, int GI> DEV void dma4k_c(u32 vb, const void* sbase, u32 sl) {
    const u32 va = vb + (u32)GI;
    asm volatile("s_add_u32 m0, %2, %3\n\ts_nop 0\n\tglobal_load_lds_dwordx4 %0, %1 sc1\n\tglobal_load_lds_dwordx4 %0, %1 offset:1024 sc1\n\t"
                 "global_load_lds_dwordx4 %0, %1 offset:2048 sc1\n\tglobal_load_lds_dwordx4 %0, %1 offset:3072 sc1"
                 :: "v"(va), "s"(sbase), "s"(sl), "i"(LI) : "memory", "scc");
}
template <int OFF> DEV void ldg_u16(u32& d, u32 vo, const void* sb) { asm volatile("global_load_ushort %0, %1, %2 offset:%3" : "=v"(d) : "v"(vo), "s"(sb), "i"(OFF) : "memory"); }
template <int OFF> DEV void ldg_u32(u32& d, u32 vo, const void* sb) { asm volatile("global_load_dword %0, %1, %2 offset:%3" : "=v"(d) : "v"(vo), "s"(sb), "i"(OFF) : "memory"); }
DEV void lds_wait(u32 addr, u32 v) {
    u32 tmp, cnt;
    asm volatile("s_mov_b32 %1, 0x80000\n"
                 "1:\n\tds_read_b32 %0, %2\n\ts_sub_u32 %1, %1, 1\n\ts_waitcnt lgkmcnt(0)\n\ts_cbranch_scc1 2f\n\tv_cmp_gt_u32 vcc, %3, %0\n\ts_cbranch_vccnz 1b\n2:"
                 : "=&v"(tmp), "=&s"(cnt) : "v"(addr), "s"(v) : "memory", "vcc", "scc");
}
template <int N, typename F> DEV void sfor(F&& f) { if constexpr (N > 0) { sfor<N - 1>(f); f(IC<N - 1>{}); } }

DEV void pin9(f4 (&x)[4], f4 (&y)[4], f4& z) {
    asm volatile("" : "+v"(x[0]), "+v"(x[1]), "+v"(x[2]), "+v"(x[3]), "+v"(y[0]), "+v"(y[1]), "+v"(y[2]), "+v"(y[3]), "+v"(z));
}
DEV void pin4(f4 (&x)[4]) { asm volatile("" : "+v"(x[0]), "+v"(x[1]), "+v"(x[2]), "+v"(x[3])); }
DEV void dma_rt(u32 voff, const void* sbase, u32 sl) {
    asm volatile("s_mov_b32 m0, %2\n\ts_nop 0\n\tglobal_load_lds_dwordx4 %0, %1" :: "v"(voff), "s"(sbase), "s"(sl) : "memory");
}
DEV u4 geglu8(const f4 (&acc)[4], LAS char* cs, f2 st) {
    u32 hw[4];
    const f2 nm = f2{-st.x, -st.x}, ri = f2{st.y, st.y};
#pragma unroll
    for (int c = 0; c < 2; c++) {
        f4 cg_ = *(LAS f4*)(cs + c * 64), cu_ = *(LAS f4*)(cs + (2 + c) * 64);
#pragma unroll
        for (int e = 0; e < 2; e++) {
            f2 g = pkfma(e ? cg_.zw : cg_.xy, nm, e ? acc[c].zw : acc[c].xy) * ri;
            f2 u = pkfma(e ? cu_.zw : cu_.xy, nm, e ? acc[2 + c].zw : acc[2 + c].xy) * ri;
            f2 z = g * pkfma(g * g, f2{-0.10294324f, -0.10294324f}, f2{-2.3022082f, -2.3022082f});
            f2 d = f2{__builtin_amdgcn_exp2f(z.x), __builtin_amdgcn_exp2f(z.y)} + f2{1.f, 1.f};
            f2 r = f2{__builtin_amdgcn_rcpf(d.x), __builtin_amdgcn_rcpf(d.y)};
            hw[2 * c + e] = pkbf(g * r * u);
        }
    }
    return u4{hw[0], hw[1], hw[2], hw[3]};
}

enum { A_XL = 1, A_XC, A_P1, A_XB, A_S0, A_CS, A_WB, A_PB, A_D1, A_G1, A_D3 };
enum { B_XL = 1, B_W0, B_W1, B_W2, B_S0, B_W3, B_CS, B_FP, B_E1, B_E2, B_G1, B_F3 };

__global__ void __launch_bounds__(512, 2) fused(Args a) {
    extern __shared__ __attribute__((aligned(16))) char smem[];
    LAS char* lds = (LAS char*)smem;
    const int tid = threadIdx.x, w = __builtin_amdgcn_readfirstlane(tid >> 6);
    int lane = tid & 63;
    const int b = blockIdx.x, xcd = b & 7, r = b >> 3, t = r & 7, par = xcd & 1, rg = (xcd >> 1) * 4 + (r >> 3);
    const int s2 = 2 * rg + par, cg = par * 8 + t, sig = xcd * 32 + r, pid = s2 * 8 + t;
    const int rq = w & 3, hf = w >> 2, j4 = r >> 3;
#define BE(l) ((((l) >> 3) << 1) | ((l) & 1))
#define IX(l) (((l) >> 1) & 3)
    u32 l16 = lane * 16;
    u32 cpf = 0;
    if (w == 7) {
        const u64 pc = __builtin_amdgcn_s_getpc() & ~(u64)255;
        const u32 off = min((u32)((t * 64 + lane) * 128), (u32)(CODE_BYTES - 128));
        asm volatile("global_load_dword %0, %1, %2" : "=v"(cpf) : "v"(off), "s"(pc) : "memory");
    }
    if (w == 6) {
        const int g = lane >> 2, k = lane & 3;
        const char* p; u32 sz;
        if (g < 10) { p = (const char*)a.ws + (size_t)g * (2 << 20); sz = 2 << 20; }
        else if (g == 10) { p = (const char*)a.wr; sz = 2048 * 514; } else if (g == 11) { p = (const char*)a.wi; sz = 2048 * 514; }
        else if (g == 12) { p = (const char*)a.wd; sz = 1376 * 1024; } else if (g == 13) { p = (const char*)a.out; sz = 2 << 20; }
        else if (g == 14) { p = (const char*)a.wg; sz = 512 * 2752; } else { p = (const char*)a.wu; sz = 512 * 2752; }
        p += min((u32)(k * (700 << 10)), sz - 64);
        asm volatile("global_load_dword %0, %1, off" : "=v"(cpf) : "v"(p) : "memory");
    }
    *(volatile LAS u32*)(lds + L_PROG + w * 4) = 0;
    BAR();
    STAMP(0);
    auto dma_x = [&]() {
        const char* xs = (const char*)a.x + (size_t)((s2 ^ hf) + 512 * rq) * 1024;
#pragma unroll
        for (int i = 0; i < 16; i++) { const int row = (i + 2 * t) & 15; dma_rt(l16 ^ (u32)(row * (32768 + 16)), xs, (u32)(w * 16384 + row * 1024)); }
    };
    dma_x();
    int li, lg, c2p, l3, qb; u32 vpg;
#define RELANE() do { asm volatile("" : "+v"(lane)); l16 = lane * 16; li = lane & 15; lg = lane >> 4; c2p = lane & 3; l3 = (lane >> 2) & 7; qb = lane >> 5; vpg = (u32)(L_PROG + (lane & 3) * 4); } while (0)
    const __amdgpu_buffer_rsrc_t rs = __builtin_amdgcn_make_buffer_rsrc(a.ws, 0, WS_BYTES, 0x00020000);
    KC kc; kc.pm = f2{1.f, -1.f}; kc.rpm = f2{0.70710678118654752f, -0.70710678118654752f};
    asm volatile("" : "+s"(kc.pm), "+s"(kc.rpm));
    const u4 ones = {0x3F803F80u, 0x3F803F80u, 0x3F803F80u, 0x3F803F80u};
    auto post = [&](u32 v) { asm volatile("s_waitcnt lgkmcnt(0)" ::: "memory"); *(volatile LAS u32*)(lds + L_PROG + w * 4) = v; FENCE(); };
    auto wait_grp = [&](int g, u32 v) { lds_wait(vpg + g * 16, v); };
    auto wait_pair = [&](u32 v) { lds_wait((u32)(L_PROG + (w ^ 4) * 4), v); };
    u2 tag;
    auto get_tag = [&]() { u2 tg = *(LAS u2*)(lds + L_TAG); tag.x = __builtin_amdgcn_readfirstlane(tg.x); tag.y = __builtin_amdgcn_readfirstlane(tg.y); };
    auto flag_store = [&](int off) { __builtin_amdgcn_raw_buffer_store_b64(tag, rs, lane ? WS_BYTES : 0, off, 16); };
    auto flag_wait = [&](u32 voff, int soff) {
        _Pragma("unroll 1") for (int spins = 0; spins < SPIN_CAP; spins++) {
            u2 f = __builtin_amdgcn_raw_buffer_load_b64(rs, voff, soff, 16);
            if (__all(f.x == tag.x && f.y == tag.y)) break;
            __builtin_amdgcn_s_sleep(4);
            FENCE();
        }
        __builtin_amdgcn_fence(__ATOMIC_ACQUIRE, "wavefront");
        FENCE();
    };
    auto read_tile = [&](u4 (&x)[16], int tile) {
        LAS char* p = lds + tile * 65536 + (16 * rq + li) * 1024;
#pragma unroll
        for (int k = 0; k < 16; k++) x[k] = *(LAS u4*)(p + (((4 * IX(k) + lg) ^ li) * 16) + ((BE(k) + j4) & 3) * 256);
    };
    auto stats = [&](const u4 (&x)[16]) {
        f4 s1 = {0, 0, 0, 0}, sq = s1, s1b = s1, sqb = s1;
#pragma unroll
        for (int k = 0; k < 16; k += 2) { s1 = mfma(ones, x[k], s1); sq = mfma(x[k], x[k], sq); s1b = mfma(ones, x[k + 1], s1b); sqb = mfma(x[k + 1], x[k + 1], sqb); }
        sq += sqb;
        const int rr = li & 3;
        const float d = rr == 0 ? sq.x : rr == 1 ? sq.y : rr == 2 ? sq.z : sq.w;
        if (lg == (li >> 2)) {
            const float mean = (s1.x + s1b.x) * (1.0f / 512.0f);
            float var = d * (1.0f / 512.0f) - mean * mean;
            if (!(mean * mean <= 1024.0f * var)) {
                const u32* xrow = (const u32*)(a.x + (size_t)((s2 ^ hf) + 32 * (16 * rq + li)) * 512);
                float q = 0.f;
                _Pragma("unroll 1") for (int i = 0; i < 256; i++) { u32 v = xrow[i]; float d0 = bflo(v) - mean, d1 = bfhi(v) - mean; q += d0 * d0; q += d1 * d1; }
                var = q * (1.0f / 512.0f);
            }
            const float den = rbf(__builtin_amdgcn_sqrtf(rbf(rbf(var) + 1e-5f)));
            *(LAS f2*)(lds + L_STAT + (hf * 64 + 16 * rq + li) * 8) = f2{rbf(mean), __builtin_amdgcn_rcpf(den)};
        }
        FENCE();
    };
    const int nblk = (t < 6 - par) ? 3 : 2, fb2 = 32 + par * 6 + t;
    const char* wdp = (const char*)a.ws + WS_WDP + (size_t)(t * NFB) * 4096;

    f2 tw64[8], twT[8];
    int x3a[4];
    auto fft13_consts = [&](float scale) {
        int lane_ = lane; asm volatile("" : "+v"(lane_));
        const int c2p = lane_ & 3, l3 = (lane_ >> 2) & 7, qb = lane_ >> 5;
        const int wq = rq, q1 = qb ? (wq == 0 ? 4 : 8 - wq) : wq, c2 = 4 * t + c2p;
        const float f64 = (float)l3 * (1.0f / 64.0f);
#pragma unroll
        for (int k = 1; k < 8; k++) tw64[k] = cis(f64 * (float)k);
        const float fT = (float)(8 * c2 * q1 + s2 * l3) * (1.0f / 2048.0f), dT = (float)s2 * (1.0f / 256.0f);
#pragma unroll
        for (int k = 0; k < 8; k++) twT[k] = cis(fT + dT * (float)k) * scale;
#pragma unroll
        for (int k = 0; k < 4; k++) {
            const int ua = (k >> 1) * 4 + (k & 1), u1 = l3 + 8 * ua;
            const int cls = (u1 < 32) ? u1 : ((64 - u1) & 31), hi = u1 >> 5;
            int mbit, sub;
            if (wq == 0) { mbit = qb; sub = hi; } else if (cls != 0) { sub = hi; mbit = qb ^ hi; } else { mbit = hi; sub = qb ^ hi; }
            x3a[k] = L_FFT + wq * 4608 + (cls * 18 + mbit * 8 + sub * 4 + c2p) * 8 - (k == 3 ? 2304 : 0);
        }
        asm volatile("" : "+v"(tw64[1]), "+v"(tw64[2]), "+v"(tw64[3]), "+v"(tw64[4]), "+v"(tw64[5]), "+v"(tw64[6]), "+v"(tw64[7]),
                     "+v"(twT[0]), "+v"(twT[1]), "+v"(twT[2]), "+v"(twT[3]), "+v"(twT[4]), "+v"(twT[5]), "+v"(twT[6]), "+v"(twT[7]),
                     "+v"(x3a[0]), "+v"(x3a[1]), "+v"(x3a[2]), "+v"(x3a[3]));
    };

    u4 xa[16], xb[16];
    const int hoff0 = WS_H + ((s2 * 4 + rq) * NFB) * 1024, hoff1 = WS_H + (((s2 ^ 1) * 4 + rq) * NFB) * 1024;
    RELANE();
    const char* wgs = (const char*)a.wg + (size_t)(32 * rq) * 2752 + cg * 128;
    const char* wus = (const char*)a.wu + (size_t)(32 * rq) * 2752 + cg * 128;
    const u32 wvE = (u32)((lane >> 3) * 2752 + (((lane & 7) ^ (((lane >> 4) & 1) << 1)) * 16)), wvO = (u32)((lane >> 3) * 2752 + (((lane & 7) ^ (((lane >> 4) & 1) << 1) ^ 4) * 16));
    auto dma_w1 = [&](auto KK, auto I) {
        constexpr int kk = KK, g = I >> 1, mat = I & 1;
        dma_add<kk * PG + mat * 4096 + g * 1024, g * 8 * 2752>((g & 1) ? wvO : wvE, (mat ? wus : wgs) + ((BE(kk) + j4) & 3) * (128 * 2752), (u32)(rq * 2 * PG));
    };
    const int rot = (2 * t + 4 * j4) & 15;
    auto dma_wd = [&](auto LI, auto I, int p0) { dma4k_c<LI + I * 16384, 0>(l16, wdp + (size_t)(p0 + 2 * ((rq + 4 * I + rot) & 15)) * 4096, (u32)(rq * 4096)); };

    if (hf == 0) {
        __builtin_amdgcn_s_setprio(2);
        const int f1st = WS_F1 + ((lane >> 3) * 8 + (((lane >> 2) & 1) ? (rq == 0 ? 4 : 8 - rq) : rq)) * 16384 + pid * 64 + c2p * 16;
        u2 ev;
        { u32 zero = 0; asm volatile("global_load_dwordx2 %0, %1, %2 sc1" : "=v"(ev) : "v"(zero), "s"(a.ws) : "memory"); }
        fft13_consts(1.0f);
        WAITVM(1);
        post(A_XL);
        STAMP(1);
        u32 raw[8];
        const int rw = lane >> 2;
        read_tile(xa, 0);
#pragma unroll
        for (int c1 = 0; c1 < 8; c1++) raw[c1] = *(LAS u32*)(lds + (16 * rq + rw) * 1024 + (((8 * c1 + t) ^ rw) * 16) + c2p * 4);
        wait_pair(B_XL);
        read_tile(xb, 1);
        post(A_XC);
        stats(xa);
        STAMP(2);
        f2 v[8];
        {
            const f2 st = *(LAS f2*)(lds + L_STAT + (16 * rq + rw) * 8);
#pragma unroll
            for (int c1 = 0; c1 < 8; c1++) v[c1] = bfup(pkbf(bfup(pkbf(bfup(raw[c1]) - st.xx)) * st.yy));
            dft8<false>(v, kc);
            LAS f2* xb1 = (LAS f2*)(lds + L_FFT) + w * 64 + lane;
#pragma unroll
            for (int q1 = 0; q1 < 8; q1++) { const int reg = (q1 < 4) ? q1 * 2 : (((8 - q1) & 3) * 2 + 1); xb1[reg * 288] = v[q1]; }
        }
        asm volatile("s_waitcnt vmcnt(0)" : "+v"(ev) :: "memory");
        {
            const u64 epoch = ((u64)(u32)__builtin_amdgcn_readfirstlane(ev.y) << 32) | (u32)__builtin_amdgcn_readfirstlane(ev.x);
            u64 tg = (epoch + 1) * 0x9E3779B97F4A7C15ull; tg ^= tg >> 29; tg *= 0xBF58476D1CE4E5B9ull; tg ^= tg >> 32;
            tag = u2{(u32)tg, (u32)(tg >> 32)};
            *(LAS u4*)(lds + L_TAG) = u4{(u32)tg, (u32)(tg >> 32), (u32)(epoch + 1), (u32)((epoch + 1) >> 32)};
        }
        post(A_P1);
#pragma unroll
        for (int c1 = 0; c1 < 8; c1++) *(LAS u32*)(lds + ((rq >> 1) ? L_X1 : L_X0) + ((rq & 1) * 16 + rw) * 128 + ((c1 ^ (rw & 7)) * 16) + c2p * 4) = raw[c1];
        STAMP(19);
        STAMP(22);
        wait_grp(0, A_P1);
        STAMP(23);
        {
            LAS f2* reg = (LAS f2*)(lds + L_FFT) + (w * 2 + qb) * 288;
#pragma unroll
            for (int k = 0; k < 8; k++) v[k] = reg[(lane & 31) + 32 * k];
            dft8<false>(v, kc);
#pragma unroll
            for (int k = 1; k < 8; k++) v[k] = cmul<true>(v[k], tw64[k]);
#pragma unroll
            for (int k = 0; k < 8; k++) reg[36 * k + (lane & 31)] = v[k];
            FENCE();
#pragma unroll
            for (int k = 0; k < 8; k++) v[k] = reg[36 * l3 + 4 * k + c2p];
            dft8<false>(v, kc);
#pragma unroll
            for (int k = 0; k < 8; k++) v[k] = cmul<true>(v[k], twT[k]);
            FENCE();
            *(LAS f2*)(lds + x3a[0]) = v[0];
            *(LAS f2*)(lds + x3a[1]) = v[1]; *(LAS f2*)(lds + x3a[1] + 1152) = v[2]; *(LAS f2*)(lds + x3a[1] + 2304) = v[3];
            *(LAS f2*)(lds + x3a[2]) = v[4];
            *(LAS f2*)(lds + x3a[3] + 2304) = v[5]; *(LAS f2*)(lds + x3a[3] + 1152) = v[6]; *(LAS f2*)(lds + x3a[3]) = v[7];
            FENCE();
            STAMP(24);
#pragma unroll
            for (int i = 0; i < 4; i++) {
                u4 o = *(LAS u4*)(lds + L_FFT + w * 4608 + (i * 8 + (lane >> 3)) * 144 + (lane & 7) * 16);
                st128(o, rs, NOF(f1st), i * (64 * 16384));
            }
        }
        STAMP(3);
        __builtin_amdgcn_s_setprio(0);
        u32 pk[8], fr[16];
        {
            const u32 vo = NOPL((u32)(((8 * sig + 2 * rq) * 257 + lane) * 2));
            sfor<8>([&](auto I) { constexpr int rr = I >> 2, k = I & 3; ldg_u16<rr * 514 + k * 128>(fr[I], vo, a.wr); ldg_u16<rr * 514 + k * 128>(fr[8 + I], vo, a.wi); });
        }
        const int un = sig < 4 * NFB ? sig : 0, fb = un >> 2, jj = (un >> 1) & 1, tcol = (un & 1) * 4 + rq;
        {
            const u32 vd = NOPL((u32)(((fb * 32 + 4 * lg) * 512 + 64 * (4 * jj + (li >> 2)) + 8 * tcol + 2 * (li & 3)) * 2));
            sfor<4>([&](auto I) {
                ldg_u32<I * 1024>(pk[I], vd, a.wd);
                ldg_u32<I * 1024>(pk[4 + I], vd, (const char*)a.wd + 16384);
            });
        }
        WAITVM(24);
        flag_store(WS_FF1 + ((rq * 4 + ((s2 & 7) >> 1)) * 64 + (s2 >> 3) * 16 + (s2 & 1) * 8 + t) * 8);
        STAMP(27);
        asm volatile("s_waitcnt vmcnt(1)" : "+v"(pk[0]), "+v"(pk[1]), "+v"(pk[2]), "+v"(pk[3]), "+v"(pk[4]), "+v"(pk[5]), "+v"(pk[6]), "+v"(pk[7]),
                     "+v"(fr[0]), "+v"(fr[1]), "+v"(fr[2]), "+v"(fr[3]), "+v"(fr[4]), "+v"(fr[5]), "+v"(fr[6]), "+v"(fr[7]),
                     "+v"(fr[8]), "+v"(fr[9]), "+v"(fr[10]), "+v"(fr[11]), "+v"(fr[12]), "+v"(fr[13]), "+v"(fr[14]), "+v"(fr[15]) :: "memory");
        STAMP(25);
        {
            const int so = sig < 4 * NFB ? WS_WDP + ((tcol * NFB + fb) * 4 + 2 * jj) * 1024 : WS_BYTES;
            u4 oe = {__builtin_amdgcn_perm(pk[1], pk[0], 0x05040100), __builtin_amdgcn_perm(pk[3], pk[2], 0x05040100), __builtin_amdgcn_perm(pk[5], pk[4], 0x05040100), __builtin_amdgcn_perm(pk[7], pk[6], 0x05040100)};
            u4 oo = {__builtin_amdgcn_perm(pk[1], pk[0], 0x07060302), __builtin_amdgcn_perm(pk[3], pk[2], 0x07060302), __builtin_amdgcn_perm(pk[5], pk[4], 0x07060302), __builtin_amdgcn_perm(pk[7], pk[6], 0x07060302)};
            st128(oe, rs, l16, so);
            st128(oo, rs, l16, so + 1024);
        }
        STAMP(26);
        {
            const int q1 = lane >> 3, vt = lane & 7, srcl = (q1 + 8 * vt) * 4;
#pragma unroll
            for (int rr = 0; rr < 2; rr++) {
                const int u = 8 * sig + 2 * rq + rr, u1 = u & 63, u2_ = u >> 6;
                int cls, m, sub;
                if ((u1 & 31) != 0) { sub = u1 >> 5; cls = sub ? 64 - u1 : u1; m = sub ? ((8 - q1) & 7) : q1; }
                else { cls = 0; const int hi = u1 >> 5; if ((q1 & 3) == 0) { m = q1; sub = hi; } else { sub = (q1 >> 2) ^ hi; m = sub ? 8 - q1 : q1; } }
                u4 o;
                o.x = __builtin_amdgcn_ds_bpermute(srcl, fr[rr * 4 + 0] | (fr[8 + rr * 4 + 0] << 16)); o.y = __builtin_amdgcn_ds_bpermute(srcl, fr[rr * 4 + 1] | (fr[8 + rr * 4 + 1] << 16));
                o.z = __builtin_amdgcn_ds_bpermute(srcl, fr[rr * 4 + 2] | (fr[8 + rr * 4 + 2] << 16)); o.w = __builtin_amdgcn_ds_bpermute(srcl, fr[rr * 4 + 3] | (fr[8 + rr * 4 + 3] << 16));
                st128(o, rs, NOF(WS_WP + (cls * 8 + m) * 8192 + sub * 4096 + u2_ * 128 + vt * 16), 0);
            }
        }
        WAITVM(0);
        flag_store(sig < 4 * NFB ? WS_FP + (((sig & 1) * 4 + rq) * 128 + (sig >> 1)) * 8 : WS_BYTES);
        flag_store(WS_FW + (((sig & 7) * 4 + rq) * 32 + (sig >> 3)) * 8);
        STAMP(4);
    } else {
        u32 pf[3];
        {
            const u32 vo = (u32)((lane >> 1) * 2752 + (lane & 1) * 124);
            ldg_u32<0>(pf[0], vo, wgs + (j4 & 3) * (128 * 2752));
            ldg_u32<0>(pf[1], vo, wus + (j4 & 3) * (128 * 2752));
            const u32 vb = (u32)((lane & 31) * 2752 + (nblk == 3 ? fb2 : 0) * 64);
            asm volatile("global_load_dword %0, %1, off" : "=v"(pf[2]) : "v"((const char*)(qb ? a.wu : a.wg) + (size_t)((j4 * 4 + rq) * 32) * 2752 + vb) : "memory");
        }
        asm volatile("s_waitcnt vmcnt(3)" : "+v"(cpf) :: "memory");
        post(B_XL);
        read_tile(xb, 1);
        wait_pair(A_XL);
        read_tile(xa, 0);
        STAMP(1);
        wait_pair(A_XC);
        sfor<8>([&](auto I) { dma_w1(IC<0>{}, I); });
        SCHED();
        stats(xb);
        SCHED();
        sfor<8>([&](auto I) { dma_w1(IC<1>{}, I); });
        sfor<8>([&](auto I) { dma_w1(IC<8>{}, I); });
        __builtin_amdgcn_s_setprio(1);
        asm volatile("s_waitcnt vmcnt(16)" : "+v"(pf[0]), "+v"(pf[1]), "+v"(pf[2]) :: "memory");
        post(B_W0);
        STAMP(2);
    }
    f2 st0, st1;
    {
        const u32 fz = (u32)((li >> 3) | ((lg & 1) << 1));
        const u32 tb = (u32)((8 * lg + (li >> 2)) * 128 + 8 * (li & 3));
        u32 p0 = tb + (((2 * hf) ^ fz) << 5), p1 = tb + (((2 * hf + 1) ^ fz) << 5), pc = tb + (((2 * hf + (rq & 1)) ^ fz) << 5) + (rq >> 1) * 4096;
        const u32 wvB = (u32)((lane >> 2) * 2752 + (((lane & 3) ^ (((lane >> 5) & 1) << 1)) * 16));
        const char* wbs = (const char*)((rq >> 1) ? a.wu : a.wg) + (size_t)((rq & 1) * 16) * 2752 + fb2 * 64;
        f4 accA[4], accB[4], acs = {0, 0, 0, 0};
#pragma unroll
        for (int j = 0; j < 4; j++) { accA[j] = f4{0, 0, 0, 0}; accB[j] = f4{0, 0, 0, 0}; }
        u4 F[2][5];
        auto ldf = [&](int ks, u4 (&f)[5]) {
            const int off = (ks & 7) * PG;
            f[0] = trfrag2<512>(lds + p0 + off); f[1] = trfrag2<512>(lds + p1 + off); f[2] = trfrag2<512>(lds + p0 + off + 4096); f[3] = trfrag2<512>(lds + p1 + off + 4096); f[4] = trfrag2<512>(lds + pc + off);
        };
        wait_grp(1, B_W0);
        STAMP(14);
        ldf(0, F[0]);
        sfor<16>([&](auto S) {
            constexpr int s = S, ks = (s & 8) | ((s & 3) << 1) | ((s >> 2) & 1), sn = s + 1, kn = (sn & 8) | ((sn & 3) << 1) | ((sn >> 2) & 1);
            if ((s >= 1 && s <= 4) || s == 7 || s >= 9) pin9(accA, accB, acs);
            if (s == 3) {
                if (hf) { WAITVM(16); post(B_W1); }
                wait_grp(1, B_W1);
            }
            if constexpr (s < 2) if (hf) {
                STAMP(20 + 2 * s);
                sfor<4>([&](auto I) { dma_w1(IC<9>{}, IC<s * 4 + I>{}); });
                STAMP(21 + 2 * s);
            }
            if (s == 4) STAMP(15);
            if (s == 7) {
                if (hf) { WAITVM(8); post(B_W2); }
                wait_grp(1, B_W2); STAMP(16);
                p0 += 65536; p1 += 65536; pc += 65536;
                asm volatile("" : "+v"(p0), "+v"(p1), "+v"(pc));
            }
            if (s == 8) post(hf ? B_S0 : A_S0);
            if (s == 11) {
                if (hf) { WAITVM(0); post(B_W3); }
                wait_grp(1, B_W3); STAMP(17);
            }
            if constexpr (s >= 9) if (hf == 0 && nblk == 3) {
                if (s == 9) { wait_grp(0, A_S0); wait_grp(1, B_S0); sfor<2>([&](auto I) { dma_add<I * 4096, IX(I) * 32 * 2752>(wvB, wbs + ((BE(I) + j4) & 3) * (128 * 2752), (u32)(rq * 1024)); }); }
                sfor<2>([&](auto I) { constexpr int k = (s - 8) * 2 + I; dma_add<k * 4096, IX(k) * 32 * 2752>(wvB, wbs + ((BE(k) + j4) & 3) * (128 * 2752), (u32)(rq * 1024)); });
            }
            SCHED();
            if (s + 1 < 16) ldf(kn, F[(s + 1) & 1]);
            u4 (&f)[5] = F[s & 1];
#pragma unroll
            for (int j = 0; j < 4; j++) { accA[j] = mfma(f[j], xa[ks], accA[j]); accB[j] = mfma(f[j], xb[ks], accB[j]); }
            acs = mfma(f[4], ones, acs);
            if (s + 1 < 16) {
                SGB(0x100, 2);
#pragma unroll
                for (int i = 0; i < 8; i++) { SGB(0x8, 1); SGB(0x100, 1); }
                SGB(0x8, 1);
            }
            SCHED();
        });
        RELANE();
        if (li == 0) *(LAS f4*)(lds + L_CS + (hf * 4 + rq) * 64 + lg * 16) = acs;
        post(hf ? B_CS : A_CS);
        STAMP(18);
        wait_grp(hf, hf ? B_CS : A_CS);
        if (hf) wait_pair(A_P1);
        get_tag();
        LAS char* cs = lds + L_CS + hf * 256 + lg * 16;
        st0 = *(LAS f2*)(lds + L_STAT + (16 * rq + li) * 8);
        st1 = *(LAS f2*)(lds + L_STAT + (64 + 16 * rq + li) * 8);
        st128(geglu8(accA, cs, st0), rs, l16, hoff0 + (2 * cg + hf) * 1024);
        st128(geglu8(accB, cs, st1), rs, l16, hoff1 + (2 * cg + hf) * 1024);
    }
    STAMP(5);
    if (hf == 0) {
        WAITVM(2); post(A_WB);
        if (nblk == 3) {
            wait_grp(0, A_WB);
            const u32 tb = (u32)((8 * lg + (li >> 2)) * 64 + (lg & 1) * 32 + 8 * (li & 3));
            LAS char* p0 = lds + tb; LAS char* p1 = lds + (tb ^ 32);
            LAS char* pc = lds + (tb ^ ((rq & 1) * 32)) + (rq >> 1) * 2048;
            f4 acc[2][4], acs = {0, 0, 0, 0};
#pragma unroll
            for (int j = 0; j < 4; j++) { acc[0][j] = f4{0, 0, 0, 0}; acc[1][j] = f4{0, 0, 0, 0}; }
            u4 F[2][5];
            auto ldf = [&](int ks, u4 (&f)[5]) {
                const int off = ks * 4096;
                f[4] = trfrag2<256>(pc + off);
                f[0] = trfrag2<256>(p0 + off); f[1] = trfrag2<256>(p1 + off); f[2] = trfrag2<256>(p0 + off + 2048); f[3] = trfrag2<256>(p1 + off + 2048);
            };
            ldf(0, F[0]);
            sfor<16>([&](auto S) {
                constexpr int ks = S;
                if (ks == 4) { WAITVM(0); flag_store(WS_FHA + ((rg * 8 + w) * 16 + cg) * 8); wait_grp(1, B_FP); }
                if constexpr (ks >= 4 && ks % 3 == 1) dma_wd(IC<L_SET1>{}, IC<(ks - 4) / 3>{}, 0);
                SCHED();
                if (ks + 1 < 16) ldf(ks + 1, F[(ks + 1) & 1]);
                u4 (&f)[5] = F[ks & 1];
                acs = mfma(f[4], ones, acs);
#pragma unroll
                for (int j = 0; j < 4; j++) { acc[0][j] = mfma(f[j], xa[ks], acc[0][j]); acc[1][j] = mfma(f[j], xb[ks], acc[1][j]); }
                if (ks + 1 < 16) {
                    SGB(0x100, 2);
#pragma unroll
                    for (int i = 0; i < 8; i++) { SGB(0x8, 1); SGB(0x100, 1); }
                    SGB(0x8, 1);
                }
                SCHED();
            });
            if (li == 0) *(LAS f4*)(lds + L_CS + 512 + rq * 64 + lg * 16) = acs;
            post(A_PB);
            wait_grp(0, A_PB);
            st128(geglu8(acc[0], lds + L_CS + 512 + lg * 16, st0), rs, l16, hoff0 + fb2 * 1024);
            st128(geglu8(acc[1], lds + L_CS + 512 + lg * 16, st1), rs, l16, hoff1 + fb2 * 1024);
        } else {
            WAITVM(0); flag_store(WS_FHA + ((rg * 8 + w) * 16 + cg) * 8);
            wait_grp(1, B_FP);
            sfor<4>([&](auto I) { dma_wd(IC<L_SET1>{}, I, 0); });
        }
        STAMP(6);
        sfor<4>([&](auto I) { dma_wd(IC<0>{}, I, 1); });
        if (nblk == 3) { WAITVM(16); flag_store(WS_FHB + ((rg * 4 + rq) * 16 + fb2 - 32) * 8); }
    } else {
        f2 v[8], tw0[4], tw1[8], al[8], be[8];
        const int rb = qb, r3 = 2 * rq + rb;
        const int cls2 = sig >> 3, m2 = sig & 7;
        const int wq2 = (m2 < 4) ? m2 : ((8 - m2) & 3), pos1 = wq2 * 64 + cls2 * 2 + (m2 >> 2);
        const bool special = (cls2 == 0) && ((m2 & 3) == 0);
        int u1a, q1a, u1b, q1b;
        if (cls2 != 0) { u1a = cls2; q1a = m2; u1b = 64 - cls2; q1b = (8 - m2) & 7; }
        else if (special) { u1a = 0; u1b = 32; q1a = q1b = m2; }
        else { u1a = u1b = (m2 < 4) ? 0 : 32; q1a = m2; q1b = 8 - m2; }
        int sA, vH0, sB, vH1;
        if (!special) { sA = 0; vH0 = rq; sB = 1; vH1 = (u1a == 0) ? ((4 - rq) & 3) : 3 - rq; }
        else { sA = sB = rq >> 1; vH0 = rq & 1; vH1 = (rq == 0) ? 2 : (rq == 1) ? 3 : (rq == 2) ? 3 : 2; }
        const bool q0 = (m2 == 0);
        const int dy = ((sA ? u1b : u1a) == 0 && vH0 == 0) ? 8 : 7, dq = q0 ? 8 : 7;
        int y1 = lane >> 3, t1 = lane & 7, y2 = (dy - y1) & 7, t2 = (dq - t1) & 7, hA = vH0, hB = vH1;
        bool self = false;
        if (special && rq == 0) {
            const int k = lane & 31;
            if (qb) { hA = hB = 2; y1 = k >> 3; t1 = k & 7; y2 = 7 - y1; t2 = (dq - t1) & 7; }
            else {
                hA = hB = 0; const int vx = k & 3, y = k >> 2;
                if (!q0 || vx) { y1 = y; t1 = vx; y2 = (8 - y) & 7; t2 = dq - vx; }
                else { t1 = t2 = (y >= 4) ? 4 : 0; const int yy = y & 3; if (yy) { y1 = yy; y2 = 8 - yy; } else { y1 = 0; y2 = 4; self = true; } }
            }
        }
        const int uA = (sA ? u1b : u1a) + 64 * (hA + 4 * y1), uB = (sB ? u1b : u1a) + 64 * (hB + 4 * y2);
        const int qA = (sA ? q1b : q1a) + 8 * t1, qB = (sB ? q1b : q1a) + 8 * t2;
        const int rowB = L_FFT + ((qb ? (vH1 * 2 + sB) : (vH0 * 2 + sA)) * 36 + (lane & 31)) * 8;
        const bool rot = q0 && (t1 == 0);
        const int p3a1 = L_FFT + (y1 * 290 + (hA * 2 + sA) * 36 + t1 * 4) * 8;
        const int p3a2 = L_FFT + (y2 * 290 + (hB * 2 + sB) * 36 + t2 * 4) * 8;
        const float thA = (float)qA * (1.0f / 512.0f), thB = (float)qB * (1.0f / 512.0f);
#pragma unroll
        for (int k = 1; k < 4; k++) tw0[k] = cis((float)r3 * ((float)k / 32.0f));
#pragma unroll
        for (int k = 1; k < 8; k++) tw1[k] = cis((float)c2p * ((float)k / 32.0f));
        f2 csq[8];
#pragma unroll
        for (int e = 0; e < 8; e++) csq[e] = cis(((e >> 2) ? thB : thA) + 0.125f * (float)(e & 3));
        asm volatile("" : "+v"(csq[0]), "+v"(csq[1]), "+v"(csq[2]), "+v"(csq[3]), "+v"(csq[4]), "+v"(csq[5]), "+v"(csq[6]), "+v"(csq[7]),
                     "+v"(tw0[1]), "+v"(tw0[2]), "+v"(tw0[3]), "+v"(tw1[1]), "+v"(tw1[2]), "+v"(tw1[3]), "+v"(tw1[4]), "+v"(tw1[5]), "+v"(tw1[6]), "+v"(tw1[7]));
        {
            const u32 o1 = min(rq * 22 + lane, 85) * 8, o2 = (((qb ? u1b : u1a) >> 1) * 32 + (lane & 31)) * 8, o3 = lane * 8;
            bool posted = false;
            _Pragma("unroll 1") for (int spins = 0; spins < SPIN_CAP; spins++) {
                u2 f1 = __builtin_amdgcn_raw_buffer_load_b64(rs, o1, WS_FP + t * 1024, 16);
                u2 f2_ = __builtin_amdgcn_raw_buffer_load_b64(rs, o2, WS_FW, 16);
                u2 f3 = __builtin_amdgcn_raw_buffer_load_b64(rs, o3, WS_FF1 + (wq2 * 4 + rq) * 512, 16);
                const bool ok = f1.x == tag.x && f1.y == tag.y && f2_.x == tag.x && f2_.y == tag.y && f3.x == tag.x && f3.y == tag.y;
                const bool all = __all(ok);
                if (!posted) { WAITVM(0); flag_store(WS_FHA + ((rg * 8 + w) * 16 + cg) * 8); posted = true; }
                if (all) break;
                __builtin_amdgcn_s_sleep(2);
                FENCE();
            }
            __builtin_amdgcn_fence(__ATOMIC_ACQUIRE, "wavefront");
            FENCE();
        }
        post(B_FP);
        STAMP(3);
        const int f1off = WS_F1 + sig * 16384 + 2 * rq * 512;
        u4 d[4];
        const u4 wa4 = __builtin_amdgcn_raw_buffer_load_b128(rs, (hA + 4 * y1) * 128 + t1 * 16, WS_WP + sig * 8192 + sA * 4096, 16);
        const u4 wb4 = __builtin_amdgcn_raw_buffer_load_b128(rs, (hB + 4 * y2) * 128 + t2 * 16, WS_WP + sig * 8192 + sB * 4096, 16);
        SCHED();
#pragma unroll
        for (int H = 0; H < 4; H++) d[H] = __builtin_amdgcn_raw_buffer_load_b128(rs, NOF(l16), f1off + H * 4096, 16);
        SCHED();
        f2 wS[2] = {f2{0.f, 0.f}, f2{0.f, 0.f}};
        if (q0) {
            wS[0] = f2{bflo(a.wr[uA * 257 + 256]), bflo(a.wi[uA * 257 + 256])};
            wS[1] = f2{bflo(a.wr[uB * 257 + 256]), bflo(a.wi[uB * 257 + 256])};
        }
        {
            f2 W[8], WB[8];
#pragma unroll
            for (int e = 0; e < 4; e++) { W[e] = bfup(wa4[e]); W[4 + e] = bfup(wb4[e]); }
#pragma unroll
            for (int k = 0; k < 4; k++) { WB[k] = W[4 + 3 - k]; WB[4 + k] = W[3 - k]; }
            if (q0) {
#pragma unroll
                for (int k = 0; k < 4; k++) {
                    f2 n1 = rot ? W[(4 - k) & 3] : W[3 - k], n2 = rot ? W[4 + ((4 - k) & 3)] : W[4 + 3 - k];
                    WB[k] = self ? n1 : n2; WB[4 + k] = self ? n2 : n1;
                }
#pragma unroll
                for (int e = 0; e < 8; e += 4) {
                    f2 own = wS[e >> 2], oth = self ? own : wS[1 - (e >> 2)];
                    f2 S0 = pkfma(WB[e], kc.pm, W[e]) * 0.5f, S1c = pkfma(own, kc.pm, oth) * 0.5f;
                    WB[e] = rot ? S1c : WB[e];
                    al[e] = rot ? S0 : W[e];
                }
                W[0] = al[0]; W[4] = al[4];
            }
#pragma unroll
            for (int e = 0; e < 8; e++) {
                const f2 cs = csq[e];
                f2 P = pkfma(WB[e], kc.pm, W[e]), M = pkfma(WB[e], kc.pm.yx, W[e]);
                asm("v_pk_fma_f32 %0, %1, %2, %3 op_sel:[1,0,0] op_sel_hi:[1,1,1] neg_lo:[1,0,0] neg_hi:[1,0,0]" : "=v"(al[e]) : "v"(cs), "v"(M), "v"(P));
                asm("v_pk_mul_f32 %0, %1, %2 op_sel:[1,0] op_sel_hi:[0,0] neg_lo:[1,0]" : "=v"(be[e]) : "v"(M), "v"(cs));
            }
            asm volatile("" : "+v"(al[0]), "+v"(al[1]), "+v"(al[2]), "+v"(al[3]), "+v"(al[4]), "+v"(al[5]), "+v"(al[6]), "+v"(al[7]),
                         "+v"(be[0]), "+v"(be[1]), "+v"(be[2]), "+v"(be[3]), "+v"(be[4]), "+v"(be[5]), "+v"(be[6]), "+v"(be[7]),
                         "+v"(tw0[1]), "+v"(tw0[2]), "+v"(tw0[3]), "+v"(tw1[1]), "+v"(tw1[2]), "+v"(tw1[3]), "+v"(tw1[4]), "+v"(tw1[5]), "+v"(tw1[6]), "+v"(tw1[7]));
        }
        STAMP(4);
        wait_grp(0, A_S0);
        __builtin_amdgcn_s_setprio(2);
        f2 e0[4], e1[4];
#pragma unroll
        for (int H = 0; H < 4; H++) { e0[H] = f2{__uint_as_float(d[H].x), __uint_as_float(d[H].y)}; e1[H] = f2{__uint_as_float(d[H].z), __uint_as_float(d[H].w)}; }
        dft4<false>(e0[0], e0[1], e0[2], e0[3], kc); dft4<false>(e1[0], e1[1], e1[2], e1[3], kc);
        STAMP(6);
        LAS char* area = lds + L_FFT + rq * 4640;
#pragma unroll
        for (int k = 0; k < 4; k++) {
            if (k) { e0[k] = cmul<true>(e0[k], tw0[k]); e1[k] = cmul<true>(e1[k], tw0[k]); }
            *(LAS f4*)(area + (l3 * 72 + 2 * c2p + 8 * k + 32 * rb) * 8) = f4{e0[k].x, e0[k].y, e1[k].x, e1[k].y};
        }
        FENCE();
#pragma unroll
        for (int k = 0; k < 8; k++) v[k] = *(LAS f2*)(area + (k * 72 + lane) * 8);
        dft8<false>(v, kc);
#pragma unroll
        for (int k = 1; k < 8; k++) v[k] = cmul<true>(v[k], tw1[k]);
        FENCE();
        LAS char* pb = lds + L_FFT + (r3 * 290 + l3 * 36 + c2p) * 8;
#pragma unroll
        for (int k = 0; k < 8; k++) *(LAS f2*)(pb + k * 32) = v[k];
        post(B_E1);
        wait_grp(1, B_E1);
#pragma unroll
        for (int k = 0; k < 8; k++) v[k] = *(LAS f2*)(lds + rowB + k * 2320);
        dft8<false>(v, kc);
#pragma unroll
        for (int k = 0; k < 8; k++) *(LAS f2*)(lds + rowB + k * 2320) = v[k];
        FENCE();
        f4 a0 = *(LAS f4*)(lds + p3a1), a1 = *(LAS f4*)(lds + p3a1 + 16), b0 = *(LAS f4*)(lds + p3a2), b1 = *(LAS f4*)(lds + p3a2 + 16);
        f2 X[8] = {a0.xy, a0.zw, a1.xy, a1.zw, b0.xy, b0.zw, b1.xy, b1.zw};
        dft4<false>(X[0], X[1], X[2], X[3], kc); dft4<false>(X[4], X[5], X[6], X[7], kc);
        f2 Y[8];
#pragma unroll
        for (int k = 0; k < 4; k++) { Y[k] = X[4 + 3 - k]; Y[4 + k] = X[3 - k]; }
        if (q0) {
#pragma unroll
            for (int k = 0; k < 4; k++) {
                f2 n1 = rot ? X[(4 - k) & 3] : X[3 - k], n2 = rot ? X[4 + ((4 - k) & 3)] : X[4 + 3 - k];
                Y[k] = self ? n1 : n2; Y[4 + k] = self ? n2 : n1;
            }
        }
        f2 O[8];
#pragma unroll
        for (int e = 0; e < 8; e++) O[e] = cfmac_conj(cmul<false>(X[e], al[e]), be[e], Y[e]);
        dft4<true>(O[0], O[1], O[2], O[3], kc); dft4<true>(O[4], O[5], O[6], O[7], kc);
        *(LAS f4*)(lds + p3a1) = f4{O[0].x, O[0].y, O[1].x, O[1].y}; *(LAS f4*)(lds + p3a1 + 16) = f4{O[2].x, O[2].y, O[3].x, O[3].y};
        *(LAS f4*)(lds + p3a2) = f4{O[4].x, O[4].y, O[5].x, O[5].y}; *(LAS f4*)(lds + p3a2 + 16) = f4{O[6].x, O[6].y, O[7].x, O[7].y};
        FENCE();
#pragma unroll
        for (int k = 0; k < 8; k++) v[k] = *(LAS f2*)(lds + rowB + k * 2320);
        dft8<true>(v, kc);
#pragma unroll
        for (int k = 0; k < 8; k++) *(LAS f2*)(lds + rowB + k * 2320) = v[k];
        post(B_E2);
        wait_grp(1, B_E2);
#pragma unroll
        for (int k = 0; k < 8; k++) v[k] = *(LAS f2*)(pb + k * 32);
#pragma unroll
        for (int k = 1; k < 8; k++) v[k] = cmul<false>(v[k], tw1[k]);
        dft8<true>(v, kc);
        FENCE();
#pragma unroll
        for (int k = 0; k < 8; k++) *(LAS f2*)(area + (k * 72 + lane) * 8) = v[k];
        FENCE();
#pragma unroll
        for (int k = 0; k < 4; k++) {
            f4 q = *(LAS f4*)(area + (l3 * 72 + 2 * c2p + 8 * k + 32 * rb) * 8);
            e0[k] = q.xy; e1[k] = q.zw;
            if (k) { e0[k] = cmul<false>(e0[k], tw0[k]); e1[k] = cmul<false>(e1[k], tw0[k]); }
        }
        dft4<true>(e0[0], e0[1], e0[2], e0[3], kc); dft4<true>(e1[0], e1[1], e1[2], e1[3], kc);
        const int f2st = WS_F2 + (r3 * 8 + l3) * 16384 + pos1 * 64 + c2p * 16;
#pragma unroll
        for (int H = 0; H < 4; H++) {
            u4 o = {__float_as_uint(e0[H].x), __float_as_uint(e0[H].y), __float_as_uint(e1[H].x), __float_as_uint(e1[H].y)};
            st128(o, rs, NOF(f2st), H * (64 * 16384));
        }
        STAMP(7);
        __builtin_amdgcn_s_setprio(0);
        WAITVM(0);
        flag_store(WS_FF2 + (rq * 256 + pos1) * 8);
    }
    STAMP(8);
    RELANE();
    f4 acc2[4];
#pragma unroll
    for (int j = 0; j < 4; j++) acc2[j] = f4{0, 0, 0, 0};
    const int hsrc = hoff0 + hf * 1024;
    auto g2_mma = [&](auto NC, const u4* hfr, int ldsbase) {
        constexpr int N = decltype(NC)::value;
        u32 pa = (u32)ldsbase + l16; asm volatile("" : "+v"(pa));
        LAS u4* p = (LAS u4*)(lds + pa);
        u4 F[2][4];
#pragma unroll
        for (int j = 0; j < 4; j++) F[0][j] = p[j * 64];
        sfor<N>([&](auto I) {
            constexpr int i = I;
            SCHED();
            if (i + 1 < N) {
#pragma unroll
                for (int j = 0; j < 4; j++) F[(i + 1) & 1][j] = p[(i + 1) * 256 + j * 64];
            }
#pragma unroll
            for (int j = 0; j < 4; j++) acc2[j] = mfma(F[i & 1][j], hfr[i], acc2[j]);
            if (i + 1 < N) {
#pragma unroll
                for (int j = 0; j < 4; j++) { SGB(0x100, 1); SGB(0x8, 1); }
            }
            SCHED();
        });
    };
    u4 hfr[16];
    flag_wait(li * 8, WS_FHA + (rg * 8 + w) * 128);
    if (hf == 0) post(A_D1);
#pragma unroll
    for (int i = 0; i < 16; i++) { hfr[i] = __builtin_amdgcn_raw_buffer_load_b128(rs, l16, hsrc + 2 * ((i + rot) & 15) * 1024, 16); SCHED(); }
    STAMP(9);
    wait_grp(0, A_D1);
    g2_mma(IC<16>{}, hfr, hf ? 0 : L_SET1);
    post(hf ? B_G1 : A_G1);
    pin4(acc2);
    STAMP(10);
    if (hf == 0) {
        wait_grp(0, A_G1);
        sfor<3>([&](auto I) { constexpr int i = I; if (i < 2 || rq < 3) dma4k_c<L_SET1 + i * 16384, (32 + 4 * i) * 4096>(l16, wdp + rq * 4096, (u32)(rq * 4096)); });
        flag_wait(min(li, 10) * 8, WS_FHB + (rg * 4 + rq) * 128);
        post(A_D3);
#pragma unroll
        for (int i = 0; i < 11; i++) { hfr[i] = __builtin_amdgcn_raw_buffer_load_b128(rs, l16, hoff0 + (32 + i) * 1024, 16); SCHED(); }
        STAMP(13);
        wait_grp(0, A_D3);
        g2_mma(IC<11>{}, hfr, L_SET1);
    } else {
        wait_grp(1, B_G1);
        fft13_consts(0.5f / 524288.0f);
        flag_wait(lane * 8, WS_FF2 + (((s2 & 7) >> 1) * 256 + rq * 64) * 8);
        u4 d[4]; f2 v[8];
        const int off = WS_F2 + pid * 16384 + rq * 4096;
#pragma unroll
        for (int i = 0; i < 4; i++) d[i] = __builtin_amdgcn_raw_buffer_load_b128(rs, NOF(l16), off + i * 1024, 16);
        STAMP(13);
        __builtin_amdgcn_s_setprio(2);
#pragma unroll
        for (int i = 0; i < 4; i++) *(LAS u4*)(lds + L_FFT + rq * 4608 + (i * 8 + (lane >> 3)) * 144 + (lane & 7) * 16) = d[i];
        FENCE();
        v[0] = *(LAS f2*)(lds + x3a[0]);
        v[1] = *(LAS f2*)(lds + x3a[1]); v[2] = *(LAS f2*)(lds + x3a[1] + 1152); v[3] = *(LAS f2*)(lds + x3a[1] + 2304);
        v[4] = *(LAS f2*)(lds + x3a[2]);
        v[5] = *(LAS f2*)(lds + x3a[3] + 2304); v[6] = *(LAS f2*)(lds + x3a[3] + 1152); v[7] = *(LAS f2*)(lds + x3a[3]);
#pragma unroll
        for (int k = 0; k < 8; k++) v[k] = cmul<false>(v[k], twT[k]);
        dft8<true>(v, kc);
#pragma unroll
        for (int k = 1; k < 8; k++) v[k] = cmul<false>(v[k], tw64[k]);
        FENCE();
        LAS f2* reg = (LAS f2*)(lds + L_FFT) + (rq * 2 + qb) * 288;
#pragma unroll
        for (int k = 0; k < 8; k++) reg[36 * l3 + 4 * k + c2p] = v[k];
        FENCE();
#pragma unroll
        for (int k = 0; k < 8; k++) v[k] = reg[36 * k + (lane & 31)];
        dft8<true>(v, kc);
        FENCE();
#pragma unroll
        for (int k = 0; k < 8; k++) reg[(lane & 31) + 32 * k] = v[k];
        post(B_F3);
        wait_grp(1, B_F3);
        if (b == 0 && rq == 0) __builtin_amdgcn_raw_buffer_store_b64(*(LAS u2*)(lds + L_TAG + 8), rs, lane ? WS_BYTES : 0, 0, 16);
        LAS f2* xb1 = (LAS f2*)(lds + L_FFT) + rq * 64 + lane;
#pragma unroll
        for (int q1 = 0; q1 < 8; q1++) { const int rg_ = (q1 < 4) ? q1 * 2 : (((8 - q1) & 3) * 2 + 1); v[q1] = xb1[rg_ * 288]; }
        dft8<true>(v, kc);
#pragma unroll
        for (int c1 = 0; c1 < 8; c1++) *(LAS u32*)(lds + L_S + (16 * rq + (lane >> 2)) * 144 + c1 * 16 + c2p * 4) = pkbf(v[c1]);
    }
    STAMP(11);
    BAR();
    {
        LAS f4* xs = (LAS f4*)(lds + w * 2048) + lane;
        LAS f4* xr = (LAS f4*)(lds + (w ^ 4) * 2048) + lane;
        f4 me, mo;
        if (hf) { xs[0] = acc2[0]; xs[64] = acc2[1]; me = acc2[2]; mo = acc2[3]; } else { xs[0] = acc2[2]; xs[64] = acc2[3]; me = acc2[0]; mo = acc2[1]; }
        BAR();
        const int c1 = 4 * hf + lg;
        me += xr[0]; mo += xr[64];
        const u4 sv = *(LAS u4*)(lds + L_S + (16 * rq + li) * 144 + c1 * 16);
        const u4 xv = *(LAS u4*)(lds + ((rq >> 1) ? L_X1 : L_X0) + ((rq & 1) * 16 + li) * 128 + ((c1 ^ (li & 7)) * 16));
        u32 res[4];
#pragma unroll
        for (int e = 0; e < 4; e++) {
            const f2 ov = bfup(pkbf(f2{me[e], mo[e]}));
            const f2 z = ov * -1.44269504f;
            const f2 d = f2{__builtin_amdgcn_exp2f(z.x), __builtin_amdgcn_exp2f(z.y)} + f2{1.f, 1.f};
            const f2 g = bfup(pkbf(ov * f2{__builtin_amdgcn_rcpf(d.x), __builtin_amdgcn_rcpf(d.y)}));
            res[e] = pkbf(bfup(xv[e]) + bfup(pkbf(bfup(sv[e]) * g)));
        }
        *(u4*)(a.out + (size_t)(s2 + 32 * (16 * rq + li)) * 512 + 64 * c1 + 8 * t) = u4{res[0], res[1], res[2], res[3]};
    }
    STAMP(12);
}

extern "C" void kernel_launch(void* const* d_in, const int* in_sizes, int n_in,
                              void* d_out, int out_size, void* d_ws, size_t ws_size,
                              hipStream_t stream) {
    Args a;
    a.x = (const u16*)d_in[0]; a.wr = (const u16*)d_in[1]; a.wi = (const u16*)d_in[2];
    a.wg = (const u16*)d_in[4]; a.wu = (const u16*)d_in[5]; a.wd = (const u16*)d_in[6];
    a.out = (u16*)d_out; a.ws = (unsigned char*)d_ws;
    (void)hipFuncSetAttribute((const void*)fused, hipFuncAttributeMaxDynamicSharedMemorySize, L_END);
    fused<<<256, 512, L_END, stream>>>(a);
}
```

```cpp
#include <hip/hip_runtime.h>
#include <stdint.h>
#include <type_traits>

typedef unsigned short u16;
typedef unsigned u32;
typedef unsigned long long u64;
typedef float f2 __attribute__((ext_vector_type(2)));
typedef float f4 __attribute__((ext_vector_type(4)));
typedef u32 u4 __attribute__((ext_vector_type(4)));
typedef u32 u2 __attribute__((ext_vector_type(2)));
typedef short s4 __attribute__((ext_vector_type(4)));
typedef __bf16 bf8 __attribute__((ext_vector_type(8)));
typedef __bf16 bf2 __attribute__((ext_vector_type(2)));
#define LAS __attribute__((address_space(3)))
#define GAS __attribute__((address_space(1)))
#define DEV __device__ __forceinline__
template <int N> using IC = std::integral_constant<int, N>;

constexpr int NFB = 43;
constexpr int WS_FF1 = 4096;
constexpr int WS_FF2 = WS_FF1 + 8192;
constexpr int WS_FHA = WS_FF2 + 8192;
constexpr int WS_FHB = WS_FHA + 16384;
constexpr int WS_FP = WS_FHB + 16384;
constexpr int WS_FW = WS_FP + 8192;
constexpr int WS_F1 = 1 << 20, WS_F2 = WS_F1 + (4 << 20), WS_H = WS_F2 + (4 << 20);
constexpr int WS_WDP = WS_H + 32 * 4 * NFB * 1024;
constexpr int WS_WP = WS_WDP + 8 * NFB * 4 * 1024;
constexpr int WS_STAMP = WS_WP + (2 << 20);
constexpr int WS_BYTES = WS_STAMP + (1 << 20);
constexpr int PG = 8192;
constexpr int L_SET1 = 8 * PG;
constexpr int L_FFT = 16 * PG;
constexpr int L_SMALL = 19 * PG;
constexpr int L_PROG = L_SMALL;
constexpr int L_TAG = L_SMALL + 32;
constexpr int L_STAT = L_SMALL + 64;
constexpr int L_CS = L_SMALL + 2048;
constexpr int L_X0 = L_FFT + 18688, L_X1 = L_SMALL + 3072;
constexpr int L_S = 4 * PG;
constexpr int L_END = 20 * PG;
constexpr u32 SENT = 0xFFFFFFFFu;
constexpr int CODE_BYTES = 36 * 1024;
constexpr int SPIN_CAP = 1 << 16;

struct Args { const u16* x; const u16* wr; const u16* wi; const u16* wg; const u16* wu; const u16* wd; u16* out; unsigned char* ws; };

#define BAR() asm volatile("s_waitcnt lgkmcnt(0)\n\ts_barrier" ::: "memory")
#define WAITVM(n) asm volatile("s_waitcnt vmcnt(" #n ")" ::: "memory")
#define NOF(x) (x)
#define NOPL(x) (x)
#define FENCE() asm volatile("" ::: "memory")
#define SCHED() __builtin_amdgcn_sched_barrier(0)
#define SGB(mask, n) __builtin_amdgcn_sched_group_barrier(mask, n, 0)
#define STAMP(i)
#define CSTAMP(i)

DEV f2 pkfma(f2 a, f2 b, f2 c) { return __builtin_elementwise_fma(a, b, c); }
struct KC { f2 pm, rpm; };
DEV f2 addmi(f2 a, f2 b, const KC& k) { return pkfma(b.yx, k.pm, a); }
DEV f2 addpi(f2 a, f2 b, const KC& k) { return pkfma(b.yx, k.pm.yx, a); }
template <bool INV> DEV void dft4(f2& a0, f2& a1, f2& a2, f2& a3, const KC& k) {
    f2 t0 = a0 + a2, t1 = a0 - a2, t2 = a1 + a3, t3 = a1 - a3;
    a0 = t0 + t2; a2 = t0 - t2;
    if (INV) { a1 = addpi(t1, t3, k); a3 = addmi(t1, t3, k); } else { a1 = addmi(t1, t3, k); a3 = addpi(t1, t3, k); }
}
template <bool INV> DEV void dft8(f2 (&v)[8], const KC& k) {
    f2 a0 = v[0] + v[4], a4 = v[0] - v[4], a1 = v[1] + v[5], a5 = v[1] - v[5];
    f2 a2 = v[2] + v[6], a6 = v[2] - v[6], a3 = v[3] + v[7], a7 = v[3] - v[7];
    dft4<INV>(a0, a1, a2, a3, k);
    v[0] = a0; v[2] = a1; v[4] = a2; v[6] = a3;
    f2 b1, b3, t0, t1;
    if (INV) { b1 = addpi(a5, a5, k); b3 = addpi(-a7, a7, k); t0 = addpi(a4, a6, k); t1 = addmi(a4, a6, k); }
    else { b1 = addmi(a5, a5, k); b3 = addmi(-a7, a7, k); t0 = addmi(a4, a6, k); t1 = addpi(a4, a6, k); }
    f2 t2 = b1 + b3, t3 = b1 - b3;
    v[1] = pkfma(t2, k.rpm.xx, t0); v[5] = pkfma(t2, k.rpm.yy, t0);
    if (INV) { v[3] = pkfma(t3.yx, k.rpm.yx, t1); v[7] = pkfma(t3.yx, k.rpm, t1); }
    else { v[3] = pkfma(t3.yx, k.rpm, t1); v[7] = pkfma(t3.yx, k.rpm.yx, t1); }
}
template <bool CONJ> DEV f2 cmul(f2 a, f2 w) {
    f2 r;
    if (CONJ) asm("v_pk_mul_f32 %0, %1, %2 op_sel:[1,1] op_sel_hi:[0,1]\n\tv_pk_fma_f32 %0, %1, %2, %0 op_sel_hi:[1,0,1] neg_hi:[0,0,1]" : "=&v"(r) : "v"(a), "v"(w));
    else asm("v_pk_mul_f32 %0, %1, %2 op_sel:[1,1] op_sel_hi:[0,1]\n\tv_pk_fma_f32 %0, %1, %2, %0 op_sel_hi:[1,0,1] neg_lo:[0,0,1]" : "=&v"(r) : "v"(a), "v"(w));
    return r;
}
DEV f2 cfmac_conj(f2 t, f2 beta, f2 B) {
    asm("v_pk_fma_f32 %0, %2, %1, %0 op_sel_hi:[0,1,1]\n\tv_pk_fma_f32 %0, %2, %1, %0 op_sel:[1,1,0] op_sel_hi:[1,0,1] neg_hi:[0,1,0]" : "+v"(t) : "v"(beta), "v"(B));
    return t;
}
DEV f2 cis(float r) { return f2{__builtin_amdgcn_cosf(r), __builtin_amdgcn_sinf(r)}; }
DEV float bflo(u32 v) { return __uint_as_float(v << 16); }
DEV float bfhi(u32 v) { return __uint_as_float(v & 0xFFFF0000u); }
DEV f2 bfup(u32 v) { return f2{bflo(v), bfhi(v)}; }
DEV u32 pkbf(f2 v) { bf2 r; r.x = (__bf16)v.x; r.y = (__bf16)v.y; u32 o = __builtin_bit_cast(u32, r); asm("" : "+v"(o)); return o; }
DEV float rbf(float f) { return __uint_as_float(((u32)__builtin_bit_cast(u16, (__bf16)f)) << 16); }
DEV f4 mfma(u4 a, u4 b, f4 c) { return __builtin_amdgcn_mfma_f32_16x16x32_bf16(__builtin_bit_cast(bf8, a), __builtin_bit_cast(bf8, b), c, 0, 0, 0); }
DEV u4 sanitize(u4 v) { return u4{min(v.x, 0xFFFFFFFEu), min(v.y, 0xFFFFFFFEu), min(v.z, 0xFFFFFFFEu), min(v.w, 0xFFFFFFFEu)}; }
DEV u32 max4(u4 v) { return max(max(v.x, v.y), max(v.z, v.w)); }
DEV void st128(u4 v, __amdgpu_buffer_rsrc_t rs, u32 voff, int soff) {
    __builtin_amdgcn_raw_buffer_store_b128(v, rs, voff, soff, 16);
    asm volatile("s_nop 1" :: "v"(v) : "memory");
}
template <int OFF2> DEV u4 trfrag2(LAS char* p) {
    s4 r0 = __builtin_amdgcn_ds_read_tr16_b64_v4i16((LAS s4*)p);
    s4 r1 = __builtin_amdgcn_ds_read_tr16_b64_v4i16((LAS s4*)(p + OFF2));
    u2 x0 = __builtin_bit_cast(u2, r0), x1 = __builtin_bit_cast(u2, r1);
    return u4{x0.x, x0.y, x1.x, x1.y};
}
template <int LI, int GI> DEV void dma_add(u32 vb, const void* sbase, u32 sl) {
    const u32 va = vb + (u32)GI;
    asm volatile("s_add_u32 m0, %2, %3\n\ts_nop 0\n\tglobal_load_lds_dwordx4 %0, %1" :: "v"(va), "s"(sbase), "s"(sl), "i"(LI) : "memory", "scc");
}
template <int LI, int GI> DEV void dma4k_c(u32 vb, const void* sbase, u32 sl) {
    const u32 va = vb + (u32)GI;
    asm volatile("s_add_u32 m0, %2, %3\n\ts_nop 0\n\tglobal_load_lds_dwordx4 %0, %1 sc1\n\tglobal_load_lds_dwordx4 %0, %1 offset:1024 sc1\n\t"
                 "global_load_lds_dwordx4 %0, %1 offset:2048 sc1\n\tglobal_load_lds_dwordx4 %0, %1 offset:3072 sc1"
                 :: "v"(va), "s"(sbase), "s"(sl), "i"(LI) : "memory", "scc");
}
template <int OFF> DEV void ldg_u16(u32& d, u32 vo, const void* sb) { asm volatile("global_load_ushort %0, %1, %2 offset:%3" : "=v"(d) : "v"(vo), "s"(sb), "i"(OFF) : "memory"); }
template <int OFF> DEV void ldg_u32(u32& d, u32 vo, const void* sb) { asm volatile("global_load_dword %0, %1, %2 offset:%3" : "=v"(d) : "v"(vo), "s"(sb), "i"(OFF) : "memory"); }
DEV void lds_wait(u32 addr, u32 v) {
    u32 tmp, cnt;
    asm volatile("s_mov_b32 %1, 0x80000\n"
                 "1:\n\tds_read_b32 %0, %2\n\ts_sub_u32 %1, %1, 1\n\ts_waitcnt lgkmcnt(0)\n\ts_cbranch_scc1 2f\n\tv_cmp_gt_u32 vcc, %3, %0\n\ts_cbranch_vccnz 1b\n2:"
                 : "=&v"(tmp), "=&s"(cnt) : "v"(addr), "s"(v) : "memory", "vcc", "scc");
}
template <int N, typename F> DEV void sfor(F&& f) { if constexpr (N > 0) { sfor<N - 1>(f); f(IC<N - 1>{}); } }

DEV void pin9(f4 (&x)[4], f4 (&y)[4], f4& z) {
    asm volatile("" : "+v"(x[0]), "+v"(x[1]), "+v"(x[2]), "+v"(x[3]), "+v"(y[0]), "+v"(y[1]), "+v"(y[2]), "+v"(y[3]), "+v"(z));
}
DEV void pin4(f4 (&x)[4]) { asm volatile("" : "+v"(x[0]), "+v"(x[1]), "+v"(x[2]), "+v"(x[3])); }
DEV void dma_rt(u32 voff, const void* sbase, u32 sl) {
    asm volatile("s_mov_b32 m0, %2\n\ts_nop 0\n\tglobal_load_lds_dwordx4 %0, %1" :: "v"(voff), "s"(sbase), "s"(sl) : "memory");
}
DEV u4 geglu8(const f4 (&acc)[4], LAS char* cs, f2 st) {
    u32 hw[4];
    const f2 nm = f2{-st.x, -st.x}, ri = f2{st.y, st.y};
#pragma unroll
    for (int c = 0; c < 2; c++) {
        f4 cg_ = *(LAS f4*)(cs + c * 64), cu_ = *(LAS f4*)(cs + (2 + c) * 64);
#pragma unroll
        for (int e = 0; e < 2; e++) {
            f2 g = pkfma(e ? cg_.zw : cg_.xy, nm, e ? acc[c].zw : acc[c].xy) * ri;
            f2 u = pkfma(e ? cu_.zw : cu_.xy, nm, e ? acc[2 + c].zw : acc[2 + c].xy) * ri;
            f2 z = g * pkfma(g * g, f2{-0.10294324f, -0.10294324f}, f2{-2.3022082f, -2.3022082f});
            f2 d = f2{__builtin_amdgcn_exp2f(z.x), __builtin_amdgcn_exp2f(z.y)} + f2{1.f, 1.f};
            f2 r = f2{__builtin_amdgcn_rcpf(d.x), __builtin_amdgcn_rcpf(d.y)};
            hw[2 * c + e] = pkbf(g * r * u);
        }
    }
    return u4{hw[0], hw[1], hw[2], hw[3]};
}

enum { A_XL = 1, A_XC, A_P1, A_XB, A_S0, A_CS, A_WB, A_PB, A_D1, A_G1, A_D3 };
enum { B_XL = 1, B_W0, B_W1, B_W2, B_S0, B_W3, B_CS, B_FP, B_E1, B_E2, B_G1, B_F3 };

__global__ void __launch_bounds__(512, 2) fused(Args a) {
    extern __shared__ __attribute__((aligned(16))) char smem[];
    LAS char* lds = (LAS char*)smem;
    const int tid = threadIdx.x, w = __builtin_amdgcn_readfirstlane(tid >> 6);
    int lane = tid & 63;
    const int b = blockIdx.x, xcd = b & 7, r = b >> 3, t = r & 7, par = xcd & 1, rg = (xcd >> 1) * 4 + (r >> 3);
    const int s2 = 2 * rg + par, cg = par * 8 + t, sig = xcd * 32 + r, pid = s2 * 8 + t;
    const int rq = w & 3, hf = w >> 2, j4 = r >> 3;
#define BE(l) ((((l) >> 3) << 1) | ((l) & 1))
#define IX(l) (((l) >> 1) & 3)
    u32 l16 = lane * 16;
    u32 cpf = 0;
    if (w == 7) {
        const u64 pc = __builtin_amdgcn_s_getpc() & ~(u64)255;
        const u32 off = min((u32)((t * 64 + lane) * 128), (u32)(CODE_BYTES - 128));
        asm volatile("global_load_dword %0, %1, %2" : "=v"(cpf) : "v"(off), "s"(pc) : "memory");
    }
    if (w == 6) {
        const int g = lane >> 2, k = lane & 3;
        const char* p; u32 sz;
        if (g < 10) { p = (const char*)a.ws + (size_t)g * (2 << 20); sz = 2 << 20; }
        else if (g == 10) { p = (const char*)a.wr; sz = 2048 * 514; } else if (g == 11) { p = (const char*)a.wi; sz = 2048 * 514; }
        else if (g == 12) { p = (const char*)a.wd; sz = 1376 * 1024; } else if (g == 13) { p = (const char*)a.out; sz = 2 << 20; }
        else if (g == 14) { p = (const char*)a.wg; sz = 512 * 2752; } else { p = (const char*)a.wu; sz = 512 * 2752; }
        p += min((u32)(k * (700 << 10)), sz - 64);
        asm volatile("global_load_dword %0, %1, off" : "=v"(cpf) : "v"(p) : "memory");
    }
    *(volatile LAS u32*)(lds + L_PROG + w * 4) = 0;
    BAR();
    STAMP(0);
    auto dma_x = [&]() {
        const char* xs = (const char*)a.x + (size_t)((s2 ^ hf) + 512 * rq) * 1024;
#pragma unroll
        for (int i = 0; i < 16; i++) { const int row = (i + 2 * t) & 15; dma_rt(l16 ^ (u32)(row * (32768 + 16)), xs, (u32)(L_SET1 + rq * 16384 + row * 1024)); }
    };
    if (hf == 0) dma_x();
    int li, lg, c2p, l3, qb; u32 vpg;
#define RELANE() do { asm volatile("" : "+v"(lane)); l16 = lane * 16; li = lane & 15; lg = lane >> 4; c2p = lane & 3; l3 = (lane >> 2) & 7; qb = lane >> 5; vpg = (u32)(L_PROG + (lane & 3) * 4); } while (0)
    const __amdgpu_buffer_rsrc_t rs = __builtin_amdgcn_make_buffer_rsrc(a.ws, 0, WS_BYTES, 0x00020000);
    KC kc; kc.pm = f2{1.f, -1.f}; kc.rpm = f2{0.70710678118654752f, -0.70710678118654752f};
    asm volatile("" : "+s"(kc.pm), "+s"(kc.rpm));
    const u4 ones = {0x3F803F80u, 0x3F803F80u, 0x3F803F80u, 0x3F803F80u};
    auto post = [&](u32 v) { asm volatile("s_waitcnt lgkmcnt(0)" ::: "memory"); *(volatile LAS u32*)(lds + L_PROG + w * 4) = v; FENCE(); };
    auto wait_grp = [&](int g, u32 v) { lds_wait(vpg + g * 16, v); };
    auto wait_pair = [&](u32 v) { lds_wait((u32)(L_PROG + (w ^ 4) * 4), v); };
    u2 tag;
    auto get_tag = [&]() { u2 tg = *(LAS u2*)(lds + L_TAG); tag.x = __builtin_amdgcn_readfirstlane(tg.x); tag.y = __builtin_amdgcn_readfirstlane(tg.y); };
    auto flag_store = [&](int off) { __builtin_amdgcn_raw_buffer_store_b64(tag, rs, lane ? WS_BYTES : 0, off, 16); };
    auto flag_wait = [&](u32 voff, int soff) {
        _Pragma("unroll 1") for (int spins = 0; spins < SPIN_CAP; spins++) {
            u2 f = __builtin_amdgcn_raw_buffer_load_b64(rs, voff, soff, 16);
            if (__all(f.x == tag.x && f.y == tag.y)) break;
            __builtin_amdgcn_s_sleep(4);
            FENCE();
        }
        __builtin_amdgcn_fence(__ATOMIC_ACQUIRE, "wavefront");
        FENCE();
    };
    auto read_tile = [&](u4 (&x)[16], int tile) {
        LAS char* p = lds + L_SET1 + (16 * rq + li) * 1024;
#pragma unroll
        for (int k = 0; k < 16; k++) x[k] = *(LAS u4*)(p + (((4 * IX(k) + lg) ^ li) * 16) + ((BE(k) + j4) & 3) * 256);
    };
    auto stats = [&](const u4 (&x)[16]) {
        f4 s1 = {0, 0, 0, 0}, sq = s1, s1b = s1, sqb = s1;
#pragma unroll
        for (int k = 0; k < 16; k += 2) { s1 = mfma(ones, x[k], s1); sq = mfma(x[k], x[k], sq); s1b = mfma(ones, x[k + 1], s1b); sqb = mfma(x[k + 1], x[k + 1], sqb); }
        sq += sqb;
        const int rr = li & 3;
        const float d = rr == 0 ? sq.x : rr == 1 ? sq.y : rr == 2 ? sq.z : sq.w;
        if (lg == (li >> 2)) {
            const float mean = (s1.x + s1b.x) * (1.0f / 512.0f);
            float var = d * (1.0f / 512.0f) - mean * mean;
            if (!(mean * mean <= 1024.0f * var)) {
                const u32* xrow = (const u32*)(a.x + (size_t)((s2 ^ hf) + 32 * (16 * rq + li)) * 512);
                float q = 0.f;
                _Pragma("unroll 1") for (int i = 0; i < 256; i++) { u32 v = xrow[i]; float d0 = bflo(v) - mean, d1 = bfhi(v) - mean; q += d0 * d0; q += d1 * d1; }
                var = q * (1.0f / 512.0f);
            }
            const float den = rbf(__builtin_amdgcn_sqrtf(rbf(rbf(var) + 1e-5f)));
            *(LAS f2*)(lds + L_STAT + (hf * 64 + 16 * rq + li) * 8) = f2{rbf(mean), __builtin_amdgcn_rcpf(den)};
        }
        FENCE();
    };
    const int nblk = (t < 6 - par) ? 3 : 2, fb2 = 32 + par * 6 + t;
    const char* wdp = (const char*)a.ws + WS_WDP + (size_t)(t * NFB) * 4096;

    f2 tw64[8], twT[8];
    int x3a[4];
    auto fft13_consts = [&](float scale) {
        int lane_ = lane; asm volatile("" : "+v"(lane_));
        const int c2p = lane_ & 3, l3 = (lane_ >> 2) & 7, qb = lane_ >> 5;
        const int wq = rq, q1 = qb ? (wq == 0 ? 4 : 8 - wq) : wq, c2 = 4 * t + c2p;
        const float f64 = (float)l3 * (1.0f / 64.0f);
#pragma unroll
        for (int k = 1; k < 8; k++) tw64[k] = cis(f64 * (float)k);
        const float fT = (float)(8 * c2 * q1 + s2 * l3) * (1.0f / 2048.0f), dT = (float)s2 * (1.0f / 256.0f);
#pragma unroll
        for (int k = 0; k < 8; k++) twT[k] = cis(fT + dT * (float)k) * scale;
#pragma unroll
        for (int k = 0; k < 4; k++) {
            const int ua = (k >> 1) * 4 + (k & 1), u1 = l3 + 8 * ua;
            const int cls = (u1 < 32) ? u1 : ((64 - u1) & 31), hi = u1 >> 5;
            int mbit, sub;
            if (wq == 0) { mbit = qb; sub = hi; } else if (cls != 0) { sub = hi; mbit = qb ^ hi; } else { mbit = hi; sub = qb ^ hi; }
            x3a[k] = L_FFT + wq * 4608 + (cls * 18 + mbit * 8 + sub * 4 + c2p) * 8 - (k == 3 ? 2304 : 0);
        }
        asm volatile("" : "+v"(tw64[1]), "+v"(tw64[2]), "+v"(tw64[3]), "+v"(tw64[4]), "+v"(tw64[5]), "+v"(tw64[6]), "+v"(tw64[7]),
                     "+v"(twT[0]), "+v"(twT[1]), "+v"(twT[2]), "+v"(twT[3]), "+v"(twT[4]), "+v"(twT[5]), "+v"(twT[6]), "+v"(twT[7]),
                     "+v"(x3a[0]), "+v"(x3a[1]), "+v"(x3a[2]), "+v"(x3a[3]));
    };

    u4 xa[16], xb[16];
    const int hoff0 = WS_H + ((s2 * 4 + rq) * NFB) * 1024, hoff1 = WS_H + (((s2 ^ 1) * 4 + rq) * NFB) * 1024;
    RELANE();
    const char* wgs = (const char*)a.wg + (size_t)(32 * rq) * 2752 + cg * 128;
    const char* wus = (const char*)a.wu + (size_t)(32 * rq) * 2752 + cg * 128;
    const u32 wvE = (u32)((lane >> 3) * 2752 + (((lane & 7) ^ (((lane >> 4) & 1) << 1)) * 16)), wvO = (u32)((lane >> 3) * 2752 + (((lane & 7) ^ (((lane >> 4) & 1) << 1) ^ 4) * 16));
    auto dma_w1 = [&](auto KK, auto I) {
        constexpr int kk = KK, g = I >> 1, mat = I & 1;
        dma_add<kk * PG + mat * 4096 + g * 1024, g * 8 * 2752>((g & 1) ? wvO : wvE, (mat ? wus : wgs) + ((BE(kk) + j4) & 3) * (128 * 2752), (u32)(rq * 2 * PG));
    };
    const int rot = (2 * t + 4 * j4) & 15;
    auto dma_wd = [&](auto LI, auto I, int p0) { dma4k_c<LI + I * 16384, 0>(l16, wdp + (size_t)(p0 + 2 * ((rq + 4 * I + rot) & 15)) * 4096, (u32)(rq * 4096)); };

    if (hf == 0) {
        __builtin_amdgcn_s_setprio(2);
        const int f1st = WS_F1 + ((lane >> 3) * 8 + (((lane >> 2) & 1) ? (rq == 0 ? 4 : 8 - rq) : rq)) * 16384 + pid * 64 + c2p * 16;
        u2 ev;
        { u32 zero = 0; asm volatile("global_load_dwordx2 %0, %1, %2 sc1" : "=v"(ev) : "v"(zero), "s"(a.ws) : "memory"); }
        fft13_consts(1.0f);
        WAITVM(1);
        post(A_XL);
        STAMP(1);
        u32 raw[8];
        const int rw = lane >> 2;
        read_tile(xa, 0);
#pragma unroll
        for (int c1 = 0; c1 < 8; c1++) raw[c1] = *(LAS u32*)(lds + L_SET1 + (16 * rq + rw) * 1024 + (((8 * c1 + t) ^ rw) * 16) + c2p * 4);
        post(A_XC);
        stats(xa);
        STAMP(2);
        f2 v[8];
        {
            const f2 st = *(LAS f2*)(lds + L_STAT + (16 * rq + rw) * 8);
#pragma unroll
            for (int c1 = 0; c1 < 8; c1++) v[c1] = bfup(pkbf(bfup(pkbf(bfup(raw[c1]) - st.xx)) * st.yy));
            dft8<false>(v, kc);
            LAS f2* xb1 = (LAS f2*)(lds + L_FFT) + w * 64 + lane;
#pragma unroll
            for (int q1 = 0; q1 < 8; q1++) { const int reg = (q1 < 4) ? q1 * 2 : (((8 - q1) & 3) * 2 + 1); xb1[reg * 288] = v[q1]; }
        }
        asm volatile("s_waitcnt vmcnt(0)" : "+v"(ev) :: "memory");
        {
            const u64 epoch = ((u64)(u32)__builtin_amdgcn_readfirstlane(ev.y) << 32) | (u32)__builtin_amdgcn_readfirstlane(ev.x);
            u64 tg = (epoch + 1) * 0x9E3779B97F4A7C15ull; tg ^= tg >> 29; tg *= 0xBF58476D1CE4E5B9ull; tg ^= tg >> 32;
            tag = u2{(u32)tg, (u32)(tg >> 32)};
            *(LAS u4*)(lds + L_TAG) = u4{(u32)tg, (u32)(tg >> 32), (u32)(epoch + 1), (u32)((epoch + 1) >> 32)};
        }
        post(A_P1);
#pragma unroll
        for (int c1 = 0; c1 < 8; c1++) *(LAS u32*)(lds + ((rq >> 1) ? L_X1 : L_X0) + ((rq & 1) * 16 + rw) * 128 + ((c1 ^ (rw & 7)) * 16) + c2p * 4) = raw[c1];
        STAMP(19);
        wait_pair(B_XL);
        STAMP(20);
        read_tile(xb, 1);
        post(A_XB);
        STAMP(22);
        wait_grp(0, A_P1);
        STAMP(23);
        {
            LAS f2* reg = (LAS f2*)(lds + L_FFT) + (w * 2 + qb) * 288;
#pragma unroll
            for (int k = 0; k < 8; k++) v[k] = reg[(lane & 31) + 32 * k];
            dft8<false>(v, kc);
#pragma unroll
            for (int k = 1; k < 8; k++) v[k] = cmul<true>(v[k], tw64[k]);
#pragma unroll
            for (int k = 0; k < 8; k++) reg[36 * k + (lane & 31)] = v[k];
            FENCE();
#pragma unroll
            for (int k = 0; k < 8; k++) v[k] = reg[36 * l3 + 4 * k + c2p];
            dft8<false>(v, kc);
#pragma unroll
            for (int k = 0; k < 8; k++) v[k] = cmul<true>(v[k], twT[k]);
            FENCE();
            *(LAS f2*)(lds + x3a[0]) = v[0];
            *(LAS f2*)(lds + x3a[1]) = v[1]; *(LAS f2*)(lds + x3a[1] + 1152) = v[2]; *(LAS f2*)(lds + x3a[1] + 2304) = v[3];
            *(LAS f2*)(lds + x3a[2]) = v[4];
            *(LAS f2*)(lds + x3a[3] + 2304) = v[5]; *(LAS f2*)(lds + x3a[3] + 1152) = v[6]; *(LAS f2*)(lds + x3a[3]) = v[7];
            FENCE();
            STAMP(24);
#pragma unroll
            for (int i = 0; i < 4; i++) {
                u4 o = *(LAS u4*)(lds + L_FFT + w * 4608 + (i * 8 + (lane >> 3)) * 144 + (lane & 7) * 16);
                st128(o, rs, NOF(f1st), i * (64 * 16384));
            }
        }
        STAMP(3);
        __builtin_amdgcn_s_setprio(0);
        STAMP(4);
    } else {
        u32 pk[8];
        const int un = sig < 4 * NFB ? sig : 0, fb = un >> 2, jj = (un >> 1) & 1, tcol = (un & 1) * 4 + rq;
        __builtin_amdgcn_s_sleep(2);
        {
            const u32 vd = (u32)(((fb * 32 + 4 * lg) * 512 + 64 * (4 * jj + (li >> 2)) + 8 * tcol + 2 * (li & 3)) * 2);
            sfor<4>([&](auto I) {
                ldg_u32<I * 1024>(pk[I], vd, a.wd);
                ldg_u32<I * 1024>(pk[4 + I], vd, (const char*)a.wd + 16384);
            });
        }
        sfor<8>([&](auto I) { dma_w1(IC<0>{}, I); });
        wait_pair(A_XL);
        read_tile(xa, 0);
        asm volatile("s_waitcnt lgkmcnt(0)" ::: "memory");
        wait_pair(A_XC);
        dma_x();
        asm volatile("s_waitcnt vmcnt(24)" : "+v"(cpf), "+v"(pk[0]), "+v"(pk[1]), "+v"(pk[2]), "+v"(pk[3]), "+v"(pk[4]), "+v"(pk[5]), "+v"(pk[6]), "+v"(pk[7]) :: "memory");
        sfor<8>([&](auto I) { dma_w1(IC<1>{}, I); });
        {
            const int so = sig < 4 * NFB ? WS_WDP + ((tcol * NFB + fb) * 4 + 2 * jj) * 1024 : WS_BYTES;
            u4 oe = {__builtin_amdgcn_perm(pk[1], pk[0], 0x05040100), __builtin_amdgcn_perm(pk[3], pk[2], 0x05040100), __builtin_amdgcn_perm(pk[5], pk[4], 0x05040100), __builtin_amdgcn_perm(pk[7], pk[6], 0x05040100)};
            u4 oo = {__builtin_amdgcn_perm(pk[1], pk[0], 0x07060302), __builtin_amdgcn_perm(pk[3], pk[2], 0x07060302), __builtin_amdgcn_perm(pk[5], pk[4], 0x07060302), __builtin_amdgcn_perm(pk[7], pk[6], 0x07060302)};
            st128(oe, rs, l16, so);
            st128(oo, rs, l16, so + 1024);
        }
        STAMP(1);
        WAITVM(10);
        post(B_W0);
        read_tile(xb, 1);
        __builtin_amdgcn_s_setprio(1);
        STAMP(2);
    }
    f2 st0, st1;
    {
        const u32 fz = (u32)((li >> 3) | ((lg & 1) << 1));
        const u32 tb = (u32)((8 * lg + (li >> 2)) * 128 + 8 * (li & 3));
        u32 p0 = tb + (((2 * hf) ^ fz) << 5), p1 = tb + (((2 * hf + 1) ^ fz) << 5), pc = tb + (((2 * hf + (rq & 1)) ^ fz) << 5) + (rq >> 1) * 4096;
        const u32 wvB = (u32)((lane >> 2) * 2752 + (((lane & 3) ^ (((lane >> 5) & 1) << 1)) * 16));
        const char* wbs = (const char*)((rq >> 1) ? a.wu : a.wg) + (size_t)((rq & 1) * 16) * 2752 + fb2 * 64;
        f4 accA[4], accB[4], acs = {0, 0, 0, 0};
#pragma unroll
        for (int j = 0; j < 4; j++) { accA[j] = f4{0, 0, 0, 0}; accB[j] = f4{0, 0, 0, 0}; }
        u4 F[2][5];
        auto ldf = [&](int ks, u4 (&f)[5]) {
            const int off = (ks & 7) * PG;
            f[0] = trfrag2<512>(lds + p0 + off); f[1] = trfrag2<512>(lds + p1 + off); f[2] = trfrag2<512>(lds + p0 + off + 4096); f[3] = trfrag2<512>(lds + p1 + off + 4096); f[4] = trfrag2<512>(lds + pc + off);
        };
        wait_grp(1, B_W0);
        STAMP(14);
        ldf(0, F[0]);
        sfor<16>([&](auto S) {
            constexpr int s = S, ks = (s & 8) | ((s & 3) << 1) | ((s >> 2) & 1), sn = s + 1, kn = (sn & 8) | ((sn & 3) << 1) | ((sn >> 2) & 1);
            if ((s >= 1 && s <= 4) || s == 7 || s >= 9) pin9(accA, accB, acs);
            if (s == 3) {
                if (hf) { WAITVM(10); post(B_W1); }
                else { WAITVM(0); flag_store(WS_FF1 + ((rq * 4 + ((s2 & 7) >> 1)) * 64 + (s2 >> 3) * 16 + (s2 & 1) * 8 + t) * 8); }
                wait_grp(1, B_W1);
            }
            if constexpr (s >= 1 && s <= 4) if (hf) {
                if (s == 1) { wait_pair(A_XB); get_tag(); }
                sfor<4>([&](auto I) { dma_w1(IC<8 + ((s - 1) >> 1)>{}, IC<((s - 1) & 1) * 4 + I>{}); });
            }
            if (s == 4) STAMP(15);
            if (s == 7) {
                if (hf) { WAITVM(8); post(B_W2); flag_store(sig < 4 * NFB ? WS_FP + (((sig & 1) * 4 + rq) * 128 + (sig >> 1)) * 8 : WS_BYTES); }
                wait_grp(1, B_W2); STAMP(16);
                p0 += 65536; p1 += 65536; pc += 65536;
                asm volatile("" : "+v"(p0), "+v"(p1), "+v"(pc));
            }
            if (s == 8) post(hf ? B_S0 : A_S0);
            if (s == 11) {
                if (hf) { WAITVM(0); post(B_W3); }
                wait_grp(1, B_W3); STAMP(17);
            }
            if constexpr (s >= 9) if (hf == 0 && nblk == 3) {
                if (s == 9) { wait_grp(0, A_S0); wait_grp(1, B_S0); sfor<2>([&](auto I) { dma_add<I * 4096, IX(I) * 32 * 2752>(wvB, wbs + ((BE(I) + j4) & 3) * (128 * 2752), (u32)(rq * 1024)); }); }
                sfor<2>([&](auto I) { constexpr int k = (s - 8) * 2 + I; dma_add<k * 4096, IX(k) * 32 * 2752>(wvB, wbs + ((BE(k) + j4) & 3) * (128 * 2752), (u32)(rq * 1024)); });
            }
            SCHED();
            if (s + 1 < 16) ldf(kn, F[(s + 1) & 1]);
            u4 (&f)[5] = F[s & 1];
#pragma unroll
            for (int j = 0; j < 4; j++) { accA[j] = mfma(f[j], xa[ks], accA[j]); accB[j] = mfma(f[j], xb[ks], accB[j]); }
            acs = mfma(f[4], ones, acs);
            if (s + 1 < 16) {
                SGB(0x100, 2);
#pragma unroll
                for (int i = 0; i < 8; i++) { SGB(0x8, 1); SGB(0x100, 1); }
                SGB(0x8, 1);
            }
            SCHED();
        });
        RELANE();
        if (hf) stats(xb);
        if (li == 0) *(LAS f4*)(lds + L_CS + (hf * 4 + rq) * 64 + lg * 16) = acs;
        post(hf ? B_CS : A_CS);
        STAMP(18);
        wait_grp(hf, hf ? B_CS : A_CS);
        if (hf == 0) wait_pair(B_CS);
        LAS char* cs = lds + L_CS + hf * 256 + lg * 16;
        st0 = *(LAS f2*)(lds + L_STAT + (16 * rq + li) * 8);
        st1 = *(LAS f2*)(lds + L_STAT + (64 + 16 * rq + li) * 8);
        st128(geglu8(accA, cs, st0), rs, l16, hoff0 + (2 * cg + hf) * 1024);
        st128(geglu8(accB, cs, st1), rs, l16, hoff1 + (2 * cg + hf) * 1024);
    }
    STAMP(5);
    if (hf == 0) {
        WAITVM(2); post(A_WB);
        if (nblk == 3) {
            wait_grp(0, A_WB);
            const u32 tb = (u32)((8 * lg + (li >> 2)) * 64 + (lg & 1) * 32 + 8 * (li & 3));
            LAS char* p0 = lds + tb; LAS char* p1 = lds + (tb ^ 32);
            LAS char* pc = lds + (tb ^ ((rq & 1) * 32)) + (rq >> 1) * 2048;
            f4 acc[2][4], acs = {0, 0, 0, 0};
#pragma unroll
            for (int j = 0; j < 4; j++) { acc[0][j] = f4{0, 0, 0, 0}; acc[1][j] = f4{0, 0, 0, 0}; }
            u4 F[2][5];
            auto ldf = [&](int ks, u4 (&f)[5]) {
                const int off = ks * 4096;
                f[4] = trfrag2<256>(pc + off);
                f[0] = trfrag2<256>(p0 + off); f[1] = trfrag2<256>(p1 + off); f[2] = trfrag2<256>(p0 + off + 2048); f[3] = trfrag2<256>(p1 + off + 2048);
            };
            ldf(0, F[0]);
            sfor<16>([&](auto S) {
                constexpr int ks = S;
                if (ks == 4) { WAITVM(0); flag_store(WS_FHA + ((rg * 8 + w) * 16 + cg) * 8); wait_grp(1, B_FP); }
                if constexpr (ks >= 4 && ks % 3 == 1) dma_wd(IC<L_SET1>{}, IC<(ks - 4) / 3>{}, 0);
                SCHED();
                if (ks + 1 < 16) ldf(ks + 1, F[(ks + 1) & 1]);
                u4 (&f)[5] = F[ks & 1];
                acs = mfma(f[4], ones, acs);
#pragma unroll
                for (int j = 0; j < 4; j++) { acc[0][j] = mfma(f[j], xa[ks], acc[0][j]); acc[1][j] = mfma(f[j], xb[ks], acc[1][j]); }
                if (ks + 1 < 16) {
                    SGB(0x100, 2);
#pragma unroll
                    for (int i = 0; i < 8; i++) { SGB(0x8, 1); SGB(0x100, 1); }
                    SGB(0x8, 1);
                }
                SCHED();
            });
            if (li == 0) *(LAS f4*)(lds + L_CS + 512 + rq * 64 + lg * 16) = acs;
            post(A_PB);
            wait_grp(0, A_PB);
            st128(geglu8(acc[0], lds + L_CS + 512 + lg * 16, st0), rs, l16, hoff0 + fb2 * 1024);
            st128(geglu8(acc[1], lds + L_CS + 512 + lg * 16, st1), rs, l16, hoff1 + fb2 * 1024);
        } else {
            WAITVM(0); flag_store(WS_FHA + ((rg * 8 + w) * 16 + cg) * 8);
            wait_grp(1, B_FP);
            sfor<4>([&](auto I) { dma_wd(IC<L_SET1>{}, I, 0); });
        }
        STAMP(6);
        sfor<4>([&](auto I) { dma_wd(IC<0>{}, I, 1); });
        if (nblk == 3) { WAITVM(16); flag_store(WS_FHB + ((rg * 4 + rq) * 16 + fb2 - 32) * 8); }
    } else {
        f2 v[8], tw0[4], tw1[8], al[8], be[8];
        const int rb = qb, r3 = 2 * rq + rb;
        const int cls2 = sig >> 3, m2 = sig & 7;
        const int wq2 = (m2 < 4) ? m2 : ((8 - m2) & 3), pos1 = wq2 * 64 + cls2 * 2 + (m2 >> 2);
        const bool special = (cls2 == 0) && ((m2 & 3) == 0);
        int u1a, q1a, u1b, q1b;
        if (cls2 != 0) { u1a = cls2; q1a = m2; u1b = 64 - cls2; q1b = (8 - m2) & 7; }
        else if (special) { u1a = 0; u1b = 32; q1a = q1b = m2; }
        else { u1a = u1b = (m2 < 4) ? 0 : 32; q1a = m2; q1b = 8 - m2; }
        int sA, vH0, sB, vH1;
        if (!special) { sA = 0; vH0 = rq; sB = 1; vH1 = (u1a == 0) ? ((4 - rq) & 3) : 3 - rq; }
        else { sA = sB = rq >> 1; vH0 = rq & 1; vH1 = (rq == 0) ? 2 : (rq == 1) ? 3 : (rq == 2) ? 3 : 2; }
        const bool q0 = (m2 == 0);
        const int dy = ((sA ? u1b : u1a) == 0 && vH0 == 0) ? 8 : 7, dq = q0 ? 8 : 7;
        int y1 = lane >> 3, t1 = lane & 7, y2 = (dy - y1) & 7, t2 = (dq - t1) & 7, hA = vH0, hB = vH1;
        bool self = false;
        if (special && rq == 0) {
            const int k = lane & 31;
            if (qb) { hA = hB = 2; y1 = k >> 3; t1 = k & 7; y2 = 7 - y1; t2 = (dq - t1) & 7; }
            else {
                hA = hB = 0; const int vx = k & 3, y = k >> 2;
                if (!q0 || vx) { y1 = y; t1 = vx; y2 = (8 - y) & 7; t2 = dq - vx; }
                else { t1 = t2 = (y >= 4) ? 4 : 0; const int yy = y & 3; if (yy) { y1 = yy; y2 = 8 - yy; } else { y1 = 0; y2 = 4; self = true; } }
            }
        }
        const int uA = (sA ? u1b : u1a) + 64 * (hA + 4 * y1), uB = (sB ? u1b : u1a) + 64 * (hB + 4 * y2);
        const int qA = (sA ? q1b : q1a) + 8 * t1, qB = (sB ? q1b : q1a) + 8 * t2;
        u32 wl[20];
        {
            const u32 vA = (u32)((uA * 257 + qA) * 2), vB = (u32)((uB * 257 + qB) * 2);
            sfor<4>([&](auto K) { ldg_u16<K * 128>(wl[K], vA, a.wr); ldg_u16<K * 128>(wl[8 + K], vA, a.wi); ldg_u16<K * 128>(wl[4 + K], vB, a.wr); ldg_u16<K * 128>(wl[12 + K], vB, a.wi); });
            const u32 vA2 = (u32)(uA * 514 + 512), vB2 = (u32)(uB * 514 + 512);
            ldg_u16<0>(wl[16], vA2, a.wr); ldg_u16<0>(wl[17], vA2, a.wi); ldg_u16<0>(wl[18], vB2, a.wr); ldg_u16<0>(wl[19], vB2, a.wi);
        }
        const int rowB = L_FFT + ((qb ? (vH1 * 2 + sB) : (vH0 * 2 + sA)) * 36 + (lane & 31)) * 8;
        const bool rot = q0 && (t1 == 0);
        const int p3a1 = L_FFT + (y1 * 290 + (hA * 2 + sA) * 36 + t1 * 4) * 8;
        const int p3a2 = L_FFT + (y2 * 290 + (hB * 2 + sB) * 36 + t2 * 4) * 8;
        const float thA = (float)qA * (1.0f / 512.0f), thB = (float)qB * (1.0f / 512.0f);
#pragma unroll
        for (int k = 1; k < 4; k++) tw0[k] = cis((float)r3 * ((float)k / 32.0f));
#pragma unroll
        for (int k = 1; k < 8; k++) tw1[k] = cis((float)c2p * ((float)k / 32.0f));
        f2 csq[8];
#pragma unroll
        for (int e = 0; e < 8; e++) csq[e] = cis(((e >> 2) ? thB : thA) + 0.125f * (float)(e & 3));
        asm volatile("" : "+v"(csq[0]), "+v"(csq[1]), "+v"(csq[2]), "+v"(csq[3]), "+v"(csq[4]), "+v"(csq[5]), "+v"(csq[6]), "+v"(csq[7]),
                     "+v"(tw0[1]), "+v"(tw0[2]), "+v"(tw0[3]), "+v"(tw1[1]), "+v"(tw1[2]), "+v"(tw1[3]), "+v"(tw1[4]), "+v"(tw1[5]), "+v"(tw1[6]), "+v"(tw1[7]));
        {
            const u32 o1 = min(rq * 22 + lane, 85) * 8, o3 = lane * 8;
            bool posted = false;
            _Pragma("unroll 1") for (int spins = 0; spins < SPIN_CAP; spins++) {
                u2 f1 = __builtin_amdgcn_raw_buffer_load_b64(rs, o1, WS_FP + t * 1024, 16);
                u2 f3 = __builtin_amdgcn_raw_buffer_load_b64(rs, o3, WS_FF1 + (wq2 * 4 + rq) * 512, 16);
                const bool ok = f1.x == tag.x && f1.y == tag.y && f3.x == tag.x && f3.y == tag.y;
                const bool all = __all(ok);
                if (!posted) { WAITVM(0); flag_store(WS_FHA + ((rg * 8 + w) * 16 + cg) * 8); posted = true; }
                if (all) break;
                __builtin_amdgcn_s_sleep(2);
                FENCE();
            }
            __builtin_amdgcn_fence(__ATOMIC_ACQUIRE, "wavefront");
            FENCE();
        }
        post(B_FP);
        STAMP(3);
        const int f1off = WS_F1 + sig * 16384 + 2 * rq * 512;
        u4 d[4];
        SCHED();
#pragma unroll
        for (int H = 0; H < 4; H++) d[H] = __builtin_amdgcn_raw_buffer_load_b128(rs, NOF(l16), f1off + H * 4096, 16);
        SCHED();
        asm volatile("" : "+v"(wl[0]), "+v"(wl[1]), "+v"(wl[2]), "+v"(wl[3]), "+v"(wl[4]), "+v"(wl[5]), "+v"(wl[6]), "+v"(wl[7]), "+v"(wl[8]), "+v"(wl[9]),
                     "+v"(wl[10]), "+v"(wl[11]), "+v"(wl[12]), "+v"(wl[13]), "+v"(wl[14]), "+v"(wl[15]), "+v"(wl[16]), "+v"(wl[17]), "+v"(wl[18]), "+v"(wl[19]) :: "memory");
        const f2 wS[2] = {f2{bflo(wl[16]), bflo(wl[17])}, f2{bflo(wl[18]), bflo(wl[19])}};
        {
            f2 W[8], WB[8];
#pragma unroll
            for (int e = 0; e < 8; e++) W[e] = f2{bflo(wl[e]), bflo(wl[8 + e])};
#pragma unroll
            for (int k = 0; k < 4; k++) { WB[k] = W[4 + 3 - k]; WB[4 + k] = W[3 - k]; }
            if (q0) {
#pragma unroll
                for (int k = 0; k < 4; k++) {
                    f2 n1 = rot ? W[(4 - k) & 3] : W[3 - k], n2 = rot ? W[4 + ((4 - k) & 3)] : W[4 + 3 - k];
                    WB[k] = self ? n1 : n2; WB[4 + k] = self ? n2 : n1;
                }
#pragma unroll
                for (int e = 0; e < 8; e += 4) {
                    f2 own = wS[e >> 2], oth = self ? own : wS[1 - (e >> 2)];
                    f2 S0 = pkfma(WB[e], kc.pm, W[e]) * 0.5f, S1c = pkfma(own, kc.pm, oth) * 0.5f;
                    WB[e] = rot ? S1c : WB[e];
                    al[e] = rot ? S0 : W[e];
                }
                W[0] = al[0]; W[4] = al[4];
            }
#pragma unroll
            for (int e = 0; e < 8; e++) {
                const f2 cs = csq[e];
                f2 P = pkfma(WB[e], kc.pm, W[e]), M = pkfma(WB[e], kc.pm.yx, W[e]);
                asm("v_pk_fma_f32 %0, %1, %2, %3 op_sel:[1,0,0] op_sel_hi:[1,1,1] neg_lo:[1,0,0] neg_hi:[1,0,0]" : "=v"(al[e]) : "v"(cs), "v"(M), "v"(P));
                asm("v_pk_mul_f32 %0, %1, %2 op_sel:[1,0] op_sel_hi:[0,0] neg_lo:[1,0]" : "=v"(be[e]) : "v"(M), "v"(cs));
            }
            asm volatile("" : "+v"(al[0]), "+v"(al[1]), "+v"(al[2]), "+v"(al[3]), "+v"(al[4]), "+v"(al[5]), "+v"(al[6]), "+v"(al[7]),
                         "+v"(be[0]), "+v"(be[1]), "+v"(be[2]), "+v"(be[3]), "+v"(be[4]), "+v"(be[5]), "+v"(be[6]), "+v"(be[7]),
                         "+v"(tw0[1]), "+v"(tw0[2]), "+v"(tw0[3]), "+v"(tw1[1]), "+v"(tw1[2]), "+v"(tw1[3]), "+v"(tw1[4]), "+v"(tw1[5]), "+v"(tw1[6]), "+v"(tw1[7]));
        }
        STAMP(4);
        wait_grp(0, A_S0);
        __builtin_amdgcn_s_setprio(2);
        f2 e0[4], e1[4];
#pragma unroll
        for (int H = 0; H < 4; H++) { e0[H] = f2{__uint_as_float(d[H].x), __uint_as_float(d[H].y)}; e1[H] = f2{__uint_as_float(d[H].z), __uint_as_float(d[H].w)}; }
        dft4<false>(e0[0], e0[1], e0[2], e0[3], kc); dft4<false>(e1[0], e1[1], e1[2], e1[3], kc);
        STAMP(6);
        LAS char* area = lds + L_FFT + rq * 4640;
#pragma unroll
        for (int k = 0; k < 4; k++) {
            if (k) { e0[k] = cmul<true>(e0[k], tw0[k]); e1[k] = cmul<true>(e1[k], tw0[k]); }
            *(LAS f4*)(area + (l3 * 72 + 2 * c2p + 8 * k + 32 * rb) * 8) = f4{e0[k].x, e0[k].y, e1[k].x, e1[k].y};
        }
        FENCE();
#pragma unroll
        for (int k = 0; k < 8; k++) v[k] = *(LAS f2*)(area + (k * 72 + lane) * 8);
        dft8<false>(v, kc);
#pragma unroll
        for (int k = 1; k < 8; k++) v[k] = cmul<true>(v[k], tw1[k]);
        FENCE();
        LAS char* pb = lds + L_FFT + (r3 * 290 + l3 * 36 + c2p) * 8;
#pragma unroll
        for (int k = 0; k < 8; k++) *(LAS f2*)(pb + k * 32) = v[k];
        post(B_E1);
        wait_grp(1, B_E1);
#pragma unroll
        for (int k = 0; k < 8; k++) v[k] = *(LAS f2*)(lds + rowB + k * 2320);
        dft8<false>(v, kc);
#pragma unroll
        for (int k = 0; k < 8; k++) *(LAS f2*)(lds + rowB + k * 2320) = v[k];
        FENCE();
        f4 a0 = *(LAS f4*)(lds + p3a1), a1 = *(LAS f4*)(lds + p3a1 + 16), b0 = *(LAS f4*)(lds + p3a2), b1 = *(LAS f4*)(lds + p3a2 + 16);
        f2 X[8] = {a0.xy, a0.zw, a1.xy, a1.zw, b0.xy, b0.zw, b1.xy, b1.zw};
        dft4<false>(X[0], X[1], X[2], X[3], kc); dft4<false>(X[4], X[5], X[6], X[7], kc);
        f2 Y[8];
#pragma unroll
        for (int k = 0; k < 4; k++) { Y[k] = X[4 + 3 - k]; Y[4 + k] = X[3 - k]; }
        if (q0) {
#pragma unroll
            for (int k = 0; k < 4; k++) {
                f2 n1 = rot ? X[(4 - k) & 3] : X[3 - k], n2 = rot ? X[4 + ((4 - k) & 3)] : X[4 + 3 - k];
                Y[k] = self ? n1 : n2; Y[4 + k] = self ? n2 : n1;
            }
        }
        f2 O[8];
#pragma unroll
        for (int e = 0; e < 8; e++) O[e] = cfmac_conj(cmul<false>(X[e], al[e]), be[e], Y[e]);
        dft4<true>(O[0], O[1], O[2], O[3], kc); dft4<true>(O[4], O[5], O[6], O[7], kc);
        *(LAS f4*)(lds + p3a1) = f4{O[0].x, O[0].y, O[1].x, O[1].y}; *(LAS f4*)(lds + p3a1 + 16) = f4{O[2].x, O[2].y, O[3].x, O[3].y};
        *(LAS f4*)(lds + p3a2) = f4{O[4].x, O[4].y, O[5].x, O[5].y}; *(LAS f4*)(lds + p3a2 + 16) = f4{O[6].x, O[6].y, O[7].x, O[7].y};
        FENCE();
#pragma unroll
        for (int k = 0; k < 8; k++) v[k] = *(LAS f2*)(lds + rowB + k * 2320);
        dft8<true>(v, kc);
#pragma unroll
        for (int k = 0; k < 8; k++) *(LAS f2*)(lds + rowB + k * 2320) = v[k];
        post(B_E2);
        wait_grp(1, B_E2);
#pragma unroll
        for (int k = 0; k < 8; k++) v[k] = *(LAS f2*)(pb + k * 32);
#pragma unroll
        for (int k = 1; k < 8; k++) v[k] = cmul<false>(v[k], tw1[k]);
        dft8<true>(v, kc);
        FENCE();
#pragma unroll
        for (int k = 0; k < 8; k++) *(LAS f2*)(area + (k * 72 + lane) * 8) = v[k];
        FENCE();
#pragma unroll
        for (int k = 0; k < 4; k++) {
            f4 q = *(LAS f4*)(area + (l3 * 72 + 2 * c2p + 8 * k + 32 * rb) * 8);
            e0[k] = q.xy; e1[k] = q.zw;
            if (k) { e0[k] = cmul<false>(e0[k], tw0[k]); e1[k] = cmul<false>(e1[k], tw0[k]); }
        }
        dft4<true>(e0[0], e0[1], e0[2], e0[3], kc); dft4<true>(e1[0], e1[1], e1[2], e1[3], kc);
        const int f2st = WS_F2 + (r3 * 8 + l3) * 16384 + pos1 * 64 + c2p * 16;
#pragma unroll
        for (int H = 0; H < 4; H++) {
            u4 o = {__float_as_uint(e0[H].x), __float_as_uint(e0[H].y), __float_as_uint(e1[H].x), __float_as_uint(e1[H].y)};
            st128(o, rs, NOF(f2st), H * (64 * 16384));
        }
        STAMP(7);
        __builtin_amdgcn_s_setprio(0);
        WAITVM(0);
        flag_store(WS_FF2 + (rq * 256 + pos1) * 8);
    }
    STAMP(8);
    RELANE();
    f4 acc2[4];
#pragma unroll
    for (int j = 0; j < 4; j++) acc2[j] = f4{0, 0, 0, 0};
    const int hsrc = hoff0 + hf * 1024;
    auto g2_mma = [&](auto NC, const u4* hfr, int ldsbase) {
        constexpr int N = decltype(NC)::value;
        u32 pa = (u32)ldsbase + l16; asm volatile("" : "+v"(pa));
        LAS u4* p = (LAS u4*)(lds + pa);
        u4 F[2][4];
#pragma unroll
        for (int j = 0; j < 4; j++) F[0][j] = p[j * 64];
        sfor<N>([&](auto I) {
            constexpr int i = I;
            SCHED();
            if (i + 1 < N) {
#pragma unroll
                for (int j = 0; j < 4; j++) F[(i + 1) & 1][j] = p[(i + 1) * 256 + j * 64];
            }
#pragma unroll
            for (int j = 0; j < 4; j++) acc2[j] = mfma(F[i & 1][j], hfr[i], acc2[j]);
            if (i + 1 < N) {
#pragma unroll
                for (int j = 0; j < 4; j++) { SGB(0x100, 1); SGB(0x8, 1); }
            }
            SCHED();
        });
    };
    u4 hfr[16];
    flag_wait(li * 8, WS_FHA + (rg * 8 + w) * 128);
    if (hf == 0) post(A_D1);
#pragma unroll
    for (int i = 0; i < 16; i++) { hfr[i] = __builtin_amdgcn_raw_buffer_load_b128(rs, l16, hsrc + 2 * ((i + rot) & 15) * 1024, 16); SCHED(); }
    STAMP(9);
    wait_grp(0, A_D1);
    g2_mma(IC<16>{}, hfr, hf ? 0 : L_SET1);
    post(hf ? B_G1 : A_G1);
    pin4(acc2);
    STAMP(10);
    if (hf == 0) {
        wait_grp(0, A_G1);
        sfor<3>([&](auto I) { constexpr int i = I; if (i < 2 || rq < 3) dma4k_c<L_SET1 + i * 16384, (32 + 4 * i) * 4096>(l16, wdp + rq * 4096, (u32)(rq * 4096)); });
        flag_wait(min(li, 10) * 8, WS_FHB + (rg * 4 + rq) * 128);
        post(A_D3);
#pragma unroll
        for (int i = 0; i < 11; i++) { hfr[i] = __builtin_amdgcn_raw_buffer_load_b128(rs, l16, hoff0 + (32 + i) * 1024, 16); SCHED(); }
        STAMP(13);
        wait_grp(0, A_D3);
        g2_mma(IC<11>{}, hfr, L_SET1);
    } else {
        wait_grp(1, B_G1);
        fft13_consts(0.5f / 524288.0f);
        flag_wait(lane * 8, WS_FF2 + (((s2 & 7) >> 1) * 256 + rq * 64) * 8);
        u4 d[4]; f2 v[8];
        const int off = WS_F2 + pid * 16384 + rq * 4096;
#pragma unroll
        for (int i = 0; i < 4; i++) d[i] = __builtin_amdgcn_raw_buffer_load_b128(rs, NOF(l16), off + i * 1024, 16);
        STAMP(13);
        __builtin_amdgcn_s_setprio(2);
#pragma unroll
        for (int i = 0; i < 4; i++) *(LAS u4*)(lds + L_FFT + rq * 4608 + (i * 8 + (lane >> 3)) * 144 + (lane & 7) * 16) = d[i];
        FENCE();
        v[0] = *(LAS f2*)(lds + x3a[0]);
        v[1] = *(LAS f2*)(lds + x3a[1]); v[2] = *(LAS f2*)(lds + x3a[1] + 1152); v[3] = *(LAS f2*)(lds + x3a[1] + 2304);
        v[4] = *(LAS f2*)(lds + x3a[2]);
        v[5] = *(LAS f2*)(lds + x3a[3] + 2304); v[6] = *(LAS f2*)(lds + x3a[3] + 1152); v[7] = *(LAS f2*)(lds + x3a[3]);
#pragma unroll
        for (int k = 0; k < 8; k++) v[k] = cmul<false>(v[k], twT[k]);
        dft8<true>(v, kc);
#pragma unroll
        for (int k = 1; k < 8; k++) v[k] = cmul<false>(v[k], tw64[k]);
        FENCE();
        LAS f2* reg = (LAS f2*)(lds + L_FFT) + (rq * 2 + qb) * 288;
#pragma unroll
        for (int k = 0; k < 8; k++) reg[36 * l3 + 4 * k + c2p] = v[k];
        FENCE();
#pragma unroll
        for (int k = 0; k < 8; k++) v[k] = reg[36 * k + (lane & 31)];
        dft8<true>(v, kc);
        FENCE();
#pragma unroll
        for (int k = 0; k < 8; k++) reg[(lane & 31) + 32 * k] = v[k];
        post(B_F3);
        wait_grp(1, B_F3);
        if (b == 0 && rq == 0) __builtin_amdgcn_raw_buffer_store_b64(*(LAS u2*)(lds + L_TAG + 8), rs, lane ? WS_BYTES : 0, 0, 16);
        LAS f2* xb1 = (LAS f2*)(lds + L_FFT) + rq * 64 + lane;
#pragma unroll
        for (int q1 = 0; q1 < 8; q1++) { const int rg_ = (q1 < 4) ? q1 * 2 : (((8 - q1) & 3) * 2 + 1); v[q1] = xb1[rg_ * 288]; }
        dft8<true>(v, kc);
#pragma unroll
        for (int c1 = 0; c1 < 8; c1++) *(LAS u32*)(lds + L_S + (16 * rq + (lane >> 2)) * 144 + c1 * 16 + c2p * 4) = pkbf(v[c1]);
    }
    STAMP(11);
    BAR();
    {
        LAS f4* xs = (LAS f4*)(lds + w * 2048) + lane;
        LAS f4* xr = (LAS f4*)(lds + (w ^ 4) * 2048) + lane;
        f4 me, mo;
        if (hf) { xs[0] = acc2[0]; xs[64] = acc2[1]; me = acc2[2]; mo = acc2[3]; } else { xs[0] = acc2[2]; xs[64] = acc2[3]; me = acc2[0]; mo = acc2[1]; }
        BAR();
        const int c1 = 4 * hf + lg;
        me += xr[0]; mo += xr[64];
        const u4 sv = *(LAS u4*)(lds + L_S + (16 * rq + li) * 144 + c1 * 16);
        const u4 xv = *(LAS u4*)(lds + ((rq >> 1) ? L_X1 : L_X0) + ((rq & 1) * 16 + li) * 128 + ((c1 ^ (li & 7)) * 16));
        u32 res[4];
#pragma unroll
        for (int e = 0; e < 4; e++) {
            const f2 ov = bfup(pkbf(f2{me[e], mo[e]}));
            const f2 z = ov * -1.44269504f;
            const f2 d = f2{__builtin_amdgcn_exp2f(z.x), __builtin_amdgcn_exp2f(z.y)} + f2{1.f, 1.f};
            const f2 g = bfup(pkbf(ov * f2{__builtin_amdgcn_rcpf(d.x), __builtin_amdgcn_rcpf(d.y)}));
            res[e] = pkbf(bfup(xv[e]) + bfup(pkbf(bfup(sv[e]) * g)));
        }
        *(u4*)(a.out + (size_t)(s2 + 32 * (16 * rq + li)) * 512 + 64 * c1 + 8 * t) = u4{res[0], res[1], res[2], res[3]};
    }
    STAMP(12);
}

extern "C" void kernel_launch(void* const* d_in, const int* in_sizes, int n_in,
                              void* d_out, int out_size, void* d_ws, size_t ws_size,
                              hipStream_t stream) {
    Args a;
    a.x = (const u16*)d_in[0]; a.wr = (const u16*)d_in[1]; a.wi = (const u16*)d_in[2];
    a.wg = (const u16*)d_in[4]; a.wu = (const u16*)d_in[5]; a.wd = (const u16*)d_in[6];
    a.out = (u16*)d_out; a.ws = (unsigned char*)d_ws;
    (void)hipFuncSetAttribute((const void*)fused, hipFuncAttributeMaxDynamicSharedMemorySize, L_END);
    fused<<<256, 512, L_END, stream>>>(a);
}
```

```cpp
#include <hip/hip_runtime.h>
#include <stdint.h>
#include <type_traits>

typedef unsigned short u16;
typedef unsigned u32;
typedef unsigned long long u64;
typedef float f2 __attribute__((ext_vector_type(2)));
typedef float f4 __attribute__((ext_vector_type(4)));
typedef u32 u4 __attribute__((ext_vector_type(4)));
typedef u32 u2 __attribute__((ext_vector_type(2)));
typedef short s4 __attribute__((ext_vector_type(4)));
typedef __bf16 bf8 __attribute__((ext_vector_type(8)));
typedef __bf16 bf2 __attribute__((ext_vector_type(2)));
#define LAS __attribute__((address_space(3)))
#define GAS __attribute__((address_space(1)))
#define DEV __device__ __forceinline__
template <int N> using IC = std::integral_constant<int, N>;

constexpr int NFB = 43;
constexpr int WS_FF1 = 4096;
constexpr int WS_FF2 = WS_FF1 + 8192;
constexpr int WS_FHA = WS_FF2 + 8192;
constexpr int WS_FHB = WS_FHA + 16384;
constexpr int WS_FP = WS_FHB + 16384;
constexpr int WS_FW = WS_FP + 8192;
constexpr int WS_F1 = 1 << 20, WS_F2 = WS_F1 + (4 << 20), WS_H = WS_F2 + (4 << 20);
constexpr int WS_WDP = WS_H + 32 * 4 * NFB * 1024;
constexpr int WS_WP = WS_WDP + 8 * NFB * 4 * 1024;
constexpr int WS_STAMP = WS_WP + (2 << 20);
constexpr int WS_BYTES = WS_STAMP + (1 << 20);
constexpr int PG = 8192;
constexpr int L_SET1 = 8 * PG;
constexpr int L_FFT = 16 * PG;
constexpr int L_SMALL = 19 * PG;
constexpr int L_PROG = L_SMALL;
constexpr int L_TAG = L_SMALL + 32;
constexpr int L_STAT = L_SMALL + 64;
constexpr int L_CS = L_SMALL + 2048;
constexpr int L_X0 = L_FFT + 18688, L_X1 = L_SMALL + 3072;
constexpr int L_S = 4 * PG;
constexpr int L_END = 20 * PG;
constexpr u32 SENT = 0xFFFFFFFFu;
constexpr int CODE_BYTES = 36 * 1024;
constexpr int SPIN_CAP = 1 << 16;

struct Args { const u16* x; const u16* wr; const u16* wi; const u16* wg; const u16* wu; const u16* wd; u16* out; unsigned char* ws; };

#define BAR() asm volatile("s_waitcnt lgkmcnt(0)\n\ts_barrier" ::: "memory")
#define WAITVM(n) asm volatile("s_waitcnt vmcnt(" #n ")" ::: "memory")
#define NOF(x) (x)
#define NOPL(x) (x)
#define FENCE() asm volatile("" ::: "memory")
#define SCHED() __builtin_amdgcn_sched_barrier(0)
#define SGB(mask, n) __builtin_amdgcn_sched_group_barrier(mask, n, 0)
#define STAMP(i)
#define CSTAMP(i)

DEV f2 pkfma(f2 a, f2 b, f2 c) { return __builtin_elementwise_fma(a, b, c); }
struct KC { f2 pm, rpm; };
DEV f2 addmi(f2 a, f2 b, const KC& k) { return pkfma(b.yx, k.pm, a); }
DEV f2 addpi(f2 a, f2 b, const KC& k) { return pkfma(b.yx, k.pm.yx, a); }
template <bool INV> DEV void dft4(f2& a0, f2& a1, f2& a2, f2& a3, const KC& k) {
    f2 t0 = a0 + a2, t1 = a0 - a2, t2 = a1 + a3, t3 = a1 - a3;
    a0 = t0 + t2; a2 = t0 - t2;
    if (INV) { a1 = addpi(t1, t3, k); a3 = addmi(t1, t3, k); } else { a1 = addmi(t1, t3, k); a3 = addpi(t1, t3, k); }
}
template <bool INV> DEV void dft8(f2 (&v)[8], const KC& k) {
    f2 a0 = v[0] + v[4], a4 = v[0] - v[4], a1 = v[1] + v[5], a5 = v[1] - v[5];
    f2 a2 = v[2] + v[6], a6 = v[2] - v[6], a3 = v[3] + v[7], a7 = v[3] - v[7];
    dft4<INV>(a0, a1, a2, a3, k);
    v[0] = a0; v[2] = a1; v[4] = a2; v[6] = a3;
    f2 b1, b3, t0, t1;
    if (INV) { b1 = addpi(a5, a5, k); b3 = addpi(-a7, a7, k); t0 = addpi(a4, a6, k); t1 = addmi(a4, a6, k); }
    else { b1 = addmi(a5, a5, k); b3 = addmi(-a7, a7, k); t0 = addmi(a4, a6, k); t1 = addpi(a4, a6, k); }
    f2 t2 = b1 + b3, t3 = b1 - b3;
    v[1] = pkfma(t2, k.rpm.xx, t0); v[5] = pkfma(t2, k.rpm.yy, t0);
    if (INV) { v[3] = pkfma(t3.yx, k.rpm.yx, t1); v[7] = pkfma(t3.yx, k.rpm, t1); }
    else { v[3] = pkfma(t3.yx, k.rpm, t1); v[7] = pkfma(t3.yx, k.rpm.yx, t1); }
}
template <bool CONJ> DEV f2 cmul(f2 a, f2 w) {
    f2 r;
    if (CONJ) asm("v_pk_mul_f32 %0, %1, %2 op_sel:[1,1] op_sel_hi:[0,1]\n\tv_pk_fma_f32 %0, %1, %2, %0 op_sel_hi:[1,0,1] neg_hi:[0,0,1]" : "=&v"(r) : "v"(a), "v"(w));
    else asm("v_pk_mul_f32 %0, %1, %2 op_sel:[1,1] op_sel_hi:[0,1]\n\tv_pk_fma_f32 %0, %1, %2, %0 op_sel_hi:[1,0,1] neg_lo:[0,0,1]" : "=&v"(r) : "v"(a), "v"(w));
    return r;
}
DEV f2 cfmac_conj(f2 t, f2 beta, f2 B) {
    asm("v_pk_fma_f32 %0, %2, %1, %0 op_sel_hi:[0,1,1]\n\tv_pk_fma_f32 %0, %2, %1, %0 op_sel:[1,1,0] op_sel_hi:[1,0,1] neg_hi:[0,1,0]" : "+v"(t) : "v"(beta), "v"(B));
    return t;
}
DEV f2 cis(float r) { return f2{__builtin_amdgcn_cosf(r), __builtin_amdgcn_sinf(r)}; }
DEV float bflo(u32 v) { return __uint_as_float(v << 16); }
DEV float bfhi(u32 v) { return __uint_as_float(v & 0xFFFF0000u); }
DEV f2 bfup(u32 v) { return f2{bflo(v), bfhi(v)}; }
DEV u32 pkbf(f2 v) { bf2 r; r.x = (__bf16)v.x; r.y = (__bf16)v.y; u32 o = __builtin_bit_cast(u32, r); asm("" : "+v"(o)); return o; }
DEV float rbf(float f) { return __uint_as_float(((u32)__builtin_bit_cast(u16, (__bf16)f)) << 16); }
DEV f4 mfma(u4 a, u4 b, f4 c) { return __builtin_amdgcn_mfma_f32_16x16x32_bf16(__builtin_bit_cast(bf8, a), __builtin_bit_cast(bf8, b), c, 0, 0, 0); }
DEV u4 sanitize(u4 v) { return u4{min(v.x, 0xFFFFFFFEu), min(v.y, 0xFFFFFFFEu), min(v.z, 0xFFFFFFFEu), min(v.w, 0xFFFFFFFEu)}; }
DEV u32 max4(u4 v) { return max(max(v.x, v.y), max(v.z, v.w)); }
DEV void st128(u4 v, __amdgpu_buffer_rsrc_t rs, u32 voff, int soff) {
    __builtin_amdgcn_raw_buffer_store_b128(v, rs, voff, soff, 16);
    asm volatile("s_nop 1" :: "v"(v) : "memory");
}
template <int OFF2> DEV u4 trfrag2(LAS char* p) {
    s4 r0 = __builtin_amdgcn_ds_read_tr16_b64_v4i16((LAS s4*)p);
    s4 r1 = __builtin_amdgcn_ds_read_tr16_b64_v4i16((LAS s4*)(p + OFF2));
    u2 x0 = __builtin_bit_cast(u2, r0), x1 = __builtin_bit_cast(u2, r1);
    return u4{x0.x, x0.y, x1.x, x1.y};
}
template <int LI, int GI> DEV void dma_add(u32 vb, const void* sbase, u32 sl) {
    const u32 va = vb + (u32)GI;
    asm volatile("s_add_u32 m0, %2, %3\n\ts_nop 0\n\tglobal_load_lds_dwordx4 %0, %1" :: "v"(va), "s"(sbase), "s"(sl), "i"(LI) : "memory", "scc");
}
template <int LI, int GI> DEV void dma4k_c(u32 vb, const void* sbase, u32 sl) {
    const u32 va = vb + (u32)GI;
    asm volatile("s_add_u32 m0, %2, %3\n\ts_nop 0\n\tglobal_load_lds_dwordx4 %0, %1 sc1\n\tglobal_load_lds_dwordx4 %0, %1 offset:1024 sc1\n\t"
                 "global_load_lds_dwordx4 %0, %1 offset:2048 sc1\n\tglobal_load_lds_dwordx4 %0, %1 offset:3072 sc1"
                 :: "v"(va), "s"(sbase), "s"(sl), "i"(LI) : "memory", "scc");
}
template <int OFF> DEV void ldg_u16(u32& d, u32 vo, const void* sb) { asm volatile("global_load_ushort %0, %1, %2 offset:%3" : "=v"(d) : "v"(vo), "s"(sb), "i"(OFF) : "memory"); }
template <int OFF> DEV void ldg_u32(u32& d, u32 vo, const void* sb) { asm volatile("global_load_dword %0, %1, %2 offset:%3" : "=v"(d) : "v"(vo), "s"(sb), "i"(OFF) : "memory"); }
DEV void lds_wait(u32 addr, u32 v) {
    u32 tmp, cnt;
    asm volatile("s_mov_b32 %1, 0x80000\n"
                 "1:\n\tds_read_b32 %0, %2\n\ts_sub_u32 %1, %1, 1\n\ts_waitcnt lgkmcnt(0)\n\ts_cbranch_scc1 2f\n\tv_cmp_gt_u32 vcc, %3, %0\n\ts_cbranch_vccnz 1b\n2:"
                 : "=&v"(tmp), "=&s"(cnt) : "v"(addr), "s"(v) : "memory", "vcc", "scc");
}
template <int N, typename F> DEV void sfor(F&& f) { if constexpr (N > 0) { sfor<N - 1>(f); f(IC<N - 1>{}); } }

DEV void pin9(f4 (&x)[4], f4 (&y)[4], f4& z) {
    asm volatile("" : "+v"(x[0]), "+v"(x[1]), "+v"(x[2]), "+v"(x[3]), "+v"(y[0]), "+v"(y[1]), "+v"(y[2]), "+v"(y[3]), "+v"(z));
}
DEV void pin4(f4 (&x)[4]) { asm volatile("" : "+v"(x[0]), "+v"(x[1]), "+v"(x[2]), "+v"(x[3])); }
DEV void dma_rt(u32 voff, const void* sbase, u32 sl) {
    asm volatile("s_mov_b32 m0, %2\n\ts_nop 0\n\tglobal_load_lds_dwordx4 %0, %1" :: "v"(voff), "s"(sbase), "s"(sl) : "memory");
}
DEV u4 geglu8(const f4 (&acc)[4], LAS char* cs, f2 st) {
    u32 hw[4];
    const f2 nm = f2{-st.x, -st.x}, ri = f2{st.y, st.y};
#pragma unroll
    for (int c = 0; c < 2; c++) {
        f4 cg_ = *(LAS f4*)(cs + c * 64), cu_ = *(LAS f4*)(cs + (2 + c) * 64);
#pragma unroll
        for (int e = 0; e < 2; e++) {
            f2 g = pkfma(e ? cg_.zw : cg_.xy, nm, e ? acc[c].zw : acc[c].xy) * ri;
            f2 u = pkfma(e ? cu_.zw : cu_.xy, nm, e ? acc[2 + c].zw : acc[2 + c].xy) * ri;
            f2 z = g * pkfma(g * g, f2{-0.10294324f, -0.10294324f}, f2{-2.3022082f, -2.3022082f});
            f2 d = f2{__builtin_amdgcn_exp2f(z.x), __builtin_amdgcn_exp2f(z.y)} + f2{1.f, 1.f};
            f2 r = f2{__builtin_amdgcn_rcpf(d.x), __builtin_amdgcn_rcpf(d.y)};
            hw[2 * c + e] = pkbf(g * r * u);
        }
    }
    return u4{hw[0], hw[1], hw[2], hw[3]};
}

enum { A_XL = 1, A_XC, A_P1, A_XB, A_S0, A_CS, A_WB, A_PB, A_D1, A_G1, A_D3 };
enum { B_XL = 1, B_W0, B_W1, B_W2, B_S0, B_W3, B_CS, B_FP, B_E1, B_E2, B_G1, B_F3 };

__global__ void __launch_bounds__(512, 2) fused(Args a) {
    extern __shared__ __attribute__((aligned(16))) char smem[];
    LAS char* lds = (LAS char*)smem;
    const int tid = threadIdx.x, w = __builtin_amdgcn_readfirstlane(tid >> 6);
    int lane = tid & 63;
    const int b = blockIdx.x, xcd = b & 7, r = b >> 3, t = r & 7, par = xcd & 1, rg = (xcd >> 1) * 4 + (r >> 3);
    const int s2 = 2 * rg + par, cg = par * 8 + t, sig = xcd * 32 + r, pid = s2 * 8 + t;
    const int rq = w & 3, hf = w >> 2, j4 = r >> 3;
#define BE(l) ((((l) >> 3) << 1) | ((l) & 1))
#define IX(l) (((l) >> 1) & 3)
    u32 l16 = lane * 16;
    u32 cpf = 0;
    if (w == 7) {
        const u64 pc = __builtin_amdgcn_s_getpc() & ~(u64)255;
        const u32 off = min((u32)((t * 64 + lane) * 128), (u32)(CODE_BYTES - 128));
        asm volatile("global_load_dword %0, %1, %2" : "=v"(cpf) : "v"(off), "s"(pc) : "memory");
    }
    *(volatile LAS u32*)(lds + L_PROG + w * 4) = 0;
    BAR();
    STAMP(0);
    auto dma_x = [&]() {
        const char* xs = (const char*)a.x + (size_t)((s2 ^ hf) + 512 * rq) * 1024;
#pragma unroll
        for (int i = 0; i < 16; i++) { const int row = (i + 2 * t) & 15; dma_rt(l16 ^ (u32)(row * (32768 + 16)), xs, (u32)(L_SET1 + rq * 16384 + row * 1024)); }
    };
    if (hf == 0) dma_x();
    int li, lg, c2p, l3, qb; u32 vpg;
#define RELANE() do { asm volatile("" : "+v"(lane)); l16 = lane * 16; li = lane & 15; lg = lane >> 4; c2p = lane & 3; l3 = (lane >> 2) & 7; qb = lane >> 5; vpg = (u32)(L_PROG + (lane & 3) * 4); } while (0)
    const __amdgpu_buffer_rsrc_t rs = __builtin_amdgcn_make_buffer_rsrc(a.ws, 0, WS_BYTES, 0x00020000);
    KC kc; kc.pm = f2{1.f, -1.f}; kc.rpm = f2{0.70710678118654752f, -0.70710678118654752f};
    asm volatile("" : "+s"(kc.pm), "+s"(kc.rpm));
    const u4 ones = {0x3F803F80u, 0x3F803F80u, 0x3F803F80u, 0x3F803F80u};
    auto post = [&](u32 v) { asm volatile("s_waitcnt lgkmcnt(0)" ::: "memory"); *(volatile LAS u32*)(lds + L_PROG + w * 4) = v; FENCE(); };
    auto wait_grp = [&](int g, u32 v) { lds_wait(vpg + g * 16, v); };
    auto wait_pair = [&](u32 v) { lds_wait((u32)(L_PROG + (w ^ 4) * 4), v); };
    u2 tag;
    auto get_tag = [&]() { u2 tg = *(LAS u2*)(lds + L_TAG); tag.x = __builtin_amdgcn_readfirstlane(tg.x); tag.y = __builtin_amdgcn_readfirstlane(tg.y); };
    auto flag_store = [&](int off) { __builtin_amdgcn_raw_buffer_store_b64(tag, rs, lane ? WS_BYTES : 0, off, 16); };
    auto flag_wait = [&](u32 voff, int soff) {
        _Pragma("unroll 1") for (int spins = 0; spins < SPIN_CAP; spins++) {
            u2 f = __builtin_amdgcn_raw_buffer_load_b64(rs, voff, soff, 16);
            if (__all(f.x == tag.x && f.y == tag.y)) break;
            __builtin_amdgcn_s_sleep(4);
            FENCE();
        }
        __builtin_amdgcn_fence(__ATOMIC_ACQUIRE, "wavefront");
        FENCE();
    };
    auto read_tile = [&](u4 (&x)[16], int tile) {
        LAS char* p = lds + L_SET1 + (16 * rq + li) * 1024;
#pragma unroll
        for (int k = 0; k < 16; k++) x[k] = *(LAS u4*)(p + (((4 * IX(k) + lg) ^ li) * 16) + ((BE(k) + j4) & 3) * 256);
    };
    auto stats = [&](const u4 (&x)[16]) {
        f4 s1 = {0, 0, 0, 0}, sq = s1, s1b = s1, sqb = s1;
#pragma unroll
        for (int k = 0; k < 16; k += 2) { s1 = mfma(ones, x[k], s1); sq = mfma(x[k], x[k], sq); s1b = mfma(ones, x[k + 1], s1b); sqb = mfma(x[k + 1], x[k + 1], sqb); }
        sq += sqb;
        const int rr = li & 3;
        const float d = rr == 0 ? sq.x : rr == 1 ? sq.y : rr == 2 ? sq.z : sq.w;
        if (lg == (li >> 2)) {
            const float mean = (s1.x + s1b.x) * (1.0f / 512.0f);
            float var = d * (1.0f / 512.0f) - mean * mean;
            if (!(mean * mean <= 1024.0f * var)) {
                const u32* xrow = (const u32*)(a.x + (size_t)((s2 ^ hf) + 32 * (16 * rq + li)) * 512);
                float q = 0.f;
                _Pragma("unroll 1") for (int i = 0; i < 256; i++) { u32 v = xrow[i]; float d0 = bflo(v) - mean, d1 = bfhi(v) - mean; q += d0 * d0; q += d1 * d1; }
                var = q * (1.0f / 512.0f);
            }
            const float den = rbf(__builtin_amdgcn_sqrtf(rbf(rbf(var) + 1e-5f)));
            *(LAS f2*)(lds + L_STAT + (hf * 64 + 16 * rq + li) * 8) = f2{rbf(mean), __builtin_amdgcn_rcpf(den)};
        }
        FENCE();
    };
    const int nblk = (t < 6 - par) ? 3 : 2, fb2 = 32 + par * 6 + t;
    const char* wdp = (const char*)a.ws + WS_WDP + (size_t)(t * NFB) * 4096;

    f2 tw64[8], twT[8];
    int x3a[4];
    auto fft13_consts = [&](float scale) {
        int lane_ = lane; asm volatile("" : "+v"(lane_));
        const int c2p = lane_ & 3, l3 = (lane_ >> 2) & 7, qb = lane_ >> 5;
        const int wq = rq, q1 = qb ? (wq == 0 ? 4 : 8 - wq) : wq, c2 = 4 * t + c2p;
        const float f64 = (float)l3 * (1.0f / 64.0f);
#pragma unroll
        for (int k = 1; k < 8; k++) tw64[k] = cis(f64 * (float)k);
        const float fT = (float)(8 * c2 * q1 + s2 * l3) * (1.0f / 2048.0f), dT = (float)s2 * (1.0f / 256.0f);
#pragma unroll
        for (int k = 0; k < 8; k++) twT[k] = cis(fT + dT * (float)k) * scale;
#pragma unroll
        for (int k = 0; k < 4; k++) {
            const int ua = (k >> 1) * 4 + (k & 1), u1 = l3 + 8 * ua;
            const int cls = (u1 < 32) ? u1 : ((64 - u1) & 31), hi = u1 >> 5;
            int mbit, sub;
            if (wq == 0) { mbit = qb; sub = hi; } else if (cls != 0) { sub = hi; mbit = qb ^ hi; } else { mbit = hi; sub = qb ^ hi; }
            x3a[k] = L_FFT + wq * 4608 + (cls * 18 + mbit * 8 + sub * 4 + c2p) * 8 - (k == 3 ? 2304 : 0);
        }
        asm volatile("" : "+v"(tw64[1]), "+v"(tw64[2]), "+v"(tw64[3]), "+v"(tw64[4]), "+v"(tw64[5]), "+v"(tw64[6]), "+v"(tw64[7]),
                     "+v"(twT[0]), "+v"(twT[1]), "+v"(twT[2]), "+v"(twT[3]), "+v"(twT[4]), "+v"(twT[5]), "+v"(twT[6]), "+v"(twT[7]),
                     "+v"(x3a[0]), "+v"(x3a[1]), "+v"(x3a[2]), "+v"(x3a[3]));
    };

    u4 xa[16], xb[16];
    const int hoff0 = WS_H + ((s2 * 4 + rq) * NFB) * 1024, hoff1 = WS_H + (((s2 ^ 1) * 4 + rq) * NFB) * 1024;
    RELANE();
    const char* wgs = (const char*)a.wg + (size_t)(32 * rq) * 2752 + cg * 128;
    const char* wus = (const char*)a.wu + (size_t)(32 * rq) * 2752 + cg * 128;
    const u32 wvE = (u32)((lane >> 3) * 2752 + (((lane & 7) ^ (((lane >> 4) & 1) << 1)) * 16)), wvO = (u32)((lane >> 3) * 2752 + (((lane & 7) ^ (((lane >> 4) & 1) << 1) ^ 4) * 16));
    auto dma_w1 = [&](auto KK, auto I) {
        constexpr int kk = KK, g = I >> 1, mat = I & 1;
        dma_add<kk * PG + mat * 4096 + g * 1024, g * 8 * 2752>((g & 1) ? wvO : wvE, (mat ? wus : wgs) + ((BE(kk) + j4) & 3) * (128 * 2752), (u32)(rq * 2 * PG));
    };
    const int rot = (2 * t + 4 * j4) & 15;
    auto dma_wd = [&](auto LI, auto I, int p0) { dma4k_c<LI + I * 16384, 0>(l16, wdp + (size_t)(p0 + 2 * ((rq + 4 * I + rot) & 15)) * 4096, (u32)(rq * 4096)); };

    if (hf == 0) {
        __builtin_amdgcn_s_setprio(2);
        const int f1st = WS_F1 + ((lane >> 3) * 8 + (((lane >> 2) & 1) ? (rq == 0 ? 4 : 8 - rq) : rq)) * 16384 + pid * 64 + c2p * 16;
        u2 ev;
        { u32 zero = 0; asm volatile("global_load_dwordx2 %0, %1, %2 sc1" : "=v"(ev) : "v"(zero), "s"(a.ws) : "memory"); }
        fft13_consts(1.0f);
        WAITVM(1);
        post(A_XL);
        STAMP(1);
        u32 raw[8];
        const int rw = lane >> 2;
        read_tile(xa, 0);
#pragma unroll
        for (int c1 = 0; c1 < 8; c1++) raw[c1] = *(LAS u32*)(lds + L_SET1 + (16 * rq + rw) * 1024 + (((8 * c1 + t) ^ rw) * 16) + c2p * 4);
        post(A_XC);
        stats(xa);
        STAMP(2);
        f2 v[8];
        {
            const f2 st = *(LAS f2*)(lds + L_STAT + (16 * rq + rw) * 8);
#pragma unroll
            for (int c1 = 0; c1 < 8; c1++) v[c1] = bfup(pkbf(bfup(pkbf(bfup(raw[c1]) - st.xx)) * st.yy));
            dft8<false>(v, kc);
            LAS f2* xb1 = (LAS f2*)(lds + L_FFT) + w * 64 + lane;
#pragma unroll
            for (int q1 = 0; q1 < 8; q1++) { const int reg = (q1 < 4) ? q1 * 2 : (((8 - q1) & 3) * 2 + 1); xb1[reg * 288] = v[q1]; }
        }
        asm volatile("s_waitcnt vmcnt(0)" : "+v"(ev) :: "memory");
        {
            const u64 epoch = ((u64)(u32)__builtin_amdgcn_readfirstlane(ev.y) << 32) | (u32)__builtin_amdgcn_readfirstlane(ev.x);
            u64 tg = (epoch + 1) * 0x9E3779B97F4A7C15ull; tg ^= tg >> 29; tg *= 0xBF58476D1CE4E5B9ull; tg ^= tg >> 32;
            tag = u2{(u32)tg, (u32)(tg >> 32)};
            *(LAS u4*)(lds + L_TAG) = u4{(u32)tg, (u32)(tg >> 32), (u32)(epoch + 1), (u32)((epoch + 1) >> 32)};
        }
        post(A_P1);
#pragma unroll
        for (int c1 = 0; c1 < 8; c1++) *(LAS u32*)(lds + ((rq >> 1) ? L_X1 : L_X0) + ((rq & 1) * 16 + rw) * 128 + ((c1 ^ (rw & 7)) * 16) + c2p * 4) = raw[c1];
        STAMP(19);
        wait_pair(B_XL);
        STAMP(20);
        read_tile(xb, 1);
        post(A_XB);
        STAMP(22);
        wait_grp(0, A_P1);
        STAMP(23);
        {
            LAS f2* reg = (LAS f2*)(lds + L_FFT) + (w * 2 + qb) * 288;
#pragma unroll
            for (int k = 0; k < 8; k++) v[k] = reg[(lane & 31) + 32 * k];
            dft8<false>(v, kc);
#pragma unroll
            for (int k = 1; k < 8; k++) v[k] = cmul<true>(v[k], tw64[k]);
#pragma unroll
            for (int k = 0; k < 8; k++) reg[36 * k + (lane & 31)] = v[k];
            FENCE();
#pragma unroll
            for (int k = 0; k < 8; k++) v[k] = reg[36 * l3 + 4 * k + c2p];
            dft8<false>(v, kc);
#pragma unroll
            for (int k = 0; k < 8; k++) v[k] = cmul<true>(v[k], twT[k]);
            FENCE();
            *(LAS f2*)(lds + x3a[0]) = v[0];
            *(LAS f2*)(lds + x3a[1]) = v[1]; *(LAS f2*)(lds + x3a[1] + 1152) = v[2]; *(LAS f2*)(lds + x3a[1] + 2304) = v[3];
            *(LAS f2*)(lds + x3a[2]) = v[4];
            *(LAS f2*)(lds + x3a[3] + 2304) = v[5]; *(LAS f2*)(lds + x3a[3] + 1152) = v[6]; *(LAS f2*)(lds + x3a[3]) = v[7];
            FENCE();
            STAMP(24);
#pragma unroll
            for (int i = 0; i < 4; i++) {
                u4 o = *(LAS u4*)(lds + L_FFT + w * 4608 + (i * 8 + (lane >> 3)) * 144 + (lane & 7) * 16);
                st128(o, rs, NOF(f1st), i * (64 * 16384));
            }
        }
        STAMP(3);
        __builtin_amdgcn_s_setprio(0);
        STAMP(4);
    } else {
        u32 pk[8];
        const int un = sig < 4 * NFB ? sig : 0, fb = un >> 2, jj = (un >> 1) & 1, tcol = (un & 1) * 4 + rq;
        __builtin_amdgcn_s_sleep(2);
        {
            const u32 vd = (u32)(((fb * 32 + 4 * lg) * 512 + 64 * (4 * jj + (li >> 2)) + 8 * tcol + 2 * (li & 3)) * 2);
            sfor<4>([&](auto I) {
                ldg_u32<I * 1024>(pk[I], vd, a.wd);
                ldg_u32<I * 1024>(pk[4 + I], vd, (const char*)a.wd + 16384);
            });
        }
        sfor<8>([&](auto I) { dma_w1(IC<0>{}, I); });
        wait_pair(A_XL);
        read_tile(xa, 0);
        asm volatile("s_waitcnt lgkmcnt(0)" ::: "memory");
        wait_pair(A_XC);
        dma_x();
        asm volatile("s_waitcnt vmcnt(24)" : "+v"(cpf), "+v"(pk[0]), "+v"(pk[1]), "+v"(pk[2]), "+v"(pk[3]), "+v"(pk[4]), "+v"(pk[5]), "+v"(pk[6]), "+v"(pk[7]) :: "memory");
        sfor<8>([&](auto I) { dma_w1(IC<1>{}, I); });
        {
            const int so = sig < 4 * NFB ? WS_WDP + ((tcol * NFB + fb) * 4 + 2 * jj) * 1024 : WS_BYTES;
            u4 oe = {__builtin_amdgcn_perm(pk[1], pk[0], 0x05040100), __builtin_amdgcn_perm(pk[3], pk[2], 0x05040100), __builtin_amdgcn_perm(pk[5], pk[4], 0x05040100), __builtin_amdgcn_perm(pk[7], pk[6], 0x05040100)};
            u4 oo = {__builtin_amdgcn_perm(pk[1], pk[0], 0x07060302), __builtin_amdgcn_perm(pk[3], pk[2], 0x07060302), __builtin_amdgcn_perm(pk[5], pk[4], 0x07060302), __builtin_amdgcn_perm(pk[7], pk[6], 0x07060302)};
            st128(oe, rs, l16, so);
            st128(oo, rs, l16, so + 1024);
        }
        STAMP(1);
        WAITVM(10);
        post(B_W0);
        read_tile(xb, 1);
        __builtin_amdgcn_s_setprio(1);
        STAMP(2);
    }
    f2 st0, st1;
    {
        const u32 fz = (u32)((li >> 3) | ((lg & 1) << 1));
        const u32 tb = (u32)((8 * lg + (li >> 2)) * 128 + 8 * (li & 3));
        u32 p0 = tb + (((2 * hf) ^ fz) << 5), p1 = tb + (((2 * hf + 1) ^ fz) << 5), pc = tb + (((2 * hf + (rq & 1)) ^ fz) << 5) + (rq >> 1) * 4096;
        const u32 wvB = (u32)((lane >> 2) * 2752 + (((lane & 3) ^ (((lane >> 5) & 1) << 1)) * 16));
        const char* wbs = (const char*)((rq >> 1) ? a.wu : a.wg) + (size_t)((rq & 1) * 16) * 2752 + fb2 * 64;
        f4 accA[4], accB[4], acs = {0, 0, 0, 0};
#pragma unroll
        for (int j = 0; j < 4; j++) { accA[j] = f4{0, 0, 0, 0}; accB[j] = f4{0, 0, 0, 0}; }
        u4 F[2][5];
        auto ldf = [&](int ks, u4 (&f)[5]) {
            const int off = (ks & 7) * PG;
            f[0] = trfrag2<512>(lds + p0 + off); f[1] = trfrag2<512>(lds + p1 + off); f[2] = trfrag2<512>(lds + p0 + off + 4096); f[3] = trfrag2<512>(lds + p1 + off + 4096); f[4] = trfrag2<512>(lds + pc + off);
        };
        wait_grp(1, B_W0);
        STAMP(14);
        ldf(0, F[0]);
        sfor<16>([&](auto S) {
            constexpr int s = S, ks = (s & 8) | ((s & 3) << 1) | ((s >> 2) & 1), sn = s + 1, kn = (sn & 8) | ((sn & 3) << 1) | ((sn >> 2) & 1);
            if ((s >= 1 && s <= 4) || s == 7 || s >= 9) pin9(accA, accB, acs);
            if (s == 3) {
                if (hf) { WAITVM(10); post(B_W1); }
                else { WAITVM(0); flag_store(WS_FF1 + ((rq * 4 + ((s2 & 7) >> 1)) * 64 + (s2 >> 3) * 16 + (s2 & 1) * 8 + t) * 8); }
                wait_grp(1, B_W1);
            }
            if constexpr (s >= 1 && s <= 4) if (hf) {
                if (s == 1) { wait_pair(A_XB); get_tag(); }
                sfor<4>([&](auto I) { dma_w1(IC<8 + ((s - 1) >> 1)>{}, IC<((s - 1) & 1) * 4 + I>{}); });
            }
            if (s == 4) STAMP(15);
            if (s == 7) {
                if (hf) { WAITVM(8); post(B_W2); flag_store(sig < 4 * NFB ? WS_FP + (((sig & 1) * 4 + rq) * 128 + (sig >> 1)) * 8 : WS_BYTES); }
                wait_grp(1, B_W2); STAMP(16);
                p0 += 65536; p1 += 65536; pc += 65536;
                asm volatile("" : "+v"(p0), "+v"(p1), "+v"(pc));
            }
            if (s == 8) post(hf ? B_S0 : A_S0);
            if (s == 11) {
                if (hf) { WAITVM(0); post(B_W3); }
                wait_grp(1, B_W3); STAMP(17);
            }
            if constexpr (s >= 9) if (hf == 0 && nblk == 3) {
                if (s == 9) { wait_grp(0, A_S0); wait_grp(1, B_S0); sfor<2>([&](auto I) { dma_add<I * 4096, IX(I) * 32 * 2752>(wvB, wbs + ((BE(I) + j4) & 3) * (128 * 2752), (u32)(rq * 1024)); }); }
                sfor<2>([&](auto I) { constexpr int k = (s - 8) * 2 + I; dma_add<k * 4096, IX(k) * 32 * 2752>(wvB, wbs + ((BE(k) + j4) & 3) * (128 * 2752), (u32)(rq * 1024)); });
            }
            SCHED();
            if (s + 1 < 16) ldf(kn, F[(s + 1) & 1]);
            u4 (&f)[5] = F[s & 1];
#pragma unroll
            for (int j = 0; j < 4; j++) { accA[j] = mfma(f[j], xa[ks], accA[j]); accB[j] = mfma(f[j], xb[ks], accB[j]); }
            acs = mfma(f[4], ones, acs);
            if (s + 1 < 16) {
                SGB(0x100, 2);
#pragma unroll
                for (int i = 0; i < 8; i++) { SGB(0x8, 1); SGB(0x100, 1); }
                SGB(0x8, 1);
            }
            SCHED();
        });
        RELANE();
        if (hf) stats(xb);
        if (li == 0) *(LAS f4*)(lds + L_CS + (hf * 4 + rq) * 64 + lg * 16) = acs;
        post(hf ? B_CS : A_CS);
        STAMP(18);
        wait_grp(hf, hf ? B_CS : A_CS);
        if (hf == 0) wait_pair(B_CS);
        LAS char* cs = lds + L_CS + hf * 256 + lg * 16;
        st0 = *(LAS f2*)(lds + L_STAT + (16 * rq + li) * 8);
        st1 = *(LAS f2*)(lds + L_STAT + (64 + 16 * rq + li) * 8);
        st128(geglu8(accA, cs, st0), rs, l16, hoff0 + (2 * cg + hf) * 1024);
        st128(geglu8(accB, cs, st1), rs, l16, hoff1 + (2 * cg + hf) * 1024);
    }
    STAMP(5);
    if (hf == 0) {
        WAITVM(2); post(A_WB);
        if (nblk == 3) {
            wait_grp(0, A_WB);
            const u32 tb = (u32)((8 * lg + (li >> 2)) * 64 + (lg & 1) * 32 + 8 * (li & 3));
            LAS char* p0 = lds + tb; LAS char* p1 = lds + (tb ^ 32);
            LAS char* pc = lds + (tb ^ ((rq & 1) * 32)) + (rq >> 1) * 2048;
            f4 acc[2][4], acs = {0, 0, 0, 0};
#pragma unroll
            for (int j = 0; j < 4; j++) { acc[0][j] = f4{0, 0, 0, 0}; acc[1][j] = f4{0, 0, 0, 0}; }
            u4 F[2][5];
            auto ldf = [&](int ks, u4 (&f)[5]) {
                const int off = ks * 4096;
                f[4] = trfrag2<256>(pc + off);
                f[0] = trfrag2<256>(p0 + off); f[1] = trfrag2<256>(p1 + off); f[2] = trfrag2<256>(p0 + off + 2048); f[3] = trfrag2<256>(p1 + off + 2048);
            };
            ldf(0, F[0]);
            sfor<16>([&](auto S) {
                constexpr int ks = S;
                if (ks == 4) { WAITVM(0); flag_store(WS_FHA + ((rg * 8 + w) * 16 + cg) * 8); wait_grp(1, B_FP); }
                if constexpr (ks >= 4 && ks % 3 == 1) dma_wd(IC<L_SET1>{}, IC<(ks - 4) / 3>{}, 0);
                SCHED();
                if (ks + 1 < 16) ldf(ks + 1, F[(ks + 1) & 1]);
                u4 (&f)[5] = F[ks & 1];
                acs = mfma(f[4], ones, acs);
#pragma unroll
                for (int j = 0; j < 4; j++) { acc[0][j] = mfma(f[j], xa[ks], acc[0][j]); acc[1][j] = mfma(f[j], xb[ks], acc[1][j]); }
                if (ks + 1 < 16) {
                    SGB(0x100, 2);
#pragma unroll
                    for (int i = 0; i < 8; i++) { SGB(0x8, 1); SGB(0x100, 1); }
                    SGB(0x8, 1);
                }
                SCHED();
            });
            if (li == 0) *(LAS f4*)(lds + L_CS + 512 + rq * 64 + lg * 16) = acs;
            post(A_PB);
            wait_grp(0, A_PB);
            st128(geglu8(acc[0], lds + L_CS + 512 + lg * 16, st0), rs, l16, hoff0 + fb2 * 1024);
            st128(geglu8(acc[1], lds + L_CS + 512 + lg * 16, st1), rs, l16, hoff1 + fb2 * 1024);
        } else {
            WAITVM(0); flag_store(WS_FHA + ((rg * 8 + w) * 16 + cg) * 8);
            wait_grp(1, B_FP);
            sfor<4>([&](auto I) { dma_wd(IC<L_SET1>{}, I, 0); });
        }
        STAMP(6);
        sfor<4>([&](auto I) { dma_wd(IC<0>{}, I, 1); });
        if (nblk == 3) { WAITVM(16); flag_store(WS_FHB + ((rg * 4 + rq) * 16 + fb2 - 32) * 8); }
    } else {
        f2 v[8], tw0[4], tw1[8], al[8], be[8];
        const int rb = qb, r3 = 2 * rq + rb;
        const int cls2 = sig >> 3, m2 = sig & 7;
        const int wq2 = (m2 < 4) ? m2 : ((8 - m2) & 3), pos1 = wq2 * 64 + cls2 * 2 + (m2 >> 2);
        const bool special = (cls2 == 0) && ((m2 & 3) == 0);
        int u1a, q1a, u1b, q1b;
        if (cls2 != 0) { u1a = cls2; q1a = m2; u1b = 64 - cls2; q1b = (8 - m2) & 7; }
        else if (special) { u1a = 0; u1b = 32; q1a = q1b = m2; }
        else { u1a = u1b = (m2 < 4) ? 0 : 32; q1a = m2; q1b = 8 - m2; }
        int sA, vH0, sB, vH1;
        if (!special) { sA = 0; vH0 = rq; sB = 1; vH1 = (u1a == 0) ? ((4 - rq) & 3) : 3 - rq; }
        else { sA = sB = rq >> 1; vH0 = rq & 1; vH1 = (rq == 0) ? 2 : (rq == 1) ? 3 : (rq == 2) ? 3 : 2; }
        const bool q0 = (m2 == 0);
        const int dy = ((sA ? u1b : u1a) == 0 && vH0 == 0) ? 8 : 7, dq = q0 ? 8 : 7;
        int y1 = lane >> 3, t1 = lane & 7, y2 = (dy - y1) & 7, t2 = (dq - t1) & 7, hA = vH0, hB = vH1;
        bool self = false;
        if (special && rq == 0) {
            const int k = lane & 31;
            if (qb) { hA = hB = 2; y1 = k >> 3; t1 = k & 7; y2 = 7 - y1; t2 = (dq - t1) & 7; }
            else {
                hA = hB = 0; const int vx = k & 3, y = k >> 2;
                if (!q0 || vx) { y1 = y; t1 = vx; y2 = (8 - y) & 7; t2 = dq - vx; }
                else { t1 = t2 = (y >= 4) ? 4 : 0; const int yy = y & 3; if (yy) { y1 = yy; y2 = 8 - yy; } else { y1 = 0; y2 = 4; self = true; } }
            }
        }
        const int uA = (sA ? u1b : u1a) + 64 * (hA + 4 * y1), uB = (sB ? u1b : u1a) + 64 * (hB + 4 * y2);
        const int qA = (sA ? q1b : q1a) + 8 * t1, qB = (sB ? q1b : q1a) + 8 * t2;
        u32 wl[20];
        {
            const u32 vA = (u32)((uA * 257 + qA) * 2), vB = (u32)((uB * 257 + qB) * 2);
            sfor<4>([&](auto K) { ldg_u16<K * 128>(wl[K], vA, a.wr); ldg_u16<K * 128>(wl[8 + K], vA, a.wi); ldg_u16<K * 128>(wl[4 + K], vB, a.wr); ldg_u16<K * 128>(wl[12 + K], vB, a.wi); });
            const u32 vA2 = (u32)(uA * 514 + 512), vB2 = (u32)(uB * 514 + 512);
            ldg_u16<0>(wl[16], vA2, a.wr); ldg_u16<0>(wl[17], vA2, a.wi); ldg_u16<0>(wl[18], vB2, a.wr); ldg_u16<0>(wl[19], vB2, a.wi);
        }
        const int rowB = L_FFT + ((qb ? (vH1 * 2 + sB) : (vH0 * 2 + sA)) * 36 + (lane & 31)) * 8;
        const bool rot = q0 && (t1 == 0);
        const int p3a1 = L_FFT + (y1 * 290 + (hA * 2 + sA) * 36 + t1 * 4) * 8;
        const int p3a2 = L_FFT + (y2 * 290 + (hB * 2 + sB) * 36 + t2 * 4) * 8;
        const float thA = (float)qA * (1.0f / 512.0f), thB = (float)qB * (1.0f / 512.0f);
#pragma unroll
        for (int k = 1; k < 4; k++) tw0[k] = cis((float)r3 * ((float)k / 32.0f));
#pragma unroll
        for (int k = 1; k < 8; k++) tw1[k] = cis((float)c2p * ((float)k / 32.0f));
        f2 csq[8];
#pragma unroll
        for (int e = 0; e < 8; e++) csq[e] = cis(((e >> 2) ? thB : thA) + 0.125f * (float)(e & 3));
        asm volatile("" : "+v"(csq[0]), "+v"(csq[1]), "+v"(csq[2]), "+v"(csq[3]), "+v"(csq[4]), "+v"(csq[5]), "+v"(csq[6]), "+v"(csq[7]),
                     "+v"(tw0[1]), "+v"(tw0[2]), "+v"(tw0[3]), "+v"(tw1[1]), "+v"(tw1[2]), "+v"(tw1[3]), "+v"(tw1[4]), "+v"(tw1[5]), "+v"(tw1[6]), "+v"(tw1[7]));
        {
            const u32 o1 = min(rq * 22 + lane, 85) * 8, o3 = lane * 8;
            bool posted = false;
            _Pragma("unroll 1") for (int spins = 0; spins < SPIN_CAP; spins++) {
                u2 f1 = __builtin_amdgcn_raw_buffer_load_b64(rs, o1, WS_FP + t * 1024, 16);
                u2 f3 = __builtin_amdgcn_raw_buffer_load_b64(rs, o3, WS_FF1 + (wq2 * 4 + rq) * 512, 16);
                const bool ok = f1.x == tag.x && f1.y == tag.y && f3.x == tag.x && f3.y == tag.y;
                const bool all = __all(ok);
                if (!posted) { WAITVM(0); flag_store(WS_FHA + ((rg * 8 + w) * 16 + cg) * 8); posted = true; }
                if (all) break;
                __builtin_amdgcn_s_sleep(2);
                FENCE();
            }
            __builtin_amdgcn_fence(__ATOMIC_ACQUIRE, "wavefront");
            FENCE();
        }
        post(B_FP);
        STAMP(3);
        const int f1off = WS_F1 + sig * 16384 + 2 * rq * 512;
        u4 d[4];
        SCHED();
#pragma unroll
        for (int H = 0; H < 4; H++) d[H] = __builtin_amdgcn_raw_buffer_load_b128(rs, NOF(l16), f1off + H * 4096, 16);
        SCHED();
        asm volatile("" : "+v"(wl[0]), "+v"(wl[1]), "+v"(wl[2]), "+v"(wl[3]), "+v"(wl[4]), "+v"(wl[5]), "+v"(wl[6]), "+v"(wl[7]), "+v"(wl[8]), "+v"(wl[9]),
                     "+v"(wl[10]), "+v"(wl[11]), "+v"(wl[12]), "+v"(wl[13]), "+v"(wl[14]), "+v"(wl[15]), "+v"(wl[16]), "+v"(wl[17]), "+v"(wl[18]), "+v"(wl[19]) :: "memory");
        const f2 wS[2] = {f2{bflo(wl[16]), bflo(wl[17])}, f2{bflo(wl[18]), bflo(wl[19])}};
        {
            f2 W[8], WB[8];
#pragma unroll
            for (int e = 0; e < 8; e++) W[e] = f2{bflo(wl[e]), bflo(wl[8 + e])};
#pragma unroll
            for (int k = 0; k < 4; k++) { WB[k] = W[4 + 3 - k]; WB[4 + k] = W[3 - k]; }
            if (q0) {
#pragma unroll
                for (int k = 0; k < 4; k++) {
                    f2 n1 = rot ? W[(4 - k) & 3] : W[3 - k], n2 = rot ? W[4 + ((4 - k) & 3)] : W[4 + 3 - k];
                    WB[k] = self ? n1 : n2; WB[4 + k] = self ? n2 : n1;
                }
#pragma unroll
                for (int e = 0; e < 8; e += 4) {
                    f2 own = wS[e >> 2], oth = self ? own : wS[1 - (e >> 2)];
                    f2 S0 = pkfma(WB[e], kc.pm, W[e]) * 0.5f, S1c = pkfma(own, kc.pm, oth) * 0.5f;
                    WB[e] = rot ? S1c : WB[e];
                    al[e] = rot ? S0 : W[e];
                }
                W[0] = al[0]; W[4] = al[4];
            }
#pragma unroll
            for (int e = 0; e < 8; e++) {
                const f2 cs = csq[e];
                f2 P = pkfma(WB[e], kc.pm, W[e]), M = pkfma(WB[e], kc.pm.yx, W[e]);
                asm("v_pk_fma_f32 %0, %1, %2, %3 op_sel:[1,0,0] op_sel_hi:[1,1,1] neg_lo:[1,0,0] neg_hi:[1,0,0]" : "=v"(al[e]) : "v"(cs), "v"(M), "v"(P));
                asm("v_pk_mul_f32 %0, %1, %2 op_sel:[1,0] op_sel_hi:[0,0] neg_lo:[1,0]" : "=v"(be[e]) : "v"(M), "v"(cs));
            }
            asm volatile("" : "+v"(al[0]), "+v"(al[1]), "+v"(al[2]), "+v"(al[3]), "+v"(al[4]), "+v"(al[5]), "+v"(al[6]), "+v"(al[7]),
                         "+v"(be[0]), "+v"(be[1]), "+v"(be[2]), "+v"(be[3]), "+v"(be[4]), "+v"(be[5]), "+v"(be[6]), "+v"(be[7]),
                         "+v"(tw0[1]), "+v"(tw0[2]), "+v"(tw0[3]), "+v"(tw1[1]), "+v"(tw1[2]), "+v"(tw1[3]), "+v"(tw1[4]), "+v"(tw1[5]), "+v"(tw1[6]), "+v"(tw1[7]));
        }
        STAMP(4);
        wait_grp(0, A_S0);
        __builtin_amdgcn_s_setprio(2);
        f2 e0[4], e1[4];
#pragma unroll
        for (int H = 0; H < 4; H++) { e0[H] = f2{__uint_as_float(d[H].x), __uint_as_float(d[H].y)}; e1[H] = f2{__uint_as_float(d[H].z), __uint_as_float(d[H].w)}; }
        dft4<false>(e0[0], e0[1], e0[2], e0[3], kc); dft4<false>(e1[0], e1[1], e1[2], e1[3], kc);
        STAMP(6);
        LAS char* area = lds + L_FFT + rq * 4640;
#pragma unroll
        for (int k = 0; k < 4; k++) {
            if (k) { e0[k] = cmul<true>(e0[k], tw0[k]); e1[k] = cmul<true>(e1[k], tw0[k]); }
            *(LAS f4*)(area + (l3 * 72 + 2 * c2p + 8 * k + 32 * rb) * 8) = f4{e0[k].x, e0[k].y, e1[k].x, e1[k].y};
        }
        FENCE();
#pragma unroll
        for (int k = 0; k < 8; k++) v[k] = *(LAS f2*)(area + (k * 72 + lane) * 8);
        dft8<false>(v, kc);
#pragma unroll
        for (int k = 1; k < 8; k++) v[k] = cmul<true>(v[k], tw1[k]);
        FENCE();
        LAS char* pb = lds + L_FFT + (r3 * 290 + l3 * 36 + c2p) * 8;
#pragma unroll
        for (int k = 0; k < 8; k++) *(LAS f2*)(pb + k * 32) = v[k];
        post(B_E1);
        wait_grp(1, B_E1);
#pragma unroll
        for (int k = 0; k < 8; k++) v[k] = *(LAS f2*)(lds + rowB + k * 2320);
        dft8<false>(v, kc);
#pragma unroll
        for (int k = 0; k < 8; k++) *(LAS f2*)(lds + rowB + k * 2320) = v[k];
        FENCE();
        f4 a0 = *(LAS f4*)(lds + p3a1), a1 = *(LAS f4*)(lds + p3a1 + 16), b0 = *(LAS f4*)(lds + p3a2), b1 = *(LAS f4*)(lds + p3a2 + 16);
        f2 X[8] = {a0.xy, a0.zw, a1.xy, a1.zw, b0.xy, b0.zw, b1.xy, b1.zw};
        dft4<false>(X[0], X[1], X[2], X[3], kc); dft4<false>(X[4], X[5], X[6], X[7], kc);
        f2 Y[8];
#pragma unroll
        for (int k = 0; k < 4; k++) { Y[k] = X[4 + 3 - k]; Y[4 + k] = X[3 - k]; }
        if (q0) {
#pragma unroll
            for (int k = 0; k < 4; k++) {
                f2 n1 = rot ? X[(4 - k) & 3] : X[3 - k], n2 = rot ? X[4 + ((4 - k) & 3)] : X[4 + 3 - k];
                Y[k] = self ? n1 : n2; Y[4 + k] = self ? n2 : n1;
            }
        }
        f2 O[8];
#pragma unroll
        for (int e = 0; e < 8; e++) O[e] = cfmac_conj(cmul<false>(X[e], al[e]), be[e], Y[e]);
        dft4<true>(O[0], O[1], O[2], O[3], kc); dft4<true>(O[4], O[5], O[6], O[7], kc);
        *(LAS f4*)(lds + p3a1) = f4{O[0].x, O[0].y, O[1].x, O[1].y}; *(LAS f4*)(lds + p3a1 + 16) = f4{O[2].x, O[2].y, O[3].x, O[3].y};
        *(LAS f4*)(lds + p3a2) = f4{O[4].x, O[4].y, O[5].x, O[5].y}; *(LAS f4*)(lds + p3a2 + 16) = f4{O[6].x, O[6].y, O[7].x, O[7].y};
        FENCE();
#pragma unroll
        for (int k = 0; k < 8; k++) v[k] = *(LAS f2*)(lds + rowB + k * 2320);
        dft8<true>(v, kc);
#pragma unroll
        for (int k = 0; k < 8; k++) *(LAS f2*)(lds + rowB + k * 2320) = v[k];
        post(B_E2);
        wait_grp(1, B_E2);
#pragma unroll
        for (int k = 0; k < 8; k++) v[k] = *(LAS f2*)(pb + k * 32);
#pragma unroll
        for (int k = 1; k < 8; k++) v[k] = cmul<false>(v[k], tw1[k]);
        dft8<true>(v, kc);
        FENCE();
#pragma unroll
        for (int k = 0; k < 8; k++) *(LAS f2*)(area + (k * 72 + lane) * 8) = v[k];
        FENCE();
#pragma unroll
        for (int k = 0; k < 4; k++) {
            f4 q = *(LAS f4*)(area + (l3 * 72 + 2 * c2p + 8 * k + 32 * rb) * 8);
            e0[k] = q.xy; e1[k] = q.zw;
            if (k) { e0[k] = cmul<false>(e0[k], tw0[k]); e1[k] = cmul<false>(e1[k], tw0[k]); }
        }
        dft4<true>(e0[0], e0[1], e0[2], e0[3], kc); dft4<true>(e1[0], e1[1], e1[2], e1[3], kc);
        const int f2st = WS_F2 + (r3 * 8 + l3) * 16384 + pos1 * 64 + c2p * 16;
#pragma unroll
        for (int H = 0; H < 4; H++) {
            u4 o = {__float_as_uint(e0[H].x), __float_as_uint(e0[H].y), __float_as_uint(e1[H].x), __float_as_uint(e1[H].y)};
            st128(o, rs, NOF(f2st), H * (64 * 16384));
        }
        STAMP(7);
        __builtin_amdgcn_s_setprio(0);
        WAITVM(0);
        flag_store(WS_FF2 + (rq * 256 + pos1) * 8);
    }
    STAMP(8);
    RELANE();
    f4 acc2[4];
#pragma unroll
    for (int j = 0; j < 4; j++) acc2[j] = f4{0, 0, 0, 0};
    const int hsrc = hoff0 + hf * 1024;
    auto g2_mma = [&](auto NC, const u4* hfr, int ldsbase) {
        constexpr int N = decltype(NC)::value;
        u32 pa = (u32)ldsbase + l16; asm volatile("" : "+v"(pa));
        LAS u4* p = (LAS u4*)(lds + pa);
        u4 F[2][4];
#pragma unroll
        for (int j = 0; j < 4; j++) F[0][j] = p[j * 64];
        sfor<N>([&](auto I) {
            constexpr int i = I;
            SCHED();
            if (i + 1 < N) {
#pragma unroll
                for (int j = 0; j < 4; j++) F[(i + 1) & 1][j] = p[(i + 1) * 256 + j * 64];
            }
#pragma unroll
            for (int j = 0; j < 4; j++) acc2[j] = mfma(F[i & 1][j], hfr[i], acc2[j]);
            if (i + 1 < N) {
#pragma unroll
                for (int j = 0; j < 4; j++) { SGB(0x100, 1); SGB(0x8, 1); }
            }
            SCHED();
        });
    };
    u4 hfr[16];
    flag_wait(li * 8, WS_FHA + (rg * 8 + w) * 128);
    if (hf == 0) post(A_D1);
#pragma unroll
    for (int i = 0; i < 16; i++) { hfr[i] = __builtin_amdgcn_raw_buffer_load_b128(rs, l16, hsrc + 2 * ((i + rot) & 15) * 1024, 16); SCHED(); }
    STAMP(9);
    wait_grp(0, A_D1);
    g2_mma(IC<16>{}, hfr, hf ? 0 : L_SET1);
    post(hf ? B_G1 : A_G1);
    pin4(acc2);
    STAMP(10);
    if (hf == 0) {
        wait_grp(0, A_G1);
        sfor<3>([&](auto I) { constexpr int i = I; if (i < 2 || rq < 3) dma4k_c<L_SET1 + i * 16384, (32 + 4 * i) * 4096>(l16, wdp + rq * 4096, (u32)(rq * 4096)); });
        flag_wait(min(li, 10) * 8, WS_FHB + (rg * 4 + rq) * 128);
        post(A_D3);
#pragma unroll
        for (int i = 0; i < 11; i++) { hfr[i] = __builtin_amdgcn_raw_buffer_load_b128(rs, l16, hoff0 + (32 + i) * 1024, 16); SCHED(); }
        STAMP(13);
        wait_grp(0, A_D3);
        g2_mma(IC<11>{}, hfr, L_SET1);
    } else {
        wait_grp(1, B_G1);
        fft13_consts(0.5f / 524288.0f);
        flag_wait(lane * 8, WS_FF2 + (((s2 & 7) >> 1) * 256 + rq * 64) * 8);
        u4 d[4]; f2 v[8];
        const int off = WS_F2 + pid * 16384 + rq * 4096;
#pragma unroll
        for (int i = 0; i < 4; i++) d[i] = __builtin_amdgcn_raw_buffer_load_b128(rs, NOF(l16), off + i * 1024, 16);
        STAMP(13);
        __builtin_amdgcn_s_setprio(2);
#pragma unroll
        for (int i = 0; i < 4; i++) *(LAS u4*)(lds + L_FFT + rq * 4608 + (i * 8 + (lane >> 3)) * 144 + (lane & 7) * 16) = d[i];
        FENCE();
        v[0] = *(LAS f2*)(lds + x3a[0]);
        v[1] = *(LAS f2*)(lds + x3a[1]); v[2] = *(LAS f2*)(lds + x3a[1] + 1152); v[3] = *(LAS f2*)(lds + x3a[1] + 2304);
        v[4] = *(LAS f2*)(lds + x3a[2]);
        v[5] = *(LAS f2*)(lds + x3a[3] + 2304); v[6] = *(LAS f2*)(lds + x3a[3] + 1152); v[7] = *(LAS f2*)(lds + x3a[3]);
#pragma unroll
        for (int k = 0; k < 8; k++) v[k] = cmul<false>(v[k], twT[k]);
        dft8<true>(v, kc);
#pragma unroll
        for (int k = 1; k < 8; k++) v[k] = cmul<false>(v[k], tw64[k]);
        FENCE();
        LAS f2* reg = (LAS f2*)(lds + L_FFT) + (rq * 2 + qb) * 288;
#pragma unroll
        for (int k = 0; k < 8; k++) reg[36 * l3 + 4 * k + c2p] = v[k];
        FENCE();
#pragma unroll
        for (int k = 0; k < 8; k++) v[k] = reg[36 * k + (lane & 31)];
        dft8<true>(v, kc);
        FENCE();
#pragma unroll
        for (int k = 0; k < 8; k++) reg[(lane & 31) + 32 * k] = v[k];
        post(B_F3);
        wait_grp(1, B_F3);
        if (b == 0 && rq == 0) __builtin_amdgcn_raw_buffer_store_b64(*(LAS u2*)(lds + L_TAG + 8), rs, lane ? WS_BYTES : 0, 0, 16);
        LAS f2* xb1 = (LAS f2*)(lds + L_FFT) + rq * 64 + lane;
#pragma unroll
        for (int q1 = 0; q1 < 8; q1++) { const int rg_ = (q1 < 4) ? q1 * 2 : (((8 - q1) & 3) * 2 + 1); v[q1] = xb1[rg_ * 288]; }
        dft8<true>(v, kc);
#pragma unroll
        for (int c1 = 0; c1 < 8; c1++) *(LAS u32*)(lds + L_S + (16 * rq + (lane >> 2)) * 144 + c1 * 16 + c2p * 4) = pkbf(v[c1]);
    }
    STAMP(11);
    BAR();
    {
        LAS f4* xs = (LAS f4*)(lds + w * 2048) + lane;
        LAS f4* xr = (LAS f4*)(lds + (w ^ 4) * 2048) + lane;
        f4 me, mo;
        if (hf) { xs[0] = acc2[0]; xs[64] = acc2[1]; me = acc2[2]; mo = acc2[3]; } else { xs[0] = acc2[2]; xs[64] = acc2[3]; me = acc2[0]; mo = acc2[1]; }
        BAR();
        const int c1 = 4 * hf + lg;
        me += xr[0]; mo += xr[64];
        const u4 sv = *(LAS u4*)(lds + L_S + (16 * rq + li) * 144 + c1 * 16);
        const u4 xv = *(LAS u4*)(lds + ((rq >> 1) ? L_X1 : L_X0) + ((rq & 1) * 16 + li) * 128 + ((c1 ^ (li & 7)) * 16));
        u32 res[4];
#pragma unroll
        for (int e = 0; e < 4; e++) {
            const f2 ov = bfup(pkbf(f2{me[e], mo[e]}));
            const f2 z = ov * -1.44269504f;
            const f2 d = f2{__builtin_amdgcn_exp2f(z.x), __builtin_amdgcn_exp2f(z.y)} + f2{1.f, 1.f};
            const f2 g = bfup(pkbf(ov * f2{__builtin_amdgcn_rcpf(d.x), __builtin_amdgcn_rcpf(d.y)}));
            res[e] = pkbf(bfup(xv[e]) + bfup(pkbf(bfup(sv[e]) * g)));
        }
        *(u4*)(a.out + (size_t)(s2 + 32 * (16 * rq + li)) * 512 + 64 * c1 + 8 * t) = u4{res[0], res[1], res[2], res[3]};
    }
    STAMP(12);
}

extern "C" void kernel_launch(void* const* d_in, const int* in_sizes, int n_in,
                              void* d_out, int out_size, void* d_ws, size_t ws_size,
                              hipStream_t stream) {
    Args a;
    a.x = (const u16*)d_in[0]; a.wr = (const u16*)d_in[1]; a.wi = (const u16*)d_in[2];
    a.wg = (const u16*)d_in[4]; a.wu = (const u16*)d_in[5]; a.wd = (const u16*)d_in[6];
    a.out = (u16*)d_out; a.ws = (unsigned char*)d_ws;
    (void)hipFuncSetAttribute((const void*)fused, hipFuncAttributeMaxDynamicSharedMemorySize, L_END);
    fused<<<256, 512, L_END, stream>>>(a);
}
```

```cpp
#include <hip/hip_runtime.h>
#include <stdint.h>
#include <type_traits>

typedef unsigned short u16;
typedef unsigned u32;
typedef unsigned long long u64;
typedef float f2 __attribute__((ext_vector_type(2)));
typedef float f4 __attribute__((ext_vector_type(4)));
typedef u32 u4 __attribute__((ext_vector_type(4)));
typedef u32 u2 __attribute__((ext_vector_type(2)));
typedef short s4 __attribute__((ext_vector_type(4)));
typedef __bf16 bf8 __attribute__((ext_vector_type(8)));
typedef __bf16 bf2 __attribute__((ext_vector_type(2)));
#define LAS __attribute__((address_space(3)))
#define GAS __attribute__((address_space(1)))
#define DEV __device__ __forceinline__
template <int N> using IC = std::integral_constant<int, N>;

constexpr int NFB = 43;
constexpr int WS_FF1 = 4096;
constexpr int WS_FF2 = WS_FF1 + 8192;
constexpr int WS_FHA = WS_FF2 + 8192;
constexpr int WS_FHB = WS_FHA + 16384;
constexpr int WS_FP = WS_FHB + 16384;
constexpr int WS_FW = WS_FP + 8192;
constexpr int WS_F1 = 1 << 20, WS_F2 = WS_F1 + (4 << 20), WS_H = WS_F2 + (4 << 20);
constexpr int WS_WDP = WS_H + 32 * 4 * NFB * 1024;
constexpr int WS_WP = WS_WDP + 8 * NFB * 4 * 1024;
constexpr int WS_STAMP = WS_WP + (2 << 20);
constexpr int WS_BYTES = WS_STAMP + (1 << 20);
constexpr int PG = 8192;
constexpr int L_SET1 = 8 * PG;
constexpr int L_FFT = 16 * PG;
constexpr int L_SMALL = 19 * PG;
constexpr int L_PROG = L_SMALL;
constexpr int L_TAG = L_SMALL + 32;
constexpr int L_STAT = L_SMALL + 64;
constexpr int L_CS = L_SMALL + 2048;
constexpr int L_X0 = L_FFT + 18688, L_X1 = L_SMALL + 3072;
constexpr int L_S = L_FFT;
constexpr int L_END = 20 * PG;
constexpr u32 SENT = 0xFFFFFFFFu;
constexpr int CODE_BYTES = 44 * 1024;
constexpr int SPIN_CAP = 1 << 16;

struct Args { const u16* x; const u16* wr; const u16* wi; const u16* wg; const u16* wu; const u16* wd; u16* out; unsigned char* ws; };

#define BAR() asm volatile("s_waitcnt lgkmcnt(0)\n\ts_barrier" ::: "memory")
#define WAITVM(n) asm volatile("s_waitcnt vmcnt(" #n ")" ::: "memory")
#define NOF(x) (x)
#define NOPL(x) (x)
#define FENCE() asm volatile("" ::: "memory")
#define SCHED() __builtin_amdgcn_sched_barrier(0)
#define SGB(mask, n) __builtin_amdgcn_sched_group_barrier(mask, n, 0)
#define STAMP(i)
#define CSTAMP(i)

DEV f2 pkfma(f2 a, f2 b, f2 c) { return __builtin_elementwise_fma(a, b, c); }
struct KC { f2 pm, rpm; };
DEV f2 addmi(f2 a, f2 b, const KC& k) { return pkfma(b.yx, k.pm, a); }
DEV f2 addpi(f2 a, f2 b, const KC& k) { return pkfma(b.yx, k.pm.yx, a); }
template <bool INV> DEV void dft4(f2& a0, f2& a1, f2& a2, f2& a3, const KC& k) {
    f2 t0 = a0 + a2, t1 = a0 - a2, t2 = a1 + a3, t3 = a1 - a3;
    a0 = t0 + t2; a2 = t0 - t2;
    if (INV) { a1 = addpi(t1, t3, k); a3 = addmi(t1, t3, k); } else { a1 = addmi(t1, t3, k); a3 = addpi(t1, t3, k); }
}
template <bool INV> DEV void dft8(f2 (&v)[8], const KC& k) {
    f2 a0 = v[0] + v[4], a4 = v[0] - v[4], a1 = v[1] + v[5], a5 = v[1] - v[5];
    f2 a2 = v[2] + v[6], a6 = v[2] - v[6], a3 = v[3] + v[7], a7 = v[3] - v[7];
    dft4<INV>(a0, a1, a2, a3, k);
    v[0] = a0; v[2] = a1; v[4] = a2; v[6] = a3;
    f2 b1, b3, t0, t1;
    if (INV) { b1 = addpi(a5, a5, k); b3 = addpi(-a7, a7, k); t0 = addpi(a4, a6, k); t1 = addmi(a4, a6, k); }
    else { b1 = addmi(a5, a5, k); b3 = addmi(-a7, a7, k); t0 = addmi(a4, a6, k); t1 = addpi(a4, a6, k); }
    f2 t2 = b1 + b3, t3 = b1 - b3;
    v[1] = pkfma(t2, k.rpm.xx, t0); v[5] = pkfma(t2, k.rpm.yy, t0);
    if (INV) { v[3] = pkfma(t3.yx, k.rpm.yx, t1); v[7] = pkfma(t3.yx, k.rpm, t1); }
    else { v[3] = pkfma(t3.yx, k.rpm, t1); v[7] = pkfma(t3.yx, k.rpm.yx, t1); }
}
template <bool CONJ> DEV f2 cmul(f2 a, f2 w) {
    f2 r;
    if (CONJ) asm("v_pk_mul_f32 %0, %1, %2 op_sel:[1,1] op_sel_hi:[0,1]\n\tv_pk_fma_f32 %0, %1, %2, %0 op_sel_hi:[1,0,1] neg_hi:[0,0,1]" : "=&v"(r) : "v"(a), "v"(w));
    else asm("v_pk_mul_f32 %0, %1, %2 op_sel:[1,1] op_sel_hi:[0,1]\n\tv_pk_fma_f32 %0, %1, %2, %0 op_sel_hi:[1,0,1] neg_lo:[0,0,1]" : "=&v"(r) : "v"(a), "v"(w));
    return r;
}
DEV f2 cfmac_conj(f2 t, f2 beta, f2 B) {
    asm("v_pk_fma_f32 %0, %2, %1, %0 op_sel_hi:[0,1,1]\n\tv_pk_fma_f32 %0, %2, %1, %0 op_sel:[1,1,0] op_sel_hi:[1,0,1] neg_hi:[0,1,0]" : "+v"(t) : "v"(beta), "v"(B));
    return t;
}
DEV f2 cis(float r) { return f2{__builtin_amdgcn_cosf(r), __builtin_amdgcn_sinf(r)}; }
DEV float bflo(u32 v) { return __uint_as_float(v << 16); }
DEV float bfhi(u32 v) { return __uint_as_float(v & 0xFFFF0000u); }
DEV f2 bfup(u32 v) { return f2{bflo(v), bfhi(v)}; }
DEV u32 pkbf(f2 v) { bf2 r; r.x = (__bf16)v.x; r.y = (__bf16)v.y; u32 o = __builtin_bit_cast(u32, r); asm("" : "+v"(o)); return o; }
DEV float rbf(float f) { return __uint_as_float(((u32)__builtin_bit_cast(u16, (__bf16)f)) << 16); }
DEV f4 mfma(u4 a, u4 b, f4 c) { return __builtin_amdgcn_mfma_f32_16x16x32_bf16(__builtin_bit_cast(bf8, a), __builtin_bit_cast(bf8, b), c, 0, 0, 0); }
DEV u4 sanitize(u4 v) { return u4{min(v.x, 0xFFFFFFFEu), min(v.y, 0xFFFFFFFEu), min(v.z, 0xFFFFFFFEu), min(v.w, 0xFFFFFFFEu)}; }
DEV u32 max4(u4 v) { return max(max(v.x, v.y), max(v.z, v.w)); }
DEV void st128(u4 v, __amdgpu_buffer_rsrc_t rs, u32 voff, int soff) {
    __builtin_amdgcn_raw_buffer_store_b128(v, rs, voff, soff, 16);
    asm volatile("s_nop 1" :: "v"(v) : "memory");
}
template <int OFF2> DEV u4 trfrag2(LAS char* p) {
    s4 r0 = __builtin_amdgcn_ds_read_tr16_b64_v4i16((LAS s4*)p);
    s4 r1 = __builtin_amdgcn_ds_read_tr16_b64_v4i16((LAS s4*)(p + OFF2));
    u2 x0 = __builtin_bit_cast(u2, r0), x1 = __builtin_bit_cast(u2, r1);
    return u4{x0.x, x0.y, x1.x, x1.y};
}
template <int LI, int GI> DEV void dma_add(u32 vb, const void* sbase, u32 sl) {
    const u32 va = vb + (u32)GI;
    asm volatile("s_add_u32 m0, %2, %3\n\ts_nop 0\n\tglobal_load_lds_dwordx4 %0, %1" :: "v"(va), "s"(sbase), "s"(sl), "i"(LI) : "memory", "scc");
}
template <int LI, int GI> DEV void dma4k_c(u32 vb, const void* sbase, u32 sl) {
    const u32 va = vb + (u32)GI;
    asm volatile("s_add_u32 m0, %2, %3\n\ts_nop 0\n\tglobal_load_lds_dwordx4 %0, %1 sc1\n\tglobal_load_lds_dwordx4 %0, %1 offset:1024 sc1\n\t"
                 "global_load_lds_dwordx4 %0, %1 offset:2048 sc1\n\tglobal_load_lds_dwordx4 %0, %1 offset:3072 sc1"
                 :: "v"(va), "s"(sbase), "s"(sl), "i"(LI) : "memory", "scc");
}
template <int OFF> DEV void ldg_u16(u32& d, u32 vo, const void* sb) { asm volatile("global_load_ushort %0, %1, %2 offset:%3" : "=v"(d) : "v"(vo), "s"(sb), "i"(OFF) : "memory"); }
template <int OFF> DEV void ldg_u32(u32& d, u32 vo, const void* sb) { asm volatile("global_load_dword %0, %1, %2 offset:%3" : "=v"(d) : "v"(vo), "s"(sb), "i"(OFF) : "memory"); }
DEV void lds_wait(u32 addr, u32 v) {
    u32 tmp, cnt;
    asm volatile("s_mov_b32 %1, 0x80000\n"
                 "1:\n\tds_read_b32 %0, %2\n\ts_sub_u32 %1, %1, 1\n\ts_waitcnt lgkmcnt(0)\n\ts_cbranch_scc1 2f\n\tv_cmp_gt_u32 vcc, %3, %0\n\ts_cbranch_vccnz 1b\n2:"
                 : "=&v"(tmp), "=&s"(cnt) : "v"(addr), "s"(v) : "memory", "vcc", "scc");
}
template <int N, typename F> DEV void sfor(F&& f) { if constexpr (N > 0) { sfor<N - 1>(f); f(IC<N - 1>{}); } }

DEV void pin9(f4 (&x)[4], f4 (&y)[4], f4& z) {
    asm volatile("" : "+v"(x[0]), "+v"(x[1]), "+v"(x[2]), "+v"(x[3]), "+v"(y[0]), "+v"(y[1]), "+v"(y[2]), "+v"(y[3]), "+v"(z));
}
DEV void pin4(f4 (&x)[4]) { asm volatile("" : "+v"(x[0]), "+v"(x[1]), "+v"(x[2]), "+v"(x[3])); }
DEV void dma_rt(u32 voff, const void* sbase, u32 sl) {
    asm volatile("s_mov_b32 m0, %2\n\ts_nop 0\n\tglobal_load_lds_dwordx4 %0, %1" :: "v"(voff), "s"(sbase), "s"(sl) : "memory");
}
DEV u4 geglu8(const f4 (&acc)[4], LAS char* cs, f2 st) {
    u32 hw[4];
    const f2 nm = f2{-st.x, -st.x}, ri = f2{st.y, st.y};
#pragma unroll
    for (int c = 0; c < 2; c++) {
        f4 cg_ = *(LAS f4*)(cs + c * 64), cu_ = *(LAS f4*)(cs + (2 + c) * 64);
#pragma unroll
        for (int e = 0; e < 2; e++) {
            f2 g = pkfma(e ? cg_.zw : cg_.xy, nm, e ? acc[c].zw : acc[c].xy) * ri;
            f2 u = pkfma(e ? cu_.zw : cu_.xy, nm, e ? acc[2 + c].zw : acc[2 + c].xy) * ri;
            f2 z = g * pkfma(g * g, f2{-0.10294324f, -0.10294324f}, f2{-2.3022082f, -2.3022082f});
            f2 d = f2{__builtin_amdgcn_exp2f(z.x), __builtin_amdgcn_exp2f(z.y)} + f2{1.f, 1.f};
            f2 r = f2{__builtin_amdgcn_rcpf(d.x), __builtin_amdgcn_rcpf(d.y)};
            hw[2 * c + e] = pkbf(g * r * u);
        }
    }
    return u4{hw[0], hw[1], hw[2], hw[3]};
}

enum { A_XL = 1, A_XC, A_P1, A_XB, A_S0, A_FP, A_E1, A_E2, A_CS, A_F3, A_F4, A_D1 };
enum { B_W0 = 1, B_W1, B_W2, B_W3, B_CS, B_WB, B_PB, B_D1, B_G1, B_D3 };

__global__ void __launch_bounds__(512, 2) fused(Args a) {
    extern __shared__ __attribute__((aligned(16))) char smem[];
    LAS char* lds = (LAS char*)smem;
    const int tid = threadIdx.x, w = __builtin_amdgcn_readfirstlane(tid >> 6);
    int lane = tid & 63;
    const int b = blockIdx.x, xcd = b & 7, r = b >> 3, t = r & 7, par = xcd & 1, rg = (xcd >> 1) * 4 + (r >> 3);
    const int s2 = 2 * rg + par, cg = par * 8 + t, sig = xcd * 32 + r, pid = s2 * 8 + t;
    const int rq = w & 3, hf = w >> 2, j4 = r >> 3;
#define BE(l) ((((l) >> 3) << 1) | ((l) & 1))
#define IX(l) (((l) >> 1) & 3)
    u32 l16 = lane * 16;
    u32 cpf = 0;
    if (w == 7) {
        const u64 pc = __builtin_amdgcn_s_getpc() & ~(u64)255;
        const u32 off = min((u32)((t * 64 + lane) * 128), (u32)(CODE_BYTES - 128));
        asm volatile("global_load_dword %0, %1, %2" : "=v"(cpf) : "v"(off), "s"(pc) : "memory");
    }
    *(volatile LAS u32*)(lds + L_PROG + w * 4) = 0;
    BAR();
    STAMP(0);
    auto dma_x = [&]() {
        const char* xs = (const char*)a.x + (size_t)((s2 ^ hf) + 512 * rq) * 1024;
#pragma unroll
        for (int i = 0; i < 16; i++) { const int row = (i + 2 * t) & 15; dma_rt(l16 ^ (u32)(row * (32768 + 16)), xs, (u32)(L_SET1 + rq * 16384 + row * 1024)); }
    };
    if (hf == 0) dma_x();
    int li, lg, c2p, l3, qb; u32 vpg;
#define RELANE() do { asm volatile("" : "+v"(lane)); l16 = lane * 16; li = lane & 15; lg = lane >> 4; c2p = lane & 3; l3 = (lane >> 2) & 7; qb = lane >> 5; vpg = (u32)(L_PROG + (lane & 3) * 4); } while (0)
    const __amdgpu_buffer_rsrc_t rs = __builtin_amdgcn_make_buffer_rsrc(a.ws, 0, WS_BYTES, 0x00020000);
    KC kc; kc.pm = f2{1.f, -1.f}; kc.rpm = f2{0.70710678118654752f, -0.70710678118654752f};
    asm volatile("" : "+s"(kc.pm), "+s"(kc.rpm));
    const u4 ones = {0x3F803F80u, 0x3F803F80u, 0x3F803F80u, 0x3F803F80u};
    auto post = [&](u32 v) { asm volatile("s_waitcnt lgkmcnt(0)" ::: "memory"); *(volatile LAS u32*)(lds + L_PROG + w * 4) = v; FENCE(); };
    auto wait_grp = [&](int g, u32 v) { lds_wait(vpg + g * 16, v); };
    auto wait_pair = [&](u32 v) { lds_wait((u32)(L_PROG + (w ^ 4) * 4), v); };
    u2 tag;
    auto get_tag = [&]() { u2 tg = *(LAS u2*)(lds + L_TAG); tag.x = __builtin_amdgcn_readfirstlane(tg.x); tag.y = __builtin_amdgcn_readfirstlane(tg.y); };
    auto flag_store = [&](int off) { __builtin_amdgcn_raw_buffer_store_b64(tag, rs, lane ? WS_BYTES : 0, off, 16); };
    auto flag_wait = [&](u32 voff, int soff) {
        _Pragma("unroll 1") for (int spins = 0; spins < SPIN_CAP; spins++) {
            u2 f = __builtin_amdgcn_raw_buffer_load_b64(rs, voff, soff, 16);
            if (__all(f.x == tag.x && f.y == tag.y)) break;
            __builtin_amdgcn_s_sleep(4);
            FENCE();
        }
        __builtin_amdgcn_fence(__ATOMIC_ACQUIRE, "wavefront");
        FENCE();
    };
    auto read_tile = [&](u4 (&x)[16], int tile) {
        LAS char* p = lds + L_SET1 + (16 * rq + li) * 1024;
#pragma unroll
        for (int k = 0; k < 16; k++) x[k] = *(LAS u4*)(p + (((4 * IX(k) + lg) ^ li) * 16) + ((BE(k) + j4) & 3) * 256);
    };
    auto stats = [&](const u4 (&x)[16]) {
        f4 s1 = {0, 0, 0, 0}, sq = s1, s1b = s1, sqb = s1;
#pragma unroll
        for (int k = 0; k < 16; k += 2) { s1 = mfma(ones, x[k], s1); sq = mfma(x[k], x[k], sq); s1b = mfma(ones, x[k + 1], s1b); sqb = mfma(x[k + 1], x[k + 1], sqb); }
        sq += sqb;
        const int rr = li & 3;
        const float d = rr == 0 ? sq.x : rr == 1 ? sq.y : rr == 2 ? sq.z : sq.w;
        if (lg == (li >> 2)) {
            const float mean = (s1.x + s1b.x) * (1.0f / 512.0f);
            float var = d * (1.0f / 512.0f) - mean * mean;
            if (!(mean * mean <= 1024.0f * var)) {
                const u32* xrow = (const u32*)(a.x + (size_t)((s2 ^ hf) + 32 * (16 * rq + li)) * 512);
                float q = 0.f;
                _Pragma("unroll 1") for (int i = 0; i < 256; i++) { u32 v = xrow[i]; float d0 = bflo(v) - mean, d1 = bfhi(v) - mean; q += d0 * d0; q += d1 * d1; }
                var = q * (1.0f / 512.0f);
            }
            const float den = rbf(__builtin_amdgcn_sqrtf(rbf(rbf(var) + 1e-5f)));
            *(LAS f2*)(lds + L_STAT + (hf * 64 + 16 * rq + li) * 8) = f2{rbf(mean), __builtin_amdgcn_rcpf(den)};
        }
        FENCE();
    };
    const int nblk = (t < 6 - par) ? 3 : 2, fb2 = 32 + par * 6 + t;
    const char* wdp = (const char*)a.ws + WS_WDP + (size_t)(t * NFB) * 4096;

    f2 tw64[8], twT[8];
    int x3a[4];
    auto fft13_consts = [&](float scale) {
        int lane_ = lane; asm volatile("" : "+v"(lane_));
        const int c2p = lane_ & 3, l3 = (lane_ >> 2) & 7, qb = lane_ >> 5;
        const int wq = rq, q1 = qb ? (wq == 0 ? 4 : 8 - wq) : wq, c2 = 4 * t + c2p;
        const float f64 = (float)l3 * (1.0f / 64.0f);
#pragma unroll
        for (int k = 1; k < 8; k++) tw64[k] = cis(f64 * (float)k);
        const float fT = (float)(8 * c2 * q1 + s2 * l3) * (1.0f / 2048.0f), dT = (float)s2 * (1.0f / 256.0f);
#pragma unroll
        for (int k = 0; k < 8; k++) twT[k] = cis(fT + dT * (float)k) * scale;
#pragma unroll
        for (int k = 0; k < 4; k++) {
            const int ua = (k >> 1) * 4 + (k & 1), u1 = l3 + 8 * ua;
            const int cls = (u1 < 32) ? u1 : ((64 - u1) & 31), hi = u1 >> 5;
            int mbit, sub;
            if (wq == 0) { mbit = qb; sub = hi; } else if (cls != 0) { sub = hi; mbit = qb ^ hi; } else { mbit = hi; sub = qb ^ hi; }
            x3a[k] = L_FFT + wq * 4608 + (cls * 18 + mbit * 8 + sub * 4 + c2p) * 8 - (k == 3 ? 2304 : 0);
        }
        asm volatile("" : "+v"(tw64[1]), "+v"(tw64[2]), "+v"(tw64[3]), "+v"(tw64[4]), "+v"(tw64[5]), "+v"(tw64[6]), "+v"(tw64[7]),
                     "+v"(twT[0]), "+v"(twT[1]), "+v"(twT[2]), "+v"(twT[3]), "+v"(twT[4]), "+v"(twT[5]), "+v"(twT[6]), "+v"(twT[7]),
                     "+v"(x3a[0]), "+v"(x3a[1]), "+v"(x3a[2]), "+v"(x3a[3]));
    };

    const int hoff0 = WS_H + ((s2 * 4 + rq) * NFB) * 1024, hoff1 = WS_H + (((s2 ^ 1) * 4 + rq) * NFB) * 1024;
    RELANE();
    const char* wgs = (const char*)a.wg + (size_t)(32 * rq) * 2752 + cg * 128;
    const char* wus = (const char*)a.wu + (size_t)(32 * rq) * 2752 + cg * 128;
    const u32 wvE = (u32)((lane >> 3) * 2752 + (((lane & 7) ^ (((lane >> 4) & 1) << 1)) * 16)), wvO = (u32)((lane >> 3) * 2752 + (((lane & 7) ^ (((lane >> 4) & 1) << 1) ^ 4) * 16));
    auto dma_w1 = [&](auto KK, auto I) {
        constexpr int kk = KK, g = I >> 1, mat = I & 1;
        dma_add<kk * PG + mat * 4096 + g * 1024, g * 8 * 2752>((g & 1) ? wvO : wvE, (mat ? wus : wgs) + ((BE(kk) + j4) & 3) * (128 * 2752), (u32)(rq * 2 * PG));
    };
    const int rot = (2 * t + 4 * j4) & 15;
    auto dma_wd = [&](auto LI, auto I, int p0) { dma4k_c<LI + I * 16384, 0>(l16, wdp + (size_t)(p0 + 2 * ((rq + 4 * I + rot) & 15)) * 4096, (u32)(rq * 4096)); };

    auto gemm1_half = [&](auto S0C, const u4 (&xa)[16], const u4 (&xb)[16], f4 (&accA)[4], f4 (&accB)[4], f4& acs, auto&& hook) {
        constexpr int S0 = S0C;
        const u32 fz = (u32)((li >> 3) | ((lg & 1) << 1));
        const u32 tb = (u32)((8 * lg + (li >> 2)) * 128 + 8 * (li & 3)) + S0 * PG;
        u32 p0 = tb + (((2 * hf) ^ fz) << 5), p1 = tb + (((2 * hf + 1) ^ fz) << 5), pc = tb + (((2 * hf + (rq & 1)) ^ fz) << 5) + (rq >> 1) * 4096;
        asm volatile("" : "+v"(p0), "+v"(p1), "+v"(pc));
        u4 F[2][5];
        auto ldf = [&](int ks, u4 (&f)[5]) {
            const int off = (ks & 7) * PG;
            f[0] = trfrag2<512>(lds + p0 + off); f[1] = trfrag2<512>(lds + p1 + off); f[2] = trfrag2<512>(lds + p0 + off + 4096); f[3] = trfrag2<512>(lds + p1 + off + 4096); f[4] = trfrag2<512>(lds + pc + off);
        };
        ldf(0, F[0]);
        sfor<8>([&](auto S) {
            constexpr int s = S, ks = S0 | ((s & 3) << 1) | ((s >> 2) & 1), sn = s + 1, kn = ((sn & 3) << 1) | ((sn >> 2) & 1);
            hook(S);
            SCHED();
            if (s + 1 < 8) ldf(kn, F[(s + 1) & 1]);
            u4 (&f)[5] = F[s & 1];
#pragma unroll
            for (int j = 0; j < 4; j++) { accA[j] = mfma(f[j], xa[ks], accA[j]); accB[j] = mfma(f[j], xb[ks], accB[j]); }
            acs = mfma(f[4], ones, acs);
            if (s + 1 < 8) {
                SGB(0x100, 2);
#pragma unroll
                for (int i = 0; i < 8; i++) { SGB(0x8, 1); SGB(0x100, 1); }
                SGB(0x8, 1);
            }
            SCHED();
        });
    };
    f2 st0, st1;
    auto gemm1_epi = [&](f4 (&accA)[4], f4 (&accB)[4], f4 acs, u32 step) {
        if (li == 0) *(LAS f4*)(lds + L_CS + (hf * 4 + rq) * 64 + lg * 16) = acs;
        post(step);
        wait_grp(hf, step);
        LAS char* cs = lds + L_CS + hf * 256 + lg * 16;
        st0 = *(LAS f2*)(lds + L_STAT + (16 * rq + li) * 8);
        st1 = *(LAS f2*)(lds + L_STAT + (64 + 16 * rq + li) * 8);
        st128(geglu8(accA, cs, st0), rs, l16, hoff0 + (2 * cg + hf) * 1024);
        st128(geglu8(accB, cs, st1), rs, l16, hoff1 + (2 * cg + hf) * 1024);
    };
    f4 acc2[4];
#pragma unroll
    for (int j = 0; j < 4; j++) acc2[j] = f4{0, 0, 0, 0};
    const int hsrc = hoff0 + hf * 1024;
    auto g2_mma = [&](auto NC, const u4* hfr, int ldsbase) {
        constexpr int N = decltype(NC)::value;
        u32 pa = (u32)ldsbase + l16; asm volatile("" : "+v"(pa));
        LAS u4* p = (LAS u4*)(lds + pa);
        u4 F[2][4];
#pragma unroll
        for (int j = 0; j < 4; j++) F[0][j] = p[j * 64];
        sfor<N>([&](auto I) {
            constexpr int i = I;
            SCHED();
            if (i + 1 < N) {
#pragma unroll
                for (int j = 0; j < 4; j++) F[(i + 1) & 1][j] = p[(i + 1) * 256 + j * 64];
            }
#pragma unroll
            for (int j = 0; j < 4; j++) acc2[j] = mfma(F[i & 1][j], hfr[i], acc2[j]);
            if (i + 1 < N) {
#pragma unroll
                for (int j = 0; j < 4; j++) { SGB(0x100, 1); SGB(0x8, 1); }
            }
            SCHED();
        });
    };
    if (hf == 0) {
        u4 xa[16], xb[16];
        __builtin_amdgcn_s_setprio(2);
        const int f1st = WS_F1 + ((lane >> 3) * 8 + (((lane >> 2) & 1) ? (rq == 0 ? 4 : 8 - rq) : rq)) * 16384 + pid * 64 + c2p * 16;
        u2 ev;
        { u32 zero = 0; asm volatile("global_load_dwordx2 %0, %1, %2 sc1" : "=v"(ev) : "v"(zero), "s"(a.ws) : "memory"); }
        fft13_consts(1.0f);
        WAITVM(1);
        post(A_XL);
        STAMP(1);
        u32 raw[8];
        const int rw = lane >> 2;
        read_tile(xa, 0);
#pragma unroll
        for (int c1 = 0; c1 < 8; c1++) raw[c1] = *(LAS u32*)(lds + L_SET1 + (16 * rq + rw) * 1024 + (((8 * c1 + t) ^ rw) * 16) + c2p * 4);
        post(A_XC);
        stats(xa);
        STAMP(2);
        f2 v[8];
        {
            const f2 st = *(LAS f2*)(lds + L_STAT + (16 * rq + rw) * 8);
#pragma unroll
            for (int c1 = 0; c1 < 8; c1++) v[c1] = bfup(pkbf(bfup(pkbf(bfup(raw[c1]) - st.xx)) * st.yy));
            dft8<false>(v, kc);
            LAS f2* xb1 = (LAS f2*)(lds + L_FFT) + w * 64 + lane;
#pragma unroll
            for (int q1 = 0; q1 < 8; q1++) { const int reg = (q1 < 4) ? q1 * 2 : (((8 - q1) & 3) * 2 + 1); xb1[reg * 288] = v[q1]; }
        }
        asm volatile("s_waitcnt vmcnt(0)" : "+v"(ev) :: "memory");
        {
            const u64 epoch = ((u64)(u32)__builtin_amdgcn_readfirstlane(ev.y) << 32) | (u32)__builtin_amdgcn_readfirstlane(ev.x);
            u64 tg = (epoch + 1) * 0x9E3779B97F4A7C15ull; tg ^= tg >> 29; tg *= 0xBF58476D1CE4E5B9ull; tg ^= tg >> 32;
            tag = u2{(u32)tg, (u32)(tg >> 32)};
            *(LAS u4*)(lds + L_TAG) = u4{(u32)tg, (u32)(tg >> 32), (u32)(epoch + 1), (u32)((epoch + 1) >> 32)};
        }
        post(A_P1);
#pragma unroll
        for (int c1 = 0; c1 < 8; c1++) *(LAS u32*)(lds + ((rq >> 1) ? L_X1 : L_X0) + ((rq & 1) * 16 + rw) * 128 + ((c1 ^ (rw & 7)) * 16) + c2p * 4) = raw[c1];
        wait_pair(B_W0);
        read_tile(xb, 1);
        post(A_XB);
        u32 pk[8], fr[16];
        const int un = sig < 4 * NFB ? sig : 0, fb = un >> 2, jj = (un >> 1) & 1, tcol = (un & 1) * 4 + rq;
        {
            const u32 vd = (u32)(((fb * 32 + 4 * lg) * 512 + 64 * (4 * jj + (li >> 2)) + 8 * tcol + 2 * (li & 3)) * 2);
            sfor<4>([&](auto I) {
                ldg_u32<I * 1024>(pk[I], vd, a.wd);
                ldg_u32<I * 1024>(pk[4 + I], vd, (const char*)a.wd + 16384);
            });
            const u32 vo = (u32)(((8 * sig + 2 * rq) * 257 + lane) * 2);
            sfor<8>([&](auto I) { constexpr int rr = I >> 2, k = I & 3; ldg_u16<rr * 514 + k * 128>(fr[I], vo, a.wr); ldg_u16<rr * 514 + k * 128>(fr[8 + I], vo, a.wi); });
        }
        wait_grp(0, A_P1);
        {
            LAS f2* reg = (LAS f2*)(lds + L_FFT) + (w * 2 + qb) * 288;
#pragma unroll
            for (int k = 0; k < 8; k++) v[k] = reg[(lane & 31) + 32 * k];
            dft8<false>(v, kc);
#pragma unroll
            for (int k = 1; k < 8; k++) v[k] = cmul<true>(v[k], tw64[k]);
#pragma unroll
            for (int k = 0; k < 8; k++) reg[36 * k + (lane & 31)] = v[k];
            FENCE();
#pragma unroll
            for (int k = 0; k < 8; k++) v[k] = reg[36 * l3 + 4 * k + c2p];
            dft8<false>(v, kc);
#pragma unroll
            for (int k = 0; k < 8; k++) v[k] = cmul<true>(v[k], twT[k]);
            FENCE();
            *(LAS f2*)(lds + x3a[0]) = v[0];
            *(LAS f2*)(lds + x3a[1]) = v[1]; *(LAS f2*)(lds + x3a[1] + 1152) = v[2]; *(LAS f2*)(lds + x3a[1] + 2304) = v[3];
            *(LAS f2*)(lds + x3a[2]) = v[4];
            *(LAS f2*)(lds + x3a[3] + 2304) = v[5]; *(LAS f2*)(lds + x3a[3] + 1152) = v[6]; *(LAS f2*)(lds + x3a[3]) = v[7];
            FENCE();
#pragma unroll
            for (int i = 0; i < 4; i++) {
                u4 o = *(LAS u4*)(lds + L_FFT + w * 4608 + (i * 8 + (lane >> 3)) * 144 + (lane & 7) * 16);
                st128(o, rs, NOF(f1st), i * (64 * 16384));
            }
        }
        STAMP(3);
        __builtin_amdgcn_s_setprio(0);
        asm volatile("s_waitcnt vmcnt(4)" : "+v"(pk[0]), "+v"(pk[1]), "+v"(pk[2]), "+v"(pk[3]), "+v"(pk[4]), "+v"(pk[5]), "+v"(pk[6]), "+v"(pk[7]),
                     "+v"(fr[0]), "+v"(fr[1]), "+v"(fr[2]), "+v"(fr[3]), "+v"(fr[4]), "+v"(fr[5]), "+v"(fr[6]), "+v"(fr[7]),
                     "+v"(fr[8]), "+v"(fr[9]), "+v"(fr[10]), "+v"(fr[11]), "+v"(fr[12]), "+v"(fr[13]), "+v"(fr[14]), "+v"(fr[15]) :: "memory");
        {
            const int so = sig < 4 * NFB ? WS_WDP + ((tcol * NFB + fb) * 4 + 2 * jj) * 1024 : WS_BYTES;
            u4 oe = {__builtin_amdgcn_perm(pk[1], pk[0], 0x05040100), __builtin_amdgcn_perm(pk[3], pk[2], 0x05040100), __builtin_amdgcn_perm(pk[5], pk[4], 0x05040100), __builtin_amdgcn_perm(pk[7], pk[6], 0x05040100)};
            u4 oo = {__builtin_amdgcn_perm(pk[1], pk[0], 0x07060302), __builtin_amdgcn_perm(pk[3], pk[2], 0x07060302), __builtin_amdgcn_perm(pk[5], pk[4], 0x07060302), __builtin_amdgcn_perm(pk[7], pk[6], 0x07060302)};
            st128(oe, rs, l16, so);
            st128(oo, rs, l16, so + 1024);
        }
        {
            const int q1 = lane >> 3, vt = lane & 7, srcl = (q1 + 8 * vt) * 4;
#pragma unroll
            for (int rr = 0; rr < 2; rr++) {
                const int u = 8 * sig + 2 * rq + rr, u1 = u & 63, u2_ = u >> 6;
                int cls, m, sub;
                if ((u1 & 31) != 0) { sub = u1 >> 5; cls = sub ? 64 - u1 : u1; m = sub ? ((8 - q1) & 7) : q1; }
                else { cls = 0; const int hi = u1 >> 5; if ((q1 & 3) == 0) { m = q1; sub = hi; } else { sub = (q1 >> 2) ^ hi; m = sub ? 8 - q1 : q1; } }
                u4 o;
                o.x = __builtin_amdgcn_ds_bpermute(srcl, fr[rr * 4 + 0] | (fr[8 + rr * 4 + 0] << 16)); o.y = __builtin_amdgcn_ds_bpermute(srcl, fr[rr * 4 + 1] | (fr[8 + rr * 4 + 1] << 16));
                o.z = __builtin_amdgcn_ds_bpermute(srcl, fr[rr * 4 + 2] | (fr[8 + rr * 4 + 2] << 16)); o.w = __builtin_amdgcn_ds_bpermute(srcl, fr[rr * 4 + 3] | (fr[8 + rr * 4 + 3] << 16));
                st128(o, rs, NOF(WS_WP + (cls * 8 + m) * 8192 + sub * 4096 + u2_ * 128 + vt * 16), 0);
            }
        }
        WAITVM(4);
        flag_store(WS_FF1 + ((rq * 4 + ((s2 & 7) >> 1)) * 64 + (s2 >> 3) * 16 + (s2 & 1) * 8 + t) * 8);
        STAMP(4);
        f4 accA[4], accB[4], acs = {0, 0, 0, 0};
#pragma unroll
        for (int j = 0; j < 4; j++) { accA[j] = f4{0, 0, 0, 0}; accB[j] = f4{0, 0, 0, 0}; }
        wait_grp(1, B_W1);
        __builtin_amdgcn_s_setprio(1);
        gemm1_half(IC<0>{}, xa, xb, accA, accB, acs, [&](auto S) {
            if (S == 3) {
                WAITVM(1);
                flag_store(sig < 4 * NFB ? WS_FP + (((sig & 1) * 4 + rq) * 128 + (sig >> 1)) * 8 : WS_BYTES);
                flag_store(WS_FW + (((sig & 7) * 4 + rq) * 32 + (sig >> 3)) * 8);
            }
        });
        pin9(accA, accB, acs);
        post(A_S0);
        STAMP(19);
        RELANE();
        int pos1f;
        {
        f2 tw0[4], tw1[8], al[8], be[8];
        const int rb = qb, r3 = 2 * rq + rb;
        const int cls2 = sig >> 3, m2 = sig & 7;
        const int wq2 = (m2 < 4) ? m2 : ((8 - m2) & 3), pos1 = wq2 * 64 + cls2 * 2 + (m2 >> 2);
        const bool special = (cls2 == 0) && ((m2 & 3) == 0);
        int u1a, q1a, u1b, q1b;
        if (cls2 != 0) { u1a = cls2; q1a = m2; u1b = 64 - cls2; q1b = (8 - m2) & 7; }
        else if (special) { u1a = 0; u1b = 32; q1a = q1b = m2; }
        else { u1a = u1b = (m2 < 4) ? 0 : 32; q1a = m2; q1b = 8 - m2; }
        int sA, vH0, sB, vH1;
        if (!special) { sA = 0; vH0 = rq; sB = 1; vH1 = (u1a == 0) ? ((4 - rq) & 3) : 3 - rq; }
        else { sA = sB = rq >> 1; vH0 = rq & 1; vH1 = (rq == 0) ? 2 : (rq == 1) ? 3 : (rq == 2) ? 3 : 2; }
        const bool q0 = (m2 == 0);
        const int dy = ((sA ? u1b : u1a) == 0 && vH0 == 0) ? 8 : 7, dq = q0 ? 8 : 7;
        int y1 = lane >> 3, t1 = lane & 7, y2 = (dy - y1) & 7, t2 = (dq - t1) & 7, hA = vH0, hB = vH1;
        bool self = false;
        if (special && rq == 0) {
            const int k = lane & 31;
            if (qb) { hA = hB = 2; y1 = k >> 3; t1 = k & 7; y2 = 7 - y1; t2 = (dq - t1) & 7; }
            else {
                hA = hB = 0; const int vx = k & 3, y = k >> 2;
                if (!q0 || vx) { y1 = y; t1 = vx; y2 = (8 - y) & 7; t2 = dq - vx; }
                else { t1 = t2 = (y >= 4) ? 4 : 0; const int yy = y & 3; if (yy) { y1 = yy; y2 = 8 - yy; } else { y1 = 0; y2 = 4; self = true; } }
            }
        }
        const int uA = (sA ? u1b : u1a) + 64 * (hA + 4 * y1), uB = (sB ? u1b : u1a) + 64 * (hB + 4 * y2);
        const int qA = (sA ? q1b : q1a) + 8 * t1, qB = (sB ? q1b : q1a) + 8 * t2;
        const int rowB = L_FFT + ((qb ? (vH1 * 2 + sB) : (vH0 * 2 + sA)) * 36 + (lane & 31)) * 8;
        const bool rot = q0 && (t1 == 0);
        const int p3a1 = L_FFT + (y1 * 290 + (hA * 2 + sA) * 36 + t1 * 4) * 8;
        const int p3a2 = L_FFT + (y2 * 290 + (hB * 2 + sB) * 36 + t2 * 4) * 8;
        const float thA = (float)qA * (1.0f / 512.0f), thB = (float)qB * (1.0f / 512.0f);
#pragma unroll
        for (int k = 1; k < 4; k++) tw0[k] = cis((float)r3 * ((float)k / 32.0f));
#pragma unroll
        for (int k = 1; k < 8; k++) tw1[k] = cis((float)c2p * ((float)k / 32.0f));
        f2 csq[8];
#pragma unroll
        for (int e = 0; e < 8; e++) csq[e] = cis(((e >> 2) ? thB : thA) + 0.125f * (float)(e & 3));
        asm volatile("" : "+v"(csq[0]), "+v"(csq[1]), "+v"(csq[2]), "+v"(csq[3]), "+v"(csq[4]), "+v"(csq[5]), "+v"(csq[6]), "+v"(csq[7]),
                     "+v"(tw0[1]), "+v"(tw0[2]), "+v"(tw0[3]), "+v"(tw1[1]), "+v"(tw1[2]), "+v"(tw1[3]), "+v"(tw1[4]), "+v"(tw1[5]), "+v"(tw1[6]), "+v"(tw1[7]));
        {
            const u32 o1 = min(rq * 22 + lane, 85) * 8, o2 = (((qb ? u1b : u1a) >> 1) * 32 + (lane & 31)) * 8, o3 = lane * 8;
            _Pragma("unroll 1") for (int spins = 0; spins < SPIN_CAP; spins++) {
                u2 f1 = __builtin_amdgcn_raw_buffer_load_b64(rs, o1, WS_FP + t * 1024, 16);
                u2 f2_ = __builtin_amdgcn_raw_buffer_load_b64(rs, o2, WS_FW, 16);
                u2 f3 = __builtin_amdgcn_raw_buffer_load_b64(rs, o3, WS_FF1 + (wq2 * 4 + rq) * 512, 16);
                if (__all(f1.x == tag.x && f1.y == tag.y && f2_.x == tag.x && f2_.y == tag.y && f3.x == tag.x && f3.y == tag.y)) break;
                __builtin_amdgcn_s_sleep(2);
                FENCE();
            }
            __builtin_amdgcn_fence(__ATOMIC_ACQUIRE, "wavefront");
            FENCE();
        }
        post(A_FP);
        wait_grp(0, A_S0);
        STAMP(21);
        const int f1off = WS_F1 + sig * 16384 + 2 * rq * 512;
        u4 d[4];
        const u4 wa4 = __builtin_amdgcn_raw_buffer_load_b128(rs, (hA + 4 * y1) * 128 + t1 * 16, WS_WP + sig * 8192 + sA * 4096, 16);
        const u4 wb4 = __builtin_amdgcn_raw_buffer_load_b128(rs, (hB + 4 * y2) * 128 + t2 * 16, WS_WP + sig * 8192 + sB * 4096, 16);
        SCHED();
#pragma unroll
        for (int H = 0; H < 4; H++) d[H] = __builtin_amdgcn_raw_buffer_load_b128(rs, NOF(l16), f1off + H * 4096, 16);
        SCHED();
        f2 wS[2] = {f2{0.f, 0.f}, f2{0.f, 0.f}};
        if (q0) {
            wS[0] = f2{bflo(a.wr[uA * 257 + 256]), bflo(a.wi[uA * 257 + 256])};
            wS[1] = f2{bflo(a.wr[uB * 257 + 256]), bflo(a.wi[uB * 257 + 256])};
        }
        {
            f2 W[8], WB[8];
#pragma unroll
            for (int e = 0; e < 4; e++) { W[e] = bfup(wa4[e]); W[4 + e] = bfup(wb4[e]); }
#pragma unroll
            for (int k = 0; k < 4; k++) { WB[k] = W[4 + 3 - k]; WB[4 + k] = W[3 - k]; }
            if (q0) {
#pragma unroll
                for (int k = 0; k < 4; k++) {
                    f2 n1 = rot ? W[(4 - k) & 3] : W[3 - k], n2 = rot ? W[4 + ((4 - k) & 3)] : W[4 + 3 - k];
                    WB[k] = self ? n1 : n2; WB[4 + k] = self ? n2 : n1;
                }
#pragma unroll
                for (int e = 0; e < 8; e += 4) {
                    f2 own = wS[e >> 2], oth = self ? own : wS[1 - (e >> 2)];
                    f2 S0 = pkfma(WB[e], kc.pm, W[e]) * 0.5f, S1c = pkfma(own, kc.pm, oth) * 0.5f;
                    WB[e] = rot ? S1c : WB[e];
                    al[e] = rot ? S0 : W[e];
                }
                W[0] = al[0]; W[4] = al[4];
            }
#pragma unroll
            for (int e = 0; e < 8; e++) {
                const f2 cs = csq[e];
                f2 P = pkfma(WB[e], kc.pm, W[e]), M = pkfma(WB[e], kc.pm.yx, W[e]);
                asm("v_pk_fma_f32 %0, %1, %2, %3 op_sel:[1,0,0] op_sel_hi:[1,1,1] neg_lo:[1,0,0] neg_hi:[1,0,0]" : "=v"(al[e]) : "v"(cs), "v"(M), "v"(P));
                asm("v_pk_mul_f32 %0, %1, %2 op_sel:[1,0] op_sel_hi:[0,0] neg_lo:[1,0]" : "=v"(be[e]) : "v"(M), "v"(cs));
            }
            asm volatile("" : "+v"(al[0]), "+v"(al[1]), "+v"(al[2]), "+v"(al[3]), "+v"(al[4]), "+v"(al[5]), "+v"(al[6]), "+v"(al[7]),
                         "+v"(be[0]), "+v"(be[1]), "+v"(be[2]), "+v"(be[3]), "+v"(be[4]), "+v"(be[5]), "+v"(be[6]), "+v"(be[7]),
                         "+v"(tw0[1]), "+v"(tw0[2]), "+v"(tw0[3]), "+v"(tw1[1]), "+v"(tw1[2]), "+v"(tw1[3]), "+v"(tw1[4]), "+v"(tw1[5]), "+v"(tw1[6]), "+v"(tw1[7]));
        }
        STAMP(5);
        __builtin_amdgcn_s_setprio(2);
        f2 e0[4], e1[4];
#pragma unroll
        for (int H = 0; H < 4; H++) { e0[H] = f2{__uint_as_float(d[H].x), __uint_as_float(d[H].y)}; e1[H] = f2{__uint_as_float(d[H].z), __uint_as_float(d[H].w)}; }
        dft4<false>(e0[0], e0[1], e0[2], e0[3], kc); dft4<false>(e1[0], e1[1], e1[2], e1[3], kc);
        STAMP(6);
        LAS char* area = lds + L_FFT + rq * 4640;
#pragma unroll
        for (int k = 0; k < 4; k++) {
            if (k) { e0[k] = cmul<true>(e0[k], tw0[k]); e1[k] = cmul<true>(e1[k], tw0[k]); }
            *(LAS f4*)(area + (l3 * 72 + 2 * c2p + 8 * k + 32 * rb) * 8) = f4{e0[k].x, e0[k].y, e1[k].x, e1[k].y};
        }
        FENCE();
#pragma unroll
        for (int k = 0; k < 8; k++) v[k] = *(LAS f2*)(area + (k * 72 + lane) * 8);
        dft8<false>(v, kc);
#pragma unroll
        for (int k = 1; k < 8; k++) v[k] = cmul<true>(v[k], tw1[k]);
        FENCE();
        LAS char* pb = lds + L_FFT + (r3 * 290 + l3 * 36 + c2p) * 8;
#pragma unroll
        for (int k = 0; k < 8; k++) *(LAS f2*)(pb + k * 32) = v[k];
        post(A_E1);
        wait_grp(0, A_E1);
#pragma unroll
        for (int k = 0; k < 8; k++) v[k] = *(LAS f2*)(lds + rowB + k * 2320);
        dft8<false>(v, kc);
#pragma unroll
        for (int k = 0; k < 8; k++) *(LAS f2*)(lds + rowB + k * 2320) = v[k];
        FENCE();
        f4 a0 = *(LAS f4*)(lds + p3a1), a1 = *(LAS f4*)(lds + p3a1 + 16), b0 = *(LAS f4*)(lds + p3a2), b1 = *(LAS f4*)(lds + p3a2 + 16);
        f2 X[8] = {a0.xy, a0.zw, a1.xy, a1.zw, b0.xy, b0.zw, b1.xy, b1.zw};
        dft4<false>(X[0], X[1], X[2], X[3], kc); dft4<false>(X[4], X[5], X[6], X[7], kc);
        f2 Y[8];
#pragma unroll
        for (int k = 0; k < 4; k++) { Y[k] = X[4 + 3 - k]; Y[4 + k] = X[3 - k]; }
        if (q0) {
#pragma unroll
            for (int k = 0; k < 4; k++) {
                f2 n1 = rot ? X[(4 - k) & 3] : X[3 - k], n2 = rot ? X[4 + ((4 - k) & 3)] : X[4 + 3 - k];
                Y[k] = self ? n1 : n2; Y[4 + k] = self ? n2 : n1;
            }
        }
        f2 O[8];
#pragma unroll
        for (int e = 0; e < 8; e++) O[e] = cfmac_conj(cmul<false>(X[e], al[e]), be[e], Y[e]);
        dft4<true>(O[0], O[1], O[2], O[3], kc); dft4<true>(O[4], O[5], O[6], O[7], kc);
        *(LAS f4*)(lds + p3a1) = f4{O[0].x, O[0].y, O[1].x, O[1].y}; *(LAS f4*)(lds + p3a1 + 16) = f4{O[2].x, O[2].y, O[3].x, O[3].y};
        *(LAS f4*)(lds + p3a2) = f4{O[4].x, O[4].y, O[5].x, O[5].y}; *(LAS f4*)(lds + p3a2 + 16) = f4{O[6].x, O[6].y, O[7].x, O[7].y};
        FENCE();
#pragma unroll
        for (int k = 0; k < 8; k++) v[k] = *(LAS f2*)(lds + rowB + k * 2320);
        dft8<true>(v, kc);
#pragma unroll
        for (int k = 0; k < 8; k++) *(LAS f2*)(lds + rowB + k * 2320) = v[k];
        post(A_E2);
        wait_grp(0, A_E2);
#pragma unroll
        for (int k = 0; k < 8; k++) v[k] = *(LAS f2*)(pb + k * 32);
#pragma unroll
        for (int k = 1; k < 8; k++) v[k] = cmul<false>(v[k], tw1[k]);
        dft8<true>(v, kc);
        FENCE();
#pragma unroll
        for (int k = 0; k < 8; k++) *(LAS f2*)(area + (k * 72 + lane) * 8) = v[k];
        FENCE();
#pragma unroll
        for (int k = 0; k < 4; k++) {
            f4 q = *(LAS f4*)(area + (l3 * 72 + 2 * c2p + 8 * k + 32 * rb) * 8);
            e0[k] = q.xy; e1[k] = q.zw;
            if (k) { e0[k] = cmul<false>(e0[k], tw0[k]); e1[k] = cmul<false>(e1[k], tw0[k]); }
        }
        dft4<true>(e0[0], e0[1], e0[2], e0[3], kc); dft4<true>(e1[0], e1[1], e1[2], e1[3], kc);
        const int f2st = WS_F2 + (r3 * 8 + l3) * 16384 + pos1 * 64 + c2p * 16;
#pragma unroll
        for (int H = 0; H < 4; H++) {
            u4 o = {__float_as_uint(e0[H].x), __float_as_uint(e0[H].y), __float_as_uint(e1[H].x), __float_as_uint(e1[H].y)};
            st128(o, rs, NOF(f2st), H * (64 * 16384));
        }
            pos1f = pos1;
        }
        STAMP(7);
        __builtin_amdgcn_s_setprio(1);
        RELANE();
        wait_grp(1, B_CS);
        gemm1_half(IC<8>{}, xa, xb, accA, accB, acs, [&](auto S) { if (S == 5) { WAITVM(0); flag_store(WS_FF2 + (rq * 256 + pos1f) * 8); } });
        STAMP(8);
        RELANE();
        gemm1_epi(accA, accB, acs, A_CS);
        __builtin_amdgcn_s_setprio(0);
        sfor<4>([&](auto I) { dma_wd(IC<L_SET1>{}, I, 0); });
        WAITVM(16);
        flag_store(WS_FHA + ((rg * 8 + w) * 16 + cg) * 8);
        STAMP(9);
        {
            u4 d[4];
        fft13_consts(0.5f / 524288.0f);
        flag_wait(lane * 8, WS_FF2 + (((s2 & 7) >> 1) * 256 + rq * 64) * 8);
        const int off = WS_F2 + pid * 16384 + rq * 4096;
#pragma unroll
        for (int i = 0; i < 4; i++) d[i] = __builtin_amdgcn_raw_buffer_load_b128(rs, NOF(l16), off + i * 1024, 16);
        STAMP(13);
        __builtin_amdgcn_s_setprio(2);
#pragma unroll
        for (int i = 0; i < 4; i++) *(LAS u4*)(lds + L_FFT + rq * 4608 + (i * 8 + (lane >> 3)) * 144 + (lane & 7) * 16) = d[i];
        FENCE();
        v[0] = *(LAS f2*)(lds + x3a[0]);
        v[1] = *(LAS f2*)(lds + x3a[1]); v[2] = *(LAS f2*)(lds + x3a[1] + 1152); v[3] = *(LAS f2*)(lds + x3a[1] + 2304);
        v[4] = *(LAS f2*)(lds + x3a[2]);
        v[5] = *(LAS f2*)(lds + x3a[3] + 2304); v[6] = *(LAS f2*)(lds + x3a[3] + 1152); v[7] = *(LAS f2*)(lds + x3a[3]);
#pragma unroll
        for (int k = 0; k < 8; k++) v[k] = cmul<false>(v[k], twT[k]);
        dft8<true>(v, kc);
#pragma unroll
        for (int k = 1; k < 8; k++) v[k] = cmul<false>(v[k], tw64[k]);
        FENCE();
        LAS f2* reg = (LAS f2*)(lds + L_FFT) + (rq * 2 + qb) * 288;
#pragma unroll
        for (int k = 0; k < 8; k++) reg[36 * l3 + 4 * k + c2p] = v[k];
        FENCE();
#pragma unroll
        for (int k = 0; k < 8; k++) v[k] = reg[36 * k + (lane & 31)];
        dft8<true>(v, kc);
        FENCE();
#pragma unroll
        for (int k = 0; k < 8; k++) reg[(lane & 31) + 32 * k] = v[k];
        post(A_F3);
        wait_grp(0, A_F3);
        if (b == 0 && rq == 0) __builtin_amdgcn_raw_buffer_store_b64(*(LAS u2*)(lds + L_TAG + 8), rs, lane ? WS_BYTES : 0, 0, 16);
        LAS f2* xb1 = (LAS f2*)(lds + L_FFT) + rq * 64 + lane;
#pragma unroll
        for (int q1 = 0; q1 < 8; q1++) { const int rg_ = (q1 < 4) ? q1 * 2 : (((8 - q1) & 3) * 2 + 1); v[q1] = xb1[rg_ * 288]; }
        post(A_F4);
        dft8<true>(v, kc);
        wait_grp(0, A_F4);
#pragma unroll
        for (int c1 = 0; c1 < 8; c1++) *(LAS u32*)(lds + L_S + (16 * rq + (lane >> 2)) * 144 + c1 * 16 + c2p * 4) = pkbf(v[c1]);
        }
        STAMP(10);
        u4 hfr[16];
        flag_wait(li * 8, WS_FHA + (rg * 8 + w) * 128);
#pragma unroll
        for (int i = 0; i < 16; i++) { hfr[i] = __builtin_amdgcn_raw_buffer_load_b128(rs, l16, hsrc + 2 * ((i + rot) & 15) * 1024, 16); SCHED(); }
        STAMP(17);
        g2_mma(IC<16>{}, hfr, L_SET1);
    } else {
        u4 xa[16], xb[16];
        __builtin_amdgcn_s_sleep(2);
        sfor<8>([&](auto I) { dma_w1(IC<0>{}, I); });
        wait_pair(A_XL);
        read_tile(xa, 0);
        asm volatile("s_waitcnt lgkmcnt(0)" ::: "memory");
        wait_pair(A_XC);
        dma_x();
        sfor<8>([&](auto I) { dma_w1(IC<1>{}, I); });
        STAMP(1);
        asm volatile("s_waitcnt vmcnt(8)" : "+v"(cpf) :: "memory");
        post(B_W0);
        read_tile(xb, 1);
        STAMP(2);
        {
            f4 accA[4], accB[4], acs = {0, 0, 0, 0};
#pragma unroll
            for (int j = 0; j < 4; j++) { accA[j] = f4{0, 0, 0, 0}; accB[j] = f4{0, 0, 0, 0}; }
            wait_grp(1, B_W0);
            STAMP(3);
            gemm1_half(IC<0>{}, xa, xb, accA, accB, acs, [&](auto S) {
                constexpr int s = S;
                if (s == 1) { WAITVM(0); post(B_W1); wait_pair(A_XB); }
                if (s == 3) wait_grp(1, B_W1);
                if constexpr (s >= 1 && s <= 4) sfor<4>([&](auto I) { dma_w1(IC<8 + ((s - 1) >> 1)>{}, IC<((s - 1) & 1) * 4 + I>{}); });
                if (s == 7) { WAITVM(8); post(B_W2); }
            });
            STAMP(14);
            wait_grp(1, B_W2);
            get_tag();
            gemm1_half(IC<8>{}, xa, xb, accA, accB, acs, [&](auto S) {
                if (S == 1) { WAITVM(0); post(B_W3); }
                if (S == 3) wait_grp(1, B_W3);
            });
            STAMP(4);
            RELANE();
            pin9(accA, accB, acs);
            if (nblk == 3) {
                const u32 wvB = (u32)((lane >> 2) * 2752 + (((lane & 3) ^ (((lane >> 5) & 1) << 1)) * 16));
                const char* wbs = (const char*)((rq >> 1) ? a.wu : a.wg) + (size_t)((rq & 1) * 16) * 2752 + fb2 * 64;
                wait_grp(0, A_S0);
                sfor<16>([&](auto K) { constexpr int k = K; dma_add<k * 4096, IX(k) * 32 * 2752>(wvB, wbs + ((BE(k) + j4) & 3) * (128 * 2752), (u32)(rq * 1024)); });
            }
            SCHED();
            stats(xb);
            STAMP(15);
            gemm1_epi(accA, accB, acs, B_CS);
        }
        STAMP(5);
        if (nblk == 3) {
            WAITVM(2); post(B_WB);
            wait_grp(1, B_WB);
            const u32 tb = (u32)((8 * lg + (li >> 2)) * 64 + (lg & 1) * 32 + 8 * (li & 3));
            LAS char* p0 = lds + tb; LAS char* p1 = lds + (tb ^ 32);
            LAS char* pc = lds + (tb ^ ((rq & 1) * 32)) + (rq >> 1) * 2048;
            f4 acc[2][4], acs = {0, 0, 0, 0};
#pragma unroll
            for (int j = 0; j < 4; j++) { acc[0][j] = f4{0, 0, 0, 0}; acc[1][j] = f4{0, 0, 0, 0}; }
            u4 F[2][5];
            auto ldf = [&](int ks, u4 (&f)[5]) {
                const int off = ks * 4096;
                f[4] = trfrag2<256>(pc + off);
                f[0] = trfrag2<256>(p0 + off); f[1] = trfrag2<256>(p1 + off); f[2] = trfrag2<256>(p0 + off + 2048); f[3] = trfrag2<256>(p1 + off + 2048);
            };
            ldf(0, F[0]);
            sfor<16>([&](auto S) {
                constexpr int ks = S;
                if (ks == 5) { WAITVM(0); flag_store(WS_FHA + ((rg * 8 + w) * 16 + cg) * 8); }
                SCHED();
                if (ks + 1 < 16) ldf(ks + 1, F[(ks + 1) & 1]);
                u4 (&f)[5] = F[ks & 1];
                acs = mfma(f[4], ones, acs);
#pragma unroll
                for (int j = 0; j < 4; j++) { acc[0][j] = mfma(f[j], xa[ks], acc[0][j]); acc[1][j] = mfma(f[j], xb[ks], acc[1][j]); }
                if (ks + 1 < 16) {
                    SGB(0x100, 2);
#pragma unroll
                    for (int i = 0; i < 8; i++) { SGB(0x8, 1); SGB(0x100, 1); }
                    SGB(0x8, 1);
                }
                SCHED();
            });
            if (li == 0) *(LAS f4*)(lds + L_CS + 512 + rq * 64 + lg * 16) = acs;
            post(B_PB);
            wait_grp(1, B_PB);
            st128(geglu8(acc[0], lds + L_CS + 512 + lg * 16, st0), rs, l16, hoff0 + fb2 * 1024);
            st128(geglu8(acc[1], lds + L_CS + 512 + lg * 16, st1), rs, l16, hoff1 + fb2 * 1024);
        } else { WAITVM(0); flag_store(WS_FHA + ((rg * 8 + w) * 16 + cg) * 8); }
        STAMP(6);
        RELANE();
        wait_grp(0, A_FP);
        sfor<4>([&](auto I) { dma_wd(IC<0>{}, I, 1); });
        if (nblk == 3) { WAITVM(16); flag_store(WS_FHB + ((rg * 4 + rq) * 16 + fb2 - 32) * 8); }
        u4 hfr[16];
        flag_wait(li * 8, WS_FHA + (rg * 8 + w) * 128);
        post(B_D1);
#pragma unroll
        for (int i = 0; i < 16; i++) { hfr[i] = __builtin_amdgcn_raw_buffer_load_b128(rs, l16, hsrc + 2 * ((i + rot) & 15) * 1024, 16); SCHED(); }
        STAMP(9);
        wait_grp(1, B_D1);
        g2_mma(IC<16>{}, hfr, 0);
        post(B_G1);
        pin4(acc2);
        STAMP(10);
        wait_grp(1, B_G1);
        sfor<3>([&](auto I) { constexpr int i = I; dma4k_c<2 * PG + i * 16384, 0>(l16, wdp + (size_t)min(32 + 4 * i + rq, 42) * 4096, (u32)(rq * 4096)); });
        flag_wait(min(li, 10) * 8, WS_FHB + (rg * 4 + rq) * 128);
        post(B_D3);
#pragma unroll
        for (int i = 0; i < 11; i++) { hfr[i] = __builtin_amdgcn_raw_buffer_load_b128(rs, l16, hoff0 + (32 + i) * 1024, 16); SCHED(); }
        STAMP(13);
        wait_grp(1, B_D3);
        g2_mma(IC<11>{}, hfr, 2 * PG);
    }
    RELANE();
    STAMP(11);
    BAR();
    {
        LAS f4* xs = (LAS f4*)(lds + w * 2048) + lane;
        LAS f4* xr = (LAS f4*)(lds + (w ^ 4) * 2048) + lane;
        f4 me, mo;
        if (hf) { xs[0] = acc2[0]; xs[64] = acc2[1]; me = acc2[2]; mo = acc2[3]; } else { xs[0] = acc2[2]; xs[64] = acc2[3]; me = acc2[0]; mo = acc2[1]; }
        BAR();
        const int c1 = 4 * hf + lg;
        me += xr[0]; mo += xr[64];
        const u4 sv = *(LAS u4*)(lds + L_S + (16 * rq + li) * 144 + c1 * 16);
        const u4 xv = *(LAS u4*)(lds + ((rq >> 1) ? L_X1 : L_X0) + ((rq & 1) * 16 + li) * 128 + ((c1 ^ (li & 7)) * 16));
        u32 res[4];
#pragma unroll
        for (int e = 0; e < 4; e++) {
            const f2 ov = bfup(pkbf(f2{me[e], mo[e]}));
            const f2 z = ov * -1.44269504f;
            const f2 d = f2{__builtin_amdgcn_exp2f(z.x), __builtin_amdgcn_exp2f(z.y)} + f2{1.f, 1.f};
            const f2 g = bfup(pkbf(ov * f2{__builtin_amdgcn_rcpf(d.x), __builtin_amdgcn_rcpf(d.y)}));
            res[e] = pkbf(bfup(xv[e]) + bfup(pkbf(bfup(sv[e]) * g)));
        }
        *(u4*)(a.out + (size_t)(s2 + 32 * (16 * rq + li)) * 512 + 64 * c1 + 8 * t) = u4{res[0], res[1], res[2], res[3]};
    }
    STAMP(12);
}

extern "C" void kernel_launch(void* const* d_in, const int* in_sizes, int n_in,
                              void* d_out, int out_size, void* d_ws, size_t ws_size,
                              hipStream_t stream) {
    Args a;
    a.x = (const u16*)d_in[0]; a.wr = (const u16*)d_in[1]; a.wi = (const u16*)d_in[2];
    a.wg = (const u16*)d_in[4]; a.wu = (const u16*)d_in[5]; a.wd = (const u16*)d_in[6];
    a.out = (u16*)d_out; a.ws = (unsigned char*)d_ws;
    (void)hipFuncSetAttribute((const void*)fused, hipFuncAttributeMaxDynamicSharedMemorySize, L_END);
    fused<<<256, 512, L_END, stream>>>(a);
}
```

```cpp
#define PROBE 0
#include <hip/hip_runtime.h>
#include <stdint.h>
#include <type_traits>

typedef unsigned short u16;
typedef unsigned u32;
typedef unsigned long long u64;
typedef float f2 __attribute__((ext_vector_type(2)));
typedef float f4 __attribute__((ext_vector_type(4)));
typedef u32 u4 __attribute__((ext_vector_type(4)));
typedef u32 u2 __attribute__((ext_vector_type(2)));
typedef short s4 __attribute__((ext_vector_type(4)));
typedef __bf16 bf8 __attribute__((ext_vector_type(8)));
typedef __bf16 bf2 __attribute__((ext_vector_type(2)));
#define LAS __attribute__((address_space(3)))
#define GAS __attribute__((address_space(1)))
#define DEV __device__ __forceinline__
template <int N> using IC = std::integral_constant<int, N>;

constexpr int NFB = 43;
constexpr int WS_FF1 = 4096;
constexpr int WS_FF2 = WS_FF1 + 8192;
constexpr int WS_FHA = WS_FF2 + 8192;
constexpr int WS_FHB = WS_FHA + 16384;
constexpr int WS_FP = WS_FHB + 16384;
constexpr int WS_FW = WS_FP + 8192;
constexpr int WS_F1 = 1 << 20, WS_F2 = WS_F1 + (4 << 20), WS_H = WS_F2 + (4 << 20);
constexpr int WS_WDP = WS_H + 32 * 4 * NFB * 1024;
constexpr int WS_WP = WS_WDP + 8 * NFB * 4 * 1024;
constexpr int WS_STAMP = WS_WP + (2 << 20);
constexpr int WS_BYTES = WS_STAMP + (1 << 20);
constexpr int PG = 8192;
constexpr int L_SET1 = 8 * PG;
constexpr int L_FFT = 16 * PG;
constexpr int L_SMALL = 19 * PG;
constexpr int L_PROG = L_SMALL;
constexpr int L_TAG = L_SMALL + 32;
constexpr int L_STAT = L_SMALL + 64;
constexpr int L_CS = L_SMALL + 2048;
constexpr int L_X0 = L_FFT + 18688, L_X1 = L_SMALL + 3072;
constexpr int L_S = L_FFT;
constexpr int L_END = 20 * PG;
constexpr u32 SENT = 0xFFFFFFFFu;
constexpr int CODE_BYTES = 44 * 1024;
constexpr int SPIN_CAP = 1 << 16;

struct Args { const u16* x; const u16* wr; const u16* wi; const u16* wg; const u16* wu; const u16* wd; u16* out; unsigned char* ws; };

#define BAR() asm volatile("s_waitcnt lgkmcnt(0)\n\ts_barrier" ::: "memory")
#define WAITVM(n) asm volatile("s_waitcnt vmcnt(" #n ")" ::: "memory")
#define NOF(x) (x)
#define NOPL(x) (x)
#define FENCE() asm volatile("" ::: "memory")
#define SCHED() __builtin_amdgcn_sched_barrier(0)
#define SGB(mask, n) __builtin_amdgcn_sched_group_barrier(mask, n, 0)
#define STAMP(i) do { u64 t_ = __builtin_amdgcn_s_memrealtime(); *(volatile LAS u2*)(lds + L_FFT + 22784 + (w * 28 + (i)) * 8) = u2{(u32)t_, (u32)(t_ >> 32)}; } while (0)
#define CSTAMP(i) do { u64 t_ = __builtin_amdgcn_s_memtime(); *(volatile LAS u2*)(lds + L_FFT + 22784 + (w * 28 + (i)) * 8) = u2{(u32)t_, (u32)(t_ >> 32)}; } while (0)

DEV f2 pkfma(f2 a, f2 b, f2 c) { return __builtin_elementwise_fma(a, b, c); }
struct KC { f2 pm, rpm; };
DEV f2 addmi(f2 a, f2 b, const KC& k) { return pkfma(b.yx, k.pm, a); }
DEV f2 addpi(f2 a, f2 b, const KC& k) { return pkfma(b.yx, k.pm.yx, a); }
template <bool INV> DEV void dft4(f2& a0, f2& a1, f2& a2, f2& a3, const KC& k) {
    f2 t0 = a0 + a2, t1 = a0 - a2, t2 = a1 + a3, t3 = a1 - a3;
    a0 = t0 + t2; a2 = t0 - t2;
    if (INV) { a1 = addpi(t1, t3, k); a3 = addmi(t1, t3, k); } else { a1 = addmi(t1, t3, k); a3 = addpi(t1, t3, k); }
}
template <bool INV> DEV void dft8(f2 (&v)[8], const KC& k) {
    f2 a0 = v[0] + v[4], a4 = v[0] - v[4], a1 = v[1] + v[5], a5 = v[1] - v[5];
    f2 a2 = v[2] + v[6], a6 = v[2] - v[6], a3 = v[3] + v[7], a7 = v[3] - v[7];
    dft4<INV>(a0, a1, a2, a3, k);
    v[0] = a0; v[2] = a1; v[4] = a2; v[6] = a3;
    f2 b1, b3, t0, t1;
    if (INV) { b1 = addpi(a5, a5, k); b3 = addpi(-a7, a7, k); t0 = addpi(a4, a6, k); t1 = addmi(a4, a6, k); }
    else { b1 = addmi(a5, a5, k); b3 = addmi(-a7, a7, k); t0 = addmi(a4, a6, k); t1 = addpi(a4, a6, k); }
    f2 t2 = b1 + b3, t3 = b1 - b3;
    v[1] = pkfma(t2, k.rpm.xx, t0); v[5] = pkfma(t2, k.rpm.yy, t0);
    if (INV) { v[3] = pkfma(t3.yx, k.rpm.yx, t1); v[7] = pkfma(t3.yx, k.rpm, t1); }
    else { v[3] = pkfma(t3.yx, k.rpm, t1); v[7] = pkfma(t3.yx, k.rpm.yx, t1); }
}
template <bool CONJ> DEV f2 cmul(f2 a, f2 w) {
    f2 r;
    if (CONJ) asm("v_pk_mul_f32 %0, %1, %2 op_sel:[1,1] op_sel_hi:[0,1]\n\tv_pk_fma_f32 %0, %1, %2, %0 op_sel_hi:[1,0,1] neg_hi:[0,0,1]" : "=&v"(r) : "v"(a), "v"(w));
    else asm("v_pk_mul_f32 %0, %1, %2 op_sel:[1,1] op_sel_hi:[0,1]\n\tv_pk_fma_f32 %0, %1, %2, %0 op_sel_hi:[1,0,1] neg_lo:[0,0,1]" : "=&v"(r) : "v"(a), "v"(w));
    return r;
}
DEV f2 cfmac_conj(f2 t, f2 beta, f2 B) {
    asm("v_pk_fma_f32 %0, %2, %1, %0 op_sel_hi:[0,1,1]\n\tv_pk_fma_f32 %0, %2, %1, %0 op_sel:[1,1,0] op_sel_hi:[1,0,1] neg_hi:[0,1,0]" : "+v"(t) : "v"(beta), "v"(B));
    return t;
}
DEV f2 cis(float r) { return f2{__builtin_amdgcn_cosf(r), __builtin_amdgcn_sinf(r)}; }
DEV float bflo(u32 v) { return __uint_as_float(v << 16); }
DEV float bfhi(u32 v) { return __uint_as_float(v & 0xFFFF0000u); }
DEV f2 bfup(u32 v) { return f2{bflo(v), bfhi(v)}; }
DEV u32 pkbf(f2 v) { bf2 r; r.x = (__bf16)v.x; r.y = (__bf16)v.y; u32 o = __builtin_bit_cast(u32, r); asm("" : "+v"(o)); return o; }
DEV float rbf(float f) { return __uint_as_float(((u32)__builtin_bit_cast(u16, (__bf16)f)) << 16); }
DEV f4 mfma(u4 a, u4 b, f4 c) { return __builtin_amdgcn_mfma_f32_16x16x32_bf16(__builtin_bit_cast(bf8, a), __builtin_bit_cast(bf8, b), c, 0, 0, 0); }
DEV u4 sanitize(u4 v) { return u4{min(v.x, 0xFFFFFFFEu), min(v.y, 0xFFFFFFFEu), min(v.z, 0xFFFFFFFEu), min(v.w, 0xFFFFFFFEu)}; }
DEV u32 max4(u4 v) { return max(max(v.x, v.y), max(v.z, v.w)); }
DEV void st128(u4 v, __amdgpu_buffer_rsrc_t rs, u32 voff, int soff) {
    __builtin_amdgcn_raw_buffer_store_b128(v, rs, voff, soff, 16);
    asm volatile("s_nop 1" :: "v"(v) : "memory");
}
template <int OFF2> DEV u4 trfrag2(LAS char* p) {
    s4 r0 = __builtin_amdgcn_ds_read_tr16_b64_v4i16((LAS s4*)p);
    s4 r1 = __builtin_amdgcn_ds_read_tr16_b64_v4i16((LAS s4*)(p + OFF2));
    u2 x0 = __builtin_bit_cast(u2, r0), x1 = __builtin_bit_cast(u2, r1);
    return u4{x0.x, x0.y, x1.x, x1.y};
}
template <int LI, int GI> DEV void dma_add(u32 vb, const void* sbase, u32 sl) {
    const u32 va = vb + (u32)GI;
    asm volatile("s_add_u32 m0, %2, %3\n\ts_nop 0\n\tglobal_load_lds_dwordx4 %0, %1" :: "v"(va), "s"(sbase), "s"(sl), "i"(LI) : "memory", "scc");
}
template <int LI, int GI> DEV void dma4k_c(u32 vb, const void* sbase, u32 sl) {
    const u32 va = vb + (u32)GI;
    asm volatile("s_add_u32 m0, %2, %3\n\ts_nop 0\n\tglobal_load_lds_dwordx4 %0, %1 sc1\n\tglobal_load_lds_dwordx4 %0, %1 offset:1024 sc1\n\t"
                 "global_load_lds_dwordx4 %0, %1 offset:2048 sc1\n\tglobal_load_lds_dwordx4 %0, %1 offset:3072 sc1"
                 :: "v"(va), "s"(sbase), "s"(sl), "i"(LI) : "memory", "scc");
}
template <int OFF> DEV void ldg_u16(u32& d, u32 vo, const void* sb) { asm volatile("global_load_ushort %0, %1, %2 offset:%3" : "=v"(d) : "v"(vo), "s"(sb), "i"(OFF) : "memory"); }
template <int OFF> DEV void ldg_u32(u32& d, u32 vo, const void* sb) { asm volatile("global_load_dword %0, %1, %2 offset:%3" : "=v"(d) : "v"(vo), "s"(sb), "i"(OFF) : "memory"); }
DEV void lds_wait(u32 addr, u32 v) {
    u32 tmp, cnt;
    asm volatile("s_mov_b32 %1, 0x80000\n"
                 "1:\n\tds_read_b32 %0, %2\n\ts_sub_u32 %1, %1, 1\n\ts_waitcnt lgkmcnt(0)\n\ts_cbranch_scc1 2f\n\tv_cmp_gt_u32 vcc, %3, %0\n\ts_cbranch_vccnz 1b\n2:"
                 : "=&v"(tmp), "=&s"(cnt) : "v"(addr), "s"(v) : "memory", "vcc", "scc");
}
template <int N, typename F> DEV void sfor(F&& f) { if constexpr (N > 0) { sfor<N - 1>(f); f(IC<N - 1>{}); } }

DEV void pin9(f4 (&x)[4], f4 (&y)[4], f4& z) {
    asm volatile("" : "+v"(x[0]), "+v"(x[1]), "+v"(x[2]), "+v"(x[3]), "+v"(y[0]), "+v"(y[1]), "+v"(y[2]), "+v"(y[3]), "+v"(z));
}
DEV void pin4(f4 (&x)[4]) { asm volatile("" : "+v"(x[0]), "+v"(x[1]), "+v"(x[2]), "+v"(x[3])); }
DEV void dma_rt(u32 voff, const void* sbase, u32 sl) {
    asm volatile("s_mov_b32 m0, %2\n\ts_nop 0\n\tglobal_load_lds_dwordx4 %0, %1" :: "v"(voff), "s"(sbase), "s"(sl) : "memory");
}
DEV u4 geglu8(const f4 (&acc)[4], LAS char* cs, f2 st) {
    u32 hw[4];
    const f2 nm = f2{-st.x, -st.x}, ri = f2{st.y, st.y};
#pragma unroll
    for (int c = 0; c < 2; c++) {
        f4 cg_ = *(LAS f4*)(cs + c * 64), cu_ = *(LAS f4*)(cs + (2 + c) * 64);
#pragma unroll
        for (int e = 0; e < 2; e++) {
            f2 g = pkfma(e ? cg_.zw : cg_.xy, nm, e ? acc[c].zw : acc[c].xy) * ri;
            f2 u = pkfma(e ? cu_.zw : cu_.xy, nm, e ? acc[2 + c].zw : acc[2 + c].xy) * ri;
            f2 z = g * pkfma(g * g, f2{-0.10294324f, -0.10294324f}, f2{-2.3022082f, -2.3022082f});
            f2 d = f2{__builtin_amdgcn_exp2f(z.x), __builtin_amdgcn_exp2f(z.y)} + f2{1.f, 1.f};
            f2 r = f2{__builtin_amdgcn_rcpf(d.x), __builtin_amdgcn_rcpf(d.y)};
            hw[2 * c + e] = pkbf(g * r * u);
        }
    }
    return u4{hw[0], hw[1], hw[2], hw[3]};
}

enum { A_XL = 1, A_XC, A_P1, A_XB, A_S0, A_FP, A_E1, A_E2, A_CS, A_F3, A_F4, A_D1 };
enum { B_W0 = 1, B_W1, B_W2, B_W3, B_CS, B_WB, B_PB, B_D1, B_G1, B_D3 };

__global__ void __launch_bounds__(512, 2) fused(Args a) {
    extern __shared__ __attribute__((aligned(16))) char smem[];
    LAS char* lds = (LAS char*)smem;
    const int tid = threadIdx.x, w = __builtin_amdgcn_readfirstlane(tid >> 6);
    int lane = tid & 63;
    const int b = blockIdx.x, xcd = b & 7, r = b >> 3, t = r & 7, par = xcd & 1, rg = (xcd >> 1) * 4 + (r >> 3);
    const int s2 = 2 * rg + par, cg = par * 8 + t, sig = xcd * 32 + r, pid = s2 * 8 + t;
    const int rq = w & 3, hf = w >> 2, j4 = r >> 3;
#define BE(l) ((((l) >> 3) << 1) | ((l) & 1))
#define IX(l) (((l) >> 1) & 3)
    u32 l16 = lane * 16;
    u32 cpf = 0;
    if (w == 7) {
        const u64 pc = __builtin_amdgcn_s_getpc() & ~(u64)255;
        const u32 off = min((u32)((t * 64 + lane) * 128), (u32)(CODE_BYTES - 128));
        asm volatile("global_load_dword %0, %1, %2" : "=v"(cpf) : "v"(off), "s"(pc) : "memory");
    }
    *(volatile LAS u32*)(lds + L_PROG + w * 4) = 0;
    BAR();
    STAMP(0);
    auto dma_x = [&]() {
        const char* xs = (const char*)a.x + (size_t)((s2 ^ hf) + 512 * rq) * 1024;
#pragma unroll
        for (int i = 0; i < 16; i++) { const int row = (i + 2 * t) & 15; dma_rt(l16 ^ (u32)(row * (32768 + 16)), xs, (u32)(L_SET1 + rq * 16384 + row * 1024)); }
    };
    if (hf == 0) dma_x();
    int li, lg, c2p, l3, qb; u32 vpg;
#define RELANE() do { asm volatile("" : "+v"(lane)); l16 = lane * 16; li = lane & 15; lg = lane >> 4; c2p = lane & 3; l3 = (lane >> 2) & 7; qb = lane >> 5; vpg = (u32)(L_PROG + (lane & 3) * 4); } while (0)
    const __amdgpu_buffer_rsrc_t rs = __builtin_amdgcn_make_buffer_rsrc(a.ws, 0, WS_BYTES, 0x00020000);
    KC kc; kc.pm = f2{1.f, -1.f}; kc.rpm = f2{0.70710678118654752f, -0.70710678118654752f};
    asm volatile("" : "+s"(kc.pm), "+s"(kc.rpm));
    const u4 ones = {0x3F803F80u, 0x3F803F80u, 0x3F803F80u, 0x3F803F80u};
    auto post = [&](u32 v) { asm volatile("s_waitcnt lgkmcnt(0)" ::: "memory"); *(volatile LAS u32*)(lds + L_PROG + w * 4) = v; FENCE(); };
    auto wait_grp = [&](int g, u32 v) { lds_wait(vpg + g * 16, v); };
    auto wait_pair = [&](u32 v) { lds_wait((u32)(L_PROG + (w ^ 4) * 4), v); };
    u2 tag;
    auto get_tag = [&]() { u2 tg = *(LAS u2*)(lds + L_TAG); tag.x = __builtin_amdgcn_readfirstlane(tg.x); tag.y = __builtin_amdgcn_readfirstlane(tg.y); };
    auto flag_store = [&](int off) { __builtin_amdgcn_raw_buffer_store_b64(tag, rs, lane ? WS_BYTES : 0, off, 16); };
    auto flag_wait = [&](u32 voff, int soff) {
        _Pragma("unroll 1") for (int spins = 0; spins < SPIN_CAP; spins++) {
            u2 f = __builtin_amdgcn_raw_buffer_load_b64(rs, voff, soff, 16);
            if (__all(f.x == tag.x && f.y == tag.y)) break;
            __builtin_amdgcn_s_sleep(4);
            FENCE();
        }
        __builtin_amdgcn_fence(__ATOMIC_ACQUIRE, "wavefront");
        FENCE();
    };
    auto read_tile = [&](u4 (&x)[16], int tile) {
        LAS char* p = lds + L_SET1 + (16 * rq + li) * 1024;
#pragma unroll
        for (int k = 0; k < 16; k++) x[k] = *(LAS u4*)(p + (((4 * IX(k) + lg) ^ li) * 16) + ((BE(k) + j4) & 3) * 256);
    };
    auto stats = [&](const u4 (&x)[16]) {
        f4 s1 = {0, 0, 0, 0}, sq = s1, s1b = s1, sqb = s1;
#pragma unroll
        for (int k = 0; k < 16; k += 2) { s1 = mfma(ones, x[k], s1); sq = mfma(x[k], x[k], sq); s1b = mfma(ones, x[k + 1], s1b); sqb = mfma(x[k + 1], x[k + 1], sqb); }
        sq += sqb;
        const int rr = li & 3;
        const float d = rr == 0 ? sq.x : rr == 1 ? sq.y : rr == 2 ? sq.z : sq.w;
        if (lg == (li >> 2)) {
            const float mean = (s1.x + s1b.x) * (1.0f / 512.0f);
            float var = d * (1.0f / 512.0f) - mean * mean;
            if (!(mean * mean <= 1024.0f * var)) {
                const u32* xrow = (const u32*)(a.x + (size_t)((s2 ^ hf) + 32 * (16 * rq + li)) * 512);
                float q = 0.f;
                _Pragma("unroll 1") for (int i = 0; i < 256; i++) { u32 v = xrow[i]; float d0 = bflo(v) - mean, d1 = bfhi(v) - mean; q += d0 * d0; q += d1 * d1; }
                var = q * (1.0f / 512.0f);
            }
            const float den = rbf(__builtin_amdgcn_sqrtf(rbf(rbf(var) + 1e-5f)));
            *(LAS f2*)(lds + L_STAT + (hf * 64 + 16 * rq + li) * 8) = f2{rbf(mean), __builtin_amdgcn_rcpf(den)};
        }
        FENCE();
    };
    const int nblk = (t < 6 - par) ? 3 : 2, fb2 = 32 + par * 6 + t;
    const char* wdp = (const char*)a.ws + WS_WDP + (size_t)(t * NFB) * 4096;

    f2 tw64[8], twT[8];
    int x3a[4];
    auto fft13_consts = [&](float scale) {
        int lane_ = lane; asm volatile("" : "+v"(lane_));
        const int c2p = lane_ & 3, l3 = (lane_ >> 2) & 7, qb = lane_ >> 5;
        const int wq = rq, q1 = qb ? (wq == 0 ? 4 : 8 - wq) : wq, c2 = 4 * t + c2p;
        const float f64 = (float)l3 * (1.0f / 64.0f);
#pragma unroll
        for (int k = 1; k < 8; k++) tw64[k] = cis(f64 * (float)k);
        const float fT = (float)(8 * c2 * q1 + s2 * l3) * (1.0f / 2048.0f), dT = (float)s2 * (1.0f / 256.0f);
#pragma unroll
        for (int k = 0; k < 8; k++) twT[k] = cis(fT + dT * (float)k) * scale;
#pragma unroll
        for (int k = 0; k < 4; k++) {
            const int ua = (k >> 1) * 4 + (k & 1), u1 = l3 + 8 * ua;
            const int cls = (u1 < 32) ? u1 : ((64 - u1) & 31), hi = u1 >> 5;
            int mbit, sub;
            if (wq == 0) { mbit = qb; sub = hi; } else if (cls != 0) { sub = hi; mbit = qb ^ hi; } else { mbit = hi; sub = qb ^ hi; }
            x3a[k] = L_FFT + wq * 4608 + (cls * 18 + mbit * 8 + sub * 4 + c2p) * 8 - (k == 3 ? 2304 : 0);
        }
        asm volatile("" : "+v"(tw64[1]), "+v"(tw64[2]), "+v"(tw64[3]), "+v"(tw64[4]), "+v"(tw64[5]), "+v"(tw64[6]), "+v"(tw64[7]),
                     "+v"(twT[0]), "+v"(twT[1]), "+v"(twT[2]), "+v"(twT[3]), "+v"(twT[4]), "+v"(twT[5]), "+v"(twT[6]), "+v"(twT[7]),
                     "+v"(x3a[0]), "+v"(x3a[1]), "+v"(x3a[2]), "+v"(x3a[3]));
    };

    const int hoff0 = WS_H + ((s2 * 4 + rq) * NFB) * 1024, hoff1 = WS_H + (((s2 ^ 1) * 4 + rq) * NFB) * 1024;
    RELANE();
    const char* wgs = (const char*)a.wg + (size_t)(32 * rq) * 2752 + cg * 128;
    const char* wus = (const char*)a.wu + (size_t)(32 * rq) * 2752 + cg * 128;
    const u32 wvE = (u32)((lane >> 3) * 2752 + (((lane & 7) ^ (((lane >> 4) & 1) << 1)) * 16)), wvO = (u32)((lane >> 3) * 2752 + (((lane & 7) ^ (((lane >> 4) & 1) << 1) ^ 4) * 16));
    auto dma_w1 = [&](auto KK, auto I) {
        constexpr int kk = KK, g = I >> 1, mat = I & 1;
        dma_add<kk * PG + mat * 4096 + g * 1024, g * 8 * 2752>((g & 1) ? wvO : wvE, (mat ? wus : wgs) + ((BE(kk) + j4) & 3) * (128 * 2752), (u32)(rq * 2 * PG));
    };
    const int rot = (2 * t + 4 * j4) & 15;
    auto dma_wd = [&](auto LI, auto I, int p0) { dma4k_c<LI + I * 16384, 0>(l16, wdp + (size_t)(p0 + 2 * ((rq + 4 * I + rot) & 15)) * 4096, (u32)(rq * 4096)); };

    auto gemm1_half = [&](auto S0C, const u4 (&xa)[16], const u4 (&xb)[16], f4 (&accA)[4], f4 (&accB)[4], f4& acs, auto&& hook) {
        constexpr int S0 = S0C;
        const u32 fz = (u32)((li >> 3) | ((lg & 1) << 1));
        const u32 tb = (u32)((8 * lg + (li >> 2)) * 128 + 8 * (li & 3)) + S0 * PG;
        u32 p0 = tb + (((2 * hf) ^ fz) << 5), p1 = tb + (((2 * hf + 1) ^ fz) << 5), pc = tb + (((2 * hf + (rq & 1)) ^ fz) << 5) + (rq >> 1) * 4096;
        asm volatile("" : "+v"(p0), "+v"(p1), "+v"(pc));
        u4 F[2][5];
        auto ldf = [&](int ks, u4 (&f)[5]) {
            const int off = (ks & 7) * PG;
            f[0] = trfrag2<512>(lds + p0 + off); f[1] = trfrag2<512>(lds + p1 + off); f[2] = trfrag2<512>(lds + p0 + off + 4096); f[3] = trfrag2<512>(lds + p1 + off + 4096); f[4] = trfrag2<512>(lds + pc + off);
        };
        ldf(0, F[0]);
        sfor<8>([&](auto S) {
            constexpr int s = S, ks = S0 | ((s & 3) << 1) | ((s >> 2) & 1), sn = s + 1, kn = ((sn & 3) << 1) | ((sn >> 2) & 1);
            hook(S);
            SCHED();
            if (s + 1 < 8) ldf(kn, F[(s + 1) & 1]);
            u4 (&f)[5] = F[s & 1];
#pragma unroll
            for (int j = 0; j < 4; j++) { accA[j] = mfma(f[j], xa[ks], accA[j]); accB[j] = mfma(f[j], xb[ks], accB[j]); }
            acs = mfma(f[4], ones, acs);
            if (s + 1 < 8) {
                SGB(0x100, 2);
#pragma unroll
                for (int i = 0; i < 8; i++) { SGB(0x8, 1); SGB(0x100, 1); }
                SGB(0x8, 1);
            }
            SCHED();
        });
    };
    f2 st0, st1;
    auto gemm1_epi = [&](f4 (&accA)[4], f4 (&accB)[4], f4 acs, u32 step) {
        if (li == 0) *(LAS f4*)(lds + L_CS + (hf * 4 + rq) * 64 + lg * 16) = acs;
        post(step);
        wait_grp(hf, step);
        LAS char* cs = lds + L_CS + hf * 256 + lg * 16;
        st0 = *(LAS f2*)(lds + L_STAT + (16 * rq + li) * 8);
        st1 = *(LAS f2*)(lds + L_STAT + (64 + 16 * rq + li) * 8);
        st128(geglu8(accA, cs, st0), rs, l16, hoff0 + (2 * cg + hf) * 1024);
        st128(geglu8(accB, cs, st1), rs, l16, hoff1 + (2 * cg + hf) * 1024);
    };
    f4 acc2[4];
#pragma unroll
    for (int j = 0; j < 4; j++) acc2[j] = f4{0, 0, 0, 0};
    const int hsrc = hoff0 + hf * 1024;
    auto g2_mma = [&](auto NC, const u4* hfr, int ldsbase) {
        constexpr int N = decltype(NC)::value;
        u32 pa = (u32)ldsbase + l16; asm volatile("" : "+v"(pa));
        LAS u4* p = (LAS u4*)(lds + pa);
        u4 F[2][4];
#pragma unroll
        for (int j = 0; j < 4; j++) F[0][j] = p[j * 64];
        sfor<N>([&](auto I) {
            constexpr int i = I;
            SCHED();
            if (i + 1 < N) {
#pragma unroll
                for (int j = 0; j < 4; j++) F[(i + 1) & 1][j] = p[(i + 1) * 256 + j * 64];
            }
#pragma unroll
            for (int j = 0; j < 4; j++) acc2[j] = mfma(F[i & 1][j], hfr[i], acc2[j]);
            if (i + 1 < N) {
#pragma unroll
                for (int j = 0; j < 4; j++) { SGB(0x100, 1); SGB(0x8, 1); }
            }
            SCHED();
        });
    };
    if (hf == 0) {
        u4 xa[16], xb[16];
        __builtin_amdgcn_s_setprio(2);
        const int f1st = WS_F1 + ((lane >> 3) * 8 + (((lane >> 2) & 1) ? (rq == 0 ? 4 : 8 - rq) : rq)) * 16384 + pid * 64 + c2p * 16;
        u2 ev;
        { u32 zero = 0; asm volatile("global_load_dwordx2 %0, %1, %2 sc1" : "=v"(ev) : "v"(zero), "s"(a.ws) : "memory"); }
        fft13_consts(1.0f);
        WAITVM(1);
        post(A_XL);
        STAMP(1);
        u32 raw[8];
        const int rw = lane >> 2;
        read_tile(xa, 0);
#pragma unroll
        for (int c1 = 0; c1 < 8; c1++) raw[c1] = *(LAS u32*)(lds + L_SET1 + (16 * rq + rw) * 1024 + (((8 * c1 + t) ^ rw) * 16) + c2p * 4);
        post(A_XC);
        stats(xa);
        STAMP(2);
        f2 v[8];
        {
            const f2 st = *(LAS f2*)(lds + L_STAT + (16 * rq + rw) * 8);
#pragma unroll
            for (int c1 = 0; c1 < 8; c1++) v[c1] = bfup(pkbf(bfup(pkbf(bfup(raw[c1]) - st.xx)) * st.yy));
            dft8<false>(v, kc);
            LAS f2* xb1 = (LAS f2*)(lds + L_FFT) + w * 64 + lane;
#pragma unroll
            for (int q1 = 0; q1 < 8; q1++) { const int reg = (q1 < 4) ? q1 * 2 : (((8 - q1) & 3) * 2 + 1); xb1[reg * 288] = v[q1]; }
        }
        asm volatile("s_waitcnt vmcnt(0)" : "+v"(ev) :: "memory");
        {
            const u64 epoch = ((u64)(u32)__builtin_amdgcn_readfirstlane(ev.y) << 32) | (u32)__builtin_amdgcn_readfirstlane(ev.x);
            u64 tg = (epoch + 1) * 0x9E3779B97F4A7C15ull; tg ^= tg >> 29; tg *= 0xBF58476D1CE4E5B9ull; tg ^= tg >> 32;
            tag = u2{(u32)tg, (u32)(tg >> 32)};
            *(LAS u4*)(lds + L_TAG) = u4{(u32)tg, (u32)(tg >> 32), (u32)(epoch + 1), (u32)((epoch + 1) >> 32)};
        }
        post(A_P1);
#pragma unroll
        for (int c1 = 0; c1 < 8; c1++) *(LAS u32*)(lds + ((rq >> 1) ? L_X1 : L_X0) + ((rq & 1) * 16 + rw) * 128 + ((c1 ^ (rw & 7)) * 16) + c2p * 4) = raw[c1];
        wait_pair(B_W0);
        read_tile(xb, 1);
        post(A_XB);
        u32 pk[8], fr[16];
        const int un = sig < 4 * NFB ? sig : 0, fb = un >> 2, jj = (un >> 1) & 1, tcol = (un & 1) * 4 + rq;
        {
            const u32 vd = (u32)(((fb * 32 + 4 * lg) * 512 + 64 * (4 * jj + (li >> 2)) + 8 * tcol + 2 * (li & 3)) * 2);
            sfor<4>([&](auto I) {
                ldg_u32<I * 1024>(pk[I], vd, a.wd);
                ldg_u32<I * 1024>(pk[4 + I], vd, (const char*)a.wd + 16384);
            });
            const u32 vo = (u32)(((8 * sig + 2 * rq) * 257 + lane) * 2);
            sfor<8>([&](auto I) { constexpr int rr = I >> 2, k = I & 3; ldg_u16<rr * 514 + k * 128>(fr[I], vo, a.wr); ldg_u16<rr * 514 + k * 128>(fr[8 + I], vo, a.wi); });
        }
        wait_grp(0, A_P1);
        {
            LAS f2* reg = (LAS f2*)(lds + L_FFT) + (w * 2 + qb) * 288;
#pragma unroll
            for (int k = 0; k < 8; k++) v[k] = reg[(lane & 31) + 32 * k];
            dft8<false>(v, kc);
#pragma unroll
            for (int k = 1; k < 8; k++) v[k] = cmul<true>(v[k], tw64[k]);
#pragma unroll
            for (int k = 0; k < 8; k++) reg[36 * k + (lane & 31)] = v[k];
            FENCE();
#pragma unroll
            for (int k = 0; k < 8; k++) v[k] = reg[36 * l3 + 4 * k + c2p];
            dft8<false>(v, kc);
#pragma unroll
            for (int k = 0; k < 8; k++) v[k] = cmul<true>(v[k], twT[k]);
            FENCE();
            *(LAS f2*)(lds + x3a[0]) = v[0];
            *(LAS f2*)(lds + x3a[1]) = v[1]; *(LAS f2*)(lds + x3a[1] + 1152) = v[2]; *(LAS f2*)(lds + x3a[1] + 2304) = v[3];
            *(LAS f2*)(lds + x3a[2]) = v[4];
            *(LAS f2*)(lds + x3a[3] + 2304) = v[5]; *(LAS f2*)(lds + x3a[3] + 1152) = v[6]; *(LAS f2*)(lds + x3a[3]) = v[7];
            FENCE();
#pragma unroll
            for (int i = 0; i < 4; i++) {
                u4 o = *(LAS u4*)(lds + L_FFT + w * 4608 + (i * 8 + (lane >> 3)) * 144 + (lane & 7) * 16);
                st128(o, rs, NOF(f1st), i * (64 * 16384));
            }
        }
        STAMP(3);
        __builtin_amdgcn_s_setprio(0);
        asm volatile("s_waitcnt vmcnt(4)" : "+v"(pk[0]), "+v"(pk[1]), "+v"(pk[2]), "+v"(pk[3]), "+v"(pk[4]), "+v"(pk[5]), "+v"(pk[6]), "+v"(pk[7]),
                     "+v"(fr[0]), "+v"(fr[1]), "+v"(fr[2]), "+v"(fr[3]), "+v"(fr[4]), "+v"(fr[5]), "+v"(fr[6]), "+v"(fr[7]),
                     "+v"(fr[8]), "+v"(fr[9]), "+v"(fr[10]), "+v"(fr[11]), "+v"(fr[12]), "+v"(fr[13]), "+v"(fr[14]), "+v"(fr[15]) :: "memory");
        {
            const int so = sig < 4 * NFB ? WS_WDP + ((tcol * NFB + fb) * 4 + 2 * jj) * 1024 : WS_BYTES;
            u4 oe = {__builtin_amdgcn_perm(pk[1], pk[0], 0x05040100), __builtin_amdgcn_perm(pk[3], pk[2], 0x05040100), __builtin_amdgcn_perm(pk[5], pk[4], 0x05040100), __builtin_amdgcn_perm(pk[7], pk[6], 0x05040100)};
            u4 oo = {__builtin_amdgcn_perm(pk[1], pk[0], 0x07060302), __builtin_amdgcn_perm(pk[3], pk[2], 0x07060302), __builtin_amdgcn_perm(pk[5], pk[4], 0x07060302), __builtin_amdgcn_perm(pk[7], pk[6], 0x07060302)};
            st128(oe, rs, l16, so);
            st128(oo, rs, l16, so + 1024);
        }
        {
            const int q1 = lane >> 3, vt = lane & 7, srcl = (q1 + 8 * vt) * 4;
#pragma unroll
            for (int rr = 0; rr < 2; rr++) {
                const int u = 8 * sig + 2 * rq + rr, u1 = u & 63, u2_ = u >> 6;
                int cls, m, sub;
                if ((u1 & 31) != 0) { sub = u1 >> 5; cls = sub ? 64 - u1 : u1; m = sub ? ((8 - q1) & 7) : q1; }
                else { cls = 0; const int hi = u1 >> 5; if ((q1 & 3) == 0) { m = q1; sub = hi; } else { sub = (q1 >> 2) ^ hi; m = sub ? 8 - q1 : q1; } }
                u4 o;
                o.x = __builtin_amdgcn_ds_bpermute(srcl, fr[rr * 4 + 0] | (fr[8 + rr * 4 + 0] << 16)); o.y = __builtin_amdgcn_ds_bpermute(srcl, fr[rr * 4 + 1] | (fr[8 + rr * 4 + 1] << 16));
                o.z = __builtin_amdgcn_ds_bpermute(srcl, fr[rr * 4 + 2] | (fr[8 + rr * 4 + 2] << 16)); o.w = __builtin_amdgcn_ds_bpermute(srcl, fr[rr * 4 + 3] | (fr[8 + rr * 4 + 3] << 16));
                st128(o, rs, NOF(WS_WP + (cls * 8 + m) * 8192 + sub * 4096 + u2_ * 128 + vt * 16), 0);
            }
        }
        WAITVM(4);
        flag_store(WS_FF1 + ((rq * 4 + ((s2 & 7) >> 1)) * 64 + (s2 >> 3) * 16 + (s2 & 1) * 8 + t) * 8);
        STAMP(4);
        f4 accA[4], accB[4], acs = {0, 0, 0, 0};
#pragma unroll
        for (int j = 0; j < 4; j++) { accA[j] = f4{0, 0, 0, 0}; accB[j] = f4{0, 0, 0, 0}; }
        wait_grp(1, B_W1);
        __builtin_amdgcn_s_setprio(1);
        gemm1_half(IC<0>{}, xa, xb, accA, accB, acs, [&](auto S) {
            if (S == 3) {
                WAITVM(1);
                flag_store(sig < 4 * NFB ? WS_FP + (((sig & 1) * 4 + rq) * 128 + (sig >> 1)) * 8 : WS_BYTES);
                flag_store(WS_FW + (((sig & 7) * 4 + rq) * 32 + (sig >> 3)) * 8);
            }
        });
        pin9(accA, accB, acs);
        post(A_S0);
        STAMP(19);
        RELANE();
        int pos1f;
        {
        f2 tw0[4], tw1[8], al[8], be[8];
        const int rb = qb, r3 = 2 * rq + rb;
        const int cls2 = sig >> 3, m2 = sig & 7;
        const int wq2 = (m2 < 4) ? m2 : ((8 - m2) & 3), pos1 = wq2 * 64 + cls2 * 2 + (m2 >> 2);
        const bool special = (cls2 == 0) && ((m2 & 3) == 0);
        int u1a, q1a, u1b, q1b;
        if (cls2 != 0) { u1a = cls2; q1a = m2; u1b = 64 - cls2; q1b = (8 - m2) & 7; }
        else if (special) { u1a = 0; u1b = 32; q1a = q1b = m2; }
        else { u1a = u1b = (m2 < 4) ? 0 : 32; q1a = m2; q1b = 8 - m2; }
        int sA, vH0, sB, vH1;
        if (!special) { sA = 0; vH0 = rq; sB = 1; vH1 = (u1a == 0) ? ((4 - rq) & 3) : 3 - rq; }
        else { sA = sB = rq >> 1; vH0 = rq & 1; vH1 = (rq == 0) ? 2 : (rq == 1) ? 3 : (rq == 2) ? 3 : 2; }
        const bool q0 = (m2 == 0);
        const int dy = ((sA ? u1b : u1a) == 0 && vH0 == 0) ? 8 : 7, dq = q0 ? 8 : 7;
        int y1 = lane >> 3, t1 = lane & 7, y2 = (dy - y1) & 7, t2 = (dq - t1) & 7, hA = vH0, hB = vH1;
        bool self = false;
        if (special && rq == 0) {
            const int k = lane & 31;
            if (qb) { hA = hB = 2; y1 = k >> 3; t1 = k & 7; y2 = 7 - y1; t2 = (dq - t1) & 7; }
            else {
                hA = hB = 0; const int vx = k & 3, y = k >> 2;
                if (!q0 || vx) { y1 = y; t1 = vx; y2 = (8 - y) & 7; t2 = dq - vx; }
                else { t1 = t2 = (y >= 4) ? 4 : 0; const int yy = y & 3; if (yy) { y1 = yy; y2 = 8 - yy; } else { y1 = 0; y2 = 4; self = true; } }
            }
        }
        const int uA = (sA ? u1b : u1a) + 64 * (hA + 4 * y1), uB = (sB ? u1b : u1a) + 64 * (hB + 4 * y2);
        const int qA = (sA ? q1b : q1a) + 8 * t1, qB = (sB ? q1b : q1a) + 8 * t2;
        const int rowB = L_FFT + ((qb ? (vH1 * 2 + sB) : (vH0 * 2 + sA)) * 36 + (lane & 31)) * 8;
        const bool rot = q0 && (t1 == 0);
        const int p3a1 = L_FFT + (y1 * 290 + (hA * 2 + sA) * 36 + t1 * 4) * 8;
        const int p3a2 = L_FFT + (y2 * 290 + (hB * 2 + sB) * 36 + t2 * 4) * 8;
        const float thA = (float)qA * (1.0f / 512.0f), thB = (float)qB * (1.0f / 512.0f);
#pragma unroll
        for (int k = 1; k < 4; k++) tw0[k] = cis((float)r3 * ((float)k / 32.0f));
#pragma unroll
        for (int k = 1; k < 8; k++) tw1[k] = cis((float)c2p * ((float)k / 32.0f));
        f2 csq[8];
#pragma unroll
        for (int e = 0; e < 8; e++) csq[e] = cis(((e >> 2) ? thB : thA) + 0.125f * (float)(e & 3));
        asm volatile("" : "+v"(csq[0]), "+v"(csq[1]), "+v"(csq[2]), "+v"(csq[3]), "+v"(csq[4]), "+v"(csq[5]), "+v"(csq[6]), "+v"(csq[7]),
                     "+v"(tw0[1]), "+v"(tw0[2]), "+v"(tw0[3]), "+v"(tw1[1]), "+v"(tw1[2]), "+v"(tw1[3]), "+v"(tw1[4]), "+v"(tw1[5]), "+v"(tw1[6]), "+v"(tw1[7]));
        {
            const u32 o1 = min(rq * 22 + lane, 85) * 8, o2 = (((qb ? u1b : u1a) >> 1) * 32 + (lane & 31)) * 8, o3 = lane * 8;
            _Pragma("unroll 1") for (int spins = 0; spins < SPIN_CAP; spins++) {
                u2 f1 = __builtin_amdgcn_raw_buffer_load_b64(rs, o1, WS_FP + t * 1024, 16);
                u2 f2_ = __builtin_amdgcn_raw_buffer_load_b64(rs, o2, WS_FW, 16);
                u2 f3 = __builtin_amdgcn_raw_buffer_load_b64(rs, o3, WS_FF1 + (wq2 * 4 + rq) * 512, 16);
                if (__all(f1.x == tag.x && f1.y == tag.y && f2_.x == tag.x && f2_.y == tag.y && f3.x == tag.x && f3.y == tag.y)) break;
                __builtin_amdgcn_s_sleep(2);
                FENCE();
            }
            __builtin_amdgcn_fence(__ATOMIC_ACQUIRE, "wavefront");
            FENCE();
        }
        post(A_FP);
        wait_grp(0, A_S0);
        STAMP(21);
        const int f1off = WS_F1 + sig * 16384 + 2 * rq * 512;
        u4 d[4];
        const u4 wa4 = __builtin_amdgcn_raw_buffer_load_b128(rs, (hA + 4 * y1) * 128 + t1 * 16, WS_WP + sig * 8192 + sA * 4096, 16);
        const u4 wb4 = __builtin_amdgcn_raw_buffer_load_b128(rs, (hB + 4 * y2) * 128 + t2 * 16, WS_WP + sig * 8192 + sB * 4096, 16);
        SCHED();
#pragma unroll
        for (int H = 0; H < 4; H++) d[H] = __builtin_amdgcn_raw_buffer_load_b128(rs, NOF(l16), f1off + H * 4096, 16);
        SCHED();
        f2 wS[2] = {f2{0.f, 0.f}, f2{0.f, 0.f}};
        if (q0) {
            wS[0] = f2{bflo(a.wr[uA * 257 + 256]), bflo(a.wi[uA * 257 + 256])};
            wS[1] = f2{bflo(a.wr[uB * 257 + 256]), bflo(a.wi[uB * 257 + 256])};
        }
        {
            f2 W[8], WB[8];
#pragma unroll
            for (int e = 0; e < 4; e++) { W[e] = bfup(wa4[e]); W[4 + e] = bfup(wb4[e]); }
#pragma unroll
            for (int k = 0; k < 4; k++) { WB[k] = W[4 + 3 - k]; WB[4 + k] = W[3 - k]; }
            if (q0) {
#pragma unroll
                for (int k = 0; k < 4; k++) {
                    f2 n1 = rot ? W[(4 - k) & 3] : W[3 - k], n2 = rot ? W[4 + ((4 - k) & 3)] : W[4 + 3 - k];
                    WB[k] = self ? n1 : n2; WB[4 + k] = self ? n2 : n1;
                }
#pragma unroll
                for (int e = 0; e < 8; e += 4) {
                    f2 own = wS[e >> 2], oth = self ? own : wS[1 - (e >> 2)];
                    f2 S0 = pkfma(WB[e], kc.pm, W[e]) * 0.5f, S1c = pkfma(own, kc.pm, oth) * 0.5f;
                    WB[e] = rot ? S1c : WB[e];
                    al[e] = rot ? S0 : W[e];
                }
                W[0] = al[0]; W[4] = al[4];
            }
#pragma unroll
            for (int e = 0; e < 8; e++) {
                const f2 cs = csq[e];
                f2 P = pkfma(WB[e], kc.pm, W[e]), M = pkfma(WB[e], kc.pm.yx, W[e]);
                asm("v_pk_fma_f32 %0, %1, %2, %3 op_sel:[1,0,0] op_sel_hi:[1,1,1] neg_lo:[1,0,0] neg_hi:[1,0,0]" : "=v"(al[e]) : "v"(cs), "v"(M), "v"(P));
                asm("v_pk_mul_f32 %0, %1, %2 op_sel:[1,0] op_sel_hi:[0,0] neg_lo:[1,0]" : "=v"(be[e]) : "v"(M), "v"(cs));
            }
            asm volatile("" : "+v"(al[0]), "+v"(al[1]), "+v"(al[2]), "+v"(al[3]), "+v"(al[4]), "+v"(al[5]), "+v"(al[6]), "+v"(al[7]),
                         "+v"(be[0]), "+v"(be[1]), "+v"(be[2]), "+v"(be[3]), "+v"(be[4]), "+v"(be[5]), "+v"(be[6]), "+v"(be[7]),
                         "+v"(tw0[1]), "+v"(tw0[2]), "+v"(tw0[3]), "+v"(tw1[1]), "+v"(tw1[2]), "+v"(tw1[3]), "+v"(tw1[4]), "+v"(tw1[5]), "+v"(tw1[6]), "+v"(tw1[7]));
        }
        STAMP(5);
        __builtin_amdgcn_s_setprio(2);
        f2 e0[4], e1[4];
#pragma unroll
        for (int H = 0; H < 4; H++) { e0[H] = f2{__uint_as_float(d[H].x), __uint_as_float(d[H].y)}; e1[H] = f2{__uint_as_float(d[H].z), __uint_as_float(d[H].w)}; }
        dft4<false>(e0[0], e0[1], e0[2], e0[3], kc); dft4<false>(e1[0], e1[1], e1[2], e1[3], kc);
        STAMP(6);
        LAS char* area = lds + L_FFT + rq * 4640;
#pragma unroll
        for (int k = 0; k < 4; k++) {
            if (k) { e0[k] = cmul<true>(e0[k], tw0[k]); e1[k] = cmul<true>(e1[k], tw0[k]); }
            *(LAS f4*)(area + (l3 * 72 + 2 * c2p + 8 * k + 32 * rb) * 8) = f4{e0[k].x, e0[k].y, e1[k].x, e1[k].y};
        }
        FENCE();
#pragma unroll
        for (int k = 0; k < 8; k++) v[k] = *(LAS f2*)(area + (k * 72 + lane) * 8);
        dft8<false>(v, kc);
#pragma unroll
        for (int k = 1; k < 8; k++) v[k] = cmul<true>(v[k], tw1[k]);
        FENCE();
        LAS char* pb = lds + L_FFT + (r3 * 290 + l3 * 36 + c2p) * 8;
#pragma unroll
        for (int k = 0; k < 8; k++) *(LAS f2*)(pb + k * 32) = v[k];
        post(A_E1);
        wait_grp(0, A_E1);
#pragma unroll
        for (int k = 0; k < 8; k++) v[k] = *(LAS f2*)(lds + rowB + k * 2320);
        dft8<false>(v, kc);
#pragma unroll
        for (int k = 0; k < 8; k++) *(LAS f2*)(lds + rowB + k * 2320) = v[k];
        FENCE();
        f4 a0 = *(LAS f4*)(lds + p3a1), a1 = *(LAS f4*)(lds + p3a1 + 16), b0 = *(LAS f4*)(lds + p3a2), b1 = *(LAS f4*)(lds + p3a2 + 16);
        f2 X[8] = {a0.xy, a0.zw, a1.xy, a1.zw, b0.xy, b0.zw, b1.xy, b1.zw};
        dft4<false>(X[0], X[1], X[2], X[3], kc); dft4<false>(X[4], X[5], X[6], X[7], kc);
        f2 Y[8];
#pragma unroll
        for (int k = 0; k < 4; k++) { Y[k] = X[4 + 3 - k]; Y[4 + k] = X[3 - k]; }
        if (q0) {
#pragma unroll
            for (int k = 0; k < 4; k++) {
                f2 n1 = rot ? X[(4 - k) & 3] : X[3 - k], n2 = rot ? X[4 + ((4 - k) & 3)] : X[4 + 3 - k];
                Y[k] = self ? n1 : n2; Y[4 + k] = self ? n2 : n1;
            }
        }
        f2 O[8];
#pragma unroll
        for (int e = 0; e < 8; e++) O[e] = cfmac_conj(cmul<false>(X[e], al[e]), be[e], Y[e]);
        dft4<true>(O[0], O[1], O[2], O[3], kc); dft4<true>(O[4], O[5], O[6], O[7], kc);
        *(LAS f4*)(lds + p3a1) = f4{O[0].x, O[0].y, O[1].x, O[1].y}; *(LAS f4*)(lds + p3a1 + 16) = f4{O[2].x, O[2].y, O[3].x, O[3].y};
        *(LAS f4*)(lds + p3a2) = f4{O[4].x, O[4].y, O[5].x, O[5].y}; *(LAS f4*)(lds + p3a2 + 16) = f4{O[6].x, O[6].y, O[7].x, O[7].y};
        FENCE();
#pragma unroll
        for (int k = 0; k < 8; k++) v[k] = *(LAS f2*)(lds + rowB + k * 2320);
        dft8<true>(v, kc);
#pragma unroll
        for (int k = 0; k < 8; k++) *(LAS f2*)(lds + rowB + k * 2320) = v[k];
        post(A_E2);
        wait_grp(0, A_E2);
#pragma unroll
        for (int k = 0; k < 8; k++) v[k] = *(LAS f2*)(pb + k * 32);
#pragma unroll
        for (int k = 1; k < 8; k++) v[k] = cmul<false>(v[k], tw1[k]);
        dft8<true>(v, kc);
        FENCE();
#pragma unroll
        for (int k = 0; k < 8; k++) *(LAS f2*)(area + (k * 72 + lane) * 8) = v[k];
        FENCE();
#pragma unroll
        for (int k = 0; k < 4; k++) {
            f4 q = *(LAS f4*)(area + (l3 * 72 + 2 * c2p + 8 * k + 32 * rb) * 8);
            e0[k] = q.xy; e1[k] = q.zw;
            if (k) { e0[k] = cmul<false>(e0[k], tw0[k]); e1[k] = cmul<false>(e1[k], tw0[k]); }
        }
        dft4<true>(e0[0], e0[1], e0[2], e0[3], kc); dft4<true>(e1[0], e1[1], e1[2], e1[3], kc);
        const int f2st = WS_F2 + (r3 * 8 + l3) * 16384 + pos1 * 64 + c2p * 16;
#pragma unroll
        for (int H = 0; H < 4; H++) {
            u4 o = {__float_as_uint(e0[H].x), __float_as_uint(e0[H].y), __float_as_uint(e1[H].x), __float_as_uint(e1[H].y)};
            st128(o, rs, NOF(f2st), H * (64 * 16384));
        }
            pos1f = pos1;
        }
        STAMP(7);
        __builtin_amdgcn_s_setprio(1);
        RELANE();
        wait_grp(1, B_CS);
        gemm1_half(IC<8>{}, xa, xb, accA, accB, acs, [&](auto S) { if (S == 5) { WAITVM(0); flag_store(WS_FF2 + (rq * 256 + pos1f) * 8); } });
        STAMP(8);
        RELANE();
        gemm1_epi(accA, accB, acs, A_CS);
        __builtin_amdgcn_s_setprio(0);
        sfor<4>([&](auto I) { dma_wd(IC<L_SET1>{}, I, 0); });
        WAITVM(16);
        flag_store(WS_FHA + ((rg * 8 + w) * 16 + cg) * 8);
        STAMP(9);
        {
            u4 d[4];
        fft13_consts(0.5f / 524288.0f);
        flag_wait(lane * 8, WS_FF2 + (((s2 & 7) >> 1) * 256 + rq * 64) * 8);
        const int off = WS_F2 + pid * 16384 + rq * 4096;
#pragma unroll
        for (int i = 0; i < 4; i++) d[i] = __builtin_amdgcn_raw_buffer_load_b128(rs, NOF(l16), off + i * 1024, 16);
        STAMP(13);
        __builtin_amdgcn_s_setprio(2);
#pragma unroll
        for (int i = 0; i < 4; i++) *(LAS u4*)(lds + L_FFT + rq * 4608 + (i * 8 + (lane >> 3)) * 144 + (lane & 7) * 16) = d[i];
        FENCE();
        v[0] = *(LAS f2*)(lds + x3a[0]);
        v[1] = *(LAS f2*)(lds + x3a[1]); v[2] = *(LAS f2*)(lds + x3a[1] + 1152); v[3] = *(LAS f2*)(lds + x3a[1] + 2304);
        v[4] = *(LAS f2*)(lds + x3a[2]);
        v[5] = *(LAS f2*)(lds + x3a[3] + 2304); v[6] = *(LAS f2*)(lds + x3a[3] + 1152); v[7] = *(LAS f2*)(lds + x3a[3]);
#pragma unroll
        for (int k = 0; k < 8; k++) v[k] = cmul<false>(v[k], twT[k]);
        dft8<true>(v, kc);
#pragma unroll
        for (int k = 1; k < 8; k++) v[k] = cmul<false>(v[k], tw64[k]);
        FENCE();
        LAS f2* reg = (LAS f2*)(lds + L_FFT) + (rq * 2 + qb) * 288;
#pragma unroll
        for (int k = 0; k < 8; k++) reg[36 * l3 + 4 * k + c2p] = v[k];
        FENCE();
#pragma unroll
        for (int k = 0; k < 8; k++) v[k] = reg[36 * k + (lane & 31)];
        dft8<true>(v, kc);
        FENCE();
#pragma unroll
        for (int k = 0; k < 8; k++) reg[(lane & 31) + 32 * k] = v[k];
        post(A_F3);
        wait_grp(0, A_F3);
        if (b == 0 && rq == 0) __builtin_amdgcn_raw_buffer_store_b64(*(LAS u2*)(lds + L_TAG + 8), rs, lane ? WS_BYTES : 0, 0, 16);
        LAS f2* xb1 = (LAS f2*)(lds + L_FFT) + rq * 64 + lane;
#pragma unroll
        for (int q1 = 0; q1 < 8; q1++) { const int rg_ = (q1 < 4) ? q1 * 2 : (((8 - q1) & 3) * 2 + 1); v[q1] = xb1[rg_ * 288]; }
        post(A_F4);
        dft8<true>(v, kc);
        wait_grp(0, A_F4);
#pragma unroll
        for (int c1 = 0; c1 < 8; c1++) *(LAS u32*)(lds + L_S + (16 * rq + (lane >> 2)) * 144 + c1 * 16 + c2p * 4) = pkbf(v[c1]);
        }
        STAMP(10);
        u4 hfr[16];
        flag_wait(li * 8, WS_FHA + (rg * 8 + w) * 128);
#pragma unroll
        for (int i = 0; i < 16; i++) { hfr[i] = __builtin_amdgcn_raw_buffer_load_b128(rs, l16, hsrc + 2 * ((i + rot) & 15) * 1024, 16); SCHED(); }
        STAMP(17);
        g2_mma(IC<16>{}, hfr, L_SET1);
    } else {
        u4 xa[16], xb[16];
        __builtin_amdgcn_s_sleep(2);
        sfor<8>([&](auto I) { dma_w1(IC<0>{}, I); });
        wait_pair(A_XL);
        read_tile(xa, 0);
        asm volatile("s_waitcnt lgkmcnt(0)" ::: "memory");
        wait_pair(A_XC);
        dma_x();
        sfor<8>([&](auto I) { dma_w1(IC<1>{}, I); });
        STAMP(1);
        asm volatile("s_waitcnt vmcnt(8)" : "+v"(cpf) :: "memory");
        post(B_W0);
        read_tile(xb, 1);
        STAMP(2);
        {
            f4 accA[4], accB[4], acs = {0, 0, 0, 0};
#pragma unroll
            for (int j = 0; j < 4; j++) { accA[j] = f4{0, 0, 0, 0}; accB[j] = f4{0, 0, 0, 0}; }
            wait_grp(1, B_W0);
            STAMP(3);
            gemm1_half(IC<0>{}, xa, xb, accA, accB, acs, [&](auto S) {
                constexpr int s = S;
                if (s == 1) { WAITVM(0); post(B_W1); wait_pair(A_XB); }
                if (s == 3) wait_grp(1, B_W1);
                if constexpr (s >= 1 && s <= 4) sfor<4>([&](auto I) { dma_w1(IC<8 + ((s - 1) >> 1)>{}, IC<((s - 1) & 1) * 4 + I>{}); });
                if (s == 7) { WAITVM(8); post(B_W2); }
            });
            STAMP(14);
            wait_grp(1, B_W2);
            get_tag();
            gemm1_half(IC<8>{}, xa, xb, accA, accB, acs, [&](auto S) {
                if (S == 1) { WAITVM(0); post(B_W3); }
                if (S == 3) wait_grp(1, B_W3);
            });
            STAMP(4);
            RELANE();
            pin9(accA, accB, acs);
            if (nblk == 3) {
                const u32 wvB = (u32)((lane >> 2) * 2752 + (((lane & 3) ^ (((lane >> 5) & 1) << 1)) * 16));
                const char* wbs = (const char*)((rq >> 1) ? a.wu : a.wg) + (size_t)((rq & 1) * 16) * 2752 + fb2 * 64;
                wait_grp(0, A_S0);
                sfor<16>([&](auto K) { constexpr int k = K; dma_add<k * 4096, IX(k) * 32 * 2752>(wvB, wbs + ((BE(k) + j4) & 3) * (128 * 2752), (u32)(rq * 1024)); });
            }
            SCHED();
            stats(xb);
            STAMP(15);
            gemm1_epi(accA, accB, acs, B_CS);
        }
        STAMP(5);
        if (nblk == 3) {
            WAITVM(2); post(B_WB);
            wait_grp(1, B_WB);
            const u32 tb = (u32)((8 * lg + (li >> 2)) * 64 + (lg & 1) * 32 + 8 * (li & 3));
            LAS char* p0 = lds + tb; LAS char* p1 = lds + (tb ^ 32);
            LAS char* pc = lds + (tb ^ ((rq & 1) * 32)) + (rq >> 1) * 2048;
            f4 acc[2][4], acs = {0, 0, 0, 0};
#pragma unroll
            for (int j = 0; j < 4; j++) { acc[0][j] = f4{0, 0, 0, 0}; acc[1][j] = f4{0, 0, 0, 0}; }
            u4 F[2][5];
            auto ldf = [&](int ks, u4 (&f)[5]) {
                const int off = ks * 4096;
                f[4] = trfrag2<256>(pc + off);
                f[0] = trfrag2<256>(p0 + off); f[1] = trfrag2<256>(p1 + off); f[2] = trfrag2<256>(p0 + off + 2048); f[3] = trfrag2<256>(p1 + off + 2048);
            };
            ldf(0, F[0]);
            sfor<16>([&](auto S) {
                constexpr int ks = S;
                if (ks == 5) { WAITVM(0); flag_store(WS_FHA + ((rg * 8 + w) * 16 + cg) * 8); }
                SCHED();
                if (ks + 1 < 16) ldf(ks + 1, F[(ks + 1) & 1]);
                u4 (&f)[5] = F[ks & 1];
                acs = mfma(f[4], ones, acs);
#pragma unroll
                for (int j = 0; j < 4; j++) { acc[0][j] = mfma(f[j], xa[ks], acc[0][j]); acc[1][j] = mfma(f[j], xb[ks], acc[1][j]); }
                if (ks + 1 < 16) {
                    SGB(0x100, 2);
#pragma unroll
                    for (int i = 0; i < 8; i++) { SGB(0x8, 1); SGB(0x100, 1); }
                    SGB(0x8, 1);
                }
                SCHED();
            });
            if (li == 0) *(LAS f4*)(lds + L_CS + 512 + rq * 64 + lg * 16) = acs;
            post(B_PB);
            wait_grp(1, B_PB);
            st128(geglu8(acc[0], lds + L_CS + 512 + lg * 16, st0), rs, l16, hoff0 + fb2 * 1024);
            st128(geglu8(acc[1], lds + L_CS + 512 + lg * 16, st1), rs, l16, hoff1 + fb2 * 1024);
        } else { WAITVM(0); flag_store(WS_FHA + ((rg * 8 + w) * 16 + cg) * 8); }
        STAMP(6);
        RELANE();
        wait_grp(0, A_FP);
        sfor<4>([&](auto I) { dma_wd(IC<0>{}, I, 1); });
        if (nblk == 3) { WAITVM(16); flag_store(WS_FHB + ((rg * 4 + rq) * 16 + fb2 - 32) * 8); }
        u4 hfr[16];
        flag_wait(li * 8, WS_FHA + (rg * 8 + w) * 128);
        post(B_D1);
#pragma unroll
        for (int i = 0; i < 16; i++) { hfr[i] = __builtin_amdgcn_raw_buffer_load_b128(rs, l16, hsrc + 2 * ((i + rot) & 15) * 1024, 16); SCHED(); }
        STAMP(9);
        wait_grp(1, B_D1);
        g2_mma(IC<16>{}, hfr, 0);
        post(B_G1);
        pin4(acc2);
        STAMP(10);
        wait_grp(1, B_G1);
        sfor<3>([&](auto I) { constexpr int i = I; dma4k_c<2 * PG + i * 16384, 0>(l16, wdp + (size_t)min(32 + 4 * i + rq, 42) * 4096, (u32)(rq * 4096)); });
        flag_wait(min(li, 10) * 8, WS_FHB + (rg * 4 + rq) * 128);
        post(B_D3);
#pragma unroll
        for (int i = 0; i < 11; i++) { hfr[i] = __builtin_amdgcn_raw_buffer_load_b128(rs, l16, hoff0 + (32 + i) * 1024, 16); SCHED(); }
        STAMP(13);
        wait_grp(1, B_D3);
        g2_mma(IC<11>{}, hfr, 2 * PG);
    }
    RELANE();
    STAMP(11);
    BAR();
    {
        LAS f4* xs = (LAS f4*)(lds + w * 2048) + lane;
        LAS f4* xr = (LAS f4*)(lds + (w ^ 4) * 2048) + lane;
        f4 me, mo;
        if (hf) { xs[0] = acc2[0]; xs[64] = acc2[1]; me = acc2[2]; mo = acc2[3]; } else { xs[0] = acc2[2]; xs[64] = acc2[3]; me = acc2[0]; mo = acc2[1]; }
        BAR();
        const int c1 = 4 * hf + lg;
        me += xr[0]; mo += xr[64];
        const u4 sv = *(LAS u4*)(lds + L_S + (16 * rq + li) * 144 + c1 * 16);
        const u4 xv = *(LAS u4*)(lds + ((rq >> 1) ? L_X1 : L_X0) + ((rq & 1) * 16 + li) * 128 + ((c1 ^ (li & 7)) * 16));
        u32 res[4];
#pragma unroll
        for (int e = 0; e < 4; e++) {
            const f2 ov = bfup(pkbf(f2{me[e], mo[e]}));
            const f2 z = ov * -1.44269504f;
            const f2 d = f2{__builtin_amdgcn_exp2f(z.x), __builtin_amdgcn_exp2f(z.y)} + f2{1.f, 1.f};
            const f2 g = bfup(pkbf(ov * f2{__builtin_amdgcn_rcpf(d.x), __builtin_amdgcn_rcpf(d.y)}));
            res[e] = pkbf(bfup(xv[e]) + bfup(pkbf(bfup(sv[e]) * g)));
        }
        *(u4*)(a.out + (size_t)(s2 + 32 * (16 * rq + li)) * 512 + 64 * c1 + 8 * t) = u4{res[0], res[1], res[2], res[3]};
    }
    STAMP(12);
    if (b == 0 && w == 0) {
        asm volatile("s_waitcnt lgkmcnt(0)" ::: "memory");
        u32 t0 = *(volatile LAS u32*)(lds + L_FFT + 22784), tk = *(volatile LAS u32*)(lds + L_FFT + 22784 + PROBE * 8);
        u32 d = __builtin_amdgcn_readfirstlane(min(tk - t0, 5000u)) * 10;
        u64 now = __builtin_amdgcn_s_memrealtime();
        while (__builtin_amdgcn_s_memrealtime() < now + d) __builtin_amdgcn_s_sleep(8);
    }
}

extern "C" void kernel_launch(void* const* d_in, const int* in_sizes, int n_in,
                              void* d_out, int out_size, void* d_ws, size_t ws_size,
                              hipStream_t stream) {
    Args a;
    a.x = (const u16*)d_in[0]; a.wr = (const u16*)d_in[1]; a.wi = (const u16*)d_in[2];
    a.wg = (const u16*)d_in[4]; a.wu = (const u16*)d_in[5]; a.wd = (const u16*)d_in[6];
    a.out = (u16*)d_out; a.ws = (unsigned char*)d_ws;
    (void)hipFuncSetAttribute((const void*)fused, hipFuncAttributeMaxDynamicSharedMemorySize, L_END);
    fused<<<256, 512, L_END, stream>>>(a);
}
```

```cpp
#define PROBE 27
#include <hip/hip_runtime.h>
#include <stdint.h>
#include <type_traits>

typedef unsigned short u16;
typedef unsigned u32;
typedef unsigned long long u64;
typedef float f2 __attribute__((ext_vector_type(2)));
typedef float f4 __attribute__((ext_vector_type(4)));
typedef u32 u4 __attribute__((ext_vector_type(4)));
typedef u32 u2 __attribute__((ext_vector_type(2)));
typedef short s4 __attribute__((ext_vector_type(4)));
typedef __bf16 bf8 __attribute__((ext_vector_type(8)));
typedef __bf16 bf2 __attribute__((ext_vector_type(2)));
#define LAS __attribute__((address_space(3)))
#define GAS __attribute__((address_space(1)))
#define DEV __device__ __forceinline__
template <int N> using IC = std::integral_constant<int, N>;

constexpr int NFB = 43;
constexpr int WS_FF1 = 4096;
constexpr int WS_FF2 = WS_FF1 + 8192;
constexpr int WS_FHA = WS_FF2 + 8192;
constexpr int WS_FHB = WS_FHA + 16384;
constexpr int WS_FP = WS_FHB + 16384;
constexpr int WS_FW = WS_FP + 8192;
constexpr int WS_F1 = 1 << 20, WS_F2 = WS_F1 + (4 << 20), WS_H = WS_F2 + (4 << 20);
constexpr int WS_WDP = WS_H + 32 * 4 * NFB * 1024;
constexpr int WS_WP = WS_WDP + 8 * NFB * 4 * 1024;
constexpr int WS_STAMP = WS_WP + (2 << 20);
constexpr int WS_BYTES = WS_STAMP + (1 << 20);
constexpr int PG = 8192;
constexpr int L_SET1 = 8 * PG;
constexpr int L_FFT = 16 * PG;
constexpr int L_SMALL = 19 * PG;
constexpr int L_PROG = L_SMALL;
constexpr int L_TAG = L_SMALL + 32;
constexpr int L_STAT = L_SMALL + 64;
constexpr int L_CS = L_SMALL + 2048;
constexpr int L_X0 = L_FFT + 18688, L_X1 = L_SMALL + 3072;
constexpr int L_S = L_FFT;
constexpr int L_END = 20 * PG;
constexpr u32 SENT = 0xFFFFFFFFu;
constexpr int CODE_BYTES = 44 * 1024;
constexpr int SPIN_CAP = 1 << 16;

struct Args { const u16* x; const u16* wr; const u16* wi; const u16* wg; const u16* wu; const u16* wd; u16* out; unsigned char* ws; };

#define BAR() asm volatile("s_waitcnt lgkmcnt(0)\n\ts_barrier" ::: "memory")
#define WAITVM(n) asm volatile("s_waitcnt vmcnt(" #n ")" ::: "memory")
#define NOF(x) (x)
#define NOPL(x) (x)
#define FENCE() asm volatile("" ::: "memory")
#define SCHED() __builtin_amdgcn_sched_barrier(0)
#define SGB(mask, n) __builtin_amdgcn_sched_group_barrier(mask, n, 0)
#define STAMP(i) do { u64 t_ = __builtin_amdgcn_s_memrealtime(); *(volatile LAS u2*)(lds + L_FFT + 22784 + (w * 28 + (i)) * 8) = u2{(u32)t_, (u32)(t_ >> 32)}; } while (0)
#define CSTAMP(i) do { u64 t_ = __builtin_amdgcn_s_memtime(); *(volatile LAS u2*)(lds + L_FFT + 22784 + (w * 28 + (i)) * 8) = u2{(u32)t_, (u32)(t_ >> 32)}; } while (0)

DEV f2 pkfma(f2 a, f2 b, f2 c) { return __builtin_elementwise_fma(a, b, c); }
struct KC { f2 pm, rpm; };
DEV f2 addmi(f2 a, f2 b, const KC& k) { return pkfma(b.yx, k.pm, a); }
DEV f2 addpi(f2 a, f2 b, const KC& k) { return pkfma(b.yx, k.pm.yx, a); }
template <bool INV> DEV void dft4(f2& a0, f2& a1, f2& a2, f2& a3, const KC& k) {
    f2 t0 = a0 + a2, t1 = a0 - a2, t2 = a1 + a3, t3 = a1 - a3;
    a0 = t0 + t2; a2 = t0 - t2;
    if (INV) { a1 = addpi(t1, t3, k); a3 = addmi(t1, t3, k); } else { a1 = addmi(t1, t3, k); a3 = addpi(t1, t3, k); }
}
template <bool INV> DEV void dft8(f2 (&v)[8], const KC& k) {
    f2 a0 = v[0] + v[4], a4 = v[0] - v[4], a1 = v[1] + v[5], a5 = v[1] - v[5];
    f2 a2 = v[2] + v[6], a6 = v[2] - v[6], a3 = v[3] + v[7], a7 = v[3] - v[7];
    dft4<INV>(a0, a1, a2, a3, k);
    v[0] = a0; v[2] = a1; v[4] = a2; v[6] = a3;
    f2 b1, b3, t0, t1;
    if (INV) { b1 = addpi(a5, a5, k); b3 = addpi(-a7, a7, k); t0 = addpi(a4, a6, k); t1 = addmi(a4, a6, k); }
    else { b1 = addmi(a5, a5, k); b3 = addmi(-a7, a7, k); t0 = addmi(a4, a6, k); t1 = addpi(a4, a6, k); }
    f2 t2 = b1 + b3, t3 = b1 - b3;
    v[1] = pkfma(t2, k.rpm.xx, t0); v[5] = pkfma(t2, k.rpm.yy, t0);
    if (INV) { v[3] = pkfma(t3.yx, k.rpm.yx, t1); v[7] = pkfma(t3.yx, k.rpm, t1); }
    else { v[3] = pkfma(t3.yx, k.rpm, t1); v[7] = pkfma(t3.yx, k.rpm.yx, t1); }
}
template <bool CONJ> DEV f2 cmul(f2 a, f2 w) {
    f2 r;
    if (CONJ) asm("v_pk_mul_f32 %0, %1, %2 op_sel:[1,1] op_sel_hi:[0,1]\n\tv_pk_fma_f32 %0, %1, %2, %0 op_sel_hi:[1,0,1] neg_hi:[0,0,1]" : "=&v"(r) : "v"(a), "v"(w));
    else asm("v_pk_mul_f32 %0, %1, %2 op_sel:[1,1] op_sel_hi:[0,1]\n\tv_pk_fma_f32 %0, %1, %2, %0 op_sel_hi:[1,0,1] neg_lo:[0,0,1]" : "=&v"(r) : "v"(a), "v"(w));
    return r;
}
DEV f2 cfmac_conj(f2 t, f2 beta, f2 B) {
    asm("v_pk_fma_f32 %0, %2, %1, %0 op_sel_hi:[0,1,1]\n\tv_pk_fma_f32 %0, %2, %1, %0 op_sel:[1,1,0] op_sel_hi:[1,0,1] neg_hi:[0,1,0]" : "+v"(t) : "v"(beta), "v"(B));
    return t;
}
DEV f2 cis(float r) { return f2{__builtin_amdgcn_cosf(r), __builtin_amdgcn_sinf(r)}; }
DEV float bflo(u32 v) { return __uint_as_float(v << 16); }
DEV float bfhi(u32 v) { return __uint_as_float(v & 0xFFFF0000u); }
DEV f2 bfup(u32 v) { return f2{bflo(v), bfhi(v)}; }
DEV u32 pkbf(f2 v) { bf2 r; r.x = (__bf16)v.x; r.y = (__bf16)v.y; u32 o = __builtin_bit_cast(u32, r); asm("" : "+v"(o)); return o; }
DEV float rbf(float f) { return __uint_as_float(((u32)__builtin_bit_cast(u16, (__bf16)f)) << 16); }
DEV f4 mfma(u4 a, u4 b, f4 c) { return __builtin_amdgcn_mfma_f32_16x16x32_bf16(__builtin_bit_cast(bf8, a), __builtin_bit_cast(bf8, b), c, 0, 0, 0); }
DEV u4 sanitize(u4 v) { return u4{min(v.x, 0xFFFFFFFEu), min(v.y, 0xFFFFFFFEu), min(v.z, 0xFFFFFFFEu), min(v.w, 0xFFFFFFFEu)}; }
DEV u32 max4(u4 v) { return max(max(v.x, v.y), max(v.z, v.w)); }
DEV void st128(u4 v, __amdgpu_buffer_rsrc_t rs, u32 voff, int soff) {
    __builtin_amdgcn_raw_buffer_store_b128(v, rs, voff, soff, 16);
    asm volatile("s_nop 1" :: "v"(v) : "memory");
}
template <int OFF2> DEV u4 trfrag2(LAS char* p) {
    s4 r0 = __builtin_amdgcn_ds_read_tr16_b64_v4i16((LAS s4*)p);
    s4 r1 = __builtin_amdgcn_ds_read_tr16_b64_v4i16((LAS s4*)(p + OFF2));
    u2 x0 = __builtin_bit_cast(u2, r0), x1 = __builtin_bit_cast(u2, r1);
    return u4{x0.x, x0.y, x1.x, x1.y};
}
template <int LI, int GI> DEV void dma_add(u32 vb, const void* sbase, u32 sl) {
    const u32 va = vb + (u32)GI;
    asm volatile("s_add_u32 m0, %2, %3\n\ts_nop 0\n\tglobal_load_lds_dwordx4 %0, %1" :: "v"(va), "s"(sbase), "s"(sl), "i"(LI) : "memory", "scc");
}
template <int LI, int GI> DEV void dma4k_c(u32 vb, const void* sbase, u32 sl) {
    const u32 va = vb + (u32)GI;
    asm volatile("s_add_u32 m0, %2, %3\n\ts_nop 0\n\tglobal_load_lds_dwordx4 %0, %1 sc1\n\tglobal_load_lds_dwordx4 %0, %1 offset:1024 sc1\n\t"
                 "global_load_lds_dwordx4 %0, %1 offset:2048 sc1\n\tglobal_load_lds_dwordx4 %0, %1 offset:3072 sc1"
                 :: "v"(va), "s"(sbase), "s"(sl), "i"(LI) : "memory", "scc");
}
template <int OFF> DEV void ldg_u16(u32& d, u32 vo, const void* sb) { asm volatile("global_load_ushort %0, %1, %2 offset:%3" : "=v"(d) : "v"(vo), "s"(sb), "i"(OFF) : "memory"); }
template <int OFF> DEV void ldg_u32(u32& d, u32 vo, const void* sb) { asm volatile("global_load_dword %0, %1, %2 offset:%3" : "=v"(d) : "v"(vo), "s"(sb), "i"(OFF) : "memory"); }
DEV void lds_wait(u32 addr, u32 v) {
    u32 tmp, cnt;
    asm volatile("s_mov_b32 %1, 0x80000\n"
                 "1:\n\tds_read_b32 %0, %2\n\ts_sub_u32 %1, %1, 1\n\ts_waitcnt lgkmcnt(0)\n\ts_cbranch_scc1 2f\n\tv_cmp_gt_u32 vcc, %3, %0\n\ts_cbranch_vccnz 1b\n2:"
                 : "=&v"(tmp), "=&s"(cnt) : "v"(addr), "s"(v) : "memory", "vcc", "scc");
}
template <int N, typename F> DEV void sfor(F&& f) { if constexpr (N > 0) { sfor<N - 1>(f); f(IC<N - 1>{}); } }

DEV void pin9(f4 (&x)[4], f4 (&y)[4], f4& z) {
    asm volatile("" : "+v"(x[0]), "+v"(x[1]), "+v"(x[2]), "+v"(x[3]), "+v"(y[0]), "+v"(y[1]), "+v"(y[2]), "+v"(y[3]), "+v"(z));
}
DEV void pin4(f4 (&x)[4]) { asm volatile("" : "+v"(x[0]), "+v"(x[1]), "+v"(x[2]), "+v"(x[3])); }
DEV void dma_rt(u32 voff, const void* sbase, u32 sl) {
    asm volatile("s_mov_b32 m0, %2\n\ts_nop 0\n\tglobal_load_lds_dwordx4 %0, %1" :: "v"(voff), "s"(sbase), "s"(sl) : "memory");
}
DEV u4 geglu8(const f4 (&acc)[4], LAS char* cs, f2 st) {
    u32 hw[4];
    const f2 nm = f2{-st.x, -st.x}, ri = f2{st.y, st.y};
#pragma unroll
    for (int c = 0; c < 2; c++) {
        f4 cg_ = *(LAS f4*)(cs + c * 64), cu_ = *(LAS f4*)(cs + (2 + c) * 64);
#pragma unroll
        for (int e = 0; e < 2; e++) {
            f2 g = pkfma(e ? cg_.zw : cg_.xy, nm, e ? acc[c].zw : acc[c].xy) * ri;
            f2 u = pkfma(e ? cu_.zw : cu_.xy, nm, e ? acc[2 + c].zw : acc[2 + c].xy) * ri;
            f2 z = g * pkfma(g * g, f2{-0.10294324f, -0.10294324f}, f2{-2.3022082f, -2.3022082f});
            f2 d = f2{__builtin_amdgcn_exp2f(z.x), __builtin_amdgcn_exp2f(z.y)} + f2{1.f, 1.f};
            f2 r = f2{__builtin_amdgcn_rcpf(d.x), __builtin_amdgcn_rcpf(d.y)};
            hw[2 * c + e] = pkbf(g * r * u);
        }
    }
    return u4{hw[0], hw[1], hw[2], hw[3]};
}

enum { A_XL = 1, A_XC, A_P1, A_XB, A_S0, A_FP, A_E1, A_E2, A_CS, A_F3, A_F4, A_D1 };
enum { B_W0 = 1, B_W1, B_W2, B_W3, B_CS, B_WB, B_PB, B_D1, B_G1, B_D3 };

__global__ void __launch_bounds__(512, 2) fused(Args a) {
    extern __shared__ __attribute__((aligned(16))) char smem[];
    LAS char* lds = (LAS char*)smem;
    const int tid = threadIdx.x, w = __builtin_amdgcn_readfirstlane(tid >> 6);
    int lane = tid & 63;
    const int b = blockIdx.x, xcd = b & 7, r = b >> 3, t = r & 7, par = xcd & 1, rg = (xcd >> 1) * 4 + (r >> 3);
    const int s2 = 2 * rg + par, cg = par * 8 + t, sig = xcd * 32 + r, pid = s2 * 8 + t;
    const int rq = w & 3, hf = w >> 2, j4 = r >> 3;
#define BE(l) ((((l) >> 3) << 1) | ((l) & 1))
#define IX(l) (((l) >> 1) & 3)
    u32 l16 = lane * 16;
    const u64 tfirst = __builtin_amdgcn_s_memrealtime();
    asm volatile("" ::: "memory");
    u32 cpf = 0;
    if (w == 7) {
        const u64 pc = __builtin_amdgcn_s_getpc() & ~(u64)255;
        const u32 off = min((u32)((t * 64 + lane) * 128), (u32)(CODE_BYTES - 128));
        asm volatile("global_load_dword %0, %1, %2" : "=v"(cpf) : "v"(off), "s"(pc) : "memory");
    }
    *(volatile LAS u32*)(lds + L_PROG + w * 4) = 0;
    BAR();
    *(volatile LAS u2*)(lds + L_FFT + 22784 + (w * 28) * 8) = u2{(u32)tfirst, (u32)(tfirst >> 32)};
    STAMP(27);
    auto dma_x = [&]() {
        const char* xs = (const char*)a.x + (size_t)((s2 ^ hf) + 512 * rq) * 1024;
#pragma unroll
        for (int i = 0; i < 16; i++) { const int row = (i + 2 * t) & 15; dma_rt(l16 ^ (u32)(row * (32768 + 16)), xs, (u32)(L_SET1 + rq * 16384 + row * 1024)); }
    };
    if (hf == 0) dma_x();
    int li, lg, c2p, l3, qb; u32 vpg;
#define RELANE() do { asm volatile("" : "+v"(lane)); l16 = lane * 16; li = lane & 15; lg = lane >> 4; c2p = lane & 3; l3 = (lane >> 2) & 7; qb = lane >> 5; vpg = (u32)(L_PROG + (lane & 3) * 4); } while (0)
    const __amdgpu_buffer_rsrc_t rs = __builtin_amdgcn_make_buffer_rsrc(a.ws, 0, WS_BYTES, 0x00020000);
    KC kc; kc.pm = f2{1.f, -1.f}; kc.rpm = f2{0.70710678118654752f, -0.70710678118654752f};
    asm volatile("" : "+s"(kc.pm), "+s"(kc.rpm));
    const u4 ones = {0x3F803F80u, 0x3F803F80u, 0x3F803F80u, 0x3F803F80u};
    auto post = [&](u32 v) { asm volatile("s_waitcnt lgkmcnt(0)" ::: "memory"); *(volatile LAS u32*)(lds + L_PROG + w * 4) = v; FENCE(); };
    auto wait_grp = [&](int g, u32 v) { lds_wait(vpg + g * 16, v); };
    auto wait_pair = [&](u32 v) { lds_wait((u32)(L_PROG + (w ^ 4) * 4), v); };
    u2 tag;
    auto get_tag = [&]() { u2 tg = *(LAS u2*)(lds + L_TAG); tag.x = __builtin_amdgcn_readfirstlane(tg.x); tag.y = __builtin_amdgcn_readfirstlane(tg.y); };
    auto flag_store = [&](int off) { __builtin_amdgcn_raw_buffer_store_b64(tag, rs, lane ? WS_BYTES : 0, off, 16); };
    auto flag_wait = [&](u32 voff, int soff) {
        _Pragma("unroll 1") for (int spins = 0; spins < SPIN_CAP; spins++) {
            u2 f = __builtin_amdgcn_raw_buffer_load_b64(rs, voff, soff, 16);
            if (__all(f.x == tag.x && f.y == tag.y)) break;
            __builtin_amdgcn_s_sleep(4);
            FENCE();
        }
        __builtin_amdgcn_fence(__ATOMIC_ACQUIRE, "wavefront");
        FENCE();
    };
    auto read_tile = [&](u4 (&x)[16], int tile) {
        LAS char* p = lds + L_SET1 + (16 * rq + li) * 1024;
#pragma unroll
        for (int k = 0; k < 16; k++) x[k] = *(LAS u4*)(p + (((4 * IX(k) + lg) ^ li) * 16) + ((BE(k) + j4) & 3) * 256);
    };
    auto stats = [&](const u4 (&x)[16]) {
        f4 s1 = {0, 0, 0, 0}, sq = s1, s1b = s1, sqb = s1;
#pragma unroll
        for (int k = 0; k < 16; k += 2) { s1 = mfma(ones, x[k], s1); sq = mfma(x[k], x[k], sq); s1b = mfma(ones, x[k + 1], s1b); sqb = mfma(x[k + 1], x[k + 1], sqb); }
        sq += sqb;
        const int rr = li & 3;
        const float d = rr == 0 ? sq.x : rr == 1 ? sq.y : rr == 2 ? sq.z : sq.w;
        if (lg == (li >> 2)) {
            const float mean = (s1.x + s1b.x) * (1.0f / 512.0f);
            float var = d * (1.0f / 512.0f) - mean * mean;
            if (!(mean * mean <= 1024.0f * var)) {
                const u32* xrow = (const u32*)(a.x + (size_t)((s2 ^ hf) + 32 * (16 * rq + li)) * 512);
                float q = 0.f;
                _Pragma("unroll 1") for (int i = 0; i < 256; i++) { u32 v = xrow[i]; float d0 = bflo(v) - mean, d1 = bfhi(v) - mean; q += d0 * d0; q += d1 * d1; }
                var = q * (1.0f / 512.0f);
            }
            const float den = rbf(__builtin_amdgcn_sqrtf(rbf(rbf(var) + 1e-5f)));
            *(LAS f2*)(lds + L_STAT + (hf * 64 + 16 * rq + li) * 8) = f2{rbf(mean), __builtin_amdgcn_rcpf(den)};
        }
        FENCE();
    };
    const int nblk = (t < 6 - par) ? 3 : 2, fb2 = 32 + par * 6 + t;
    const char* wdp = (const char*)a.ws + WS_WDP + (size_t)(t * NFB) * 4096;

    f2 tw64[8], twT[8];
    int x3a[4];
    auto fft13_consts = [&](float scale) {
        int lane_ = lane; asm volatile("" : "+v"(lane_));
        const int c2p = lane_ & 3, l3 = (lane_ >> 2) & 7, qb = lane_ >> 5;
        const int wq = rq, q1 = qb ? (wq == 0 ? 4 : 8 - wq) : wq, c2 = 4 * t + c2p;
        const float f64 = (float)l3 * (1.0f / 64.0f);
#pragma unroll
        for (int k = 1; k < 8; k++) tw64[k] = cis(f64 * (float)k);
        const float fT = (float)(8 * c2 * q1 + s2 * l3) * (1.0f / 2048.0f), dT = (float)s2 * (1.0f / 256.0f);
#pragma unroll
        for (int k = 0; k < 8; k++) twT[k] = cis(fT + dT * (float)k) * scale;
#pragma unroll
        for (int k = 0; k < 4; k++) {
            const int ua = (k >> 1) * 4 + (k & 1), u1 = l3 + 8 * ua;
            const int cls = (u1 < 32) ? u1 : ((64 - u1) & 31), hi = u1 >> 5;
            int mbit, sub;
            if (wq == 0) { mbit = qb; sub = hi; } else if (cls != 0) { sub = hi; mbit = qb ^ hi; } else { mbit = hi; sub = qb ^ hi; }
            x3a[k] = L_FFT + wq * 4608 + (cls * 18 + mbit * 8 + sub * 4 + c2p) * 8 - (k == 3 ? 2304 : 0);
        }
        asm volatile("" : "+v"(tw64[1]), "+v"(tw64[2]), "+v"(tw64[3]), "+v"(tw64[4]), "+v"(tw64[5]), "+v"(tw64[6]), "+v"(tw64[7]),
                     "+v"(twT[0]), "+v"(twT[1]), "+v"(twT[2]), "+v"(twT[3]), "+v"(twT[4]), "+v"(twT[5]), "+v"(twT[6]), "+v"(twT[7]),
                     "+v"(x3a[0]), "+v"(x3a[1]), "+v"(x3a[2]), "+v"(x3a[3]));
    };

    const int hoff0 = WS_H + ((s2 * 4 + rq) * NFB) * 1024, hoff1 = WS_H + (((s2 ^ 1) * 4 + rq) * NFB) * 1024;
    RELANE();
    const char* wgs = (const char*)a.wg + (size_t)(32 * rq) * 2752 + cg * 128;
    const char* wus = (const char*)a.wu + (size_t)(32 * rq) * 2752 + cg * 128;
    const u32 wvE = (u32)((lane >> 3) * 2752 + (((lane & 7) ^ (((lane >> 4) & 1) << 1)) * 16)), wvO = (u32)((lane >> 3) * 2752 + (((lane & 7) ^ (((lane >> 4) & 1) << 1) ^ 4) * 16));
    auto dma_w1 = [&](auto KK, auto I) {
        constexpr int kk = KK, g = I >> 1, mat = I & 1;
        dma_add<kk * PG + mat * 4096 + g * 1024, g * 8 * 2752>((g & 1) ? wvO : wvE, (mat ? wus : wgs) + ((BE(kk) + j4) & 3) * (128 * 2752), (u32)(rq * 2 * PG));
    };
    const int rot = (2 * t + 4 * j4) & 15;
    auto dma_wd = [&](auto LI, auto I, int p0) { dma4k_c<LI + I * 16384, 0>(l16, wdp + (size_t)(p0 + 2 * ((rq + 4 * I + rot) & 15)) * 4096, (u32)(rq * 4096)); };

    auto gemm1_half = [&](auto S0C, const u4 (&xa)[16], const u4 (&xb)[16], f4 (&accA)[4], f4 (&accB)[4], f4& acs, auto&& hook) {
        constexpr int S0 = S0C;
        const u32 fz = (u32)((li >> 3) | ((lg & 1) << 1));
        const u32 tb = (u32)((8 * lg + (li >> 2)) * 128 + 8 * (li & 3)) + S0 * PG;
        u32 p0 = tb + (((2 * hf) ^ fz) << 5), p1 = tb + (((2 * hf + 1) ^ fz) << 5), pc = tb + (((2 * hf + (rq & 1)) ^ fz) << 5) + (rq >> 1) * 4096;
        asm volatile("" : "+v"(p0), "+v"(p1), "+v"(pc));
        u4 F[2][5];
        auto ldf = [&](int ks, u4 (&f)[5]) {
            const int off = (ks & 7) * PG;
            f[0] = trfrag2<512>(lds + p0 + off); f[1] = trfrag2<512>(lds + p1 + off); f[2] = trfrag2<512>(lds + p0 + off + 4096); f[3] = trfrag2<512>(lds + p1 + off + 4096); f[4] = trfrag2<512>(lds + pc + off);
        };
        ldf(0, F[0]);
        sfor<8>([&](auto S) {
            constexpr int s = S, ks = S0 | ((s & 3) << 1) | ((s >> 2) & 1), sn = s + 1, kn = ((sn & 3) << 1) | ((sn >> 2) & 1);
            hook(S);
            SCHED();
            if (s + 1 < 8) ldf(kn, F[(s + 1) & 1]);
            u4 (&f)[5] = F[s & 1];
#pragma unroll
            for (int j = 0; j < 4; j++) { accA[j] = mfma(f[j], xa[ks], accA[j]); accB[j] = mfma(f[j], xb[ks], accB[j]); }
            acs = mfma(f[4], ones, acs);
            if (s + 1 < 8) {
                SGB(0x100, 2);
#pragma unroll
                for (int i = 0; i < 8; i++) { SGB(0x8, 1); SGB(0x100, 1); }
                SGB(0x8, 1);
            }
            SCHED();
        });
    };
    f2 st0, st1;
    auto gemm1_epi = [&](f4 (&accA)[4], f4 (&accB)[4], f4 acs, u32 step) {
        if (li == 0) *(LAS f4*)(lds + L_CS + (hf * 4 + rq) * 64 + lg * 16) = acs;
        post(step);
        wait_grp(hf, step);
        LAS char* cs = lds + L_CS + hf * 256 + lg * 16;
        st0 = *(LAS f2*)(lds + L_STAT + (16 * rq + li) * 8);
        st1 = *(LAS f2*)(lds + L_STAT + (64 + 16 * rq + li) * 8);
        st128(geglu8(accA, cs, st0), rs, l16, hoff0 + (2 * cg + hf) * 1024);
        st128(geglu8(accB, cs, st1), rs, l16, hoff1 + (2 * cg + hf) * 1024);
    };
    f4 acc2[4];
#pragma unroll
    for (int j = 0; j < 4; j++) acc2[j] = f4{0, 0, 0, 0};
    const int hsrc = hoff0 + hf * 1024;
    auto g2_mma = [&](auto NC, const u4* hfr, int ldsbase) {
        constexpr int N = decltype(NC)::value;
        u32 pa = (u32)ldsbase + l16; asm volatile("" : "+v"(pa));
        LAS u4* p = (LAS u4*)(lds + pa);
        u4 F[2][4];
#pragma unroll
        for (int j = 0; j < 4; j++) F[0][j] = p[j * 64];
        sfor<N>([&](auto I) {
            constexpr int i = I;
            SCHED();
            if (i + 1 < N) {
#pragma unroll
                for (int j = 0; j < 4; j++) F[(i + 1) & 1][j] = p[(i + 1) * 256 + j * 64];
            }
#pragma unroll
            for (int j = 0; j < 4; j++) acc2[j] = mfma(F[i & 1][j], hfr[i], acc2[j]);
            if (i + 1 < N) {
#pragma unroll
                for (int j = 0; j < 4; j++) { SGB(0x100, 1); SGB(0x8, 1); }
            }
            SCHED();
        });
    };
    if (hf == 0) {
        u4 xa[16], xb[16];
        __builtin_amdgcn_s_setprio(2);
        const int f1st = WS_F1 + ((lane >> 3) * 8 + (((lane >> 2) & 1) ? (rq == 0 ? 4 : 8 - rq) : rq)) * 16384 + pid * 64 + c2p * 16;
        u2 ev;
        { u32 zero = 0; asm volatile("global_load_dwordx2 %0, %1, %2 sc1" : "=v"(ev) : "v"(zero), "s"(a.ws) : "memory"); }
        fft13_consts(1.0f);
        WAITVM(1);
        post(A_XL);
        STAMP(1);
        u32 raw[8];
        const int rw = lane >> 2;
        read_tile(xa, 0);
#pragma unroll
        for (int c1 = 0; c1 < 8; c1++) raw[c1] = *(LAS u32*)(lds + L_SET1 + (16 * rq + rw) * 1024 + (((8 * c1 + t) ^ rw) * 16) + c2p * 4);
        post(A_XC);
        stats(xa);
        STAMP(2);
        f2 v[8];
        {
            const f2 st = *(LAS f2*)(lds + L_STAT + (16 * rq + rw) * 8);
#pragma unroll
            for (int c1 = 0; c1 < 8; c1++) v[c1] = bfup(pkbf(bfup(pkbf(bfup(raw[c1]) - st.xx)) * st.yy));
            dft8<false>(v, kc);
            LAS f2* xb1 = (LAS f2*)(lds + L_FFT) + w * 64 + lane;
#pragma unroll
            for (int q1 = 0; q1 < 8; q1++) { const int reg = (q1 < 4) ? q1 * 2 : (((8 - q1) & 3) * 2 + 1); xb1[reg * 288] = v[q1]; }
        }
        asm volatile("s_waitcnt vmcnt(0)" : "+v"(ev) :: "memory");
        {
            const u64 epoch = ((u64)(u32)__builtin_amdgcn_readfirstlane(ev.y) << 32) | (u32)__builtin_amdgcn_readfirstlane(ev.x);
            u64 tg = (epoch + 1) * 0x9E3779B97F4A7C15ull; tg ^= tg >> 29; tg *= 0xBF58476D1CE4E5B9ull; tg ^= tg >> 32;
            tag = u2{(u32)tg, (u32)(tg >> 32)};
            *(LAS u4*)(lds + L_TAG) = u4{(u32)tg, (u32)(tg >> 32), (u32)(epoch + 1), (u32)((epoch + 1) >> 32)};
        }
        post(A_P1);
#pragma unroll
        for (int c1 = 0; c1 < 8; c1++) *(LAS u32*)(lds + ((rq >> 1) ? L_X1 : L_X0) + ((rq & 1) * 16 + rw) * 128 + ((c1 ^ (rw & 7)) * 16) + c2p * 4) = raw[c1];
        wait_pair(B_W0);
        read_tile(xb, 1);
        post(A_XB);
        u32 pk[8], fr[16];
        const int un = sig < 4 * NFB ? sig : 0, fb = un >> 2, jj = (un >> 1) & 1, tcol = (un & 1) * 4 + rq;
        {
            const u32 vd = (u32)(((fb * 32 + 4 * lg) * 512 + 64 * (4 * jj + (li >> 2)) + 8 * tcol + 2 * (li & 3)) * 2);
            sfor<4>([&](auto I) {
                ldg_u32<I * 1024>(pk[I], vd, a.wd);
                ldg_u32<I * 1024>(pk[4 + I], vd, (const char*)a.wd + 16384);
            });
            const u32 vo = (u32)(((8 * sig + 2 * rq) * 257 + lane) * 2);
            sfor<8>([&](auto I) { constexpr int rr = I >> 2, k = I & 3; ldg_u16<rr * 514 + k * 128>(fr[I], vo, a.wr); ldg_u16<rr * 514 + k * 128>(fr[8 + I], vo, a.wi); });
        }
        wait_grp(0, A_P1);
        {
            LAS f2* reg = (LAS f2*)(lds + L_FFT) + (w * 2 + qb) * 288;
#pragma unroll
            for (int k = 0; k < 8; k++) v[k] = reg[(lane & 31) + 32 * k];
            dft8<false>(v, kc);
#pragma unroll
            for (int k = 1; k < 8; k++) v[k] = cmul<true>(v[k], tw64[k]);
#pragma unroll
            for (int k = 0; k < 8; k++) reg[36 * k + (lane & 31)] = v[k];
            FENCE();
#pragma unroll
            for (int k = 0; k < 8; k++) v[k] = reg[36 * l3 + 4 * k + c2p];
            dft8<false>(v, kc);
#pragma unroll
            for (int k = 0; k < 8; k++) v[k] = cmul<true>(v[k], twT[k]);
            FENCE();
            *(LAS f2*)(lds + x3a[0]) = v[0];
            *(LAS f2*)(lds + x3a[1]) = v[1]; *(LAS f2*)(lds + x3a[1] + 1152) = v[2]; *(LAS f2*)(lds + x3a[1] + 2304) = v[3];
            *(LAS f2*)(lds + x3a[2]) = v[4];
            *(LAS f2*)(lds + x3a[3] + 2304) = v[5]; *(LAS f2*)(lds + x3a[3] + 1152) = v[6]; *(LAS f2*)(lds + x3a[3]) = v[7];
            FENCE();
#pragma unroll
            for (int i = 0; i < 4; i++) {
                u4 o = *(LAS u4*)(lds + L_FFT + w * 4608 + (i * 8 + (lane >> 3)) * 144 + (lane & 7) * 16);
                st128(o, rs, NOF(f1st), i * (64 * 16384));
            }
        }
        STAMP(3);
        __builtin_amdgcn_s_setprio(0);
        asm volatile("s_waitcnt vmcnt(4)" : "+v"(pk[0]), "+v"(pk[1]), "+v"(pk[2]), "+v"(pk[3]), "+v"(pk[4]), "+v"(pk[5]), "+v"(pk[6]), "+v"(pk[7]),
                     "+v"(fr[0]), "+v"(fr[1]), "+v"(fr[2]), "+v"(fr[3]), "+v"(fr[4]), "+v"(fr[5]), "+v"(fr[6]), "+v"(fr[7]),
                     "+v"(fr[8]), "+v"(fr[9]), "+v"(fr[10]), "+v"(fr[11]), "+v"(fr[12]), "+v"(fr[13]), "+v"(fr[14]), "+v"(fr[15]) :: "memory");
        {
            const int so = sig < 4 * NFB ? WS_WDP + ((tcol * NFB + fb) * 4 + 2 * jj) * 1024 : WS_BYTES;
            u4 oe = {__builtin_amdgcn_perm(pk[1], pk[0], 0x05040100), __builtin_amdgcn_perm(pk[3], pk[2], 0x05040100), __builtin_amdgcn_perm(pk[5], pk[4], 0x05040100), __builtin_amdgcn_perm(pk[7], pk[6], 0x05040100)};
            u4 oo = {__builtin_amdgcn_perm(pk[1], pk[0], 0x07060302), __builtin_amdgcn_perm(pk[3], pk[2], 0x07060302), __builtin_amdgcn_perm(pk[5], pk[4], 0x07060302), __builtin_amdgcn_perm(pk[7], pk[6], 0x07060302)};
            st128(oe, rs, l16, so);
            st128(oo, rs, l16, so + 1024);
        }
        {
            const int q1 = lane >> 3, vt = lane & 7, srcl = (q1 + 8 * vt) * 4;
#pragma unroll
            for (int rr = 0; rr < 2; rr++) {
                const int u = 8 * sig + 2 * rq + rr, u1 = u & 63, u2_ = u >> 6;
                int cls, m, sub;
                if ((u1 & 31) != 0) { sub = u1 >> 5; cls = sub ? 64 - u1 : u1; m = sub ? ((8 - q1) & 7) : q1; }
                else { cls = 0; const int hi = u1 >> 5; if ((q1 & 3) == 0) { m = q1; sub = hi; } else { sub = (q1 >> 2) ^ hi; m = sub ? 8 - q1 : q1; } }
                u4 o;
                o.x = __builtin_amdgcn_ds_bpermute(srcl, fr[rr * 4 + 0] | (fr[8 + rr * 4 + 0] << 16)); o.y = __builtin_amdgcn_ds_bpermute(srcl, fr[rr * 4 + 1] | (fr[8 + rr * 4 + 1] << 16));
                o.z = __builtin_amdgcn_ds_bpermute(srcl, fr[rr * 4 + 2] | (fr[8 + rr * 4 + 2] << 16)); o.w = __builtin_amdgcn_ds_bpermute(srcl, fr[rr * 4 + 3] | (fr[8 + rr * 4 + 3] << 16));
                st128(o, rs, NOF(WS_WP + (cls * 8 + m) * 8192 + sub * 4096 + u2_ * 128 + vt * 16), 0);
            }
        }
        WAITVM(4);
        flag_store(WS_FF1 + ((rq * 4 + ((s2 & 7) >> 1)) * 64 + (s2 >> 3) * 16 + (s2 & 1) * 8 + t) * 8);
        STAMP(4);
        f4 accA[4], accB[4], acs = {0, 0, 0, 0};
#pragma unroll
        for (int j = 0; j < 4; j++) { accA[j] = f4{0, 0, 0, 0}; accB[j] = f4{0, 0, 0, 0}; }
        wait_grp(1, B_W1);
        __builtin_amdgcn_s_setprio(1);
        gemm1_half(IC<0>{}, xa, xb, accA, accB, acs, [&](auto S) {
            if (S == 3) {
                WAITVM(1);
                flag_store(sig < 4 * NFB ? WS_FP + (((sig & 1) * 4 + rq) * 128 + (sig >> 1)) * 8 : WS_BYTES);
                flag_store(WS_FW + (((sig & 7) * 4 + rq) * 32 + (sig >> 3)) * 8);
            }
        });
        pin9(accA, accB, acs);
        post(A_S0);
        STAMP(19);
        RELANE();
        int pos1f;
        {
        f2 tw0[4], tw1[8], al[8], be[8];
        const int rb = qb, r3 = 2 * rq + rb;
        const int cls2 = sig >> 3, m2 = sig & 7;
        const int wq2 = (m2 < 4) ? m2 : ((8 - m2) & 3), pos1 = wq2 * 64 + cls2 * 2 + (m2 >> 2);
        const bool special = (cls2 == 0) && ((m2 & 3) == 0);
        int u1a, q1a, u1b, q1b;
        if (cls2 != 0) { u1a = cls2; q1a = m2; u1b = 64 - cls2; q1b = (8 - m2) & 7; }
        else if (special) { u1a = 0; u1b = 32; q1a = q1b = m2; }
        else { u1a = u1b = (m2 < 4) ? 0 : 32; q1a = m2; q1b = 8 - m2; }
        int sA, vH0, sB, vH1;
        if (!special) { sA = 0; vH0 = rq; sB = 1; vH1 = (u1a == 0) ? ((4 - rq) & 3) : 3 - rq; }
        else { sA = sB = rq >> 1; vH0 = rq & 1; vH1 = (rq == 0) ? 2 : (rq == 1) ? 3 : (rq == 2) ? 3 : 2; }
        const bool q0 = (m2 == 0);
        const int dy = ((sA ? u1b : u1a) == 0 && vH0 == 0) ? 8 : 7, dq = q0 ? 8 : 7;
        int y1 = lane >> 3, t1 = lane & 7, y2 = (dy - y1) & 7, t2 = (dq - t1) & 7, hA = vH0, hB = vH1;
        bool self = false;
        if (special && rq == 0) {
            const int k = lane & 31;
            if (qb) { hA = hB = 2; y1 = k >> 3; t1 = k & 7; y2 = 7 - y1; t2 = (dq - t1) & 7; }
            else {
                hA = hB = 0; const int vx = k & 3, y = k >> 2;
                if (!q0 || vx) { y1 = y; t1 = vx; y2 = (8 - y) & 7; t2 = dq - vx; }
                else { t1 = t2 = (y >= 4) ? 4 : 0; const int yy = y & 3; if (yy) { y1 = yy; y2 = 8 - yy; } else { y1 = 0; y2 = 4; self = true; } }
            }
        }
        const int uA = (sA ? u1b : u1a) + 64 * (hA + 4 * y1), uB = (sB ? u1b : u1a) + 64 * (hB + 4 * y2);
        const int qA = (sA ? q1b : q1a) + 8 * t1, qB = (sB ? q1b : q1a) + 8 * t2;
        const int rowB = L_FFT + ((qb ? (vH1 * 2 + sB) : (vH0 * 2 + sA)) * 36 + (lane & 31)) * 8;
        const bool rot = q0 && (t1 == 0);
        const int p3a1 = L_FFT + (y1 * 290 + (hA * 2 + sA) * 36 + t1 * 4) * 8;
        const int p3a2 = L_FFT + (y2 * 290 + (hB * 2 + sB) * 36 + t2 * 4) * 8;
        const float thA = (float)qA * (1.0f / 512.0f), thB = (float)qB * (1.0f / 512.0f);
#pragma unroll
        for (int k = 1; k < 4; k++) tw0[k] = cis((float)r3 * ((float)k / 32.0f));
#pragma unroll
        for (int k = 1; k < 8; k++) tw1[k] = cis((float)c2p * ((float)k / 32.0f));
        f2 csq[8];
#pragma unroll
        for (int e = 0; e < 8; e++) csq[e] = cis(((e >> 2) ? thB : thA) + 0.125f * (float)(e & 3));
        asm volatile("" : "+v"(csq[0]), "+v"(csq[1]), "+v"(csq[2]), "+v"(csq[3]), "+v"(csq[4]), "+v"(csq[5]), "+v"(csq[6]), "+v"(csq[7]),
                     "+v"(tw0[1]), "+v"(tw0[2]), "+v"(tw0[3]), "+v"(tw1[1]), "+v"(tw1[2]), "+v"(tw1[3]), "+v"(tw1[4]), "+v"(tw1[5]), "+v"(tw1[6]), "+v"(tw1[7]));
        {
            const u32 o1 = min(rq * 22 + lane, 85) * 8, o2 = (((qb ? u1b : u1a) >> 1) * 32 + (lane & 31)) * 8, o3 = lane * 8;
            _Pragma("unroll 1") for (int spins = 0; spins < SPIN_CAP; spins++) {
                u2 f1 = __builtin_amdgcn_raw_buffer_load_b64(rs, o1, WS_FP + t * 1024, 16);
                u2 f2_ = __builtin_amdgcn_raw_buffer_load_b64(rs, o2, WS_FW, 16);
                u2 f3 = __builtin_amdgcn_raw_buffer_load_b64(rs, o3, WS_FF1 + (wq2 * 4 + rq) * 512, 16);
                if (__all(f1.x == tag.x && f1.y == tag.y && f2_.x == tag.x && f2_.y == tag.y && f3.x == tag.x && f3.y == tag.y)) break;
                __builtin_amdgcn_s_sleep(2);
                FENCE();
            }
            __builtin_amdgcn_fence(__ATOMIC_ACQUIRE, "wavefront");
            FENCE();
        }
        post(A_FP);
        wait_grp(0, A_S0);
        STAMP(21);
        const int f1off = WS_F1 + sig * 16384 + 2 * rq * 512;
        u4 d[4];
        const u4 wa4 = __builtin_amdgcn_raw_buffer_load_b128(rs, (hA + 4 * y1) * 128 + t1 * 16, WS_WP + sig * 8192 + sA * 4096, 16);
        const u4 wb4 = __builtin_amdgcn_raw_buffer_load_b128(rs, (hB + 4 * y2) * 128 + t2 * 16, WS_WP + sig * 8192 + sB * 4096, 16);
        SCHED();
#pragma unroll
        for (int H = 0; H < 4; H++) d[H] = __builtin_amdgcn_raw_buffer_load_b128(rs, NOF(l16), f1off + H * 4096, 16);
        SCHED();
        f2 wS[2] = {f2{0.f, 0.f}, f2{0.f, 0.f}};
        if (q0) {
            wS[0] = f2{bflo(a.wr[uA * 257 + 256]), bflo(a.wi[uA * 257 + 256])};
            wS[1] = f2{bflo(a.wr[uB * 257 + 256]), bflo(a.wi[uB * 257 + 256])};
        }
        {
            f2 W[8], WB[8];
#pragma unroll
            for (int e = 0; e < 4; e++) { W[e] = bfup(wa4[e]); W[4 + e] = bfup(wb4[e]); }
#pragma unroll
            for (int k = 0; k < 4; k++) { WB[k] = W[4 + 3 - k]; WB[4 + k] = W[3 - k]; }
            if (q0) {
#pragma unroll
                for (int k = 0; k < 4; k++) {
                    f2 n1 = rot ? W[(4 - k) & 3] : W[3 - k], n2 = rot ? W[4 + ((4 - k) & 3)] : W[4 + 3 - k];
                    WB[k] = self ? n1 : n2; WB[4 + k] = self ? n2 : n1;
                }
#pragma unroll
                for (int e = 0; e < 8; e += 4) {
                    f2 own = wS[e >> 2], oth = self ? own : wS[1 - (e >> 2)];
                    f2 S0 = pkfma(WB[e], kc.pm, W[e]) * 0.5f, S1c = pkfma(own, kc.pm, oth) * 0.5f;
                    WB[e] = rot ? S1c : WB[e];
                    al[e] = rot ? S0 : W[e];
                }
                W[0] = al[0]; W[4] = al[4];
            }
#pragma unroll
            for (int e = 0; e < 8; e++) {
                const f2 cs = csq[e];
                f2 P = pkfma(WB[e], kc.pm, W[e]), M = pkfma(WB[e], kc.pm.yx, W[e]);
                asm("v_pk_fma_f32 %0, %1, %2, %3 op_sel:[1,0,0] op_sel_hi:[1,1,1] neg_lo:[1,0,0] neg_hi:[1,0,0]" : "=v"(al[e]) : "v"(cs), "v"(M), "v"(P));
                asm("v_pk_mul_f32 %0, %1, %2 op_sel:[1,0] op_sel_hi:[0,0] neg_lo:[1,0]" : "=v"(be[e]) : "v"(M), "v"(cs));
            }
            asm volatile("" : "+v"(al[0]), "+v"(al[1]), "+v"(al[2]), "+v"(al[3]), "+v"(al[4]), "+v"(al[5]), "+v"(al[6]), "+v"(al[7]),
                         "+v"(be[0]), "+v"(be[1]), "+v"(be[2]), "+v"(be[3]), "+v"(be[4]), "+v"(be[5]), "+v"(be[6]), "+v"(be[7]),
                         "+v"(tw0[1]), "+v"(tw0[2]), "+v"(tw0[3]), "+v"(tw1[1]), "+v"(tw1[2]), "+v"(tw1[3]), "+v"(tw1[4]), "+v"(tw1[5]), "+v"(tw1[6]), "+v"(tw1[7]));
        }
        STAMP(5);
        __builtin_amdgcn_s_setprio(2);
        f2 e0[4], e1[4];
#pragma unroll
        for (int H = 0; H < 4; H++) { e0[H] = f2{__uint_as_float(d[H].x), __uint_as_float(d[H].y)}; e1[H] = f2{__uint_as_float(d[H].z), __uint_as_float(d[H].w)}; }
        dft4<false>(e0[0], e0[1], e0[2], e0[3], kc); dft4<false>(e1[0], e1[1], e1[2], e1[3], kc);
        STAMP(6);
        LAS char* area = lds + L_FFT + rq * 4640;
#pragma unroll
        for (int k = 0; k < 4; k++) {
            if (k) { e0[k] = cmul<true>(e0[k], tw0[k]); e1[k] = cmul<true>(e1[k], tw0[k]); }
            *(LAS f4*)(area + (l3 * 72 + 2 * c2p + 8 * k + 32 * rb) * 8) = f4{e0[k].x, e0[k].y, e1[k].x, e1[k].y};
        }
        FENCE();
#pragma unroll
        for (int k = 0; k < 8; k++) v[k] = *(LAS f2*)(area + (k * 72 + lane) * 8);
        dft8<false>(v, kc);
#pragma unroll
        for (int k = 1; k < 8; k++) v[k] = cmul<true>(v[k], tw1[k]);
        FENCE();
        LAS char* pb = lds + L_FFT + (r3 * 290 + l3 * 36 + c2p) * 8;
#pragma unroll
        for (int k = 0; k < 8; k++) *(LAS f2*)(pb + k * 32) = v[k];
        post(A_E1);
        wait_grp(0, A_E1);
#pragma unroll
        for (int k = 0; k < 8; k++) v[k] = *(LAS f2*)(lds + rowB + k * 2320);
        dft8<false>(v, kc);
#pragma unroll
        for (int k = 0; k < 8; k++) *(LAS f2*)(lds + rowB + k * 2320) = v[k];
        FENCE();
        f4 a0 = *(LAS f4*)(lds + p3a1), a1 = *(LAS f4*)(lds + p3a1 + 16), b0 = *(LAS f4*)(lds + p3a2), b1 = *(LAS f4*)(lds + p3a2 + 16);
        f2 X[8] = {a0.xy, a0.zw, a1.xy, a1.zw, b0.xy, b0.zw, b1.xy, b1.zw};
        dft4<false>(X[0], X[1], X[2], X[3], kc); dft4<false>(X[4], X[5], X[6], X[7], kc);
        f2 Y[8];
#pragma unroll
        for (int k = 0; k < 4; k++) { Y[k] = X[4 + 3 - k]; Y[4 + k] = X[3 - k]; }
        if (q0) {
#pragma unroll
            for (int k = 0; k < 4; k++) {
                f2 n1 = rot ? X[(4 - k) & 3] : X[3 - k], n2 = rot ? X[4 + ((4 - k) & 3)] : X[4 + 3 - k];
                Y[k] = self ? n1 : n2; Y[4 + k] = self ? n2 : n1;
            }
        }
        f2 O[8];
#pragma unroll
        for (int e = 0; e < 8; e++) O[e] = cfmac_conj(cmul<false>(X[e], al[e]), be[e], Y[e]);
        dft4<true>(O[0], O[1], O[2], O[3], kc); dft4<true>(O[4], O[5], O[6], O[7], kc);
        *(LAS f4*)(lds + p3a1) = f4{O[0].x, O[0].y, O[1].x, O[1].y}; *(LAS f4*)(lds + p3a1 + 16) = f4{O[2].x, O[2].y, O[3].x, O[3].y};
        *(LAS f4*)(lds + p3a2) = f4{O[4].x, O[4].y, O[5].x, O[5].y}; *(LAS f4*)(lds + p3a2 + 16) = f4{O[6].x, O[6].y, O[7].x, O[7].y};
        FENCE();
#pragma unroll
        for (int k = 0; k < 8; k++) v[k] = *(LAS f2*)(lds + rowB + k * 2320);
        dft8<true>(v, kc);
#pragma unroll
        for (int k = 0; k < 8; k++) *(LAS f2*)(lds + rowB + k * 2320) = v[k];
        post(A_E2);
        wait_grp(0, A_E2);
#pragma unroll
        for (int k = 0; k < 8; k++) v[k] = *(LAS f2*)(pb + k * 32);
#pragma unroll
        for (int k = 1; k < 8; k++) v[k] = cmul<false>(v[k], tw1[k]);
        dft8<true>(v, kc);
        FENCE();
#pragma unroll
        for (int k = 0; k < 8; k++) *(LAS f2*)(area + (k * 72 + lane) * 8) = v[k];
        FENCE();
#pragma unroll
        for (int k = 0; k < 4; k++) {
            f4 q = *(LAS f4*)(area + (l3 * 72 + 2 * c2p + 8 * k + 32 * rb) * 8);
            e0[k] = q.xy; e1[k] = q.zw;
            if (k) { e0[k] = cmul<false>(e0[k], tw0[k]); e1[k] = cmul<false>(e1[k], tw0[k]); }
        }
        dft4<true>(e0[0], e0[1], e0[2], e0[3], kc); dft4<true>(e1[0], e1[1], e1[2], e1[3], kc);
        const int f2st = WS_F2 + (r3 * 8 + l3) * 16384 + pos1 * 64 + c2p * 16;
#pragma unroll
        for (int H = 0; H < 4; H++) {
            u4 o = {__float_as_uint(e0[H].x), __float_as_uint(e0[H].y), __float_as_uint(e1[H].x), __float_as_uint(e1[H].y)};
            st128(o, rs, NOF(f2st), H * (64 * 16384));
        }
            pos1f = pos1;
        }
        STAMP(7);
        __builtin_amdgcn_s_setprio(1);
        RELANE();
        wait_grp(1, B_CS);
        gemm1_half(IC<8>{}, xa, xb, accA, accB, acs, [&](auto S) { if (S == 5) { WAITVM(0); flag_store(WS_FF2 + (rq * 256 + pos1f) * 8); } });
        STAMP(8);
        RELANE();
        gemm1_epi(accA, accB, acs, A_CS);
        __builtin_amdgcn_s_setprio(0);
        sfor<4>([&](auto I) { dma_wd(IC<L_SET1>{}, I, 0); });
        WAITVM(16);
        flag_store(WS_FHA + ((rg * 8 + w) * 16 + cg) * 8);
        STAMP(9);
        {
            u4 d[4];
        fft13_consts(0.5f / 524288.0f);
        flag_wait(lane * 8, WS_FF2 + (((s2 & 7) >> 1) * 256 + rq * 64) * 8);
        const int off = WS_F2 + pid * 16384 + rq * 4096;
#pragma unroll
        for (int i = 0; i < 4; i++) d[i] = __builtin_amdgcn_raw_buffer_load_b128(rs, NOF(l16), off + i * 1024, 16);
        STAMP(13);
        __builtin_amdgcn_s_setprio(2);
#pragma unroll
        for (int i = 0; i < 4; i++) *(LAS u4*)(lds + L_FFT + rq * 4608 + (i * 8 + (lane >> 3)) * 144 + (lane & 7) * 16) = d[i];
        FENCE();
        v[0] = *(LAS f2*)(lds + x3a[0]);
        v[1] = *(LAS f2*)(lds + x3a[1]); v[2] = *(LAS f2*)(lds + x3a[1] + 1152); v[3] = *(LAS f2*)(lds + x3a[1] + 2304);
        v[4] = *(LAS f2*)(lds + x3a[2]);
        v[5] = *(LAS f2*)(lds + x3a[3] + 2304); v[6] = *(LAS f2*)(lds + x3a[3] + 1152); v[7] = *(LAS f2*)(lds + x3a[3]);
#pragma unroll
        for (int k = 0; k < 8; k++) v[k] = cmul<false>(v[k], twT[k]);
        dft8<true>(v, kc);
#pragma unroll
        for (int k = 1; k < 8; k++) v[k] = cmul<false>(v[k], tw64[k]);
        FENCE();
        LAS f2* reg = (LAS f2*)(lds + L_FFT) + (rq * 2 + qb) * 288;
#pragma unroll
        for (int k = 0; k < 8; k++) reg[36 * l3 + 4 * k + c2p] = v[k];
        FENCE();
#pragma unroll
        for (int k = 0; k < 8; k++) v[k] = reg[36 * k + (lane & 31)];
        dft8<true>(v, kc);
        FENCE();
#pragma unroll
        for (int k = 0; k < 8; k++) reg[(lane & 31) + 32 * k] = v[k];
        post(A_F3);
        wait_grp(0, A_F3);
        if (b == 0 && rq == 0) __builtin_amdgcn_raw_buffer_store_b64(*(LAS u2*)(lds + L_TAG + 8), rs, lane ? WS_BYTES : 0, 0, 16);
        LAS f2* xb1 = (LAS f2*)(lds + L_FFT) + rq * 64 + lane;
#pragma unroll
        for (int q1 = 0; q1 < 8; q1++) { const int rg_ = (q1 < 4) ? q1 * 2 : (((8 - q1) & 3) * 2 + 1); v[q1] = xb1[rg_ * 288]; }
        post(A_F4);
        dft8<true>(v, kc);
        wait_grp(0, A_F4);
#pragma unroll
        for (int c1 = 0; c1 < 8; c1++) *(LAS u32*)(lds + L_S + (16 * rq + (lane >> 2)) * 144 + c1 * 16 + c2p * 4) = pkbf(v[c1]);
        }
        STAMP(10);
        u4 hfr[16];
        flag_wait(li * 8, WS_FHA + (rg * 8 + w) * 128);
#pragma unroll
        for (int i = 0; i < 16; i++) { hfr[i] = __builtin_amdgcn_raw_buffer_load_b128(rs, l16, hsrc + 2 * ((i + rot) & 15) * 1024, 16); SCHED(); }
        STAMP(17);
        g2_mma(IC<16>{}, hfr, L_SET1);
    } else {
        u4 xa[16], xb[16];
        __builtin_amdgcn_s_sleep(2);
        sfor<8>([&](auto I) { dma_w1(IC<0>{}, I); });
        wait_pair(A_XL);
        read_tile(xa, 0);
        asm volatile("s_waitcnt lgkmcnt(0)" ::: "memory");
        wait_pair(A_XC);
        dma_x();
        sfor<8>([&](auto I) { dma_w1(IC<1>{}, I); });
        STAMP(1);
        asm volatile("s_waitcnt vmcnt(8)" : "+v"(cpf) :: "memory");
        post(B_W0);
        read_tile(xb, 1);
        STAMP(2);
        {
            f4 accA[4], accB[4], acs = {0, 0, 0, 0};
#pragma unroll
            for (int j = 0; j < 4; j++) { accA[j] = f4{0, 0, 0, 0}; accB[j] = f4{0, 0, 0, 0}; }
            wait_grp(1, B_W0);
            STAMP(3);
            gemm1_half(IC<0>{}, xa, xb, accA, accB, acs, [&](auto S) {
                constexpr int s = S;
                if (s == 1) { WAITVM(0); post(B_W1); wait_pair(A_XB); }
                if (s == 3) wait_grp(1, B_W1);
                if constexpr (s >= 1 && s <= 4) sfor<4>([&](auto I) { dma_w1(IC<8 + ((s - 1) >> 1)>{}, IC<((s - 1) & 1) * 4 + I>{}); });
                if (s == 7) { WAITVM(8); post(B_W2); }
            });
            STAMP(14);
            wait_grp(1, B_W2);
            get_tag();
            gemm1_half(IC<8>{}, xa, xb, accA, accB, acs, [&](auto S) {
                if (S == 1) { WAITVM(0); post(B_W3); }
                if (S == 3) wait_grp(1, B_W3);
            });
            STAMP(4);
            RELANE();
            pin9(accA, accB, acs);
            if (nblk == 3) {
                const u32 wvB = (u32)((lane >> 2) * 2752 + (((lane & 3) ^ (((lane >> 5) & 1) << 1)) * 16));
                const char* wbs = (const char*)((rq >> 1) ? a.wu : a.wg) + (size_t)((rq & 1) * 16) * 2752 + fb2 * 64;
                wait_grp(0, A_S0);
                sfor<16>([&](auto K) { constexpr int k = K; dma_add<k * 4096, IX(k) * 32 * 2752>(wvB, wbs + ((BE(k) + j4) & 3) * (128 * 2752), (u32)(rq * 1024)); });
            }
            SCHED();
            stats(xb);
            STAMP(15);
            gemm1_epi(accA, accB, acs, B_CS);
        }
        STAMP(5);
        if (nblk == 3) {
            WAITVM(2); post(B_WB);
            wait_grp(1, B_WB);
            const u32 tb = (u32)((8 * lg + (li >> 2)) * 64 + (lg & 1) * 32 + 8 * (li & 3));
            LAS char* p0 = lds + tb; LAS char* p1 = lds + (tb ^ 32);
            LAS char* pc = lds + (tb ^ ((rq & 1) * 32)) + (rq >> 1) * 2048;
            f4 acc[2][4], acs = {0, 0, 0, 0};
#pragma unroll
            for (int j = 0; j < 4; j++) { acc[0][j] = f4{0, 0, 0, 0}; acc[1][j] = f4{0, 0, 0, 0}; }
            u4 F[2][5];
            auto ldf = [&](int ks, u4 (&f)[5]) {
                const int off = ks * 4096;
                f[4] = trfrag2<256>(pc + off);
                f[0] = trfrag2<256>(p0 + off); f[1] = trfrag2<256>(p1 + off); f[2] = trfrag2<256>(p0 + off + 2048); f[3] = trfrag2<256>(p1 + off + 2048);
            };
            ldf(0, F[0]);
            sfor<16>([&](auto S) {
                constexpr int ks = S;
                if (ks == 5) { WAITVM(0); flag_store(WS_FHA + ((rg * 8 + w) * 16 + cg) * 8); }
                SCHED();
                if (ks + 1 < 16) ldf(ks + 1, F[(ks + 1) & 1]);
                u4 (&f)[5] = F[ks & 1];
                acs = mfma(f[4], ones, acs);
#pragma unroll
                for (int j = 0; j < 4; j++) { acc[0][j] = mfma(f[j], xa[ks], acc[0][j]); acc[1][j] = mfma(f[j], xb[ks], acc[1][j]); }
                if (ks + 1 < 16) {
                    SGB(0x100, 2);
#pragma unroll
                    for (int i = 0; i < 8; i++) { SGB(0x8, 1); SGB(0x100, 1); }
                    SGB(0x8, 1);
                }
                SCHED();
            });
            if (li == 0) *(LAS f4*)(lds + L_CS + 512 + rq * 64 + lg * 16) = acs;
            post(B_PB);
            wait_grp(1, B_PB);
            st128(geglu8(acc[0], lds + L_CS + 512 + lg * 16, st0), rs, l16, hoff0 + fb2 * 1024);
            st128(geglu8(acc[1], lds + L_CS + 512 + lg * 16, st1), rs, l16, hoff1 + fb2 * 1024);
        } else { WAITVM(0); flag_store(WS_FHA + ((rg * 8 + w) * 16 + cg) * 8); }
        STAMP(6);
        RELANE();
        wait_grp(0, A_FP);
        sfor<4>([&](auto I) { dma_wd(IC<0>{}, I, 1); });
        if (nblk == 3) { WAITVM(16); flag_store(WS_FHB + ((rg * 4 + rq) * 16 + fb2 - 32) * 8); }
        u4 hfr[16];
        flag_wait(li * 8, WS_FHA + (rg * 8 + w) * 128);
        post(B_D1);
#pragma unroll
        for (int i = 0; i < 16; i++) { hfr[i] = __builtin_amdgcn_raw_buffer_load_b128(rs, l16, hsrc + 2 * ((i + rot) & 15) * 1024, 16); SCHED(); }
        STAMP(9);
        wait_grp(1, B_D1);
        g2_mma(IC<16>{}, hfr, 0);
        post(B_G1);
        pin4(acc2);
        STAMP(10);
        wait_grp(1, B_G1);
        sfor<3>([&](auto I) { constexpr int i = I; dma4k_c<2 * PG + i * 16384, 0>(l16, wdp + (size_t)min(32 + 4 * i + rq, 42) * 4096, (u32)(rq * 4096)); });
        flag_wait(min(li, 10) * 8, WS_FHB + (rg * 4 + rq) * 128);
        post(B_D3);
#pragma unroll
        for (int i = 0; i < 11; i++) { hfr[i] = __builtin_amdgcn_raw_buffer_load_b128(rs, l16, hoff0 + (32 + i) * 1024, 16); SCHED(); }
        STAMP(13);
        wait_grp(1, B_D3);
        g2_mma(IC<11>{}, hfr, 2 * PG);
    }
    RELANE();
    STAMP(11);
    BAR();
    {
        LAS f4* xs = (LAS f4*)(lds + w * 2048) + lane;
        LAS f4* xr = (LAS f4*)(lds + (w ^ 4) * 2048) + lane;
        f4 me, mo;
        if (hf) { xs[0] = acc2[0]; xs[64] = acc2[1]; me = acc2[2]; mo = acc2[3]; } else { xs[0] = acc2[2]; xs[64] = acc2[3]; me = acc2[0]; mo = acc2[1]; }
        BAR();
        const int c1 = 4 * hf + lg;
        me += xr[0]; mo += xr[64];
        const u4 sv = *(LAS u4*)(lds + L_S + (16 * rq + li) * 144 + c1 * 16);
        const u4 xv = *(LAS u4*)(lds + ((rq >> 1) ? L_X1 : L_X0) + ((rq & 1) * 16 + li) * 128 + ((c1 ^ (li & 7)) * 16));
        u32 res[4];
#pragma unroll
        for (int e = 0; e < 4; e++) {
            const f2 ov = bfup(pkbf(f2{me[e], mo[e]}));
            const f2 z = ov * -1.44269504f;
            const f2 d = f2{__builtin_amdgcn_exp2f(z.x), __builtin_amdgcn_exp2f(z.y)} + f2{1.f, 1.f};
            const f2 g = bfup(pkbf(ov * f2{__builtin_amdgcn_rcpf(d.x), __builtin_amdgcn_rcpf(d.y)}));
            res[e] = pkbf(bfup(xv[e]) + bfup(pkbf(bfup(sv[e]) * g)));
        }
        *(u4*)(a.out + (size_t)(s2 + 32 * (16 * rq + li)) * 512 + 64 * c1 + 8 * t) = u4{res[0], res[1], res[2], res[3]};
    }
    STAMP(12);
    if (b == 0 && w == 0) {
        asm volatile("s_waitcnt lgkmcnt(0)" ::: "memory");
        u32 t0 = *(volatile LAS u32*)(lds + L_FFT + 22784), tk = *(volatile LAS u32*)(lds + L_FFT + 22784 + PROBE * 8);
        u32 d = __builtin_amdgcn_readfirstlane(min(tk - t0, 5000u)) * 10;
        u64 now = __builtin_amdgcn_s_memrealtime();
        while (__builtin_amdgcn_s_memrealtime() < now + d) __builtin_amdgcn_s_sleep(8);
    }
}

extern "C" void kernel_launch(void* const* d_in, const int* in_sizes, int n_in,
                              void* d_out, int out_size, void* d_ws, size_t ws_size,
                              hipStream_t stream) {
    Args a;
    a.x = (const u16*)d_in[0]; a.wr = (const u16*)d_in[1]; a.wi = (const u16*)d_in[2];
    a.wg = (const u16*)d_in[4]; a.wu = (const u16*)d_in[5]; a.wd = (const u16*)d_in[6];
    a.out = (u16*)d_out; a.ws = (unsigned char*)d_ws;
    (void)hipFuncSetAttribute((const void*)fused, hipFuncAttributeMaxDynamicSharedMemorySize, L_END);
    fused<<<256, 512, L_END, stream>>>(a);
}
```

```cpp
#include <hip/hip_runtime.h>
#include <stdint.h>
#include <type_traits>

typedef unsigned short u16;
typedef unsigned u32;
typedef unsigned long long u64;
typedef float f2 __attribute__((ext_vector_type(2)));
typedef float f4 __attribute__((ext_vector_type(4)));
typedef u32 u4 __attribute__((ext_vector_type(4)));
typedef u32 u2 __attribute__((ext_vector_type(2)));
typedef short s4 __attribute__((ext_vector_type(4)));
typedef __bf16 bf8 __attribute__((ext_vector_type(8)));
typedef __bf16 bf2 __attribute__((ext_vector_type(2)));
#define LAS __attribute__((address_space(3)))
#define GAS __attribute__((address_space(1)))
#define DEV __device__ __forceinline__
template <int N> using IC = std::integral_constant<int, N>;

constexpr int NFB = 43;
constexpr int WS_FF1 = 4096;
constexpr int WS_FF2 = WS_FF1 + 8192;
constexpr int WS_FHA = WS_FF2 + 8192;
constexpr int WS_FHB = WS_FHA + 16384;
constexpr int WS_FP = WS_FHB + 16384;
constexpr int WS_FW = WS_FP + 8192;
constexpr int WS_F1 = 1 << 20, WS_F2 = WS_F1 + (4 << 20), WS_H = WS_F2 + (4 << 20);
constexpr int WS_WDP = WS_H + 32 * 4 * NFB * 1024;
constexpr int WS_WP = WS_WDP + 8 * NFB * 4 * 1024;
constexpr int WS_STAMP = WS_WP + (2 << 20);
constexpr int WS_BYTES = WS_STAMP + (1 << 20);
constexpr int PG = 8192;
constexpr int L_SET1 = 8 * PG;
constexpr int L_FFT = 16 * PG;
constexpr int L_SMALL = 19 * PG;
constexpr int L_PROG = L_SMALL;
constexpr int L_TAG = L_SMALL + 32;
constexpr int L_STAT = L_SMALL + 64;
constexpr int L_CS = L_SMALL + 2048;
constexpr int L_X0 = L_FFT + 18688, L_X1 = L_SMALL + 3072;
constexpr int L_S = 4 * PG;
constexpr int L_END = 20 * PG;
constexpr u32 SENT = 0xFFFFFFFFu;
constexpr int CODE_BYTES = 40 * 1024;
constexpr int SPIN_CAP = 1 << 16;

struct Args { const u16* x; const u16* wr; const u16* wi; const u16* wg; const u16* wu; const u16* wd; u16* out; unsigned char* ws; };

#define BAR() asm volatile("s_waitcnt lgkmcnt(0)\n\ts_barrier" ::: "memory")
#define WAITVM(n) asm volatile("s_waitcnt vmcnt(" #n ")" ::: "memory")
#define NOF(x) (x)
#define NOPL(x) (x)
#define FENCE() asm volatile("" ::: "memory")
#define SCHED() __builtin_amdgcn_sched_barrier(0)
#define SGB(mask, n) __builtin_amdgcn_sched_group_barrier(mask, n, 0)
#define STAMP(i)
#define CSTAMP(i)

DEV f2 pkfma(f2 a, f2 b, f2 c) { return __builtin_elementwise_fma(a, b, c); }
struct KC { f2 pm, rpm; };
DEV f2 addmi(f2 a, f2 b, const KC& k) { return pkfma(b.yx, k.pm, a); }
DEV f2 addpi(f2 a, f2 b, const KC& k) { return pkfma(b.yx, k.pm.yx, a); }
template <bool INV> DEV void dft4(f2& a0, f2& a1, f2& a2, f2& a3, const KC& k) {
    f2 t0 = a0 + a2, t1 = a0 - a2, t2 = a1 + a3, t3 = a1 - a3;
    a0 = t0 + t2; a2 = t0 - t2;
    if (INV) { a1 = addpi(t1, t3, k); a3 = addmi(t1, t3, k); } else { a1 = addmi(t1, t3, k); a3 = addpi(t1, t3, k); }
}
template <bool INV> DEV void dft8(f2 (&v)[8], const KC& k) {
    f2 a0 = v[0] + v[4], a4 = v[0] - v[4], a1 = v[1] + v[5], a5 = v[1] - v[5];
    f2 a2 = v[2] + v[6], a6 = v[2] - v[6], a3 = v[3] + v[7], a7 = v[3] - v[7];
    dft4<INV>(a0, a1, a2, a3, k);
    v[0] = a0; v[2] = a1; v[4] = a2; v[6] = a3;
    f2 b1, b3, t0, t1;
    if (INV) { b1 = addpi(a5, a5, k); b3 = addpi(-a7, a7, k); t0 = addpi(a4, a6, k); t1 = addmi(a4, a6, k); }
    else { b1 = addmi(a5, a5, k); b3 = addmi(-a7, a7, k); t0 = addmi(a4, a6, k); t1 = addpi(a4, a6, k); }
    f2 t2 = b1 + b3, t3 = b1 - b3;
    v[1] = pkfma(t2, k.rpm.xx, t0); v[5] = pkfma(t2, k.rpm.yy, t0);
    if (INV) { v[3] = pkfma(t3.yx, k.rpm.yx, t1); v[7] = pkfma(t3.yx, k.rpm, t1); }
    else { v[3] = pkfma(t3.yx, k.rpm, t1); v[7] = pkfma(t3.yx, k.rpm.yx, t1); }
}
template <bool CONJ> DEV f2 cmul(f2 a, f2 w) {
    f2 r;
    if (CONJ) asm("v_pk_mul_f32 %0, %1, %2 op_sel:[1,1] op_sel_hi:[0,1]\n\tv_pk_fma_f32 %0, %1, %2, %0 op_sel_hi:[1,0,1] neg_hi:[0,0,1]" : "=&v"(r) : "v"(a), "v"(w));
    else asm("v_pk_mul_f32 %0, %1, %2 op_sel:[1,1] op_sel_hi:[0,1]\n\tv_pk_fma_f32 %0, %1, %2, %0 op_sel_hi:[1,0,1] neg_lo:[0,0,1]" : "=&v"(r) : "v"(a), "v"(w));
    return r;
}
DEV f2 cfmac_conj(f2 t, f2 beta, f2 B) {
    asm("v_pk_fma_f32 %0, %2, %1, %0 op_sel_hi:[0,1,1]\n\tv_pk_fma_f32 %0, %2, %1, %0 op_sel:[1,1,0] op_sel_hi:[1,0,1] neg_hi:[0,1,0]" : "+v"(t) : "v"(beta), "v"(B));
    return t;
}
DEV f2 cis(float r) { return f2{__builtin_amdgcn_cosf(r), __builtin_amdgcn_sinf(r)}; }
DEV float bflo(u32 v) { return __uint_as_float(v << 16); }
DEV float bfhi(u32 v) { return __uint_as_float(v & 0xFFFF0000u); }
DEV f2 bfup(u32 v) { return f2{bflo(v), bfhi(v)}; }
DEV u32 pkbf(f2 v) { bf2 r; r.x = (__bf16)v.x; r.y = (__bf16)v.y; u32 o = __builtin_bit_cast(u32, r); asm("" : "+v"(o)); return o; }
DEV float rbf(float f) { return __uint_as_float(((u32)__builtin_bit_cast(u16, (__bf16)f)) << 16); }
DEV f4 mfma(u4 a, u4 b, f4 c) { return __builtin_amdgcn_mfma_f32_16x16x32_bf16(__builtin_bit_cast(bf8, a), __builtin_bit_cast(bf8, b), c, 0, 0, 0); }
DEV u4 sanitize(u4 v) { return u4{min(v.x, 0xFFFFFFFEu), min(v.y, 0xFFFFFFFEu), min(v.z, 0xFFFFFFFEu), min(v.w, 0xFFFFFFFEu)}; }
DEV u32 max4(u4 v) { return max(max(v.x, v.y), max(v.z, v.w)); }
DEV void st128(u4 v, __amdgpu_buffer_rsrc_t rs, u32 voff, int soff) {
    __builtin_amdgcn_raw_buffer_store_b128(v, rs, voff, soff, 16);
    asm volatile("s_nop 1" :: "v"(v) : "memory");
}
template <int OFF2> DEV u4 trfrag2(LAS char* p) {
    s4 r0 = __builtin_amdgcn_ds_read_tr16_b64_v4i16((LAS s4*)p);
    s4 r1 = __builtin_amdgcn_ds_read_tr16_b64_v4i16((LAS s4*)(p + OFF2));
    u2 x0 = __builtin_bit_cast(u2, r0), x1 = __builtin_bit_cast(u2, r1);
    return u4{x0.x, x0.y, x1.x, x1.y};
}
template <int LI, int GI> DEV void dma_add(u32 vb, const void* sbase, u32 sl) {
    const u32 va = vb + (u32)GI;
    asm volatile("s_add_u32 m0, %2, %3\n\ts_nop 0\n\tglobal_load_lds_dwordx4 %0, %1" :: "v"(va), "s"(sbase), "s"(sl), "i"(LI) : "memory", "scc");
}
template <int LI, int GI> DEV void dma4k_c(u32 vb, const void* sbase, u32 sl) {
    const u32 va = vb + (u32)GI;
    asm volatile("s_add_u32 m0, %2, %3\n\ts_nop 0\n\tglobal_load_lds_dwordx4 %0, %1 sc1\n\tglobal_load_lds_dwordx4 %0, %1 offset:1024 sc1\n\t"
                 "global_load_lds_dwordx4 %0, %1 offset:2048 sc1\n\tglobal_load_lds_dwordx4 %0, %1 offset:3072 sc1"
                 :: "v"(va), "s"(sbase), "s"(sl), "i"(LI) : "memory", "scc");
}
template <int OFF> DEV void ldg_u16(u32& d, u32 vo, const void* sb) { asm volatile("global_load_ushort %0, %1, %2 offset:%3" : "=v"(d) : "v"(vo), "s"(sb), "i"(OFF) : "memory"); }
template <int OFF> DEV void ldg_u32(u32& d, u32 vo, const void* sb) { asm volatile("global_load_dword %0, %1, %2 offset:%3" : "=v"(d) : "v"(vo), "s"(sb), "i"(OFF) : "memory"); }
DEV void lds_wait(u32 addr, u32 v) {
    u32 tmp, cnt;
    asm volatile("s_mov_b32 %1, 0x80000\n"
                 "1:\n\tds_read_b32 %0, %2\n\ts_sub_u32 %1, %1, 1\n\ts_waitcnt lgkmcnt(0)\n\ts_cbranch_scc1 2f\n\tv_cmp_gt_u32 vcc, %3, %0\n\ts_cbranch_vccnz 1b\n2:"
                 : "=&v"(tmp), "=&s"(cnt) : "v"(addr), "s"(v) : "memory", "vcc", "scc");
}
template <int N, typename F> DEV void sfor(F&& f) { if constexpr (N > 0) { sfor<N - 1>(f); f(IC<N - 1>{}); } }

DEV void pin9(f4 (&x)[4], f4 (&y)[4], f4& z) {
    asm volatile("" : "+v"(x[0]), "+v"(x[1]), "+v"(x[2]), "+v"(x[3]), "+v"(y[0]), "+v"(y[1]), "+v"(y[2]), "+v"(y[3]), "+v"(z));
}
DEV void pin4(f4 (&x)[4]) { asm volatile("" : "+v"(x[0]), "+v"(x[1]), "+v"(x[2]), "+v"(x[3])); }
DEV void dma_rt(u32 voff, const void* sbase, u32 sl) {
    asm volatile("s_mov_b32 m0, %2\n\ts_nop 0\n\tglobal_load_lds_dwordx4 %0, %1" :: "v"(voff), "s"(sbase), "s"(sl) : "memory");
}
DEV u4 geglu8(const f4 (&acc)[4], LAS char* cs, f2 st) {
    u32 hw[4];
    const f2 nm = f2{-st.x, -st.x}, ri = f2{st.y, st.y};
#pragma unroll
    for (int c = 0; c < 2; c++) {
        f4 cg_ = *(LAS f4*)(cs + c * 64), cu_ = *(LAS f4*)(cs + (2 + c) * 64);
#pragma unroll
        for (int e = 0; e < 2; e++) {
            f2 g = pkfma(e ? cg_.zw : cg_.xy, nm, e ? acc[c].zw : acc[c].xy) * ri;
            f2 u = pkfma(e ? cu_.zw : cu_.xy, nm, e ? acc[2 + c].zw : acc[2 + c].xy) * ri;
            f2 z = g * pkfma(g * g, f2{-0.10294324f, -0.10294324f}, f2{-2.3022082f, -2.3022082f});
            f2 d = f2{__builtin_amdgcn_exp2f(z.x), __builtin_amdgcn_exp2f(z.y)} + f2{1.f, 1.f};
            f2 r = f2{__builtin_amdgcn_rcpf(d.x), __builtin_amdgcn_rcpf(d.y)};
            hw[2 * c + e] = pkbf(g * r * u);
        }
    }
    return u4{hw[0], hw[1], hw[2], hw[3]};
}

enum { A_XL = 1, A_XC, A_P1, A_XB, A_S0, A_CS, A_WB, A_PB, A_D1, A_G1, A_D3 };
enum { B_XL = 1, B_W0, B_W1, B_W2, B_S0, B_W3, B_CS, B_FP, B_E1, B_E2, B_G1, B_F3 };

__global__ void __launch_bounds__(512, 2) fused(Args a) {
    extern __shared__ __attribute__((aligned(16))) char smem[];
    LAS char* lds = (LAS char*)smem;
    const int tid = threadIdx.x, w = __builtin_amdgcn_readfirstlane(tid >> 6);
    int lane = tid & 63;
    const int b = blockIdx.x, xcd = b & 7, r = b >> 3, t = r & 7, par = xcd & 1, rg = (xcd >> 1) * 4 + (r >> 3);
    const int s2 = 2 * rg + par, cg = par * 8 + t, sig = xcd * 32 + r, pid = s2 * 8 + t;
    const int rq = w & 3, hf = w >> 2, j4 = r >> 3;
#define BE(l) ((((l) >> 3) << 1) | ((l) & 1))
#define IX(l) (((l) >> 1) & 3)
    u32 l16 = lane * 16;
    u32 cpf = 0;
    if (w == 7) {
        const u64 pc = __builtin_amdgcn_s_getpc() & ~(u64)255;
        const u32 off = min((u32)((t * 64 + lane) * 128), (u32)(CODE_BYTES - 128));
        asm volatile("global_load_dword %0, %1, %2" : "=v"(cpf) : "v"(off), "s"(pc) : "memory");
    }
    *(volatile LAS u32*)(lds + L_PROG + w * 4) = 0;
    BAR();
    STAMP(0);
    auto dma_x = [&]() {
        const char* xs = (const char*)a.x + (size_t)((s2 ^ hf) + 512 * rq) * 1024;
#pragma unroll
        for (int i = 0; i < 16; i++) { const int row = (i + 2 * t) & 15; dma_rt(l16 ^ (u32)(row * (32768 + 16)), xs, (u32)(L_SET1 + rq * 16384 + row * 1024)); }
    };
    if (hf == 0) dma_x();
    int li, lg, c2p, l3, qb; u32 vpg;
#define RELANE() do { asm volatile("" : "+v"(lane)); l16 = lane * 16; li = lane & 15; lg = lane >> 4; c2p = lane & 3; l3 = (lane >> 2) & 7; qb = lane >> 5; vpg = (u32)(L_PROG + (lane & 3) * 4); } while (0)
    const __amdgpu_buffer_rsrc_t rs = __builtin_amdgcn_make_buffer_rsrc(a.ws, 0, WS_BYTES, 0x00020000);
    KC kc; kc.pm = f2{1.f, -1.f}; kc.rpm = f2{0.70710678118654752f, -0.70710678118654752f};
    asm volatile("" : "+s"(kc.pm), "+s"(kc.rpm));
    const u4 ones = {0x3F803F80u, 0x3F803F80u, 0x3F803F80u, 0x3F803F80u};
    auto post = [&](u32 v) { asm volatile("s_waitcnt lgkmcnt(0)" ::: "memory"); *(volatile LAS u32*)(lds + L_PROG + w * 4) = v; FENCE(); };
    auto wait_grp = [&](int g, u32 v) { lds_wait(vpg + g * 16, v); };
    auto wait_pair = [&](u32 v) { lds_wait((u32)(L_PROG + (w ^ 4) * 4), v); };
    u2 tag;
    auto get_tag = [&]() { u2 tg = *(LAS u2*)(lds + L_TAG); tag.x = __builtin_amdgcn_readfirstlane(tg.x); tag.y = __builtin_amdgcn_readfirstlane(tg.y); };
    auto flag_store = [&](int off) { __builtin_amdgcn_raw_buffer_store_b64(tag, rs, lane ? WS_BYTES : 0, off, 16); };
    auto flag_wait = [&](u32 voff, int soff) {
        _Pragma("unroll 1") for (int spins = 0; spins < SPIN_CAP; spins++) {
            u2 f = __builtin_amdgcn_raw_buffer_load_b64(rs, voff, soff, 16);
            if (__all(f.x == tag.x && f.y == tag.y)) break;
            __builtin_amdgcn_s_sleep(16);
            FENCE();
        }
        __builtin_amdgcn_fence(__ATOMIC_ACQUIRE, "wavefront");
        FENCE();
    };
    auto read_tile = [&](u4 (&x)[16], int tile) {
        LAS char* p = lds + L_SET1 + (16 * rq + li) * 1024;
#pragma unroll
        for (int k = 0; k < 16; k++) x[k] = *(LAS u4*)(p + (((4 * IX(k) + lg) ^ li) * 16) + ((BE(k) + j4) & 3) * 256);
    };
    auto stats = [&](const u4 (&x)[16]) {
        f4 s1 = {0, 0, 0, 0}, sq = s1, s1b = s1, sqb = s1;
#pragma unroll
        for (int k = 0; k < 16; k += 2) { s1 = mfma(ones, x[k], s1); sq = mfma(x[k], x[k], sq); s1b = mfma(ones, x[k + 1], s1b); sqb = mfma(x[k + 1], x[k + 1], sqb); }
        sq += sqb;
        const int rr = li & 3;
        const float d = rr == 0 ? sq.x : rr == 1 ? sq.y : rr == 2 ? sq.z : sq.w;
        if (lg == (li >> 2)) {
            const float mean = (s1.x + s1b.x) * (1.0f / 512.0f);
            float var = d * (1.0f / 512.0f) - mean * mean;
            if (!(mean * mean <= 1024.0f * var)) {
                const u32* xrow = (const u32*)(a.x + (size_t)((s2 ^ hf) + 32 * (16 * rq + li)) * 512);
                float q = 0.f;
                _Pragma("unroll 1") for (int i = 0; i < 256; i++) { u32 v = xrow[i]; float d0 = bflo(v) - mean, d1 = bfhi(v) - mean; q += d0 * d0; q += d1 * d1; }
                var = q * (1.0f / 512.0f);
            }
            const float den = rbf(__builtin_amdgcn_sqrtf(rbf(rbf(var) + 1e-5f)));
            *(LAS f2*)(lds + L_STAT + (hf * 64 + 16 * rq + li) * 8) = f2{rbf(mean), __builtin_amdgcn_rcpf(den)};
        }
        FENCE();
    };
    const int nblk = (t < 6 - par) ? 3 : 2, fb2 = 32 + par * 6 + t;
    const char* wdp = (const char*)a.ws + WS_WDP + (size_t)(t * NFB) * 4096;

    f2 tw64[8], twT[8];
    int x3a[4];
    auto fft13_consts = [&](float scale) {
        int lane_ = lane; asm volatile("" : "+v"(lane_));
        const int c2p = lane_ & 3, l3 = (lane_ >> 2) & 7, qb = lane_ >> 5;
        const int wq = rq, q1 = qb ? (wq == 0 ? 4 : 8 - wq) : wq, c2 = 4 * t + c2p;
        const float f64 = (float)l3 * (1.0f / 64.0f);
#pragma unroll
        for (int k = 1; k < 8; k++) tw64[k] = cis(f64 * (float)k);
        const float fT = (float)(8 * c2 * q1 + s2 * l3) * (1.0f / 2048.0f), dT = (float)s2 * (1.0f / 256.0f);
#pragma unroll
        for (int k = 0; k < 8; k++) twT[k] = cis(fT + dT * (float)k) * scale;
#pragma unroll
        for (int k = 0; k < 4; k++) {
            const int ua = (k >> 1) * 4 + (k & 1), u1 = l3 + 8 * ua;
            const int cls = (u1 < 32) ? u1 : ((64 - u1) & 31), hi = u1 >> 5;
            int mbit, sub;
            if (wq == 0) { mbit = qb; sub = hi; } else if (cls != 0) { sub = hi; mbit = qb ^ hi; } else { mbit = hi; sub = qb ^ hi; }
            x3a[k] = L_FFT + wq * 4608 + (cls * 18 + mbit * 8 + sub * 4 + c2p) * 8 - (k == 3 ? 2304 : 0);
        }
        asm volatile("" : "+v"(tw64[1]), "+v"(tw64[2]), "+v"(tw64[3]), "+v"(tw64[4]), "+v"(tw64[5]), "+v"(tw64[6]), "+v"(tw64[7]),
                     "+v"(twT[0]), "+v"(twT[1]), "+v"(twT[2]), "+v"(twT[3]), "+v"(twT[4]), "+v"(twT[5]), "+v"(twT[6]), "+v"(twT[7]),
                     "+v"(x3a[0]), "+v"(x3a[1]), "+v"(x3a[2]), "+v"(x3a[3]));
    };

    u4 xa[16], xb[16];
    const int hoff0 = WS_H + ((s2 * 4 + rq) * NFB) * 1024, hoff1 = WS_H + (((s2 ^ 1) * 4 + rq) * NFB) * 1024;
    RELANE();
    const char* wgs = (const char*)a.wg + (size_t)(32 * rq) * 2752 + cg * 128;
    const char* wus = (const char*)a.wu + (size_t)(32 * rq) * 2752 + cg * 128;
    const u32 wvE = (u32)((lane >> 3) * 2752 + (((lane & 7) ^ (((lane >> 4) & 1) << 1)) * 16)), wvO = (u32)((lane >> 3) * 2752 + (((lane & 7) ^ (((lane >> 4) & 1) << 1) ^ 4) * 16));
    auto dma_w1 = [&](auto KK, auto I) {
        constexpr int kk = KK, g = I >> 1, mat = I & 1;
        dma_add<kk * PG + mat * 4096 + g * 1024, g * 8 * 2752>((g & 1) ? wvO : wvE, (mat ? wus : wgs) + ((BE(kk) + j4) & 3) * (128 * 2752), (u32)(rq * 2 * PG));
    };
    const int rot = (2 * t + 4 * j4) & 15;
    auto dma_wd = [&](auto LI, auto I, int p0) { dma4k_c<LI + I * 16384, 0>(l16, wdp + (size_t)(p0 + 2 * ((rq + 4 * I + rot) & 15)) * 4096, (u32)(rq * 4096)); };

    if (hf == 0) {
        __builtin_amdgcn_s_setprio(2);
        const int f1st = WS_F1 + ((lane >> 3) * 8 + (((lane >> 2) & 1) ? (rq == 0 ? 4 : 8 - rq) : rq)) * 16384 + pid * 64 + c2p * 16;
        u2 ev;
        { u32 zero = 0; asm volatile("global_load_dwordx2 %0, %1, %2 sc1" : "=v"(ev) : "v"(zero), "s"(a.ws) : "memory"); }
        fft13_consts(1.0f);
        WAITVM(1);
        post(A_XL);
        STAMP(1);
        u32 raw[8];
        const int rw = lane >> 2;
        read_tile(xa, 0);
#pragma unroll
        for (int c1 = 0; c1 < 8; c1++) raw[c1] = *(LAS u32*)(lds + L_SET1 + (16 * rq + rw) * 1024 + (((8 * c1 + t) ^ rw) * 16) + c2p * 4);
        post(A_XC);
        stats(xa);
        STAMP(2);
        f2 v[8];
        {
            const f2 st = *(LAS f2*)(lds + L_STAT + (16 * rq + rw) * 8);
#pragma unroll
            for (int c1 = 0; c1 < 8; c1++) v[c1] = bfup(pkbf(bfup(pkbf(bfup(raw[c1]) - st.xx)) * st.yy));
            dft8<false>(v, kc);
            LAS f2* xb1 = (LAS f2*)(lds + L_FFT) + w * 64 + lane;
#pragma unroll
            for (int q1 = 0; q1 < 8; q1++) { const int reg = (q1 < 4) ? q1 * 2 : (((8 - q1) & 3) * 2 + 1); xb1[reg * 288] = v[q1]; }
        }
        asm volatile("s_waitcnt vmcnt(0)" : "+v"(ev) :: "memory");
        {
            const u64 epoch = ((u64)(u32)__builtin_amdgcn_readfirstlane(ev.y) << 32) | (u32)__builtin_amdgcn_readfirstlane(ev.x);
            u64 tg = (epoch + 1) * 0x9E3779B97F4A7C15ull; tg ^= tg >> 29; tg *= 0xBF58476D1CE4E5B9ull; tg ^= tg >> 32;
            tag = u2{(u32)tg, (u32)(tg >> 32)};
            *(LAS u4*)(lds + L_TAG) = u4{(u32)tg, (u32)(tg >> 32), (u32)(epoch + 1), (u32)((epoch + 1) >> 32)};
        }
        post(A_P1);
#pragma unroll
        for (int c1 = 0; c1 < 8; c1++) *(LAS u32*)(lds + ((rq >> 1) ? L_X1 : L_X0) + ((rq & 1) * 16 + rw) * 128 + ((c1 ^ (rw & 7)) * 16) + c2p * 4) = raw[c1];
        STAMP(19);
        wait_pair(B_XL);
        STAMP(20);
        read_tile(xb, 1);
        post(A_XB);
        STAMP(22);
        wait_grp(0, A_P1);
        STAMP(23);
        {
            LAS f2* reg = (LAS f2*)(lds + L_FFT) + (w * 2 + qb) * 288;
#pragma unroll
            for (int k = 0; k < 8; k++) v[k] = reg[(lane & 31) + 32 * k];
            dft8<false>(v, kc);
#pragma unroll
            for (int k = 1; k < 8; k++) v[k] = cmul<true>(v[k], tw64[k]);
#pragma unroll
            for (int k = 0; k < 8; k++) reg[36 * k + (lane & 31)] = v[k];
            FENCE();
#pragma unroll
            for (int k = 0; k < 8; k++) v[k] = reg[36 * l3 + 4 * k + c2p];
            dft8<false>(v, kc);
#pragma unroll
            for (int k = 0; k < 8; k++) v[k] = cmul<true>(v[k], twT[k]);
            FENCE();
            *(LAS f2*)(lds + x3a[0]) = v[0];
            *(LAS f2*)(lds + x3a[1]) = v[1]; *(LAS f2*)(lds + x3a[1] + 1152) = v[2]; *(LAS f2*)(lds + x3a[1] + 2304) = v[3];
            *(LAS f2*)(lds + x3a[2]) = v[4];
            *(LAS f2*)(lds + x3a[3] + 2304) = v[5]; *(LAS f2*)(lds + x3a[3] + 1152) = v[6]; *(LAS f2*)(lds + x3a[3]) = v[7];
            FENCE();
            STAMP(24);
#pragma unroll
            for (int i = 0; i < 4; i++) {
                u4 o = *(LAS u4*)(lds + L_FFT + w * 4608 + (i * 8 + (lane >> 3)) * 144 + (lane & 7) * 16);
                st128(o, rs, NOF(f1st), i * (64 * 16384));
            }
        }
        STAMP(3);
        __builtin_amdgcn_s_setprio(0);
        STAMP(4);
    } else {
        u32 pk[8];
        const int un = sig < 4 * NFB ? sig : 0, fb = un >> 2, jj = (un >> 1) & 1, tcol = (un & 1) * 4 + rq;
        __builtin_amdgcn_s_sleep(2);
        {
            const u32 vd = (u32)(((fb * 32 + 4 * lg) * 512 + 64 * (4 * jj + (li >> 2)) + 8 * tcol + 2 * (li & 3)) * 2);
            sfor<4>([&](auto I) {
                ldg_u32<I * 1024>(pk[I], vd, a.wd);
                ldg_u32<I * 1024>(pk[4 + I], vd, (const char*)a.wd + 16384);
            });
        }
        sfor<8>([&](auto I) { dma_w1(IC<0>{}, I); });
        wait_pair(A_XL);
        read_tile(xa, 0);
        asm volatile("s_waitcnt lgkmcnt(0)" ::: "memory");
        wait_pair(A_XC);
        dma_x();
        asm volatile("s_waitcnt vmcnt(24)" : "+v"(cpf), "+v"(pk[0]), "+v"(pk[1]), "+v"(pk[2]), "+v"(pk[3]), "+v"(pk[4]), "+v"(pk[5]), "+v"(pk[6]), "+v"(pk[7]) :: "memory");
        sfor<8>([&](auto I) { dma_w1(IC<1>{}, I); });
        {
            const int so = sig < 4 * NFB ? WS_WDP + ((tcol * NFB + fb) * 4 + 2 * jj) * 1024 : WS_BYTES;
            u4 oe = {__builtin_amdgcn_perm(pk[1], pk[0], 0x05040100), __builtin_amdgcn_perm(pk[3], pk[2], 0x05040100), __builtin_amdgcn_perm(pk[5], pk[4], 0x05040100), __builtin_amdgcn_perm(pk[7], pk[6], 0x05040100)};
            u4 oo = {__builtin_amdgcn_perm(pk[1], pk[0], 0x07060302), __builtin_amdgcn_perm(pk[3], pk[2], 0x07060302), __builtin_amdgcn_perm(pk[5], pk[4], 0x07060302), __builtin_amdgcn_perm(pk[7], pk[6], 0x07060302)};
            st128(oe, rs, l16, so);
            st128(oo, rs, l16, so + 1024);
        }
        STAMP(1);
        WAITVM(10);
        post(B_W0);
        read_tile(xb, 1);
        __builtin_amdgcn_s_setprio(1);
        STAMP(2);
    }
    f2 st0, st1;
    {
        const u32 fz = (u32)((li >> 3) | ((lg & 1) << 1));
        const u32 tb = (u32)((8 * lg + (li >> 2)) * 128 + 8 * (li & 3));
        u32 p0 = tb + (((2 * hf) ^ fz) << 5), p1 = tb + (((2 * hf + 1) ^ fz) << 5), pc = tb + (((2 * hf + (rq & 1)) ^ fz) << 5) + (rq >> 1) * 4096;
        const u32 wvB = (u32)((lane >> 2) * 2752 + (((lane & 3) ^ (((lane >> 5) & 1) << 1)) * 16));
        const char* wbs = (const char*)((rq >> 1) ? a.wu : a.wg) + (size_t)((rq & 1) * 16) * 2752 + fb2 * 64;
        f4 accA[4], accB[4], acs = {0, 0, 0, 0};
#pragma unroll
        for (int j = 0; j < 4; j++) { accA[j] = f4{0, 0, 0, 0}; accB[j] = f4{0, 0, 0, 0}; }
        u4 F[2][5];
        auto ldf = [&](int ks, u4 (&f)[5]) {
            const int off = (ks & 7) * PG;
            f[0] = trfrag2<512>(lds + p0 + off); f[1] = trfrag2<512>(lds + p1 + off); f[2] = trfrag2<512>(lds + p0 + off + 4096); f[3] = trfrag2<512>(lds + p1 + off + 4096); f[4] = trfrag2<512>(lds + pc + off);
        };
        wait_grp(1, B_W0);
        STAMP(14);
        ldf(0, F[0]);
        sfor<16>([&](auto S) {
            constexpr int s = S, ks = (s & 8) | ((s & 3) << 1) | ((s >> 2) & 1), sn = s + 1, kn = (sn & 8) | ((sn & 3) << 1) | ((sn >> 2) & 1);
            if ((s >= 1 && s <= 4) || s == 7 || s >= 9) pin9(accA, accB, acs);
            if (s == 3) {
                if (hf) { WAITVM(10); post(B_W1); }
                else { WAITVM(0); flag_store(WS_FF1 + ((rq * 4 + ((s2 & 7) >> 1)) * 64 + (s2 >> 3) * 16 + (s2 & 1) * 8 + t) * 8); }
                wait_grp(1, B_W1);
            }
            if constexpr (s >= 1 && s <= 4) if (hf) {
                if (s == 1) { wait_pair(A_XB); get_tag(); }
                sfor<4>([&](auto I) { dma_w1(IC<8 + ((s - 1) >> 1)>{}, IC<((s - 1) & 1) * 4 + I>{}); });
            }
            if (s == 4) STAMP(15);
            if (s == 7) {
                if (hf) { WAITVM(8); post(B_W2); flag_store(sig < 4 * NFB ? WS_FP + (((sig & 1) * 4 + rq) * 128 + (sig >> 1)) * 8 : WS_BYTES); }
                wait_grp(1, B_W2); STAMP(16);
                p0 += 65536; p1 += 65536; pc += 65536;
                asm volatile("" : "+v"(p0), "+v"(p1), "+v"(pc));
            }
            if (s == 8) post(hf ? B_S0 : A_S0);
            if (s == 11) {
                if (hf) { WAITVM(0); post(B_W3); }
                wait_grp(1, B_W3); STAMP(17);
            }
            if constexpr (s >= 9) if (hf == 0 && nblk == 3) {
                if (s == 9) { wait_grp(0, A_S0); wait_grp(1, B_S0); sfor<2>([&](auto I) { dma_add<I * 4096, IX(I) * 32 * 2752>(wvB, wbs + ((BE(I) + j4) & 3) * (128 * 2752), (u32)(rq * 1024)); }); }
                sfor<2>([&](auto I) { constexpr int k = (s - 8) * 2 + I; dma_add<k * 4096, IX(k) * 32 * 2752>(wvB, wbs + ((BE(k) + j4) & 3) * (128 * 2752), (u32)(rq * 1024)); });
            }
            SCHED();
            if (s + 1 < 16) ldf(kn, F[(s + 1) & 1]);
            u4 (&f)[5] = F[s & 1];
#pragma unroll
            for (int j = 0; j < 4; j++) { accA[j] = mfma(f[j], xa[ks], accA[j]); accB[j] = mfma(f[j], xb[ks], accB[j]); }
            acs = mfma(f[4], ones, acs);
            if (s + 1 < 16) {
                SGB(0x100, 2);
#pragma unroll
                for (int i = 0; i < 8; i++) { SGB(0x8, 1); SGB(0x100, 1); }
                SGB(0x8, 1);
            }
            SCHED();
        });
        RELANE();
        if (hf) stats(xb);
        if (li == 0) *(LAS f4*)(lds + L_CS + (hf * 4 + rq) * 64 + lg * 16) = acs;
        post(hf ? B_CS : A_CS);
        STAMP(18);
        wait_grp(hf, hf ? B_CS : A_CS);
        if (hf == 0) wait_pair(B_CS);
        LAS char* cs = lds + L_CS + hf * 256 + lg * 16;
        st0 = *(LAS f2*)(lds + L_STAT + (16 * rq + li) * 8);
        st1 = *(LAS f2*)(lds + L_STAT + (64 + 16 * rq + li) * 8);
        st128(geglu8(accA, cs, st0), rs, l16, hoff0 + (2 * cg + hf) * 1024);
        st128(geglu8(accB, cs, st1), rs, l16, hoff1 + (2 * cg + hf) * 1024);
    }
    STAMP(5);
    if (hf == 0) {
        WAITVM(2); post(A_WB);
        if (nblk == 3) {
            wait_grp(0, A_WB);
            const u32 tb = (u32)((8 * lg + (li >> 2)) * 64 + (lg & 1) * 32 + 8 * (li & 3));
            LAS char* p0 = lds + tb; LAS char* p1 = lds + (tb ^ 32);
            LAS char* pc = lds + (tb ^ ((rq & 1) * 32)) + (rq >> 1) * 2048;
            f4 acc[2][4], acs = {0, 0, 0, 0};
#pragma unroll
            for (int j = 0; j < 4; j++) { acc[0][j] = f4{0, 0, 0, 0}; acc[1][j] = f4{0, 0, 0, 0}; }
            u4 F[2][5];
            auto ldf = [&](int ks, u4 (&f)[5]) {
                const int off = ks * 4096;
                f[4] = trfrag2<256>(pc + off);
                f[0] = trfrag2<256>(p0 + off); f[1] = trfrag2<256>(p1 + off); f[2] = trfrag2<256>(p0 + off + 2048); f[3] = trfrag2<256>(p1 + off + 2048);
            };
            ldf(0, F[0]);
            sfor<16>([&](auto S) {
                constexpr int ks = S;
                if (ks == 4) { WAITVM(0); flag_store(WS_FHA + ((rg * 8 + w) * 16 + cg) * 8); wait_grp(1, B_FP); }
                if constexpr (ks >= 4 && ks % 3 == 1) dma_wd(IC<L_SET1>{}, IC<(ks - 4) / 3>{}, 0);
                SCHED();
                if (ks + 1 < 16) ldf(ks + 1, F[(ks + 1) & 1]);
                u4 (&f)[5] = F[ks & 1];
                acs = mfma(f[4], ones, acs);
#pragma unroll
                for (int j = 0; j < 4; j++) { acc[0][j] = mfma(f[j], xa[ks], acc[0][j]); acc[1][j] = mfma(f[j], xb[ks], acc[1][j]); }
                if (ks + 1 < 16) {
                    SGB(0x100, 2);
#pragma unroll
                    for (int i = 0; i < 8; i++) { SGB(0x8, 1); SGB(0x100, 1); }
                    SGB(0x8, 1);
                }
                SCHED();
            });
            if (li == 0) *(LAS f4*)(lds + L_CS + 512 + rq * 64 + lg * 16) = acs;
            post(A_PB);
            wait_grp(0, A_PB);
            st128(geglu8(acc[0], lds + L_CS + 512 + lg * 16, st0), rs, l16, hoff0 + fb2 * 1024);
            st128(geglu8(acc[1], lds + L_CS + 512 + lg * 16, st1), rs, l16, hoff1 + fb2 * 1024);
        } else {
            WAITVM(0); flag_store(WS_FHA + ((rg * 8 + w) * 16 + cg) * 8);
            wait_grp(1, B_FP);
            sfor<4>([&](auto I) { dma_wd(IC<L_SET1>{}, I, 0); });
        }
        STAMP(6);
        sfor<4>([&](auto I) { dma_wd(IC<0>{}, I, 1); });
        if (nblk == 3) { WAITVM(16); flag_store(WS_FHB + ((rg * 4 + rq) * 16 + fb2 - 32) * 8); }
    } else {
        f2 v[8], tw0[4], tw1[8], al[8], be[8];
        const int rb = qb, r3 = 2 * rq + rb;
        const int cls2 = sig >> 3, m2 = sig & 7;
        const int wq2 = (m2 < 4) ? m2 : ((8 - m2) & 3), pos1 = wq2 * 64 + cls2 * 2 + (m2 >> 2);
        const bool special = (cls2 == 0) && ((m2 & 3) == 0);
        int u1a, q1a, u1b, q1b;
        if (cls2 != 0) { u1a = cls2; q1a = m2; u1b = 64 - cls2; q1b = (8 - m2) & 7; }
        else if (special) { u1a = 0; u1b = 32; q1a = q1b = m2; }
        else { u1a = u1b = (m2 < 4) ? 0 : 32; q1a = m2; q1b = 8 - m2; }
        int sA, vH0, sB, vH1;
        if (!special) { sA = 0; vH0 = rq; sB = 1; vH1 = (u1a == 0) ? ((4 - rq) & 3) : 3 - rq; }
        else { sA = sB = rq >> 1; vH0 = rq & 1; vH1 = (rq == 0) ? 2 : (rq == 1) ? 3 : (rq == 2) ? 3 : 2; }
        const bool q0 = (m2 == 0);
        const int dy = ((sA ? u1b : u1a) == 0 && vH0 == 0) ? 8 : 7, dq = q0 ? 8 : 7;
        int y1 = lane >> 3, t1 = lane & 7, y2 = (dy - y1) & 7, t2 = (dq - t1) & 7, hA = vH0, hB = vH1;
        bool self = false;
        if (special && rq == 0) {
            const int k = lane & 31;
            if (qb) { hA = hB = 2; y1 = k >> 3; t1 = k & 7; y2 = 7 - y1; t2 = (dq - t1) & 7; }
            else {
                hA = hB = 0; const int vx = k & 3, y = k >> 2;
                if (!q0 || vx) { y1 = y; t1 = vx; y2 = (8 - y) & 7; t2 = dq - vx; }
                else { t1 = t2 = (y >= 4) ? 4 : 0; const int yy = y & 3; if (yy) { y1 = yy; y2 = 8 - yy; } else { y1 = 0; y2 = 4; self = true; } }
            }
        }
        const int uA = (sA ? u1b : u1a) + 64 * (hA + 4 * y1), uB = (sB ? u1b : u1a) + 64 * (hB + 4 * y2);
        const int qA = (sA ? q1b : q1a) + 8 * t1, qB = (sB ? q1b : q1a) + 8 * t2;
        u32 wl[20];
        {
            const u32 vA = (u32)((uA * 257 + qA) * 2), vB = (u32)((uB * 257 + qB) * 2);
            sfor<4>([&](auto K) { ldg_u16<K * 128>(wl[K], vA, a.wr); ldg_u16<K * 128>(wl[8 + K], vA, a.wi); ldg_u16<K * 128>(wl[4 + K], vB, a.wr); ldg_u16<K * 128>(wl[12 + K], vB, a.wi); });
            const u32 vA2 = (u32)(uA * 514 + 512), vB2 = (u32)(uB * 514 + 512);
            ldg_u16<0>(wl[16], vA2, a.wr); ldg_u16<0>(wl[17], vA2, a.wi); ldg_u16<0>(wl[18], vB2, a.wr); ldg_u16<0>(wl[19], vB2, a.wi);
        }
        const int rowB = L_FFT + ((qb ? (vH1 * 2 + sB) : (vH0 * 2 + sA)) * 36 + (lane & 31)) * 8;
        const bool rot = q0 && (t1 == 0);
        const int p3a1 = L_FFT + (y1 * 290 + (hA * 2 + sA) * 36 + t1 * 4) * 8;
        const int p3a2 = L_FFT + (y2 * 290 + (hB * 2 + sB) * 36 + t2 * 4) * 8;
        const float thA = (float)qA * (1.0f / 512.0f), thB = (float)qB * (1.0f / 512.0f);
#pragma unroll
        for (int k = 1; k < 4; k++) tw0[k] = cis((float)r3 * ((float)k / 32.0f));
#pragma unroll
        for (int k = 1; k < 8; k++) tw1[k] = cis((float)c2p * ((float)k / 32.0f));
        f2 csq[8];
#pragma unroll
        for (int e = 0; e < 8; e++) csq[e] = cis(((e >> 2) ? thB : thA) + 0.125f * (float)(e & 3));
        asm volatile("" : "+v"(csq[0]), "+v"(csq[1]), "+v"(csq[2]), "+v"(csq[3]), "+v"(csq[4]), "+v"(csq[5]), "+v"(csq[6]), "+v"(csq[7]),
                     "+v"(tw0[1]), "+v"(tw0[2]), "+v"(tw0[3]), "+v"(tw1[1]), "+v"(tw1[2]), "+v"(tw1[3]), "+v"(tw1[4]), "+v"(tw1[5]), "+v"(tw1[6]), "+v"(tw1[7]));
        {
            const u32 o1 = min(rq * 22 + lane, 85) * 8, o3 = lane * 8;
            bool posted = false;
            _Pragma("unroll 1") for (int spins = 0; spins < SPIN_CAP; spins++) {
                u2 f1 = __builtin_amdgcn_raw_buffer_load_b64(rs, o1, WS_FP + t * 1024, 16);
                u2 f3 = __builtin_amdgcn_raw_buffer_load_b64(rs, o3, WS_FF1 + (wq2 * 4 + rq) * 512, 16);
                const bool ok = f1.x == tag.x && f1.y == tag.y && f3.x == tag.x && f3.y == tag.y;
                const bool all = __all(ok);
                if (!posted) { WAITVM(0); flag_store(WS_FHA + ((rg * 8 + w) * 16 + cg) * 8); posted = true; }
                if (all) break;
                __builtin_amdgcn_s_sleep(16);
                FENCE();
            }
            __builtin_amdgcn_fence(__ATOMIC_ACQUIRE, "wavefront");
            FENCE();
        }
        post(B_FP);
        STAMP(3);
        const int f1off = WS_F1 + sig * 16384 + 2 * rq * 512;
        u4 d[4];
        SCHED();
#pragma unroll
        for (int H = 0; H < 4; H++) d[H] = __builtin_amdgcn_raw_buffer_load_b128(rs, NOF(l16), f1off + H * 4096, 16);
        SCHED();
        asm volatile("" : "+v"(wl[0]), "+v"(wl[1]), "+v"(wl[2]), "+v"(wl[3]), "+v"(wl[4]), "+v"(wl[5]), "+v"(wl[6]), "+v"(wl[7]), "+v"(wl[8]), "+v"(wl[9]),
                     "+v"(wl[10]), "+v"(wl[11]), "+v"(wl[12]), "+v"(wl[13]), "+v"(wl[14]), "+v"(wl[15]), "+v"(wl[16]), "+v"(wl[17]), "+v"(wl[18]), "+v"(wl[19]) :: "memory");
        const f2 wS[2] = {f2{bflo(wl[16]), bflo(wl[17])}, f2{bflo(wl[18]), bflo(wl[19])}};
        {
            f2 W[8], WB[8];
#pragma unroll
            for (int e = 0; e < 8; e++) W[e] = f2{bflo(wl[e]), bflo(wl[8 + e])};
#pragma unroll
            for (int k = 0; k < 4; k++) { WB[k] = W[4 + 3 - k]; WB[4 + k] = W[3 - k]; }
            if (q0) {
#pragma unroll
                for (int k = 0; k < 4; k++) {
                    f2 n1 = rot ? W[(4 - k) & 3] : W[3 - k], n2 = rot ? W[4 + ((4 - k) & 3)] : W[4 + 3 - k];
                    WB[k] = self ? n1 : n2; WB[4 + k] = self ? n2 : n1;
                }
#pragma unroll
                for (int e = 0; e < 8; e += 4) {
                    f2 own = wS[e >> 2], oth = self ? own : wS[1 - (e >> 2)];
                    f2 S0 = pkfma(WB[e], kc.pm, W[e]) * 0.5f, S1c = pkfma(own, kc.pm, oth) * 0.5f;
                    WB[e] = rot ? S1c : WB[e];
                    al[e] = rot ? S0 : W[e];
                }
                W[0] = al[0]; W[4] = al[4];
            }
#pragma unroll
            for (int e = 0; e < 8; e++) {
                const f2 cs = csq[e];
                f2 P = pkfma(WB[e], kc.pm, W[e]), M = pkfma(WB[e], kc.pm.yx, W[e]);
                asm("v_pk_fma_f32 %0, %1, %2, %3 op_sel:[1,0,0] op_sel_hi:[1,1,1] neg_lo:[1,0,0] neg_hi:[1,0,0]" : "=v"(al[e]) : "v"(cs), "v"(M), "v"(P));
                asm("v_pk_mul_f32 %0, %1, %2 op_sel:[1,0] op_sel_hi:[0,0] neg_lo:[1,0]" : "=v"(be[e]) : "v"(M), "v"(cs));
            }
            asm volatile("" : "+v"(al[0]), "+v"(al[1]), "+v"(al[2]), "+v"(al[3]), "+v"(al[4]), "+v"(al[5]), "+v"(al[6]), "+v"(al[7]),
                         "+v"(be[0]), "+v"(be[1]), "+v"(be[2]), "+v"(be[3]), "+v"(be[4]), "+v"(be[5]), "+v"(be[6]), "+v"(be[7]),
                         "+v"(tw0[1]), "+v"(tw0[2]), "+v"(tw0[3]), "+v"(tw1[1]), "+v"(tw1[2]), "+v"(tw1[3]), "+v"(tw1[4]), "+v"(tw1[5]), "+v"(tw1[6]), "+v"(tw1[7]));
        }
        STAMP(4);
        wait_grp(0, A_S0);
        __builtin_amdgcn_s_setprio(2);
        f2 e0[4], e1[4];
#pragma unroll
        for (int H = 0; H < 4; H++) { e0[H] = f2{__uint_as_float(d[H].x), __uint_as_float(d[H].y)}; e1[H] = f2{__uint_as_float(d[H].z), __uint_as_float(d[H].w)}; }
        dft4<false>(e0[0], e0[1], e0[2], e0[3], kc); dft4<false>(e1[0], e1[1], e1[2], e1[3], kc);
        STAMP(6);
        LAS char* area = lds + L_FFT + rq * 4640;
#pragma unroll
        for (int k = 0; k < 4; k++) {
            if (k) { e0[k] = cmul<true>(e0[k], tw0[k]); e1[k] = cmul<true>(e1[k], tw0[k]); }
            *(LAS f4*)(area + (l3 * 72 + 2 * c2p + 8 * k + 32 * rb) * 8) = f4{e0[k].x, e0[k].y, e1[k].x, e1[k].y};
        }
        FENCE();
#pragma unroll
        for (int k = 0; k < 8; k++) v[k] = *(LAS f2*)(area + (k * 72 + lane) * 8);
        dft8<false>(v, kc);
#pragma unroll
        for (int k = 1; k < 8; k++) v[k] = cmul<true>(v[k], tw1[k]);
        FENCE();
        LAS char* pb = lds + L_FFT + (r3 * 290 + l3 * 36 + c2p) * 8;
#pragma unroll
        for (int k = 0; k < 8; k++) *(LAS f2*)(pb + k * 32) = v[k];
        post(B_E1);
        wait_grp(1, B_E1);
#pragma unroll
        for (int k = 0; k < 8; k++) v[k] = *(LAS f2*)(lds + rowB + k * 2320);
        dft8<false>(v, kc);
#pragma unroll
        for (int k = 0; k < 8; k++) *(LAS f2*)(lds + rowB + k * 2320) = v[k];
        FENCE();
        f4 a0 = *(LAS f4*)(lds + p3a1), a1 = *(LAS f4*)(lds + p3a1 + 16), b0 = *(LAS f4*)(lds + p3a2), b1 = *(LAS f4*)(lds + p3a2 + 16);
        f2 X[8] = {a0.xy, a0.zw, a1.xy, a1.zw, b0.xy, b0.zw, b1.xy, b1.zw};
        dft4<false>(X[0], X[1], X[2], X[3], kc); dft4<false>(X[4], X[5], X[6], X[7], kc);
        f2 Y[8];
#pragma unroll
        for (int k = 0; k < 4; k++) { Y[k] = X[4 + 3 - k]; Y[4 + k] = X[3 - k]; }
        if (q0) {
#pragma unroll
            for (int k = 0; k < 4; k++) {
                f2 n1 = rot ? X[(4 - k) & 3] : X[3 - k], n2 = rot ? X[4 + ((4 - k) & 3)] : X[4 + 3 - k];
                Y[k] = self ? n1 : n2; Y[4 + k] = self ? n2 : n1;
            }
        }
        f2 O[8];
#pragma unroll
        for (int e = 0; e < 8; e++) O[e] = cfmac_conj(cmul<false>(X[e], al[e]), be[e], Y[e]);
        dft4<true>(O[0], O[1], O[2], O[3], kc); dft4<true>(O[4], O[5], O[6], O[7], kc);
        *(LAS f4*)(lds + p3a1) = f4{O[0].x, O[0].y, O[1].x, O[1].y}; *(LAS f4*)(lds + p3a1 + 16) = f4{O[2].x, O[2].y, O[3].x, O[3].y};
        *(LAS f4*)(lds + p3a2) = f4{O[4].x, O[4].y, O[5].x, O[5].y}; *(LAS f4*)(lds + p3a2 + 16) = f4{O[6].x, O[6].y, O[7].x, O[7].y};
        FENCE();
#pragma unroll
        for (int k = 0; k < 8; k++) v[k] = *(LAS f2*)(lds + rowB + k * 2320);
        dft8<true>(v, kc);
#pragma unroll
        for (int k = 0; k < 8; k++) *(LAS f2*)(lds + rowB + k * 2320) = v[k];
        post(B_E2);
        wait_grp(1, B_E2);
#pragma unroll
        for (int k = 0; k < 8; k++) v[k] = *(LAS f2*)(pb + k * 32);
#pragma unroll
        for (int k = 1; k < 8; k++) v[k] = cmul<false>(v[k], tw1[k]);
        dft8<true>(v, kc);
        FENCE();
#pragma unroll
        for (int k = 0; k < 8; k++) *(LAS f2*)(area + (k * 72 + lane) * 8) = v[k];
        FENCE();
#pragma unroll
        for (int k = 0; k < 4; k++) {
            f4 q = *(LAS f4*)(area + (l3 * 72 + 2 * c2p + 8 * k + 32 * rb) * 8);
            e0[k] = q.xy; e1[k] = q.zw;
            if (k) { e0[k] = cmul<false>(e0[k], tw0[k]); e1[k] = cmul<false>(e1[k], tw0[k]); }
        }
        dft4<true>(e0[0], e0[1], e0[2], e0[3], kc); dft4<true>(e1[0], e1[1], e1[2], e1[3], kc);
        const int f2st = WS_F2 + (r3 * 8 + l3) * 16384 + pos1 * 64 + c2p * 16;
#pragma unroll
        for (int H = 0; H < 4; H++) {
            u4 o = {__float_as_uint(e0[H].x), __float_as_uint(e0[H].y), __float_as_uint(e1[H].x), __float_as_uint(e1[H].y)};
            st128(o, rs, NOF(f2st), H * (64 * 16384));
        }
        STAMP(7);
        __builtin_amdgcn_s_setprio(0);
        WAITVM(0);
        flag_store(WS_FF2 + (rq * 256 + pos1) * 8);
    }
    STAMP(8);
    RELANE();
    f4 acc2[4];
#pragma unroll
    for (int j = 0; j < 4; j++) acc2[j] = f4{0, 0, 0, 0};
    const int hsrc = hoff0 + hf * 1024;
    auto g2_mma = [&](auto NC, const u4* hfr, int ldsbase) {
        constexpr int N = decltype(NC)::value;
        u32 pa = (u32)ldsbase + l16; asm volatile("" : "+v"(pa));
        LAS u4* p = (LAS u4*)(lds + pa);
        u4 F[2][4];
#pragma unroll
        for (int j = 0; j < 4; j++) F[0][j] = p[j * 64];
        sfor<N>([&](auto I) {
            constexpr int i = I;
            SCHED();
            if (i + 1 < N) {
#pragma unroll
                for (int j = 0; j < 4; j++) F[(i + 1) & 1][j] = p[(i + 1) * 256 + j * 64];
            }
#pragma unroll
            for (int j = 0; j < 4; j++) acc2[j] = mfma(F[i & 1][j], hfr[i], acc2[j]);
            if (i + 1 < N) {
#pragma unroll
                for (int j = 0; j < 4; j++) { SGB(0x100, 1); SGB(0x8, 1); }
            }
            SCHED();
        });
    };
    u4 hfr[16];
    flag_wait(li * 8, WS_FHA + (rg * 8 + w) * 128);
    if (hf == 0) post(A_D1);
#pragma unroll
    for (int i = 0; i < 16; i++) { hfr[i] = __builtin_amdgcn_raw_buffer_load_b128(rs, l16, hsrc + 2 * ((i + rot) & 15) * 1024, 16); SCHED(); }
    STAMP(9);
    wait_grp(0, A_D1);
    g2_mma(IC<16>{}, hfr, hf ? 0 : L_SET1);
    post(hf ? B_G1 : A_G1);
    pin4(acc2);
    STAMP(10);
    if (hf == 0) {
        wait_grp(0, A_G1);
        sfor<3>([&](auto I) { constexpr int i = I; if (i < 2 || rq < 3) dma4k_c<L_SET1 + i * 16384, (32 + 4 * i) * 4096>(l16, wdp + rq * 4096, (u32)(rq * 4096)); });
        flag_wait(min(li, 10) * 8, WS_FHB + (rg * 4 + rq) * 128);
        post(A_D3);
#pragma unroll
        for (int i = 0; i < 11; i++) { hfr[i] = __builtin_amdgcn_raw_buffer_load_b128(rs, l16, hoff0 + (32 + i) * 1024, 16); SCHED(); }
        STAMP(13);
        wait_grp(0, A_D3);
        g2_mma(IC<11>{}, hfr, L_SET1);
    } else {
        wait_grp(1, B_G1);
        fft13_consts(0.5f / 524288.0f);
        flag_wait(lane * 8, WS_FF2 + (((s2 & 7) >> 1) * 256 + rq * 64) * 8);
        u4 d[4]; f2 v[8];
        const int off = WS_F2 + pid * 16384 + rq * 4096;
#pragma unroll
        for (int i = 0; i < 4; i++) d[i] = __builtin_amdgcn_raw_buffer_load_b128(rs, NOF(l16), off + i * 1024, 16);
        STAMP(13);
        __builtin_amdgcn_s_setprio(2);
#pragma unroll
        for (int i = 0; i < 4; i++) *(LAS u4*)(lds + L_FFT + rq * 4608 + (i * 8 + (lane >> 3)) * 144 + (lane & 7) * 16) = d[i];
        FENCE();
        v[0] = *(LAS f2*)(lds + x3a[0]);
        v[1] = *(LAS f2*)(lds + x3a[1]); v[2] = *(LAS f2*)(lds + x3a[1] + 1152); v[3] = *(LAS f2*)(lds + x3a[1] + 2304);
        v[4] = *(LAS f2*)(lds + x3a[2]);
        v[5] = *(LAS f2*)(lds + x3a[3] + 2304); v[6] = *(LAS f2*)(lds + x3a[3] + 1152); v[7] = *(LAS f2*)(lds + x3a[3]);
#pragma unroll
        for (int k = 0; k < 8; k++) v[k] = cmul<false>(v[k], twT[k]);
        dft8<true>(v, kc);
#pragma unroll
        for (int k = 1; k < 8; k++) v[k] = cmul<false>(v[k], tw64[k]);
        FENCE();
        LAS f2* reg = (LAS f2*)(lds + L_FFT) + (rq * 2 + qb) * 288;
#pragma unroll
        for (int k = 0; k < 8; k++) reg[36 * l3 + 4 * k + c2p] = v[k];
        FENCE();
#pragma unroll
        for (int k = 0; k < 8; k++) v[k] = reg[36 * k + (lane & 31)];
        dft8<true>(v, kc);
        FENCE();
#pragma unroll
        for (int k = 0; k < 8; k++) reg[(lane & 31) + 32 * k] = v[k];
        post(B_F3);
        wait_grp(1, B_F3);
        if (b == 0 && rq == 0) __builtin_amdgcn_raw_buffer_store_b64(*(LAS u2*)(lds + L_TAG + 8), rs, lane ? WS_BYTES : 0, 0, 16);
        LAS f2* xb1 = (LAS f2*)(lds + L_FFT) + rq * 64 + lane;
#pragma unroll
        for (int q1 = 0; q1 < 8; q1++) { const int rg_ = (q1 < 4) ? q1 * 2 : (((8 - q1) & 3) * 2 + 1); v[q1] = xb1[rg_ * 288]; }
        dft8<true>(v, kc);
#pragma unroll
        for (int c1 = 0; c1 < 8; c1++) *(LAS u32*)(lds + L_S + (16 * rq + (lane >> 2)) * 144 + c1 * 16 + c2p * 4) = pkbf(v[c1]);
    }
    STAMP(11);
    BAR();
    {
        LAS f4* xs = (LAS f4*)(lds + w * 2048) + lane;
        LAS f4* xr = (LAS f4*)(lds + (w ^ 4) * 2048) + lane;
        f4 me, mo;
        if (hf) { xs[0] = acc2[0]; xs[64] = acc2[1]; me = acc2[2]; mo = acc2[3]; } else { xs[0] = acc2[2]; xs[64] = acc2[3]; me = acc2[0]; mo = acc2[1]; }
        BAR();
        const int c1 = 4 * hf + lg;
        me += xr[0]; mo += xr[64];
        const u4 sv = *(LAS u4*)(lds + L_S + (16 * rq + li) * 144 + c1 * 16);
        const u4 xv = *(LAS u4*)(lds + ((rq >> 1) ? L_X1 : L_X0) + ((rq & 1) * 16 + li) * 128 + ((c1 ^ (li & 7)) * 16));
        u32 res[4];
#pragma unroll
        for (int e = 0; e < 4; e++) {
            const f2 ov = bfup(pkbf(f2{me[e], mo[e]}));
            const f2 z = ov * -1.44269504f;
            const f2 d = f2{__builtin_amdgcn_exp2f(z.x), __builtin_amdgcn_exp2f(z.y)} + f2{1.f, 1.f};
            const f2 g = bfup(pkbf(ov * f2{__builtin_amdgcn_rcpf(d.x), __builtin_amdgcn_rcpf(d.y)}));
            res[e] = pkbf(bfup(xv[e]) + bfup(pkbf(bfup(sv[e]) * g)));
        }
        *(u4*)(a.out + (size_t)(s2 + 32 * (16 * rq + li)) * 512 + 64 * c1 + 8 * t) = u4{res[0], res[1], res[2], res[3]};
    }
    STAMP(12);
}

extern "C" void kernel_launch(void* const* d_in, const int* in_sizes, int n_in,
                              void* d_out, int out_size, void* d_ws, size_t ws_size,
                              hipStream_t stream) {
    Args a;
    a.x = (const u16*)d_in[0]; a.wr = (const u16*)d_in[1]; a.wi = (const u16*)d_in[2];
    a.wg = (const u16*)d_in[4]; a.wu = (const u16*)d_in[5]; a.wd = (const u16*)d_in[6];
    a.out = (u16*)d_out; a.ws = (unsigned char*)d_ws;
    (void)hipFuncSetAttribute((const void*)fused, hipFuncAttributeMaxDynamicSharedMemorySize, L_END);
    fused<<<256, 512, L_END, stream>>>(a);
}
```

```cpp
#define PROBE 19
#include <hip/hip_runtime.h>
#include <stdint.h>
#include <type_traits>

typedef unsigned short u16;
typedef unsigned u32;
typedef unsigned long long u64;
typedef float f2 __attribute__((ext_vector_type(2)));
typedef float f4 __attribute__((ext_vector_type(4)));
typedef u32 u4 __attribute__((ext_vector_type(4)));
typedef u32 u2 __attribute__((ext_vector_type(2)));
typedef short s4 __attribute__((ext_vector_type(4)));
typedef __bf16 bf8 __attribute__((ext_vector_type(8)));
typedef __bf16 bf2 __attribute__((ext_vector_type(2)));
#define LAS __attribute__((address_space(3)))
#define GAS __attribute__((address_space(1)))
#define DEV __device__ __forceinline__
template <int N> using IC = std::integral_constant<int, N>;

constexpr int NFB = 43;
constexpr int WS_FF1 = 4096;
constexpr int WS_FF2 = WS_FF1 + 8192;
constexpr int WS_FHA = WS_FF2 + 8192;
constexpr int WS_FHB = WS_FHA + 16384;
constexpr int WS_FP = WS_FHB + 16384;
constexpr int WS_FW = WS_FP + 8192;
constexpr int WS_F1 = 1 << 20, WS_F2 = WS_F1 + (4 << 20), WS_H = WS_F2 + (4 << 20);
constexpr int WS_WDP = WS_H + 32 * 4 * NFB * 1024;
constexpr int WS_WP = WS_WDP + 8 * NFB * 4 * 1024;
constexpr int WS_STAMP = WS_WP + (2 << 20);
constexpr int WS_BYTES = WS_STAMP + (1 << 20);
constexpr int PG = 8192;
constexpr int L_SET1 = 8 * PG;
constexpr int L_FFT = 16 * PG;
constexpr int L_SMALL = 19 * PG;
constexpr int L_PROG = L_SMALL;
constexpr int L_TAG = L_SMALL + 32;
constexpr int L_STAT = L_SMALL + 64;
constexpr int L_CS = L_SMALL + 2048;
constexpr int L_X0 = L_FFT + 18688, L_X1 = L_SMALL + 3072;
constexpr int L_S = L_FFT;
constexpr int L_END = 20 * PG;
constexpr u32 SENT = 0xFFFFFFFFu;
constexpr int CODE_BYTES = 44 * 1024;
constexpr int SPIN_CAP = 1 << 16;

struct Args { const u16* x; const u16* wr; const u16* wi; const u16* wg; const u16* wu; const u16* wd; u16* out; unsigned char* ws; };

#define BAR() asm volatile("s_waitcnt lgkmcnt(0)\n\ts_barrier" ::: "memory")
#define WAITVM(n) asm volatile("s_waitcnt vmcnt(" #n ")" ::: "memory")
#define NOF(x) (x)
#define NOPL(x) (x)
#define FENCE() asm volatile("" ::: "memory")
#define SCHED() __builtin_amdgcn_sched_barrier(0)
#define SGB(mask, n) __builtin_amdgcn_sched_group_barrier(mask, n, 0)
#define STAMP(i) do { u64 t_ = __builtin_amdgcn_s_memrealtime(); *(volatile LAS u2*)(lds + L_FFT + 22784 + (w * 28 + (i)) * 8) = u2{(u32)t_, (u32)(t_ >> 32)}; } while (0)
#define CSTAMP(i) do { u64 t_ = __builtin_amdgcn_s_memtime(); *(volatile LAS u2*)(lds + L_FFT + 22784 + (w * 28 + (i)) * 8) = u2{(u32)t_, (u32)(t_ >> 32)}; } while (0)

DEV f2 pkfma(f2 a, f2 b, f2 c) { return __builtin_elementwise_fma(a, b, c); }
struct KC { f2 pm, rpm; };
DEV f2 addmi(f2 a, f2 b, const KC& k) { return pkfma(b.yx, k.pm, a); }
DEV f2 addpi(f2 a, f2 b, const KC& k) { return pkfma(b.yx, k.pm.yx, a); }
template <bool INV> DEV void dft4(f2& a0, f2& a1, f2& a2, f2& a3, const KC& k) {
    f2 t0 = a0 + a2, t1 = a0 - a2, t2 = a1 + a3, t3 = a1 - a3;
    a0 = t0 + t2; a2 = t0 - t2;
    if (INV) { a1 = addpi(t1, t3, k); a3 = addmi(t1, t3, k); } else { a1 = addmi(t1, t3, k); a3 = addpi(t1, t3, k); }
}
template <bool INV> DEV void dft8(f2 (&v)[8], const KC& k) {
    f2 a0 = v[0] + v[4], a4 = v[0] - v[4], a1 = v[1] + v[5], a5 = v[1] - v[5];
    f2 a2 = v[2] + v[6], a6 = v[2] - v[6], a3 = v[3] + v[7], a7 = v[3] - v[7];
    dft4<INV>(a0, a1, a2, a3, k);
    v[0] = a0; v[2] = a1; v[4] = a2; v[6] = a3;
    f2 b1, b3, t0, t1;
    if (INV) { b1 = addpi(a5, a5, k); b3 = addpi(-a7, a7, k); t0 = addpi(a4, a6, k); t1 = addmi(a4, a6, k); }
    else { b1 = addmi(a5, a5, k); b3 = addmi(-a7, a7, k); t0 = addmi(a4, a6, k); t1 = addpi(a4, a6, k); }
    f2 t2 = b1 + b3, t3 = b1 - b3;
    v[1] = pkfma(t2, k.rpm.xx, t0); v[5] = pkfma(t2, k.rpm.yy, t0);
    if (INV) { v[3] = pkfma(t3.yx, k.rpm.yx, t1); v[7] = pkfma(t3.yx, k.rpm, t1); }
    else { v[3] = pkfma(t3.yx, k.rpm, t1); v[7] = pkfma(t3.yx, k.rpm.yx, t1); }
}
template <bool CONJ> DEV f2 cmul(f2 a, f2 w) {
    f2 r;
    if (CONJ) asm("v_pk_mul_f32 %0, %1, %2 op_sel:[1,1] op_sel_hi:[0,1]\n\tv_pk_fma_f32 %0, %1, %2, %0 op_sel_hi:[1,0,1] neg_hi:[0,0,1]" : "=&v"(r) : "v"(a), "v"(w));
    else asm("v_pk_mul_f32 %0, %1, %2 op_sel:[1,1] op_sel_hi:[0,1]\n\tv_pk_fma_f32 %0, %1, %2, %0 op_sel_hi:[1,0,1] neg_lo:[0,0,1]" : "=&v"(r) : "v"(a), "v"(w));
    return r;
}
DEV f2 cfmac_conj(f2 t, f2 beta, f2 B) {
    asm("v_pk_fma_f32 %0, %2, %1, %0 op_sel_hi:[0,1,1]\n\tv_pk_fma_f32 %0, %2, %1, %0 op_sel:[1,1,0] op_sel_hi:[1,0,1] neg_hi:[0,1,0]" : "+v"(t) : "v"(beta), "v"(B));
    return t;
}
DEV f2 cis(float r) { return f2{__builtin_amdgcn_cosf(r), __builtin_amdgcn_sinf(r)}; }
DEV float bflo(u32 v) { return __uint_as_float(v << 16); }
DEV float bfhi(u32 v) { return __uint_as_float(v & 0xFFFF0000u); }
DEV f2 bfup(u32 v) { return f2{bflo(v), bfhi(v)}; }
DEV u32 pkbf(f2 v) { bf2 r; r.x = (__bf16)v.x; r.y = (__bf16)v.y; u32 o = __builtin_bit_cast(u32, r); asm("" : "+v"(o)); return o; }
DEV float rbf(float f) { return __uint_as_float(((u32)__builtin_bit_cast(u16, (__bf16)f)) << 16); }
DEV f4 mfma(u4 a, u4 b, f4 c) { return __builtin_amdgcn_mfma_f32_16x16x32_bf16(__builtin_bit_cast(bf8, a), __builtin_bit_cast(bf8, b), c, 0, 0, 0); }
DEV u4 sanitize(u4 v) { return u4{min(v.x, 0xFFFFFFFEu), min(v.y, 0xFFFFFFFEu), min(v.z, 0xFFFFFFFEu), min(v.w, 0xFFFFFFFEu)}; }
DEV u32 max4(u4 v) { return max(max(v.x, v.y), max(v.z, v.w)); }
DEV void st128(u4 v, __amdgpu_buffer_rsrc_t rs, u32 voff, int soff) {
    __builtin_amdgcn_raw_buffer_store_b128(v, rs, voff, soff, 16);
    asm volatile("s_nop 1" :: "v"(v) : "memory");
}
template <int OFF2> DEV u4 trfrag2(LAS char* p) {
    s4 r0 = __builtin_amdgcn_ds_read_tr16_b64_v4i16((LAS s4*)p);
    s4 r1 = __builtin_amdgcn_ds_read_tr16_b64_v4i16((LAS s4*)(p + OFF2));
    u2 x0 = __builtin_bit_cast(u2, r0), x1 = __builtin_bit_cast(u2, r1);
    return u4{x0.x, x0.y, x1.x, x1.y};
}
template <int LI, int GI> DEV void dma_add(u32 vb, const void* sbase, u32 sl) {
    const u32 va = vb + (u32)GI;
    asm volatile("s_add_u32 m0, %2, %3\n\ts_nop 0\n\tglobal_load_lds_dwordx4 %0, %1" :: "v"(va), "s"(sbase), "s"(sl), "i"(LI) : "memory", "scc");
}
template <int LI, int GI> DEV void dma4k_c(u32 vb, const void* sbase, u32 sl) {
    const u32 va = vb + (u32)GI;
    asm volatile("s_add_u32 m0, %2, %3\n\ts_nop 0\n\tglobal_load_lds_dwordx4 %0, %1 sc1\n\tglobal_load_lds_dwordx4 %0, %1 offset:1024 sc1\n\t"
                 "global_load_lds_dwordx4 %0, %1 offset:2048 sc1\n\tglobal_load_lds_dwordx4 %0, %1 offset:3072 sc1"
                 :: "v"(va), "s"(sbase), "s"(sl), "i"(LI) : "memory", "scc");
}
template <int OFF> DEV void ldg_u16(u32& d, u32 vo, const void* sb) { asm volatile("global_load_ushort %0, %1, %2 offset:%3" : "=v"(d) : "v"(vo), "s"(sb), "i"(OFF) : "memory"); }
template <int OFF> DEV void ldg_u32(u32& d, u32 vo, const void* sb) { asm volatile("global_load_dword %0, %1, %2 offset:%3" : "=v"(d) : "v"(vo), "s"(sb), "i"(OFF) : "memory"); }
DEV void lds_wait(u32 addr, u32 v) {
    u32 tmp, cnt;
    asm volatile("s_mov_b32 %1, 0x80000\n"
                 "1:\n\tds_read_b32 %0, %2\n\ts_sub_u32 %1, %1, 1\n\ts_waitcnt lgkmcnt(0)\n\ts_cbranch_scc1 2f\n\tv_cmp_gt_u32 vcc, %3, %0\n\ts_cbranch_vccnz 1b\n2:"
                 : "=&v"(tmp), "=&s"(cnt) : "v"(addr), "s"(v) : "memory", "vcc", "scc");
}
template <int N, typename F> DEV void sfor(F&& f) { if constexpr (N > 0) { sfor<N - 1>(f); f(IC<N - 1>{}); } }

DEV void pin9(f4 (&x)[4], f4 (&y)[4], f4& z) {
    asm volatile("" : "+v"(x[0]), "+v"(x[1]), "+v"(x[2]), "+v"(x[3]), "+v"(y[0]), "+v"(y[1]), "+v"(y[2]), "+v"(y[3]), "+v"(z));
}
DEV void pin4(f4 (&x)[4]) { asm volatile("" : "+v"(x[0]), "+v"(x[1]), "+v"(x[2]), "+v"(x[3])); }
DEV void dma_rt(u32 voff, const void* sbase, u32 sl) {
    asm volatile("s_mov_b32 m0, %2\n\ts_nop 0\n\tglobal_load_lds_dwordx4 %0, %1" :: "v"(voff), "s"(sbase), "s"(sl) : "memory");
}
DEV u4 geglu8(const f4 (&acc)[4], LAS char* cs, f2 st) {
    u32 hw[4];
    const f2 nm = f2{-st.x, -st.x}, ri = f2{st.y, st.y};
#pragma unroll
    for (int c = 0; c < 2; c++) {
        f4 cg_ = *(LAS f4*)(cs + c * 64), cu_ = *(LAS f4*)(cs + (2 + c) * 64);
#pragma unroll
        for (int e = 0; e < 2; e++) {
            f2 g = pkfma(e ? cg_.zw : cg_.xy, nm, e ? acc[c].zw : acc[c].xy) * ri;
            f2 u = pkfma(e ? cu_.zw : cu_.xy, nm, e ? acc[2 + c].zw : acc[2 + c].xy) * ri;
            f2 z = g * pkfma(g * g, f2{-0.10294324f, -0.10294324f}, f2{-2.3022082f, -2.3022082f});
            f2 d = f2{__builtin_amdgcn_exp2f(z.x), __builtin_amdgcn_exp2f(z.y)} + f2{1.f, 1.f};
            f2 r = f2{__builtin_amdgcn_rcpf(d.x), __builtin_amdgcn_rcpf(d.y)};
            hw[2 * c + e] = pkbf(g * r * u);
        }
    }
    return u4{hw[0], hw[1], hw[2], hw[3]};
}

enum { A_XL = 1, A_XC, A_P1, A_XB, A_S0, A_FP, A_E1, A_E2, A_CS, A_F3, A_F4, A_D1 };
enum { B_W0 = 1, B_W1, B_W2, B_W3, B_CS, B_WB, B_PB, B_D1, B_G1, B_D3 };

__global__ void __launch_bounds__(512, 2) fused(Args a) {
    extern __shared__ __attribute__((aligned(16))) char smem[];
    LAS char* lds = (LAS char*)smem;
    const int tid = threadIdx.x, w = __builtin_amdgcn_readfirstlane(tid >> 6);
    int lane = tid & 63;
    const int b = blockIdx.x, xcd = b & 7, r = b >> 3, t = r & 7, par = xcd & 1, rg = (xcd >> 1) * 4 + (r >> 3);
    const int s2 = 2 * rg + par, cg = par * 8 + t, sig = xcd * 32 + r, pid = s2 * 8 + t;
    const int rq = w & 3, hf = w >> 2, j4 = r >> 3;
#define BE(l) ((((l) >> 3) << 1) | ((l) & 1))
#define IX(l) (((l) >> 1) & 3)
    u32 l16 = lane * 16;
    u32 cpf = 0;
    if (w == 7) {
        const u64 pc = __builtin_amdgcn_s_getpc() & ~(u64)255;
        const u32 off = min((u32)((t * 64 + lane) * 128), (u32)(CODE_BYTES - 128));
        asm volatile("global_load_dword %0, %1, %2" : "=v"(cpf) : "v"(off), "s"(pc) : "memory");
    }
    *(volatile LAS u32*)(lds + L_PROG + w * 4) = 0;
    BAR();
    STAMP(0);
    auto dma_x = [&]() {
        const char* xs = (const char*)a.x + (size_t)((s2 ^ hf) + 512 * rq) * 1024;
#pragma unroll
        for (int i = 0; i < 16; i++) { const int row = (i + 2 * t) & 15; dma_rt(l16 ^ (u32)(row * (32768 + 16)), xs, (u32)(L_SET1 + rq * 16384 + row * 1024)); }
    };
    if (hf == 0) dma_x();
    int li, lg, c2p, l3, qb; u32 vpg;
#define RELANE() do { asm volatile("" : "+v"(lane)); l16 = lane * 16; li = lane & 15; lg = lane >> 4; c2p = lane & 3; l3 = (lane >> 2) & 7; qb = lane >> 5; vpg = (u32)(L_PROG + (lane & 3) * 4); } while (0)
    const __amdgpu_buffer_rsrc_t rs = __builtin_amdgcn_make_buffer_rsrc(a.ws, 0, WS_BYTES, 0x00020000);
    KC kc; kc.pm = f2{1.f, -1.f}; kc.rpm = f2{0.70710678118654752f, -0.70710678118654752f};
    asm volatile("" : "+s"(kc.pm), "+s"(kc.rpm));
    const u4 ones = {0x3F803F80u, 0x3F803F80u, 0x3F803F80u, 0x3F803F80u};
    auto post = [&](u32 v) { asm volatile("s_waitcnt lgkmcnt(0)" ::: "memory"); *(volatile LAS u32*)(lds + L_PROG + w * 4) = v; FENCE(); };
    auto wait_grp = [&](int g, u32 v) { lds_wait(vpg + g * 16, v); };
    auto wait_pair = [&](u32 v) { lds_wait((u32)(L_PROG + (w ^ 4) * 4), v); };
    u2 tag;
    auto get_tag = [&]() { u2 tg = *(LAS u2*)(lds + L_TAG); tag.x = __builtin_amdgcn_readfirstlane(tg.x); tag.y = __builtin_amdgcn_readfirstlane(tg.y); };
    auto flag_store = [&](int off) { __builtin_amdgcn_raw_buffer_store_b64(tag, rs, lane ? WS_BYTES : 0, off, 16); };
    auto flag_wait = [&](u32 voff, int soff) {
        _Pragma("unroll 1") for (int spins = 0; spins < SPIN_CAP; spins++) {
            u2 f = __builtin_amdgcn_raw_buffer_load_b64(rs, voff, soff, 16);
            if (__all(f.x == tag.x && f.y == tag.y)) break;
            __builtin_amdgcn_s_sleep(16);
            FENCE();
        }
        __builtin_amdgcn_fence(__ATOMIC_ACQUIRE, "wavefront");
        FENCE();
    };
    auto read_tile = [&](u4 (&x)[16], int tile) {
        LAS char* p = lds + L_SET1 + (16 * rq + li) * 1024;
#pragma unroll
        for (int k = 0; k < 16; k++) x[k] = *(LAS u4*)(p + (((4 * IX(k) + lg) ^ li) * 16) + ((BE(k) + j4) & 3) * 256);
    };
    auto stats = [&](const u4 (&x)[16]) {
        f4 s1 = {0, 0, 0, 0}, sq = s1, s1b = s1, sqb = s1;
#pragma unroll
        for (int k = 0; k < 16; k += 2) { s1 = mfma(ones, x[k], s1); sq = mfma(x[k], x[k], sq); s1b = mfma(ones, x[k + 1], s1b); sqb = mfma(x[k + 1], x[k + 1], sqb); }
        sq += sqb;
        const int rr = li & 3;
        const float d = rr == 0 ? sq.x : rr == 1 ? sq.y : rr == 2 ? sq.z : sq.w;
        if (lg == (li >> 2)) {
            const float mean = (s1.x + s1b.x) * (1.0f / 512.0f);
            float var = d * (1.0f / 512.0f) - mean * mean;
            if (!(mean * mean <= 1024.0f * var)) {
                const u32* xrow = (const u32*)(a.x + (size_t)((s2 ^ hf) + 32 * (16 * rq + li)) * 512);
                float q = 0.f;
                _Pragma("unroll 1") for (int i = 0; i < 256; i++) { u32 v = xrow[i]; float d0 = bflo(v) - mean, d1 = bfhi(v) - mean; q += d0 * d0; q += d1 * d1; }
                var = q * (1.0f / 512.0f);
            }
            const float den = rbf(__builtin_amdgcn_sqrtf(rbf(rbf(var) + 1e-5f)));
            *(LAS f2*)(lds + L_STAT + (hf * 64 + 16 * rq + li) * 8) = f2{rbf(mean), __builtin_amdgcn_rcpf(den)};
        }
        FENCE();
    };
    const int nblk = (t < 6 - par) ? 3 : 2, fb2 = 32 + par * 6 + t;
    const char* wdp = (const char*)a.ws + WS_WDP + (size_t)(t * NFB) * 4096;

    f2 tw64[8], twT[8];
    int x3a[4];
    auto fft13_consts = [&](float scale) {
        int lane_ = lane; asm volatile("" : "+v"(lane_));
        const int c2p = lane_ & 3, l3 = (lane_ >> 2) & 7, qb = lane_ >> 5;
        const int wq = rq, q1 = qb ? (wq == 0 ? 4 : 8 - wq) : wq, c2 = 4 * t + c2p;
        const float f64 = (float)l3 * (1.0f / 64.0f);
#pragma unroll
        for (int k = 1; k < 8; k++) tw64[k] = cis(f64 * (float)k);
        const float fT = (float)(8 * c2 * q1 + s2 * l3) * (1.0f / 2048.0f), dT = (float)s2 * (1.0f / 256.0f);
#pragma unroll
        for (int k = 0; k < 8; k++) twT[k] = cis(fT + dT * (float)k) * scale;
#pragma unroll
        for (int k = 0; k < 4; k++) {
            const int ua = (k >> 1) * 4 + (k & 1), u1 = l3 + 8 * ua;
            const int cls = (u1 < 32) ? u1 : ((64 - u1) & 31), hi = u1 >> 5;
            int mbit, sub;
            if (wq == 0) { mbit = qb; sub = hi; } else if (cls != 0) { sub = hi; mbit = qb ^ hi; } else { mbit = hi; sub = qb ^ hi; }
            x3a[k] = L_FFT + wq * 4608 + (cls * 18 + mbit * 8 + sub * 4 + c2p) * 8 - (k == 3 ? 2304 : 0);
        }
        asm volatile("" : "+v"(tw64[1]), "+v"(tw64[2]), "+v"(tw64[3]), "+v"(tw64[4]), "+v"(tw64[5]), "+v"(tw64[6]), "+v"(tw64[7]),
                     "+v"(twT[0]), "+v"(twT[1]), "+v"(twT[2]), "+v"(twT[3]), "+v"(twT[4]), "+v"(twT[5]), "+v"(twT[6]), "+v"(twT[7]),
                     "+v"(x3a[0]), "+v"(x3a[1]), "+v"(x3a[2]), "+v"(x3a[3]));
    };

    const int hoff0 = WS_H + ((s2 * 4 + rq) * NFB) * 1024, hoff1 = WS_H + (((s2 ^ 1) * 4 + rq) * NFB) * 1024;
    RELANE();
    const char* wgs = (const char*)a.wg + (size_t)(32 * rq) * 2752 + cg * 128;
    const char* wus = (const char*)a.wu + (size_t)(32 * rq) * 2752 + cg * 128;
    const u32 wvE = (u32)((lane >> 3) * 2752 + (((lane & 7) ^ (((lane >> 4) & 1) << 1)) * 16)), wvO = (u32)((lane >> 3) * 2752 + (((lane & 7) ^ (((lane >> 4) & 1) << 1) ^ 4) * 16));
    auto dma_w1 = [&](auto KK, auto I) {
        constexpr int kk = KK, g = I >> 1, mat = I & 1;
        dma_add<kk * PG + mat * 4096 + g * 1024, g * 8 * 2752>((g & 1) ? wvO : wvE, (mat ? wus : wgs) + ((BE(kk) + j4) & 3) * (128 * 2752), (u32)(rq * 2 * PG));
    };
    const int rot = (2 * t + 4 * j4) & 15;
    auto dma_wd = [&](auto LI, auto I, int p0) { dma4k_c<LI + I * 16384, 0>(l16, wdp + (size_t)(p0 + 2 * ((rq + 4 * I + rot) & 15)) * 4096, (u32)(rq * 4096)); };

    auto gemm1_half = [&](auto S0C, const u4 (&xa)[16], const u4 (&xb)[16], f4 (&accA)[4], f4 (&accB)[4], f4& acs, auto&& hook) {
        constexpr int S0 = S0C;
        const u32 fz = (u32)((li >> 3) | ((lg & 1) << 1));
        const u32 tb = (u32)((8 * lg + (li >> 2)) * 128 + 8 * (li & 3)) + S0 * PG;
        u32 p0 = tb + (((2 * hf) ^ fz) << 5), p1 = tb + (((2 * hf + 1) ^ fz) << 5), pc = tb + (((2 * hf + (rq & 1)) ^ fz) << 5) + (rq >> 1) * 4096;
        asm volatile("" : "+v"(p0), "+v"(p1), "+v"(pc));
        u4 F[2][5];
        auto ldf = [&](int ks, u4 (&f)[5]) {
            const int off = (ks & 7) * PG;
            f[0] = trfrag2<512>(lds + p0 + off); f[1] = trfrag2<512>(lds + p1 + off); f[2] = trfrag2<512>(lds + p0 + off + 4096); f[3] = trfrag2<512>(lds + p1 + off + 4096); f[4] = trfrag2<512>(lds + pc + off);
        };
        ldf(0, F[0]);
        sfor<8>([&](auto S) {
            constexpr int s = S, ks = S0 | ((s & 3) << 1) | ((s >> 2) & 1), sn = s + 1, kn = ((sn & 3) << 1) | ((sn >> 2) & 1);
            hook(S);
            SCHED();
            if (s + 1 < 8) ldf(kn, F[(s + 1) & 1]);
            u4 (&f)[5] = F[s & 1];
#pragma unroll
            for (int j = 0; j < 4; j++) { accA[j] = mfma(f[j], xa[ks], accA[j]); accB[j] = mfma(f[j], xb[ks], accB[j]); }
            acs = mfma(f[4], ones, acs);
            if (s + 1 < 8) {
                SGB(0x100, 2);
#pragma unroll
                for (int i = 0; i < 8; i++) { SGB(0x8, 1); SGB(0x100, 1); }
                SGB(0x8, 1);
            }
            SCHED();
        });
    };
    f2 st0, st1;
    auto gemm1_epi = [&](f4 (&accA)[4], f4 (&accB)[4], f4 acs, u32 step) {
        if (li == 0) *(LAS f4*)(lds + L_CS + (hf * 4 + rq) * 64 + lg * 16) = acs;
        post(step);
        wait_grp(hf, step);
        LAS char* cs = lds + L_CS + hf * 256 + lg * 16;
        st0 = *(LAS f2*)(lds + L_STAT + (16 * rq + li) * 8);
        st1 = *(LAS f2*)(lds + L_STAT + (64 + 16 * rq + li) * 8);
        st128(geglu8(accA, cs, st0), rs, l16, hoff0 + (2 * cg + hf) * 1024);
        st128(geglu8(accB, cs, st1), rs, l16, hoff1 + (2 * cg + hf) * 1024);
    };
    f4 acc2[4];
#pragma unroll
    for (int j = 0; j < 4; j++) acc2[j] = f4{0, 0, 0, 0};
    const int hsrc = hoff0 + hf * 1024;
    auto g2_mma = [&](auto NC, const u4* hfr, int ldsbase) {
        constexpr int N = decltype(NC)::value;
        u32 pa = (u32)ldsbase + l16; asm volatile("" : "+v"(pa));
        LAS u4* p = (LAS u4*)(lds + pa);
        u4 F[2][4];
#pragma unroll
        for (int j = 0; j < 4; j++) F[0][j] = p[j * 64];
        sfor<N>([&](auto I) {
            constexpr int i = I;
            SCHED();
            if (i + 1 < N) {
#pragma unroll
                for (int j = 0; j < 4; j++) F[(i + 1) & 1][j] = p[(i + 1) * 256 + j * 64];
            }
#pragma unroll
            for (int j = 0; j < 4; j++) acc2[j] = mfma(F[i & 1][j], hfr[i], acc2[j]);
            if (i + 1 < N) {
#pragma unroll
                for (int j = 0; j < 4; j++) { SGB(0x100, 1); SGB(0x8, 1); }
            }
            SCHED();
        });
    };
    if (hf == 0) {
        u4 xa[16], xb[16];
        __builtin_amdgcn_s_setprio(2);
        const int f1st = WS_F1 + ((lane >> 3) * 8 + (((lane >> 2) & 1) ? (rq == 0 ? 4 : 8 - rq) : rq)) * 16384 + pid * 64 + c2p * 16;
        u2 ev;
        { u32 zero = 0; asm volatile("global_load_dwordx2 %0, %1, %2 sc1" : "=v"(ev) : "v"(zero), "s"(a.ws) : "memory"); }
        fft13_consts(1.0f);
        WAITVM(1);
        post(A_XL);
        STAMP(1);
        u32 raw[8];
        const int rw = lane >> 2;
        read_tile(xa, 0);
#pragma unroll
        for (int c1 = 0; c1 < 8; c1++) raw[c1] = *(LAS u32*)(lds + L_SET1 + (16 * rq + rw) * 1024 + (((8 * c1 + t) ^ rw) * 16) + c2p * 4);
        post(A_XC);
        stats(xa);
        STAMP(2);
        f2 v[8];
        {
            const f2 st = *(LAS f2*)(lds + L_STAT + (16 * rq + rw) * 8);
#pragma unroll
            for (int c1 = 0; c1 < 8; c1++) v[c1] = bfup(pkbf(bfup(pkbf(bfup(raw[c1]) - st.xx)) * st.yy));
            dft8<false>(v, kc);
            LAS f2* xb1 = (LAS f2*)(lds + L_FFT) + w * 64 + lane;
#pragma unroll
            for (int q1 = 0; q1 < 8; q1++) { const int reg = (q1 < 4) ? q1 * 2 : (((8 - q1) & 3) * 2 + 1); xb1[reg * 288] = v[q1]; }
        }
        asm volatile("s_waitcnt vmcnt(0)" : "+v"(ev) :: "memory");
        {
            const u64 epoch = ((u64)(u32)__builtin_amdgcn_readfirstlane(ev.y) << 32) | (u32)__builtin_amdgcn_readfirstlane(ev.x);
            u64 tg = (epoch + 1) * 0x9E3779B97F4A7C15ull; tg ^= tg >> 29; tg *= 0xBF58476D1CE4E5B9ull; tg ^= tg >> 32;
            tag = u2{(u32)tg, (u32)(tg >> 32)};
            *(LAS u4*)(lds + L_TAG) = u4{(u32)tg, (u32)(tg >> 32), (u32)(epoch + 1), (u32)((epoch + 1) >> 32)};
        }
        post(A_P1);
#pragma unroll
        for (int c1 = 0; c1 < 8; c1++) *(LAS u32*)(lds + ((rq >> 1) ? L_X1 : L_X0) + ((rq & 1) * 16 + rw) * 128 + ((c1 ^ (rw & 7)) * 16) + c2p * 4) = raw[c1];
        wait_pair(B_W0);
        read_tile(xb, 1);
        post(A_XB);
        u32 pk[8], fr[16];
        const int un = sig < 4 * NFB ? sig : 0, fb = un >> 2, jj = (un >> 1) & 1, tcol = (un & 1) * 4 + rq;
        {
            const u32 vd = (u32)(((fb * 32 + 4 * lg) * 512 + 64 * (4 * jj + (li >> 2)) + 8 * tcol + 2 * (li & 3)) * 2);
            sfor<4>([&](auto I) {
                ldg_u32<I * 1024>(pk[I], vd, a.wd);
                ldg_u32<I * 1024>(pk[4 + I], vd, (const char*)a.wd + 16384);
            });
            const u32 vo = (u32)(((8 * sig + 2 * rq) * 257 + lane) * 2);
            sfor<8>([&](auto I) { constexpr int rr = I >> 2, k = I & 3; ldg_u16<rr * 514 + k * 128>(fr[I], vo, a.wr); ldg_u16<rr * 514 + k * 128>(fr[8 + I], vo, a.wi); });
        }
        wait_grp(0, A_P1);
        {
            LAS f2* reg = (LAS f2*)(lds + L_FFT) + (w * 2 + qb) * 288;
#pragma unroll
            for (int k = 0; k < 8; k++) v[k] = reg[(lane & 31) + 32 * k];
            dft8<false>(v, kc);
#pragma unroll
            for (int k = 1; k < 8; k++) v[k] = cmul<true>(v[k], tw64[k]);
#pragma unroll
            for (int k = 0; k < 8; k++) reg[36 * k + (lane & 31)] = v[k];
            FENCE();
#pragma unroll
            for (int k = 0; k < 8; k++) v[k] = reg[36 * l3 + 4 * k + c2p];
            dft8<false>(v, kc);
#pragma unroll
            for (int k = 0; k < 8; k++) v[k] = cmul<true>(v[k], twT[k]);
            FENCE();
            *(LAS f2*)(lds + x3a[0]) = v[0];
            *(LAS f2*)(lds + x3a[1]) = v[1]; *(LAS f2*)(lds + x3a[1] + 1152) = v[2]; *(LAS f2*)(lds + x3a[1] + 2304) = v[3];
            *(LAS f2*)(lds + x3a[2]) = v[4];
            *(LAS f2*)(lds + x3a[3] + 2304) = v[5]; *(LAS f2*)(lds + x3a[3] + 1152) = v[6]; *(LAS f2*)(lds + x3a[3]) = v[7];
            FENCE();
#pragma unroll
            for (int i = 0; i < 4; i++) {
                u4 o = *(LAS u4*)(lds + L_FFT + w * 4608 + (i * 8 + (lane >> 3)) * 144 + (lane & 7) * 16);
                st128(o, rs, NOF(f1st), i * (64 * 16384));
            }
        }
        STAMP(3);
        __builtin_amdgcn_s_setprio(0);
        asm volatile("s_waitcnt vmcnt(4)" : "+v"(pk[0]), "+v"(pk[1]), "+v"(pk[2]), "+v"(pk[3]), "+v"(pk[4]), "+v"(pk[5]), "+v"(pk[6]), "+v"(pk[7]),
                     "+v"(fr[0]), "+v"(fr[1]), "+v"(fr[2]), "+v"(fr[3]), "+v"(fr[4]), "+v"(fr[5]), "+v"(fr[6]), "+v"(fr[7]),
                     "+v"(fr[8]), "+v"(fr[9]), "+v"(fr[10]), "+v"(fr[11]), "+v"(fr[12]), "+v"(fr[13]), "+v"(fr[14]), "+v"(fr[15]) :: "memory");
        {
            const int so = sig < 4 * NFB ? WS_WDP + ((tcol * NFB + fb) * 4 + 2 * jj) * 1024 : WS_BYTES;
            u4 oe = {__builtin_amdgcn_perm(pk[1], pk[0], 0x05040100), __builtin_amdgcn_perm(pk[3], pk[2], 0x05040100), __builtin_amdgcn_perm(pk[5], pk[4], 0x05040100), __builtin_amdgcn_perm(pk[7], pk[6], 0x05040100)};
            u4 oo = {__builtin_amdgcn_perm(pk[1], pk[0], 0x07060302), __builtin_amdgcn_perm(pk[3], pk[2], 0x07060302), __builtin_amdgcn_perm(pk[5], pk[4], 0x07060302), __builtin_amdgcn_perm(pk[7], pk[6], 0x07060302)};
            st128(oe, rs, l16, so);
            st128(oo, rs, l16, so + 1024);
        }
        {
            const int q1 = lane >> 3, vt = lane & 7, srcl = (q1 + 8 * vt) * 4;
#pragma unroll
            for (int rr = 0; rr < 2; rr++) {
                const int u = 8 * sig + 2 * rq + rr, u1 = u & 63, u2_ = u >> 6;
                int cls, m, sub;
                if ((u1 & 31) != 0) { sub = u1 >> 5; cls = sub ? 64 - u1 : u1; m = sub ? ((8 - q1) & 7) : q1; }
                else { cls = 0; const int hi = u1 >> 5; if ((q1 & 3) == 0) { m = q1; sub = hi; } else { sub = (q1 >> 2) ^ hi; m = sub ? 8 - q1 : q1; } }
                u4 o;
                o.x = __builtin_amdgcn_ds_bpermute(srcl, fr[rr * 4 + 0] | (fr[8 + rr * 4 + 0] << 16)); o.y = __builtin_amdgcn_ds_bpermute(srcl, fr[rr * 4 + 1] | (fr[8 + rr * 4 + 1] << 16));
                o.z = __builtin_amdgcn_ds_bpermute(srcl, fr[rr * 4 + 2] | (fr[8 + rr * 4 + 2] << 16)); o.w = __builtin_amdgcn_ds_bpermute(srcl, fr[rr * 4 + 3] | (fr[8 + rr * 4 + 3] << 16));
                st128(o, rs, NOF(WS_WP + (cls * 8 + m) * 8192 + sub * 4096 + u2_ * 128 + vt * 16), 0);
            }
        }
        WAITVM(4);
        flag_store(WS_FF1 + ((rq * 4 + ((s2 & 7) >> 1)) * 64 + (s2 >> 3) * 16 + (s2 & 1) * 8 + t) * 8);
        STAMP(4);
        f4 accA[4], accB[4], acs = {0, 0, 0, 0};
#pragma unroll
        for (int j = 0; j < 4; j++) { accA[j] = f4{0, 0, 0, 0}; accB[j] = f4{0, 0, 0, 0}; }
        wait_grp(1, B_W1);
        __builtin_amdgcn_s_setprio(1);
        gemm1_half(IC<0>{}, xa, xb, accA, accB, acs, [&](auto S) {
            if (S == 3) {
                WAITVM(1);
                flag_store(sig < 4 * NFB ? WS_FP + (((sig & 1) * 4 + rq) * 128 + (sig >> 1)) * 8 : WS_BYTES);
                flag_store(WS_FW + (((sig & 7) * 4 + rq) * 32 + (sig >> 3)) * 8);
            }
        });
        pin9(accA, accB, acs);
        post(A_S0);
        STAMP(19);
        RELANE();
        int pos1f;
        {
        f2 tw0[4], tw1[8], al[8], be[8];
        const int rb = qb, r3 = 2 * rq + rb;
        const int cls2 = sig >> 3, m2 = sig & 7;
        const int wq2 = (m2 < 4) ? m2 : ((8 - m2) & 3), pos1 = wq2 * 64 + cls2 * 2 + (m2 >> 2);
        const bool special = (cls2 == 0) && ((m2 & 3) == 0);
        int u1a, q1a, u1b, q1b;
        if (cls2 != 0) { u1a = cls2; q1a = m2; u1b = 64 - cls2; q1b = (8 - m2) & 7; }
        else if (special) { u1a = 0; u1b = 32; q1a = q1b = m2; }
        else { u1a = u1b = (m2 < 4) ? 0 : 32; q1a = m2; q1b = 8 - m2; }
        int sA, vH0, sB, vH1;
        if (!special) { sA = 0; vH0 = rq; sB = 1; vH1 = (u1a == 0) ? ((4 - rq) & 3) : 3 - rq; }
        else { sA = sB = rq >> 1; vH0 = rq & 1; vH1 = (rq == 0) ? 2 : (rq == 1) ? 3 : (rq == 2) ? 3 : 2; }
        const bool q0 = (m2 == 0);
        const int dy = ((sA ? u1b : u1a) == 0 && vH0 == 0) ? 8 : 7, dq = q0 ? 8 : 7;
        int y1 = lane >> 3, t1 = lane & 7, y2 = (dy - y1) & 7, t2 = (dq - t1) & 7, hA = vH0, hB = vH1;
        bool self = false;
        if (special && rq == 0) {
            const int k = lane & 31;
            if (qb) { hA = hB = 2; y1 = k >> 3; t1 = k & 7; y2 = 7 - y1; t2 = (dq - t1) & 7; }
            else {
                hA = hB = 0; const int vx = k & 3, y = k >> 2;
                if (!q0 || vx) { y1 = y; t1 = vx; y2 = (8 - y) & 7; t2 = dq - vx; }
                else { t1 = t2 = (y >= 4) ? 4 : 0; const int yy = y & 3; if (yy) { y1 = yy; y2 = 8 - yy; } else { y1 = 0; y2 = 4; self = true; } }
            }
        }
        const int uA = (sA ? u1b : u1a) + 64 * (hA + 4 * y1), uB = (sB ? u1b : u1a) + 64 * (hB + 4 * y2);
        const int qA = (sA ? q1b : q1a) + 8 * t1, qB = (sB ? q1b : q1a) + 8 * t2;
        const int rowB = L_FFT + ((qb ? (vH1 * 2 + sB) : (vH0 * 2 + sA)) * 36 + (lane & 31)) * 8;
        const bool rot = q0 && (t1 == 0);
        const int p3a1 = L_FFT + (y1 * 290 + (hA * 2 + sA) * 36 + t1 * 4) * 8;
        const int p3a2 = L_FFT + (y2 * 290 + (hB * 2 + sB) * 36 + t2 * 4) * 8;
        const float thA = (float)qA * (1.0f / 512.0f), thB = (float)qB * (1.0f / 512.0f);
#pragma unroll
        for (int k = 1; k < 4; k++) tw0[k] = cis((float)r3 * ((float)k / 32.0f));
#pragma unroll
        for (int k = 1; k < 8; k++) tw1[k] = cis((float)c2p * ((float)k / 32.0f));
        f2 csq[8];
#pragma unroll
        for (int e = 0; e < 8; e++) csq[e] = cis(((e >> 2) ? thB : thA) + 0.125f * (float)(e & 3));
        asm volatile("" : "+v"(csq[0]), "+v"(csq[1]), "+v"(csq[2]), "+v"(csq[3]), "+v"(csq[4]), "+v"(csq[5]), "+v"(csq[6]), "+v"(csq[7]),
                     "+v"(tw0[1]), "+v"(tw0[2]), "+v"(tw0[3]), "+v"(tw1[1]), "+v"(tw1[2]), "+v"(tw1[3]), "+v"(tw1[4]), "+v"(tw1[5]), "+v"(tw1[6]), "+v"(tw1[7]));
        flag_wait(lane * 8, WS_FF1 + (wq2 * 4 + rq) * 512);
        wait_grp(0, A_S0);
        STAMP(21);
        const int f1off = WS_F1 + sig * 16384 + 2 * rq * 512;
        u4 d[4];
        SCHED();
#pragma unroll
        for (int H = 0; H < 4; H++) d[H] = __builtin_amdgcn_raw_buffer_load_b128(rs, NOF(l16), f1off + H * 4096, 16);
        SCHED();
        {
            const u32 o1 = min(rq * 22 + lane, 85) * 8, o2 = (((qb ? u1b : u1a) >> 1) * 32 + (lane & 31)) * 8;
            _Pragma("unroll 1") for (int spins = 0; spins < SPIN_CAP; spins++) {
                u2 f1 = __builtin_amdgcn_raw_buffer_load_b64(rs, o1, WS_FP + t * 1024, 16);
                u2 f2_ = __builtin_amdgcn_raw_buffer_load_b64(rs, o2, WS_FW, 16);
                if (__all(f1.x == tag.x && f1.y == tag.y && f2_.x == tag.x && f2_.y == tag.y)) break;
                __builtin_amdgcn_s_sleep(8);
                FENCE();
            }
            __builtin_amdgcn_fence(__ATOMIC_ACQUIRE, "wavefront");
            FENCE();
        }
        post(A_FP);
        const u4 wa4 = __builtin_amdgcn_raw_buffer_load_b128(rs, (hA + 4 * y1) * 128 + t1 * 16, WS_WP + sig * 8192 + sA * 4096, 16);
        const u4 wb4 = __builtin_amdgcn_raw_buffer_load_b128(rs, (hB + 4 * y2) * 128 + t2 * 16, WS_WP + sig * 8192 + sB * 4096, 16);
        SCHED();
        STAMP(5);
        __builtin_amdgcn_s_setprio(2);
        f2 e0[4], e1[4];
#pragma unroll
        for (int H = 0; H < 4; H++) { e0[H] = f2{__uint_as_float(d[H].x), __uint_as_float(d[H].y)}; e1[H] = f2{__uint_as_float(d[H].z), __uint_as_float(d[H].w)}; }
        dft4<false>(e0[0], e0[1], e0[2], e0[3], kc); dft4<false>(e1[0], e1[1], e1[2], e1[3], kc);
        STAMP(6);
        LAS char* area = lds + L_FFT + rq * 4640;
#pragma unroll
        for (int k = 0; k < 4; k++) {
            if (k) { e0[k] = cmul<true>(e0[k], tw0[k]); e1[k] = cmul<true>(e1[k], tw0[k]); }
            *(LAS f4*)(area + (l3 * 72 + 2 * c2p + 8 * k + 32 * rb) * 8) = f4{e0[k].x, e0[k].y, e1[k].x, e1[k].y};
        }
        FENCE();
#pragma unroll
        for (int k = 0; k < 8; k++) v[k] = *(LAS f2*)(area + (k * 72 + lane) * 8);
        dft8<false>(v, kc);
#pragma unroll
        for (int k = 1; k < 8; k++) v[k] = cmul<true>(v[k], tw1[k]);
        FENCE();
        LAS char* pb = lds + L_FFT + (r3 * 290 + l3 * 36 + c2p) * 8;
#pragma unroll
        for (int k = 0; k < 8; k++) *(LAS f2*)(pb + k * 32) = v[k];
        post(A_E1);
        f2 wS[2] = {f2{0.f, 0.f}, f2{0.f, 0.f}};
        if (q0) {
            wS[0] = f2{bflo(a.wr[uA * 257 + 256]), bflo(a.wi[uA * 257 + 256])};
            wS[1] = f2{bflo(a.wr[uB * 257 + 256]), bflo(a.wi[uB * 257 + 256])};
        }
        {
            f2 W[8], WB[8];
#pragma unroll
            for (int e = 0; e < 4; e++) { W[e] = bfup(wa4[e]); W[4 + e] = bfup(wb4[e]); }
#pragma unroll
            for (int k = 0; k < 4; k++) { WB[k] = W[4 + 3 - k]; WB[4 + k] = W[3 - k]; }
            if (q0) {
#pragma unroll
                for (int k = 0; k < 4; k++) {
                    f2 n1 = rot ? W[(4 - k) & 3] : W[3 - k], n2 = rot ? W[4 + ((4 - k) & 3)] : W[4 + 3 - k];
                    WB[k] = self ? n1 : n2; WB[4 + k] = self ? n2 : n1;
                }
#pragma unroll
                for (int e = 0; e < 8; e += 4) {
                    f2 own = wS[e >> 2], oth = self ? own : wS[1 - (e >> 2)];
                    f2 S0 = pkfma(WB[e], kc.pm, W[e]) * 0.5f, S1c = pkfma(own, kc.pm, oth) * 0.5f;
                    WB[e] = rot ? S1c : WB[e];
                    al[e] = rot ? S0 : W[e];
                }
                W[0] = al[0]; W[4] = al[4];
            }
#pragma unroll
            for (int e = 0; e < 8; e++) {
                const f2 cs = csq[e];
                f2 P = pkfma(WB[e], kc.pm, W[e]), M = pkfma(WB[e], kc.pm.yx, W[e]);
                asm("v_pk_fma_f32 %0, %1, %2, %3 op_sel:[1,0,0] op_sel_hi:[1,1,1] neg_lo:[1,0,0] neg_hi:[1,0,0]" : "=v"(al[e]) : "v"(cs), "v"(M), "v"(P));
                asm("v_pk_mul_f32 %0, %1, %2 op_sel:[1,0] op_sel_hi:[0,0] neg_lo:[1,0]" : "=v"(be[e]) : "v"(M), "v"(cs));
            }
            asm volatile("" : "+v"(al[0]), "+v"(al[1]), "+v"(al[2]), "+v"(al[3]), "+v"(al[4]), "+v"(al[5]), "+v"(al[6]), "+v"(al[7]),
                         "+v"(be[0]), "+v"(be[1]), "+v"(be[2]), "+v"(be[3]), "+v"(be[4]), "+v"(be[5]), "+v"(be[6]), "+v"(be[7]),
                         "+v"(tw0[1]), "+v"(tw0[2]), "+v"(tw0[3]), "+v"(tw1[1]), "+v"(tw1[2]), "+v"(tw1[3]), "+v"(tw1[4]), "+v"(tw1[5]), "+v"(tw1[6]), "+v"(tw1[7]));
        }
        wait_grp(0, A_E1);
#pragma unroll
        for (int k = 0; k < 8; k++) v[k] = *(LAS f2*)(lds + rowB + k * 2320);
        dft8<false>(v, kc);
#pragma unroll
        for (int k = 0; k < 8; k++) *(LAS f2*)(lds + rowB + k * 2320) = v[k];
        FENCE();
        f4 a0 = *(LAS f4*)(lds + p3a1), a1 = *(LAS f4*)(lds + p3a1 + 16), b0 = *(LAS f4*)(lds + p3a2), b1 = *(LAS f4*)(lds + p3a2 + 16);
        f2 X[8] = {a0.xy, a0.zw, a1.xy, a1.zw, b0.xy, b0.zw, b1.xy, b1.zw};
        dft4<false>(X[0], X[1], X[2], X[3], kc); dft4<false>(X[4], X[5], X[6], X[7], kc);
        f2 Y[8];
#pragma unroll
        for (int k = 0; k < 4; k++) { Y[k] = X[4 + 3 - k]; Y[4 + k] = X[3 - k]; }
        if (q0) {
#pragma unroll
            for (int k = 0; k < 4; k++) {
                f2 n1 = rot ? X[(4 - k) & 3] : X[3 - k], n2 = rot ? X[4 + ((4 - k) & 3)] : X[4 + 3 - k];
                Y[k] = self ? n1 : n2; Y[4 + k] = self ? n2 : n1;
            }
        }
        f2 O[8];
#pragma unroll
        for (int e = 0; e < 8; e++) O[e] = cfmac_conj(cmul<false>(X[e], al[e]), be[e], Y[e]);
        dft4<true>(O[0], O[1], O[2], O[3], kc); dft4<true>(O[4], O[5], O[6], O[7], kc);
        *(LAS f4*)(lds + p3a1) = f4{O[0].x, O[0].y, O[1].x, O[1].y}; *(LAS f4*)(lds + p3a1 + 16) = f4{O[2].x, O[2].y, O[3].x, O[3].y};
        *(LAS f4*)(lds + p3a2) = f4{O[4].x, O[4].y, O[5].x, O[5].y}; *(LAS f4*)(lds + p3a2 + 16) = f4{O[6].x, O[6].y, O[7].x, O[7].y};
        FENCE();
#pragma unroll
        for (int k = 0; k < 8; k++) v[k] = *(LAS f2*)(lds + rowB + k * 2320);
        dft8<true>(v, kc);
#pragma unroll
        for (int k = 0; k < 8; k++) *(LAS f2*)(lds + rowB + k * 2320) = v[k];
        post(A_E2);
        wait_grp(0, A_E2);
#pragma unroll
        for (int k = 0; k < 8; k++) v[k] = *(LAS f2*)(pb + k * 32);
#pragma unroll
        for (int k = 1; k < 8; k++) v[k] = cmul<false>(v[k], tw1[k]);
        dft8<true>(v, kc);
        FENCE();
#pragma unroll
        for (int k = 0; k < 8; k++) *(LAS f2*)(area + (k * 72 + lane) * 8) = v[k];
        FENCE();
#pragma unroll
        for (int k = 0; k < 4; k++) {
            f4 q = *(LAS f4*)(area + (l3 * 72 + 2 * c2p + 8 * k + 32 * rb) * 8);
            e0[k] = q.xy; e1[k] = q.zw;
            if (k) { e0[k] = cmul<false>(e0[k], tw0[k]); e1[k] = cmul<false>(e1[k], tw0[k]); }
        }
        dft4<true>(e0[0], e0[1], e0[2], e0[3], kc); dft4<true>(e1[0], e1[1], e1[2], e1[3], kc);
        const int f2st = WS_F2 + (r3 * 8 + l3) * 16384 + pos1 * 64 + c2p * 16;
#pragma unroll
        for (int H = 0; H < 4; H++) {
            u4 o = {__float_as_uint(e0[H].x), __float_as_uint(e0[H].y), __float_as_uint(e1[H].x), __float_as_uint(e1[H].y)};
            st128(o, rs, NOF(f2st), H * (64 * 16384));
        }
            pos1f = pos1;
        }
        STAMP(7);
        __builtin_amdgcn_s_setprio(1);
        RELANE();
        wait_grp(1, B_CS);
        gemm1_half(IC<8>{}, xa, xb, accA, accB, acs, [&](auto S) { if (S == 5) { WAITVM(0); flag_store(WS_FF2 + (rq * 256 + pos1f) * 8); } });
        STAMP(8);
        RELANE();
        gemm1_epi(accA, accB, acs, A_CS);
        __builtin_amdgcn_s_setprio(0);
        sfor<4>([&](auto I) { dma_wd(IC<L_SET1>{}, I, 0); });
        WAITVM(16);
        flag_store(WS_FHA + ((rg * 8 + w) * 16 + cg) * 8);
        STAMP(9);
        {
            u4 d[4];
        fft13_consts(0.5f / 524288.0f);
        flag_wait(lane * 8, WS_FF2 + (((s2 & 7) >> 1) * 256 + rq * 64) * 8);
        const int off = WS_F2 + pid * 16384 + rq * 4096;
#pragma unroll
        for (int i = 0; i < 4; i++) d[i] = __builtin_amdgcn_raw_buffer_load_b128(rs, NOF(l16), off + i * 1024, 16);
        STAMP(13);
        __builtin_amdgcn_s_setprio(2);
#pragma unroll
        for (int i = 0; i < 4; i++) *(LAS u4*)(lds + L_FFT + rq * 4608 + (i * 8 + (lane >> 3)) * 144 + (lane & 7) * 16) = d[i];
        FENCE();
        v[0] = *(LAS f2*)(lds + x3a[0]);
        v[1] = *(LAS f2*)(lds + x3a[1]); v[2] = *(LAS f2*)(lds + x3a[1] + 1152); v[3] = *(LAS f2*)(lds + x3a[1] + 2304);
        v[4] = *(LAS f2*)(lds + x3a[2]);
        v[5] = *(LAS f2*)(lds + x3a[3] + 2304); v[6] = *(LAS f2*)(lds + x3a[3] + 1152); v[7] = *(LAS f2*)(lds + x3a[3]);
#pragma unroll
        for (int k = 0; k < 8; k++) v[k] = cmul<false>(v[k], twT[k]);
        dft8<true>(v, kc);
#pragma unroll
        for (int k = 1; k < 8; k++) v[k] = cmul<false>(v[k], tw64[k]);
        FENCE();
        LAS f2* reg = (LAS f2*)(lds + L_FFT) + (rq * 2 + qb) * 288;
#pragma unroll
        for (int k = 0; k < 8; k++) reg[36 * l3 + 4 * k + c2p] = v[k];
        FENCE();
#pragma unroll
        for (int k = 0; k < 8; k++) v[k] = reg[36 * k + (lane & 31)];
        dft8<true>(v, kc);
        FENCE();
#pragma unroll
        for (int k = 0; k < 8; k++) reg[(lane & 31) + 32 * k] = v[k];
        post(A_F3);
        wait_grp(0, A_F3);
        if (b == 0 && rq == 0) __builtin_amdgcn_raw_buffer_store_b64(*(LAS u2*)(lds + L_TAG + 8), rs, lane ? WS_BYTES : 0, 0, 16);
        LAS f2* xb1 = (LAS f2*)(lds + L_FFT) + rq * 64 + lane;
#pragma unroll
        for (int q1 = 0; q1 < 8; q1++) { const int rg_ = (q1 < 4) ? q1 * 2 : (((8 - q1) & 3) * 2 + 1); v[q1] = xb1[rg_ * 288]; }
        post(A_F4);
        dft8<true>(v, kc);
        wait_grp(0, A_F4);
#pragma unroll
        for (int c1 = 0; c1 < 8; c1++) *(LAS u32*)(lds + L_S + (16 * rq + (lane >> 2)) * 144 + c1 * 16 + c2p * 4) = pkbf(v[c1]);
        }
        STAMP(10);
        u4 hfr[16];
        flag_wait(li * 8, WS_FHA + (rg * 8 + w) * 128);
#pragma unroll
        for (int i = 0; i < 16; i++) { hfr[i] = __builtin_amdgcn_raw_buffer_load_b128(rs, l16, hsrc + 2 * ((i + rot) & 15) * 1024, 16); SCHED(); }
        STAMP(17);
        g2_mma(IC<16>{}, hfr, L_SET1);
    } else {
        u4 xa[16], xb[16];
        __builtin_amdgcn_s_sleep(2);
        sfor<8>([&](auto I) { dma_w1(IC<0>{}, I); });
        wait_pair(A_XL);
        read_tile(xa, 0);
        asm volatile("s_waitcnt lgkmcnt(0)" ::: "memory");
        wait_pair(A_XC);
        dma_x();
        sfor<8>([&](auto I) { dma_w1(IC<1>{}, I); });
        STAMP(1);
        asm volatile("s_waitcnt vmcnt(8)" : "+v"(cpf) :: "memory");
        post(B_W0);
        read_tile(xb, 1);
        STAMP(2);
        {
            f4 accA[4], accB[4], acs = {0, 0, 0, 0};
#pragma unroll
            for (int j = 0; j < 4; j++) { accA[j] = f4{0, 0, 0, 0}; accB[j] = f4{0, 0, 0, 0}; }
            wait_grp(1, B_W0);
            STAMP(3);
            gemm1_half(IC<0>{}, xa, xb, accA, accB, acs, [&](auto S) {
                constexpr int s = S;
                if (s == 1) { WAITVM(0); post(B_W1); wait_pair(A_XB); }
                if (s == 3) wait_grp(1, B_W1);
                if constexpr (s >= 1 && s <= 4) sfor<4>([&](auto I) { dma_w1(IC<8 + ((s - 1) >> 1)>{}, IC<((s - 1) & 1) * 4 + I>{}); });
                if (s == 7) { WAITVM(8); post(B_W2); }
            });
            STAMP(14);
            wait_grp(1, B_W2);
            get_tag();
            gemm1_half(IC<8>{}, xa, xb, accA, accB, acs, [&](auto S) {
                if (S == 1) { WAITVM(0); post(B_W3); }
                if (S == 3) wait_grp(1, B_W3);
            });
            STAMP(4);
            RELANE();
            pin9(accA, accB, acs);
            if (nblk == 3) {
                const u32 wvB = (u32)((lane >> 2) * 2752 + (((lane & 3) ^ (((lane >> 5) & 1) << 1)) * 16));
                const char* wbs = (const char*)((rq >> 1) ? a.wu : a.wg) + (size_t)((rq & 1) * 16) * 2752 + fb2 * 64;
                wait_grp(0, A_S0);
                sfor<16>([&](auto K) { constexpr int k = K; dma_add<k * 4096, IX(k) * 32 * 2752>(wvB, wbs + ((BE(k) + j4) & 3) * (128 * 2752), (u32)(rq * 1024)); });
            }
            SCHED();
            stats(xb);
            STAMP(15);
            gemm1_epi(accA, accB, acs, B_CS);
        }
        STAMP(5);
        if (nblk == 3) {
            WAITVM(2); post(B_WB);
            wait_grp(1, B_WB);
            const u32 tb = (u32)((8 * lg + (li >> 2)) * 64 + (lg & 1) * 32 + 8 * (li & 3));
            LAS char* p0 = lds + tb; LAS char* p1 = lds + (tb ^ 32);
            LAS char* pc = lds + (tb ^ ((rq & 1) * 32)) + (rq >> 1) * 2048;
            f4 acc[2][4], acs = {0, 0, 0, 0};
#pragma unroll
            for (int j = 0; j < 4; j++) { acc[0][j] = f4{0, 0, 0, 0}; acc[1][j] = f4{0, 0, 0, 0}; }
            u4 F[2][5];
            auto ldf = [&](int ks, u4 (&f)[5]) {
                const int off = ks * 4096;
                f[4] = trfrag2<256>(pc + off);
                f[0] = trfrag2<256>(p0 + off); f[1] = trfrag2<256>(p1 + off); f[2] = trfrag2<256>(p0 + off + 2048); f[3] = trfrag2<256>(p1 + off + 2048);
            };
            ldf(0, F[0]);
            sfor<16>([&](auto S) {
                constexpr int ks = S;
                if (ks == 5) { WAITVM(0); flag_store(WS_FHA + ((rg * 8 + w) * 16 + cg) * 8); }
                SCHED();
                if (ks + 1 < 16) ldf(ks + 1, F[(ks + 1) & 1]);
                u4 (&f)[5] = F[ks & 1];
                acs = mfma(f[4], ones, acs);
#pragma unroll
                for (int j = 0; j < 4; j++) { acc[0][j] = mfma(f[j], xa[ks], acc[0][j]); acc[1][j] = mfma(f[j], xb[ks], acc[1][j]); }
                if (ks + 1 < 16) {
                    SGB(0x100, 2);
#pragma unroll
                    for (int i = 0; i < 8; i++) { SGB(0x8, 1); SGB(0x100, 1); }
                    SGB(0x8, 1);
                }
                SCHED();
            });
            if (li == 0) *(LAS f4*)(lds + L_CS + 512 + rq * 64 + lg * 16) = acs;
            post(B_PB);
            wait_grp(1, B_PB);
            st128(geglu8(acc[0], lds + L_CS + 512 + lg * 16, st0), rs, l16, hoff0 + fb2 * 1024);
            st128(geglu8(acc[1], lds + L_CS + 512 + lg * 16, st1), rs, l16, hoff1 + fb2 * 1024);
        } else { WAITVM(0); flag_store(WS_FHA + ((rg * 8 + w) * 16 + cg) * 8); }
        STAMP(6);
        RELANE();
        wait_grp(0, A_FP);
        sfor<4>([&](auto I) { dma_wd(IC<0>{}, I, 1); });
        if (nblk == 3) { WAITVM(16); flag_store(WS_FHB + ((rg * 4 + rq) * 16 + fb2 - 32) * 8); }
        u4 hfr[16];
        flag_wait(li * 8, WS_FHA + (rg * 8 + w) * 128);
        post(B_D1);
#pragma unroll
        for (int i = 0; i < 16; i++) { hfr[i] = __builtin_amdgcn_raw_buffer_load_b128(rs, l16, hsrc + 2 * ((i + rot) & 15) * 1024, 16); SCHED(); }
        STAMP(9);
        wait_grp(1, B_D1);
        g2_mma(IC<16>{}, hfr, 0);
        post(B_G1);
        pin4(acc2);
        STAMP(10);
        wait_grp(1, B_G1);
        sfor<3>([&](auto I) { constexpr int i = I; dma4k_c<2 * PG + i * 16384, 0>(l16, wdp + (size_t)min(32 + 4 * i + rq, 42) * 4096, (u32)(rq * 4096)); });
        flag_wait(min(li, 10) * 8, WS_FHB + (rg * 4 + rq) * 128);
        post(B_D3);
#pragma unroll
        for (int i = 0; i < 11; i++) { hfr[i] = __builtin_amdgcn_raw_buffer_load_b128(rs, l16, hoff0 + (32 + i) * 1024, 16); SCHED(); }
        STAMP(13);
        wait_grp(1, B_D3);
        g2_mma(IC<11>{}, hfr, 2 * PG);
    }
    RELANE();
    STAMP(11);
    BAR();
    {
        LAS f4* xs = (LAS f4*)(lds + w * 2048) + lane;
        LAS f4* xr = (LAS f4*)(lds + (w ^ 4) * 2048) + lane;
        f4 me, mo;
        if (hf) { xs[0] = acc2[0]; xs[64] = acc2[1]; me = acc2[2]; mo = acc2[3]; } else { xs[0] = acc2[2]; xs[64] = acc2[3]; me = acc2[0]; mo = acc2[1]; }
        BAR();
        const int c1 = 4 * hf + lg;
        me += xr[0]; mo += xr[64];
        const u4 sv = *(LAS u4*)(lds + L_S + (16 * rq + li) * 144 + c1 * 16);
        const u4 xv = *(LAS u4*)(lds + ((rq >> 1) ? L_X1 : L_X0) + ((rq & 1) * 16 + li) * 128 + ((c1 ^ (li & 7)) * 16));
        u32 res[4];
#pragma unroll
        for (int e = 0; e < 4; e++) {
            const f2 ov = bfup(pkbf(f2{me[e], mo[e]}));
            const f2 z = ov * -1.44269504f;
            const f2 d = f2{__builtin_amdgcn_exp2f(z.x), __builtin_amdgcn_exp2f(z.y)} + f2{1.f, 1.f};
            const f2 g = bfup(pkbf(ov * f2{__builtin_amdgcn_rcpf(d.x), __builtin_amdgcn_rcpf(d.y)}));
            res[e] = pkbf(bfup(xv[e]) + bfup(pkbf(bfup(sv[e]) * g)));
        }
        *(u4*)(a.out + (size_t)(s2 + 32 * (16 * rq + li)) * 512 + 64 * c1 + 8 * t) = u4{res[0], res[1], res[2], res[3]};
    }
    STAMP(12);
    if (b == 75 && w == 1) {
        asm volatile("s_waitcnt lgkmcnt(0)" ::: "memory");
        u32 t0 = *(volatile LAS u32*)(lds + L_FFT + 22784 + 28 * 8), tk = *(volatile LAS u32*)(lds + L_FFT + 22784 + (28 + PROBE) * 8);
        u32 d = __builtin_amdgcn_readfirstlane(min(tk - t0, 5000u)) * 10;
        u64 now = __builtin_amdgcn_s_memrealtime();
        while (__builtin_amdgcn_s_memrealtime() < now + d) __builtin_amdgcn_s_sleep(8);
    }
}

extern "C" void kernel_launch(void* const* d_in, const int* in_sizes, int n_in,
                              void* d_out, int out_size, void* d_ws, size_t ws_size,
                              hipStream_t stream) {
    Args a;
    a.x = (const u16*)d_in[0]; a.wr = (const u16*)d_in[1]; a.wi = (const u16*)d_in[2];
    a.wg = (const u16*)d_in[4]; a.wu = (const u16*)d_in[5]; a.wd = (const u16*)d_in[6];
    a.out = (u16*)d_out; a.ws = (unsigned char*)d_ws;
    (void)hipFuncSetAttribute((const void*)fused, hipFuncAttributeMaxDynamicSharedMemorySize, L_END);
    fused<<<256, 512, L_END, stream>>>(a);
}
```

```cpp
#include <hip/hip_runtime.h>
#include <stdint.h>
#include <type_traits>

typedef unsigned short u16;
typedef unsigned u32;
typedef unsigned long long u64;
typedef float f2 __attribute__((ext_vector_type(2)));
typedef float f4 __attribute__((ext_vector_type(4)));
typedef u32 u4 __attribute__((ext_vector_type(4)));
typedef u32 u2 __attribute__((ext_vector_type(2)));
typedef short s4 __attribute__((ext_vector_type(4)));
typedef __bf16 bf8 __attribute__((ext_vector_type(8)));
typedef __bf16 bf2 __attribute__((ext_vector_type(2)));
#define LAS __attribute__((address_space(3)))
#define GAS __attribute__((address_space(1)))
#define DEV __device__ __forceinline__
template <int N> using IC = std::integral_constant<int, N>;

constexpr int NFB = 43;
constexpr int WS_FF1 = 4096;
constexpr int WS_FF2 = WS_FF1 + 8192;
constexpr int WS_FHA = WS_FF2 + 8192;
constexpr int WS_FHB = WS_FHA + 16384;
constexpr int WS_FP = WS_FHB + 16384;
constexpr int WS_FW = WS_FP + 8192;
constexpr int WS_F1 = 1 << 20, WS_F2 = WS_F1 + (4 << 20), WS_H = WS_F2 + (4 << 20);
constexpr int WS_WDP = WS_H + 32 * 4 * NFB * 1024;
constexpr int WS_WP = WS_WDP + 8 * NFB * 4 * 1024;
constexpr int WS_STAMP = WS_WP + (2 << 20);
constexpr int WS_BYTES = WS_STAMP + (1 << 20);
constexpr int PG = 8192;
constexpr int L_SET1 = 8 * PG;
constexpr int L_FFT = 16 * PG;
constexpr int L_SMALL = 19 * PG;
constexpr int L_PROG = L_SMALL;
constexpr int L_TAG = L_SMALL + 32;
constexpr int L_STAT = L_SMALL + 64;
constexpr int L_CS = L_SMALL + 2048;
constexpr int L_X0 = L_FFT + 18688, L_X1 = L_SMALL + 3072;
constexpr int L_S = L_FFT;
constexpr int L_END = 20 * PG;
constexpr u32 SENT = 0xFFFFFFFFu;
constexpr int CODE_BYTES = 44 * 1024;
constexpr int SPIN_CAP = 1 << 16;

struct Args { const u16* x; const u16* wr; const u16* wi; const u16* wg; const u16* wu; const u16* wd; u16* out; unsigned char* ws; };

#define BAR() asm volatile("s_waitcnt lgkmcnt(0)\n\ts_barrier" ::: "memory")
#define WAITVM(n) asm volatile("s_waitcnt vmcnt(" #n ")" ::: "memory")
#define NOF(x) (x)
#define NOPL(x) (x)
#define FENCE() asm volatile("" ::: "memory")
#define SCHED() __builtin_amdgcn_sched_barrier(0)
#define SGB(mask, n) __builtin_amdgcn_sched_group_barrier(mask, n, 0)
#define STAMP(i)
#define CSTAMP(i)

DEV f2 pkfma(f2 a, f2 b, f2 c) { return __builtin_elementwise_fma(a, b, c); }
struct KC { f2 pm, rpm; };
DEV f2 addmi(f2 a, f2 b, const KC& k) { return pkfma(b.yx, k.pm, a); }
DEV f2 addpi(f2 a, f2 b, const KC& k) { return pkfma(b.yx, k.pm.yx, a); }
template <bool INV> DEV void dft4(f2& a0, f2& a1, f2& a2, f2& a3, const KC& k) {
    f2 t0 = a0 + a2, t1 = a0 - a2, t2 = a1 + a3, t3 = a1 - a3;
    a0 = t0 + t2; a2 = t0 - t2;
    if (INV) { a1 = addpi(t1, t3, k); a3 = addmi(t1, t3, k); } else { a1 = addmi(t1, t3, k); a3 = addpi(t1, t3, k); }
}
template <bool INV> DEV void dft8(f2 (&v)[8], const KC& k) {
    f2 a0 = v[0] + v[4], a4 = v[0] - v[4], a1 = v[1] + v[5], a5 = v[1] - v[5];
    f2 a2 = v[2] + v[6], a6 = v[2] - v[6], a3 = v[3] + v[7], a7 = v[3] - v[7];
    dft4<INV>(a0, a1, a2, a3, k);
    v[0] = a0; v[2] = a1; v[4] = a2; v[6] = a3;
    f2 b1, b3, t0, t1;
    if (INV) { b1 = addpi(a5, a5, k); b3 = addpi(-a7, a7, k); t0 = addpi(a4, a6, k); t1 = addmi(a4, a6, k); }
    else { b1 = addmi(a5, a5, k); b3 = addmi(-a7, a7, k); t0 = addmi(a4, a6, k); t1 = addpi(a4, a6, k); }
    f2 t2 = b1 + b3, t3 = b1 - b3;
    v[1] = pkfma(t2, k.rpm.xx, t0); v[5] = pkfma(t2, k.rpm.yy, t0);
    if (INV) { v[3] = pkfma(t3.yx, k.rpm.yx, t1); v[7] = pkfma(t3.yx, k.rpm, t1); }
    else { v[3] = pkfma(t3.yx, k.rpm, t1); v[7] = pkfma(t3.yx, k.rpm.yx, t1); }
}
template <bool CONJ> DEV f2 cmul(f2 a, f2 w) {
    f2 r;
    if (CONJ) asm("v_pk_mul_f32 %0, %1, %2 op_sel:[1,1] op_sel_hi:[0,1]\n\tv_pk_fma_f32 %0, %1, %2, %0 op_sel_hi:[1,0,1] neg_hi:[0,0,1]" : "=&v"(r) : "v"(a), "v"(w));
    else asm("v_pk_mul_f32 %0, %1, %2 op_sel:[1,1] op_sel_hi:[0,1]\n\tv_pk_fma_f32 %0, %1, %2, %0 op_sel_hi:[1,0,1] neg_lo:[0,0,1]" : "=&v"(r) : "v"(a), "v"(w));
    return r;
}
DEV f2 cfmac_conj(f2 t, f2 beta, f2 B) {
    asm("v_pk_fma_f32 %0, %2, %1, %0 op_sel_hi:[0,1,1]\n\tv_pk_fma_f32 %0, %2, %1, %0 op_sel:[1,1,0] op_sel_hi:[1,0,1] neg_hi:[0,1,0]" : "+v"(t) : "v"(beta), "v"(B));
    return t;
}
DEV f2 cis(float r) { return f2{__builtin_amdgcn_cosf(r), __builtin_amdgcn_sinf(r)}; }
DEV float bflo(u32 v) { return __uint_as_float(v << 16); }
DEV float bfhi(u32 v) { return __uint_as_float(v & 0xFFFF0000u); }
DEV f2 bfup(u32 v) { return f2{bflo(v), bfhi(v)}; }
DEV u32 pkbf(f2 v) { bf2 r; r.x = (__bf16)v.x; r.y = (__bf16)v.y; u32 o = __builtin_bit_cast(u32, r); asm("" : "+v"(o)); return o; }
DEV float rbf(float f) { return __uint_as_float(((u32)__builtin_bit_cast(u16, (__bf16)f)) << 16); }
DEV f4 mfma(u4 a, u4 b, f4 c) { return __builtin_amdgcn_mfma_f32_16x16x32_bf16(__builtin_bit_cast(bf8, a), __builtin_bit_cast(bf8, b), c, 0, 0, 0); }
DEV u4 sanitize(u4 v) { return u4{min(v.x, 0xFFFFFFFEu), min(v.y, 0xFFFFFFFEu), min(v.z, 0xFFFFFFFEu), min(v.w, 0xFFFFFFFEu)}; }
DEV u32 max4(u4 v) { return max(max(v.x, v.y), max(v.z, v.w)); }
DEV void st128(u4 v, __amdgpu_buffer_rsrc_t rs, u32 voff, int soff) {
    __builtin_amdgcn_raw_buffer_store_b128(v, rs, voff, soff, 16);
    asm volatile("s_nop 1" :: "v"(v) : "memory");
}
template <int OFF2> DEV u4 trfrag2(LAS char* p) {
    s4 r0 = __builtin_amdgcn_ds_read_tr16_b64_v4i16((LAS s4*)p);
    s4 r1 = __builtin_amdgcn_ds_read_tr16_b64_v4i16((LAS s4*)(p + OFF2));
    u2 x0 = __builtin_bit_cast(u2, r0), x1 = __builtin_bit_cast(u2, r1);
    return u4{x0.x, x0.y, x1.x, x1.y};
}
template <int LI, int GI> DEV void dma_add(u32 vb, const void* sbase, u32 sl) {
    const u32 va = vb + (u32)GI;
    asm volatile("s_add_u32 m0, %2, %3\n\ts_nop 0\n\tglobal_load_lds_dwordx4 %0, %1" :: "v"(va), "s"(sbase), "s"(sl), "i"(LI) : "memory", "scc");
}
template <int LI, int GI> DEV void dma4k_c(u32 vb, const void* sbase, u32 sl) {
    const u32 va = vb + (u32)GI;
    asm volatile("s_add_u32 m0, %2, %3\n\ts_nop 0\n\tglobal_load_lds_dwordx4 %0, %1 sc1\n\tglobal_load_lds_dwordx4 %0, %1 offset:1024 sc1\n\t"
                 "global_load_lds_dwordx4 %0, %1 offset:2048 sc1\n\tglobal_load_lds_dwordx4 %0, %1 offset:3072 sc1"
                 :: "v"(va), "s"(sbase), "s"(sl), "i"(LI) : "memory", "scc");
}
template <int OFF> DEV void ldg_u16(u32& d, u32 vo, const void* sb) { asm volatile("global_load_ushort %0, %1, %2 offset:%3" : "=v"(d) : "v"(vo), "s"(sb), "i"(OFF) : "memory"); }
template <int OFF> DEV void ldg_u32(u32& d, u32 vo, const void* sb) { asm volatile("global_load_dword %0, %1, %2 offset:%3" : "=v"(d) : "v"(vo), "s"(sb), "i"(OFF) : "memory"); }
DEV void lds_wait(u32 addr, u32 v) {
    u32 tmp, cnt;
    asm volatile("s_mov_b32 %1, 0x80000\n"
                 "1:\n\tds_read_b32 %0, %2\n\ts_sub_u32 %1, %1, 1\n\ts_waitcnt lgkmcnt(0)\n\ts_cbranch_scc1 2f\n\tv_cmp_gt_u32 vcc, %3, %0\n\ts_cbranch_vccnz 1b\n2:"
                 : "=&v"(tmp), "=&s"(cnt) : "v"(addr), "s"(v) : "memory", "vcc", "scc");
}
template <int N, typename F> DEV void sfor(F&& f) { if constexpr (N > 0) { sfor<N - 1>(f); f(IC<N - 1>{}); } }

DEV void pin9(f4 (&x)[4], f4 (&y)[4], f4& z) {
    asm volatile("" : "+v"(x[0]), "+v"(x[1]), "+v"(x[2]), "+v"(x[3]), "+v"(y[0]), "+v"(y[1]), "+v"(y[2]), "+v"(y[3]), "+v"(z));
}
DEV void pin4(f4 (&x)[4]) { asm volatile("" : "+v"(x[0]), "+v"(x[1]), "+v"(x[2]), "+v"(x[3])); }
DEV void dma_rt(u32 voff, const void* sbase, u32 sl) {
    asm volatile("s_mov_b32 m0, %2\n\ts_nop 0\n\tglobal_load_lds_dwordx4 %0, %1" :: "v"(voff), "s"(sbase), "s"(sl) : "memory");
}
DEV u4 geglu8(const f4 (&acc)[4], LAS char* cs, f2 st) {
    u32 hw[4];
    const f2 nm = f2{-st.x, -st.x}, ri = f2{st.y, st.y};
#pragma unroll
    for (int c = 0; c < 2; c++) {
        f4 cg_ = *(LAS f4*)(cs + c * 64), cu_ = *(LAS f4*)(cs + (2 + c) * 64);
#pragma unroll
        for (int e = 0; e < 2; e++) {
            f2 g = pkfma(e ? cg_.zw : cg_.xy, nm, e ? acc[c].zw : acc[c].xy) * ri;
            f2 u = pkfma(e ? cu_.zw : cu_.xy, nm, e ? acc[2 + c].zw : acc[2 + c].xy) * ri;
            f2 z = g * pkfma(g * g, f2{-0.10294324f, -0.10294324f}, f2{-2.3022082f, -2.3022082f});
            f2 d = f2{__builtin_amdgcn_exp2f(z.x), __builtin_amdgcn_exp2f(z.y)} + f2{1.f, 1.f};
            f2 r = f2{__builtin_amdgcn_rcpf(d.x), __builtin_amdgcn_rcpf(d.y)};
            hw[2 * c + e] = pkbf(g * r * u);
        }
    }
    return u4{hw[0], hw[1], hw[2], hw[3]};
}

enum { A_XL = 1, A_XC, A_P1, A_XB, A_S0, A_FP, A_E1, A_E2, A_CS, A_F3, A_F4, A_D1 };
enum { B_W0 = 1, B_W1, B_W2, B_W3, B_CS, B_WB, B_PB, B_D1, B_G1, B_D3 };

__global__ void __launch_bounds__(512, 2) fused(Args a) {
    extern __shared__ __attribute__((aligned(16))) char smem[];
    LAS char* lds = (LAS char*)smem;
    const int tid = threadIdx.x, w = __builtin_amdgcn_readfirstlane(tid >> 6);
    int lane = tid & 63;
    const int b = blockIdx.x, xcd = b & 7, r = b >> 3, t = r & 7, par = xcd & 1, rg = (xcd >> 1) * 4 + (r >> 3);
    const int s2 = 2 * rg + par, cg = par * 8 + t, sig = xcd * 32 + r, pid = s2 * 8 + t;
    const int rq = w & 3, hf = w >> 2, j4 = r >> 3;
#define BE(l) ((((l) >> 3) << 1) | ((l) & 1))
#define IX(l) (((l) >> 1) & 3)
    u32 l16 = lane * 16;
    u32 cpf = 0;
    if (w == 7) {
        const u64 pc = __builtin_amdgcn_s_getpc() & ~(u64)255;
        const u32 off = min((u32)((t * 64 + lane) * 128), (u32)(CODE_BYTES - 128));
        asm volatile("global_load_dword %0, %1, %2" : "=v"(cpf) : "v"(off), "s"(pc) : "memory");
    }
    *(volatile LAS u32*)(lds + L_PROG + w * 4) = 0;
    BAR();
    STAMP(0);
    auto dma_x = [&]() {
        const char* xs = (const char*)a.x + (size_t)((s2 ^ hf) + 512 * rq) * 1024;
#pragma unroll
        for (int i = 0; i < 16; i++) { const int row = (i + 2 * t) & 15; dma_rt(l16 ^ (u32)(row * (32768 + 16)), xs, (u32)(L_SET1 + rq * 16384 + row * 1024)); }
    };
    if (hf == 0) dma_x();
    int li, lg, c2p, l3, qb; u32 vpg;
#define RELANE() do { asm volatile("" : "+v"(lane)); l16 = lane * 16; li = lane & 15; lg = lane >> 4; c2p = lane & 3; l3 = (lane >> 2) & 7; qb = lane >> 5; vpg = (u32)(L_PROG + (lane & 3) * 4); } while (0)
    const __amdgpu_buffer_rsrc_t rs = __builtin_amdgcn_make_buffer_rsrc(a.ws, 0, WS_BYTES, 0x00020000);
    KC kc; kc.pm = f2{1.f, -1.f}; kc.rpm = f2{0.70710678118654752f, -0.70710678118654752f};
    asm volatile("" : "+s"(kc.pm), "+s"(kc.rpm));
    const u4 ones = {0x3F803F80u, 0x3F803F80u, 0x3F803F80u, 0x3F803F80u};
    auto post = [&](u32 v) { asm volatile("s_waitcnt lgkmcnt(0)" ::: "memory"); *(volatile LAS u32*)(lds + L_PROG + w * 4) = v; FENCE(); };
    auto wait_grp = [&](int g, u32 v) { lds_wait(vpg + g * 16, v); };
    auto wait_pair = [&](u32 v) { lds_wait((u32)(L_PROG + (w ^ 4) * 4), v); };
    u2 tag;
    auto get_tag = [&]() { u2 tg = *(LAS u2*)(lds + L_TAG); tag.x = __builtin_amdgcn_readfirstlane(tg.x); tag.y = __builtin_amdgcn_readfirstlane(tg.y); };
    auto flag_store = [&](int off) { __builtin_amdgcn_raw_buffer_store_b64(tag, rs, lane ? WS_BYTES : 0, off, 16); };
    auto flag_wait = [&](u32 voff, int soff) {
        _Pragma("unroll 1") for (int spins = 0; spins < SPIN_CAP; spins++) {
            u2 f = __builtin_amdgcn_raw_buffer_load_b64(rs, voff, soff, 16);
            if (__all(f.x == tag.x && f.y == tag.y)) break;
            __builtin_amdgcn_s_sleep(16);
            FENCE();
        }
        __builtin_amdgcn_fence(__ATOMIC_ACQUIRE, "wavefront");
        FENCE();
    };
    auto read_tile = [&](u4 (&x)[16], int tile) {
        LAS char* p = lds + L_SET1 + (16 * rq + li) * 1024;
#pragma unroll
        for (int k = 0; k < 16; k++) x[k] = *(LAS u4*)(p + (((4 * IX(k) + lg) ^ li) * 16) + ((BE(k) + j4) & 3) * 256);
    };
    auto stats = [&](const u4 (&x)[16]) {
        f4 s1 = {0, 0, 0, 0}, sq = s1, s1b = s1, sqb = s1;
#pragma unroll
        for (int k = 0; k < 16; k += 2) { s1 = mfma(ones, x[k], s1); sq = mfma(x[k], x[k], sq); s1b = mfma(ones, x[k + 1], s1b); sqb = mfma(x[k + 1], x[k + 1], sqb); }
        sq += sqb;
        const int rr = li & 3;
        const float d = rr == 0 ? sq.x : rr == 1 ? sq.y : rr == 2 ? sq.z : sq.w;
        if (lg == (li >> 2)) {
            const float mean = (s1.x + s1b.x) * (1.0f / 512.0f);
            float var = d * (1.0f / 512.0f) - mean * mean;
            if (!(mean * mean <= 1024.0f * var)) {
                const u32* xrow = (const u32*)(a.x + (size_t)((s2 ^ hf) + 32 * (16 * rq + li)) * 512);
                float q = 0.f;
                _Pragma("unroll 1") for (int i = 0; i < 256; i++) { u32 v = xrow[i]; float d0 = bflo(v) - mean, d1 = bfhi(v) - mean; q += d0 * d0; q += d1 * d1; }
                var = q * (1.0f / 512.0f);
            }
            const float den = rbf(__builtin_amdgcn_sqrtf(rbf(rbf(var) + 1e-5f)));
            *(LAS f2*)(lds + L_STAT + (hf * 64 + 16 * rq + li) * 8) = f2{rbf(mean), __builtin_amdgcn_rcpf(den)};
        }
        FENCE();
    };
    const int nblk = (t < 6 - par) ? 3 : 2, fb2 = 32 + par * 6 + t;
    const char* wdp = (const char*)a.ws + WS_WDP + (size_t)(t * NFB) * 4096;

    f2 tw64[8], twT[8];
    int x3a[4];
    auto fft13_consts = [&](float scale) {
        int lane_ = lane; asm volatile("" : "+v"(lane_));
        const int c2p = lane_ & 3, l3 = (lane_ >> 2) & 7, qb = lane_ >> 5;
        const int wq = rq, q1 = qb ? (wq == 0 ? 4 : 8 - wq) : wq, c2 = 4 * t + c2p;
        const float f64 = (float)l3 * (1.0f / 64.0f);
#pragma unroll
        for (int k = 1; k < 8; k++) tw64[k] = cis(f64 * (float)k);
        const float fT = (float)(8 * c2 * q1 + s2 * l3) * (1.0f / 2048.0f), dT = (float)s2 * (1.0f / 256.0f);
#pragma unroll
        for (int k = 0; k < 8; k++) twT[k] = cis(fT + dT * (float)k) * scale;
#pragma unroll
        for (int k = 0; k < 4; k++) {
            const int ua = (k >> 1) * 4 + (k & 1), u1 = l3 + 8 * ua;
            const int cls = (u1 < 32) ? u1 : ((64 - u1) & 31), hi = u1 >> 5;
            int mbit, sub;
            if (wq == 0) { mbit = qb; sub = hi; } else if (cls != 0) { sub = hi; mbit = qb ^ hi; } else { mbit = hi; sub = qb ^ hi; }
            x3a[k] = L_FFT + wq * 4608 + (cls * 18 + mbit * 8 + sub * 4 + c2p) * 8 - (k == 3 ? 2304 : 0);
        }
        asm volatile("" : "+v"(tw64[1]), "+v"(tw64[2]), "+v"(tw64[3]), "+v"(tw64[4]), "+v"(tw64[5]), "+v"(tw64[6]), "+v"(tw64[7]),
                     "+v"(twT[0]), "+v"(twT[1]), "+v"(twT[2]), "+v"(twT[3]), "+v"(twT[4]), "+v"(twT[5]), "+v"(twT[6]), "+v"(twT[7]),
                     "+v"(x3a[0]), "+v"(x3a[1]), "+v"(x3a[2]), "+v"(x3a[3]));
    };

    const int hoff0 = WS_H + ((s2 * 4 + rq) * NFB) * 1024, hoff1 = WS_H + (((s2 ^ 1) * 4 + rq) * NFB) * 1024;
    RELANE();
    const char* wgs = (const char*)a.wg + (size_t)(32 * rq) * 2752 + cg * 128;
    const char* wus = (const char*)a.wu + (size_t)(32 * rq) * 2752 + cg * 128;
    const u32 wvE = (u32)((lane >> 3) * 2752 + (((lane & 7) ^ (((lane >> 4) & 1) << 1)) * 16)), wvO = (u32)((lane >> 3) * 2752 + (((lane & 7) ^ (((lane >> 4) & 1) << 1) ^ 4) * 16));
    auto dma_w1 = [&](auto KK, auto I) {
        constexpr int kk = KK, g = I >> 1, mat = I & 1;
        dma_add<kk * PG + mat * 4096 + g * 1024, g * 8 * 2752>((g & 1) ? wvO : wvE, (mat ? wus : wgs) + ((BE(kk) + j4) & 3) * (128 * 2752), (u32)(rq * 2 * PG));
    };
    const int rot = (2 * t + 4 * j4) & 15;
    auto dma_wd = [&](auto LI, auto I, int p0) { dma4k_c<LI + I * 16384, 0>(l16, wdp + (size_t)(p0 + 2 * ((rq + 4 * I + rot) & 15)) * 4096, (u32)(rq * 4096)); };

    auto gemm1_half = [&](auto S0C, const u4 (&xa)[16], const u4 (&xb)[16], f4 (&accA)[4], f4 (&accB)[4], f4& acs, auto&& hook) {
        constexpr int S0 = S0C;
        const u32 fz = (u32)((li >> 3) | ((lg & 1) << 1));
        const u32 tb = (u32)((8 * lg + (li >> 2)) * 128 + 8 * (li & 3)) + S0 * PG;
        u32 p0 = tb + (((2 * hf) ^ fz) << 5), p1 = tb + (((2 * hf + 1) ^ fz) << 5), pc = tb + (((2 * hf + (rq & 1)) ^ fz) << 5) + (rq >> 1) * 4096;
        asm volatile("" : "+v"(p0), "+v"(p1), "+v"(pc));
        u4 F[2][5];
        auto ldf = [&](int ks, u4 (&f)[5]) {
            const int off = (ks & 7) * PG;
            f[0] = trfrag2<512>(lds + p0 + off); f[1] = trfrag2<512>(lds + p1 + off); f[2] = trfrag2<512>(lds + p0 + off + 4096); f[3] = trfrag2<512>(lds + p1 + off + 4096); f[4] = trfrag2<512>(lds + pc + off);
        };
        ldf(0, F[0]);
        sfor<8>([&](auto S) {
            constexpr int s = S, ks = S0 | ((s & 3) << 1) | ((s >> 2) & 1), sn = s + 1, kn = ((sn & 3) << 1) | ((sn >> 2) & 1);
            hook(S);
            SCHED();
            if (s + 1 < 8) ldf(kn, F[(s + 1) & 1]);
            u4 (&f)[5] = F[s & 1];
#pragma unroll
            for (int j = 0; j < 4; j++) { accA[j] = mfma(f[j], xa[ks], accA[j]); accB[j] = mfma(f[j], xb[ks], accB[j]); }
            acs = mfma(f[4], ones, acs);
            if (s + 1 < 8) {
                SGB(0x100, 2);
#pragma unroll
                for (int i = 0; i < 8; i++) { SGB(0x8, 1); SGB(0x100, 1); }
                SGB(0x8, 1);
            }
            SCHED();
        });
    };
    f2 st0, st1;
    auto gemm1_epi = [&](f4 (&accA)[4], f4 (&accB)[4], f4 acs, u32 step) {
        if (li == 0) *(LAS f4*)(lds + L_CS + (hf * 4 + rq) * 64 + lg * 16) = acs;
        post(step);
        wait_grp(hf, step);
        LAS char* cs = lds + L_CS + hf * 256 + lg * 16;
        st0 = *(LAS f2*)(lds + L_STAT + (16 * rq + li) * 8);
        st1 = *(LAS f2*)(lds + L_STAT + (64 + 16 * rq + li) * 8);
        st128(geglu8(accA, cs, st0), rs, l16, hoff0 + (2 * cg + hf) * 1024);
        st128(geglu8(accB, cs, st1), rs, l16, hoff1 + (2 * cg + hf) * 1024);
    };
    f4 acc2[4];
#pragma unroll
    for (int j = 0; j < 4; j++) acc2[j] = f4{0, 0, 0, 0};
    const int hsrc = hoff0 + hf * 1024;
    auto g2_mma = [&](auto NC, const u4* hfr, int ldsbase) {
        constexpr int N = decltype(NC)::value;
        u32 pa = (u32)ldsbase + l16; asm volatile("" : "+v"(pa));
        LAS u4* p = (LAS u4*)(lds + pa);
        u4 F[2][4];
#pragma unroll
        for (int j = 0; j < 4; j++) F[0][j] = p[j * 64];
        sfor<N>([&](auto I) {
            constexpr int i = I;
            SCHED();
            if (i + 1 < N) {
#pragma unroll
                for (int j = 0; j < 4; j++) F[(i + 1) & 1][j] = p[(i + 1) * 256 + j * 64];
            }
#pragma unroll
            for (int j = 0; j < 4; j++) acc2[j] = mfma(F[i & 1][j], hfr[i], acc2[j]);
            if (i + 1 < N) {
#pragma unroll
                for (int j = 0; j < 4; j++) { SGB(0x100, 1); SGB(0x8, 1); }
            }
            SCHED();
        });
    };
    if (hf == 0) {
        u4 xa[16], xb[16];
        __builtin_amdgcn_s_setprio(2);
        const int f1st = WS_F1 + ((lane >> 3) * 8 + (((lane >> 2) & 1) ? (rq == 0 ? 4 : 8 - rq) : rq)) * 16384 + pid * 64 + c2p * 16;
        u2 ev;
        { u32 zero = 0; asm volatile("global_load_dwordx2 %0, %1, %2 sc1" : "=v"(ev) : "v"(zero), "s"(a.ws) : "memory"); }
        fft13_consts(1.0f);
        WAITVM(1);
        post(A_XL);
        STAMP(1);
        u32 raw[8];
        const int rw = lane >> 2;
        read_tile(xa, 0);
#pragma unroll
        for (int c1 = 0; c1 < 8; c1++) raw[c1] = *(LAS u32*)(lds + L_SET1 + (16 * rq + rw) * 1024 + (((8 * c1 + t) ^ rw) * 16) + c2p * 4);
        post(A_XC);
        stats(xa);
        STAMP(2);
        f2 v[8];
        {
            const f2 st = *(LAS f2*)(lds + L_STAT + (16 * rq + rw) * 8);
#pragma unroll
            for (int c1 = 0; c1 < 8; c1++) v[c1] = bfup(pkbf(bfup(pkbf(bfup(raw[c1]) - st.xx)) * st.yy));
            dft8<false>(v, kc);
            LAS f2* xb1 = (LAS f2*)(lds + L_FFT) + w * 64 + lane;
#pragma unroll
            for (int q1 = 0; q1 < 8; q1++) { const int reg = (q1 < 4) ? q1 * 2 : (((8 - q1) & 3) * 2 + 1); xb1[reg * 288] = v[q1]; }
        }
        asm volatile("s_waitcnt vmcnt(0)" : "+v"(ev) :: "memory");
        {
            const u64 epoch = ((u64)(u32)__builtin_amdgcn_readfirstlane(ev.y) << 32) | (u32)__builtin_amdgcn_readfirstlane(ev.x);
            u64 tg = (epoch + 1) * 0x9E3779B97F4A7C15ull; tg ^= tg >> 29; tg *= 0xBF58476D1CE4E5B9ull; tg ^= tg >> 32;
            tag = u2{(u32)tg, (u32)(tg >> 32)};
            *(LAS u4*)(lds + L_TAG) = u4{(u32)tg, (u32)(tg >> 32), (u32)(epoch + 1), (u32)((epoch + 1) >> 32)};
        }
        post(A_P1);
#pragma unroll
        for (int c1 = 0; c1 < 8; c1++) *(LAS u32*)(lds + ((rq >> 1) ? L_X1 : L_X0) + ((rq & 1) * 16 + rw) * 128 + ((c1 ^ (rw & 7)) * 16) + c2p * 4) = raw[c1];
        wait_pair(B_W0);
        read_tile(xb, 1);
        post(A_XB);
        u32 pk[8], fr[16];
        const int un = sig < 4 * NFB ? sig : 0, fb = un >> 2, jj = (un >> 1) & 1, tcol = (un & 1) * 4 + rq;
        {
            const u32 vd = (u32)(((fb * 32 + 4 * lg) * 512 + 64 * (4 * jj + (li >> 2)) + 8 * tcol + 2 * (li & 3)) * 2);
            sfor<4>([&](auto I) {
                ldg_u32<I * 1024>(pk[I], vd, a.wd);
                ldg_u32<I * 1024>(pk[4 + I], vd, (const char*)a.wd + 16384);
            });
            const u32 vo = (u32)(((8 * sig + 2 * rq) * 257 + lane) * 2);
            sfor<8>([&](auto I) { constexpr int rr = I >> 2, k = I & 3; ldg_u16<rr * 514 + k * 128>(fr[I], vo, a.wr); ldg_u16<rr * 514 + k * 128>(fr[8 + I], vo, a.wi); });
        }
        wait_grp(0, A_P1);
        {
            LAS f2* reg = (LAS f2*)(lds + L_FFT) + (w * 2 + qb) * 288;
#pragma unroll
            for (int k = 0; k < 8; k++) v[k] = reg[(lane & 31) + 32 * k];
            dft8<false>(v, kc);
#pragma unroll
            for (int k = 1; k < 8; k++) v[k] = cmul<true>(v[k], tw64[k]);
#pragma unroll
            for (int k = 0; k < 8; k++) reg[36 * k + (lane & 31)] = v[k];
            FENCE();
#pragma unroll
            for (int k = 0; k < 8; k++) v[k] = reg[36 * l3 + 4 * k + c2p];
            dft8<false>(v, kc);
#pragma unroll
            for (int k = 0; k < 8; k++) v[k] = cmul<true>(v[k], twT[k]);
            FENCE();
            *(LAS f2*)(lds + x3a[0]) = v[0];
            *(LAS f2*)(lds + x3a[1]) = v[1]; *(LAS f2*)(lds + x3a[1] + 1152) = v[2]; *(LAS f2*)(lds + x3a[1] + 2304) = v[3];
            *(LAS f2*)(lds + x3a[2]) = v[4];
            *(LAS f2*)(lds + x3a[3] + 2304) = v[5]; *(LAS f2*)(lds + x3a[3] + 1152) = v[6]; *(LAS f2*)(lds + x3a[3]) = v[7];
            FENCE();
#pragma unroll
            for (int i = 0; i < 4; i++) {
                u4 o = *(LAS u4*)(lds + L_FFT + w * 4608 + (i * 8 + (lane >> 3)) * 144 + (lane & 7) * 16);
                st128(o, rs, NOF(f1st), i * (64 * 16384));
            }
        }
        STAMP(3);
        __builtin_amdgcn_s_setprio(0);
        asm volatile("s_waitcnt vmcnt(4)" : "+v"(pk[0]), "+v"(pk[1]), "+v"(pk[2]), "+v"(pk[3]), "+v"(pk[4]), "+v"(pk[5]), "+v"(pk[6]), "+v"(pk[7]),
                     "+v"(fr[0]), "+v"(fr[1]), "+v"(fr[2]), "+v"(fr[3]), "+v"(fr[4]), "+v"(fr[5]), "+v"(fr[6]), "+v"(fr[7]),
                     "+v"(fr[8]), "+v"(fr[9]), "+v"(fr[10]), "+v"(fr[11]), "+v"(fr[12]), "+v"(fr[13]), "+v"(fr[14]), "+v"(fr[15]) :: "memory");
        {
            const int so = sig < 4 * NFB ? WS_WDP + ((tcol * NFB + fb) * 4 + 2 * jj) * 1024 : WS_BYTES;
            u4 oe = {__builtin_amdgcn_perm(pk[1], pk[0], 0x05040100), __builtin_amdgcn_perm(pk[3], pk[2], 0x05040100), __builtin_amdgcn_perm(pk[5], pk[4], 0x05040100), __builtin_amdgcn_perm(pk[7], pk[6], 0x05040100)};
            u4 oo = {__builtin_amdgcn_perm(pk[1], pk[0], 0x07060302), __builtin_amdgcn_perm(pk[3], pk[2], 0x07060302), __builtin_amdgcn_perm(pk[5], pk[4], 0x07060302), __builtin_amdgcn_perm(pk[7], pk[6], 0x07060302)};
            st128(oe, rs, l16, so);
            st128(oo, rs, l16, so + 1024);
        }
        {
            const int q1 = lane >> 3, vt = lane & 7, srcl = (q1 + 8 * vt) * 4;
#pragma unroll
            for (int rr = 0; rr < 2; rr++) {
                const int u = 8 * sig + 2 * rq + rr, u1 = u & 63, u2_ = u >> 6;
                int cls, m, sub;
                if ((u1 & 31) != 0) { sub = u1 >> 5; cls = sub ? 64 - u1 : u1; m = sub ? ((8 - q1) & 7) : q1; }
                else { cls = 0; const int hi = u1 >> 5; if ((q1 & 3) == 0) { m = q1; sub = hi; } else { sub = (q1 >> 2) ^ hi; m = sub ? 8 - q1 : q1; } }
                u4 o;
                o.x = __builtin_amdgcn_ds_bpermute(srcl, fr[rr * 4 + 0] | (fr[8 + rr * 4 + 0] << 16)); o.y = __builtin_amdgcn_ds_bpermute(srcl, fr[rr * 4 + 1] | (fr[8 + rr * 4 + 1] << 16));
                o.z = __builtin_amdgcn_ds_bpermute(srcl, fr[rr * 4 + 2] | (fr[8 + rr * 4 + 2] << 16)); o.w = __builtin_amdgcn_ds_bpermute(srcl, fr[rr * 4 + 3] | (fr[8 + rr * 4 + 3] << 16));
                st128(o, rs, NOF(WS_WP + (cls * 8 + m) * 8192 + sub * 4096 + u2_ * 128 + vt * 16), 0);
            }
        }
        WAITVM(4);
        flag_store(WS_FF1 + ((rq * 4 + ((s2 & 7) >> 1)) * 64 + (s2 >> 3) * 16 + (s2 & 1) * 8 + t) * 8);
        STAMP(4);
        f4 accA[4], accB[4], acs = {0, 0, 0, 0};
#pragma unroll
        for (int j = 0; j < 4; j++) { accA[j] = f4{0, 0, 0, 0}; accB[j] = f4{0, 0, 0, 0}; }
        wait_grp(1, B_W1);
        __builtin_amdgcn_s_setprio(1);
        gemm1_half(IC<0>{}, xa, xb, accA, accB, acs, [&](auto S) {
            if (S == 3) {
                WAITVM(1);
                flag_store(sig < 4 * NFB ? WS_FP + (((sig & 1) * 4 + rq) * 128 + (sig >> 1)) * 8 : WS_BYTES);
                flag_store(WS_FW + (((sig & 7) * 4 + rq) * 32 + (sig >> 3)) * 8);
            }
        });
        pin9(accA, accB, acs);
        post(A_S0);
        STAMP(19);
        RELANE();
        int pos1f;
        {
        f2 tw0[4], tw1[8], al[8], be[8];
        const int rb = qb, r3 = 2 * rq + rb;
        const int cls2 = sig >> 3, m2 = sig & 7;
        const int wq2 = (m2 < 4) ? m2 : ((8 - m2) & 3), pos1 = wq2 * 64 + cls2 * 2 + (m2 >> 2);
        const bool special = (cls2 == 0) && ((m2 & 3) == 0);
        int u1a, q1a, u1b, q1b;
        if (cls2 != 0) { u1a = cls2; q1a = m2; u1b = 64 - cls2; q1b = (8 - m2) & 7; }
        else if (special) { u1a = 0; u1b = 32; q1a = q1b = m2; }
        else { u1a = u1b = (m2 < 4) ? 0 : 32; q1a = m2; q1b = 8 - m2; }
        int sA, vH0, sB, vH1;
        if (!special) { sA = 0; vH0 = rq; sB = 1; vH1 = (u1a == 0) ? ((4 - rq) & 3) : 3 - rq; }
        else { sA = sB = rq >> 1; vH0 = rq & 1; vH1 = (rq == 0) ? 2 : (rq == 1) ? 3 : (rq == 2) ? 3 : 2; }
        const bool q0 = (m2 == 0);
        const int dy = ((sA ? u1b : u1a) == 0 && vH0 == 0) ? 8 : 7, dq = q0 ? 8 : 7;
        int y1 = lane >> 3, t1 = lane & 7, y2 = (dy - y1) & 7, t2 = (dq - t1) & 7, hA = vH0, hB = vH1;
        bool self = false;
        if (special && rq == 0) {
            const int k = lane & 31;
            if (qb) { hA = hB = 2; y1 = k >> 3; t1 = k & 7; y2 = 7 - y1; t2 = (dq - t1) & 7; }
            else {
                hA = hB = 0; const int vx = k & 3, y = k >> 2;
                if (!q0 || vx) { y1 = y; t1 = vx; y2 = (8 - y) & 7; t2 = dq - vx; }
                else { t1 = t2 = (y >= 4) ? 4 : 0; const int yy = y & 3; if (yy) { y1 = yy; y2 = 8 - yy; } else { y1 = 0; y2 = 4; self = true; } }
            }
        }
        const int uA = (sA ? u1b : u1a) + 64 * (hA + 4 * y1), uB = (sB ? u1b : u1a) + 64 * (hB + 4 * y2);
        const int qA = (sA ? q1b : q1a) + 8 * t1, qB = (sB ? q1b : q1a) + 8 * t2;
        const int rowB = L_FFT + ((qb ? (vH1 * 2 + sB) : (vH0 * 2 + sA)) * 36 + (lane & 31)) * 8;
        const bool rot = q0 && (t1 == 0);
        const int p3a1 = L_FFT + (y1 * 290 + (hA * 2 + sA) * 36 + t1 * 4) * 8;
        const int p3a2 = L_FFT + (y2 * 290 + (hB * 2 + sB) * 36 + t2 * 4) * 8;
        const float thA = (float)qA * (1.0f / 512.0f), thB = (float)qB * (1.0f / 512.0f);
#pragma unroll
        for (int k = 1; k < 4; k++) tw0[k] = cis((float)r3 * ((float)k / 32.0f));
#pragma unroll
        for (int k = 1; k < 8; k++) tw1[k] = cis((float)c2p * ((float)k / 32.0f));
        f2 csq[8];
#pragma unroll
        for (int e = 0; e < 8; e++) csq[e] = cis(((e >> 2) ? thB : thA) + 0.125f * (float)(e & 3));
        asm volatile("" : "+v"(csq[0]), "+v"(csq[1]), "+v"(csq[2]), "+v"(csq[3]), "+v"(csq[4]), "+v"(csq[5]), "+v"(csq[6]), "+v"(csq[7]),
                     "+v"(tw0[1]), "+v"(tw0[2]), "+v"(tw0[3]), "+v"(tw1[1]), "+v"(tw1[2]), "+v"(tw1[3]), "+v"(tw1[4]), "+v"(tw1[5]), "+v"(tw1[6]), "+v"(tw1[7]));
        flag_wait(lane * 8, WS_FF1 + (wq2 * 4 + rq) * 512);
        wait_grp(0, A_S0);
        STAMP(21);
        const int f1off = WS_F1 + sig * 16384 + 2 * rq * 512;
        u4 d[4];
        SCHED();
#pragma unroll
        for (int H = 0; H < 4; H++) d[H] = __builtin_amdgcn_raw_buffer_load_b128(rs, NOF(l16), f1off + H * 4096, 16);
        SCHED();
        {
            const u32 o1 = min(rq * 22 + lane, 85) * 8, o2 = (((qb ? u1b : u1a) >> 1) * 32 + (lane & 31)) * 8;
            _Pragma("unroll 1") for (int spins = 0; spins < SPIN_CAP; spins++) {
                u2 f1 = __builtin_amdgcn_raw_buffer_load_b64(rs, o1, WS_FP + t * 1024, 16);
                u2 f2_ = __builtin_amdgcn_raw_buffer_load_b64(rs, o2, WS_FW, 16);
                if (__all(f1.x == tag.x && f1.y == tag.y && f2_.x == tag.x && f2_.y == tag.y)) break;
                __builtin_amdgcn_s_sleep(8);
                FENCE();
            }
            __builtin_amdgcn_fence(__ATOMIC_ACQUIRE, "wavefront");
            FENCE();
        }
        post(A_FP);
        const u4 wa4 = __builtin_amdgcn_raw_buffer_load_b128(rs, (hA + 4 * y1) * 128 + t1 * 16, WS_WP + sig * 8192 + sA * 4096, 16);
        const u4 wb4 = __builtin_amdgcn_raw_buffer_load_b128(rs, (hB + 4 * y2) * 128 + t2 * 16, WS_WP + sig * 8192 + sB * 4096, 16);
        SCHED();
        STAMP(5);
        __builtin_amdgcn_s_setprio(2);
        f2 e0[4], e1[4];
#pragma unroll
        for (int H = 0; H < 4; H++) { e0[H] = f2{__uint_as_float(d[H].x), __uint_as_float(d[H].y)}; e1[H] = f2{__uint_as_float(d[H].z), __uint_as_float(d[H].w)}; }
        dft4<false>(e0[0], e0[1], e0[2], e0[3], kc); dft4<false>(e1[0], e1[1], e1[2], e1[3], kc);
        STAMP(6);
        LAS char* area = lds + L_FFT + rq * 4640;
#pragma unroll
        for (int k = 0; k < 4; k++) {
            if (k) { e0[k] = cmul<true>(e0[k], tw0[k]); e1[k] = cmul<true>(e1[k], tw0[k]); }
            *(LAS f4*)(area + (l3 * 72 + 2 * c2p + 8 * k + 32 * rb) * 8) = f4{e0[k].x, e0[k].y, e1[k].x, e1[k].y};
        }
        FENCE();
#pragma unroll
        for (int k = 0; k < 8; k++) v[k] = *(LAS f2*)(area + (k * 72 + lane) * 8);
        dft8<false>(v, kc);
#pragma unroll
        for (int k = 1; k < 8; k++) v[k] = cmul<true>(v[k], tw1[k]);
        FENCE();
        LAS char* pb = lds + L_FFT + (r3 * 290 + l3 * 36 + c2p) * 8;
#pragma unroll
        for (int k = 0; k < 8; k++) *(LAS f2*)(pb + k * 32) = v[k];
        post(A_E1);
        f2 wS[2] = {f2{0.f, 0.f}, f2{0.f, 0.f}};
        if (q0) {
            wS[0] = f2{bflo(a.wr[uA * 257 + 256]), bflo(a.wi[uA * 257 + 256])};
            wS[1] = f2{bflo(a.wr[uB * 257 + 256]), bflo(a.wi[uB * 257 + 256])};
        }
        {
            f2 W[8], WB[8];
#pragma unroll
            for (int e = 0; e < 4; e++) { W[e] = bfup(wa4[e]); W[4 + e] = bfup(wb4[e]); }
#pragma unroll
            for (int k = 0; k < 4; k++) { WB[k] = W[4 + 3 - k]; WB[4 + k] = W[3 - k]; }
            if (q0) {
#pragma unroll
                for (int k = 0; k < 4; k++) {
                    f2 n1 = rot ? W[(4 - k) & 3] : W[3 - k], n2 = rot ? W[4 + ((4 - k) & 3)] : W[4 + 3 - k];
                    WB[k] = self ? n1 : n2; WB[4 + k] = self ? n2 : n1;
                }
#pragma unroll
                for (int e = 0; e < 8; e += 4) {
                    f2 own = wS[e >> 2], oth = self ? own : wS[1 - (e >> 2)];
                    f2 S0 = pkfma(WB[e], kc.pm, W[e]) * 0.5f, S1c = pkfma(own, kc.pm, oth) * 0.5f;
                    WB[e] = rot ? S1c : WB[e];
                    al[e] = rot ? S0 : W[e];
                }
                W[0] = al[0]; W[4] = al[4];
            }
#pragma unroll
            for (int e = 0; e < 8; e++) {
                const f2 cs = csq[e];
                f2 P = pkfma(WB[e], kc.pm, W[e]), M = pkfma(WB[e], kc.pm.yx, W[e]);
                asm("v_pk_fma_f32 %0, %1, %2, %3 op_sel:[1,0,0] op_sel_hi:[1,1,1] neg_lo:[1,0,0] neg_hi:[1,0,0]" : "=v"(al[e]) : "v"(cs), "v"(M), "v"(P));
                asm("v_pk_mul_f32 %0, %1, %2 op_sel:[1,0] op_sel_hi:[0,0] neg_lo:[1,0]" : "=v"(be[e]) : "v"(M), "v"(cs));
            }
            asm volatile("" : "+v"(al[0]), "+v"(al[1]), "+v"(al[2]), "+v"(al[3]), "+v"(al[4]), "+v"(al[5]), "+v"(al[6]), "+v"(al[7]),
                         "+v"(be[0]), "+v"(be[1]), "+v"(be[2]), "+v"(be[3]), "+v"(be[4]), "+v"(be[5]), "+v"(be[6]), "+v"(be[7]),
                         "+v"(tw0[1]), "+v"(tw0[2]), "+v"(tw0[3]), "+v"(tw1[1]), "+v"(tw1[2]), "+v"(tw1[3]), "+v"(tw1[4]), "+v"(tw1[5]), "+v"(tw1[6]), "+v"(tw1[7]));
        }
        wait_grp(0, A_E1);
#pragma unroll
        for (int k = 0; k < 8; k++) v[k] = *(LAS f2*)(lds + rowB + k * 2320);
        dft8<false>(v, kc);
#pragma unroll
        for (int k = 0; k < 8; k++) *(LAS f2*)(lds + rowB + k * 2320) = v[k];
        FENCE();
        f4 a0 = *(LAS f4*)(lds + p3a1), a1 = *(LAS f4*)(lds + p3a1 + 16), b0 = *(LAS f4*)(lds + p3a2), b1 = *(LAS f4*)(lds + p3a2 + 16);
        f2 X[8] = {a0.xy, a0.zw, a1.xy, a1.zw, b0.xy, b0.zw, b1.xy, b1.zw};
        dft4<false>(X[0], X[1], X[2], X[3], kc); dft4<false>(X[4], X[5], X[6], X[7], kc);
        f2 Y[8];
#pragma unroll
        for (int k = 0; k < 4; k++) { Y[k] = X[4 + 3 - k]; Y[4 + k] = X[3 - k]; }
        if (q0) {
#pragma unroll
            for (int k = 0; k < 4; k++) {
                f2 n1 = rot ? X[(4 - k) & 3] : X[3 - k], n2 = rot ? X[4 + ((4 - k) & 3)] : X[4 + 3 - k];
                Y[k] = self ? n1 : n2; Y[4 + k] = self ? n2 : n1;
            }
        }
        f2 O[8];
#pragma unroll
        for (int e = 0; e < 8; e++) O[e] = cfmac_conj(cmul<false>(X[e], al[e]), be[e], Y[e]);
        dft4<true>(O[0], O[1], O[2], O[3], kc); dft4<true>(O[4], O[5], O[6], O[7], kc);
        *(LAS f4*)(lds + p3a1) = f4{O[0].x, O[0].y, O[1].x, O[1].y}; *(LAS f4*)(lds + p3a1 + 16) = f4{O[2].x, O[2].y, O[3].x, O[3].y};
        *(LAS f4*)(lds + p3a2) = f4{O[4].x, O[4].y, O[5].x, O[5].y}; *(LAS f4*)(lds + p3a2 + 16) = f4{O[6].x, O[6].y, O[7].x, O[7].y};
        FENCE();
#pragma unroll
        for (int k = 0; k < 8; k++) v[k] = *(LAS f2*)(lds + rowB + k * 2320);
        dft8<true>(v, kc);
#pragma unroll
        for (int k = 0; k < 8; k++) *(LAS f2*)(lds + rowB + k * 2320) = v[k];
        post(A_E2);
        wait_grp(0, A_E2);
#pragma unroll
        for (int k = 0; k < 8; k++) v[k] = *(LAS f2*)(pb + k * 32);
#pragma unroll
        for (int k = 1; k < 8; k++) v[k] = cmul<false>(v[k], tw1[k]);
        dft8<true>(v, kc);
        FENCE();
#pragma unroll
        for (int k = 0; k < 8; k++) *(LAS f2*)(area + (k * 72 + lane) * 8) = v[k];
        FENCE();
#pragma unroll
        for (int k = 0; k < 4; k++) {
            f4 q = *(LAS f4*)(area + (l3 * 72 + 2 * c2p + 8 * k + 32 * rb) * 8);
            e0[k] = q.xy; e1[k] = q.zw;
            if (k) { e0[k] = cmul<false>(e0[k], tw0[k]); e1[k] = cmul<false>(e1[k], tw0[k]); }
        }
        dft4<true>(e0[0], e0[1], e0[2], e0[3], kc); dft4<true>(e1[0], e1[1], e1[2], e1[3], kc);
        const int f2st = WS_F2 + (r3 * 8 + l3) * 16384 + pos1 * 64 + c2p * 16;
#pragma unroll
        for (int H = 0; H < 4; H++) {
            u4 o = {__float_as_uint(e0[H].x), __float_as_uint(e0[H].y), __float_as_uint(e1[H].x), __float_as_uint(e1[H].y)};
            st128(o, rs, NOF(f2st), H * (64 * 16384));
        }
            pos1f = pos1;
        }
        STAMP(7);
        __builtin_amdgcn_s_setprio(1);
        RELANE();
        wait_grp(1, B_CS);
        gemm1_half(IC<8>{}, xa, xb, accA, accB, acs, [&](auto S) { if (S == 5) { WAITVM(0); flag_store(WS_FF2 + (rq * 256 + pos1f) * 8); } });
        STAMP(8);
        RELANE();
        gemm1_epi(accA, accB, acs, A_CS);
        __builtin_amdgcn_s_setprio(0);
        sfor<4>([&](auto I) { dma_wd(IC<L_SET1>{}, I, 0); });
        WAITVM(16);
        flag_store(WS_FHA + ((rg * 8 + w) * 16 + cg) * 8);
        STAMP(9);
        {
            u4 d[4];
        fft13_consts(0.5f / 524288.0f);
        flag_wait(lane * 8, WS_FF2 + (((s2 & 7) >> 1) * 256 + rq * 64) * 8);
        const int off = WS_F2 + pid * 16384 + rq * 4096;
#pragma unroll
        for (int i = 0; i < 4; i++) d[i] = __builtin_amdgcn_raw_buffer_load_b128(rs, NOF(l16), off + i * 1024, 16);
        STAMP(13);
        __builtin_amdgcn_s_setprio(2);
#pragma unroll
        for (int i = 0; i < 4; i++) *(LAS u4*)(lds + L_FFT + rq * 4608 + (i * 8 + (lane >> 3)) * 144 + (lane & 7) * 16) = d[i];
        FENCE();
        v[0] = *(LAS f2*)(lds + x3a[0]);
        v[1] = *(LAS f2*)(lds + x3a[1]); v[2] = *(LAS f2*)(lds + x3a[1] + 1152); v[3] = *(LAS f2*)(lds + x3a[1] + 2304);
        v[4] = *(LAS f2*)(lds + x3a[2]);
        v[5] = *(LAS f2*)(lds + x3a[3] + 2304); v[6] = *(LAS f2*)(lds + x3a[3] + 1152); v[7] = *(LAS f2*)(lds + x3a[3]);
#pragma unroll
        for (int k = 0; k < 8; k++) v[k] = cmul<false>(v[k], twT[k]);
        dft8<true>(v, kc);
#pragma unroll
        for (int k = 1; k < 8; k++) v[k] = cmul<false>(v[k], tw64[k]);
        FENCE();
        LAS f2* reg = (LAS f2*)(lds + L_FFT) + (rq * 2 + qb) * 288;
#pragma unroll
        for (int k = 0; k < 8; k++) reg[36 * l3 + 4 * k + c2p] = v[k];
        FENCE();
#pragma unroll
        for (int k = 0; k < 8; k++) v[k] = reg[36 * k + (lane & 31)];
        dft8<true>(v, kc);
        FENCE();
#pragma unroll
        for (int k = 0; k < 8; k++) reg[(lane & 31) + 32 * k] = v[k];
        post(A_F3);
        wait_grp(0, A_F3);
        if (b == 0 && rq == 0) __builtin_amdgcn_raw_buffer_store_b64(*(LAS u2*)(lds + L_TAG + 8), rs, lane ? WS_BYTES : 0, 0, 16);
        LAS f2* xb1 = (LAS f2*)(lds + L_FFT) + rq * 64 + lane;
#pragma unroll
        for (int q1 = 0; q1 < 8; q1++) { const int rg_ = (q1 < 4) ? q1 * 2 : (((8 - q1) & 3) * 2 + 1); v[q1] = xb1[rg_ * 288]; }
        post(A_F4);
        dft8<true>(v, kc);
        wait_grp(0, A_F4);
#pragma unroll
        for (int c1 = 0; c1 < 8; c1++) *(LAS u32*)(lds + L_S + (16 * rq + (lane >> 2)) * 144 + c1 * 16 + c2p * 4) = pkbf(v[c1]);
        }
        STAMP(10);
        u4 hfr[16];
        flag_wait(li * 8, WS_FHA + (rg * 8 + w) * 128);
#pragma unroll
        for (int i = 0; i < 16; i++) { hfr[i] = __builtin_amdgcn_raw_buffer_load_b128(rs, l16, hsrc + 2 * ((i + rot) & 15) * 1024, 16); SCHED(); }
        STAMP(17);
        g2_mma(IC<16>{}, hfr, L_SET1);
    } else {
        u4 xa[16], xb[16];
        __builtin_amdgcn_s_sleep(2);
        sfor<8>([&](auto I) { dma_w1(IC<0>{}, I); });
        wait_pair(A_XL);
        read_tile(xa, 0);
        asm volatile("s_waitcnt lgkmcnt(0)" ::: "memory");
        wait_pair(A_XC);
        dma_x();
        sfor<8>([&](auto I) { dma_w1(IC<1>{}, I); });
        STAMP(1);
        asm volatile("s_waitcnt vmcnt(8)" : "+v"(cpf) :: "memory");
        post(B_W0);
        read_tile(xb, 1);
        STAMP(2);
        {
            f4 accA[4], accB[4], acs = {0, 0, 0, 0};
#pragma unroll
            for (int j = 0; j < 4; j++) { accA[j] = f4{0, 0, 0, 0}; accB[j] = f4{0, 0, 0, 0}; }
            wait_grp(1, B_W0);
            STAMP(3);
            gemm1_half(IC<0>{}, xa, xb, accA, accB, acs, [&](auto S) {
                constexpr int s = S;
                if (s == 1) { WAITVM(0); post(B_W1); wait_pair(A_XB); }
                if (s == 3) wait_grp(1, B_W1);
                if constexpr (s >= 1 && s <= 4) sfor<4>([&](auto I) { dma_w1(IC<8 + ((s - 1) >> 1)>{}, IC<((s - 1) & 1) * 4 + I>{}); });
                if (s == 7) { WAITVM(8); post(B_W2); }
            });
            STAMP(14);
            wait_grp(1, B_W2);
            get_tag();
            gemm1_half(IC<8>{}, xa, xb, accA, accB, acs, [&](auto S) {
                if (S == 1) { WAITVM(0); post(B_W3); }
                if (S == 3) wait_grp(1, B_W3);
            });
            STAMP(4);
            RELANE();
            pin9(accA, accB, acs);
            if (nblk == 3) {
                const u32 wvB = (u32)((lane >> 2) * 2752 + (((lane & 3) ^ (((lane >> 5) & 1) << 1)) * 16));
                const char* wbs = (const char*)((rq >> 1) ? a.wu : a.wg) + (size_t)((rq & 1) * 16) * 2752 + fb2 * 64;
                wait_grp(0, A_S0);
                sfor<16>([&](auto K) { constexpr int k = K; dma_add<k * 4096, IX(k) * 32 * 2752>(wvB, wbs + ((BE(k) + j4) & 3) * (128 * 2752), (u32)(rq * 1024)); });
            }
            SCHED();
            stats(xb);
            STAMP(15);
            gemm1_epi(accA, accB, acs, B_CS);
        }
        STAMP(5);
        if (nblk == 3) {
            WAITVM(2); post(B_WB);
            wait_grp(1, B_WB);
            const u32 tb = (u32)((8 * lg + (li >> 2)) * 64 + (lg & 1) * 32 + 8 * (li & 3));
            LAS char* p0 = lds + tb; LAS char* p1 = lds + (tb ^ 32);
            LAS char* pc = lds + (tb ^ ((rq & 1) * 32)) + (rq >> 1) * 2048;
            f4 acc[2][4], acs = {0, 0, 0, 0};
#pragma unroll
            for (int j = 0; j < 4; j++) { acc[0][j] = f4{0, 0, 0, 0}; acc[1][j] = f4{0, 0, 0, 0}; }
            u4 F[2][5];
            auto ldf = [&](int ks, u4 (&f)[5]) {
                const int off = ks * 4096;
                f[4] = trfrag2<256>(pc + off);
                f[0] = trfrag2<256>(p0 + off); f[1] = trfrag2<256>(p1 + off); f[2] = trfrag2<256>(p0 + off + 2048); f[3] = trfrag2<256>(p1 + off + 2048);
            };
            ldf(0, F[0]);
            sfor<16>([&](auto S) {
                constexpr int ks = S;
                if (ks == 5) { WAITVM(0); flag_store(WS_FHA + ((rg * 8 + w) * 16 + cg) * 8); }
                SCHED();
                if (ks + 1 < 16) ldf(ks + 1, F[(ks + 1) & 1]);
                u4 (&f)[5] = F[ks & 1];
                acs = mfma(f[4], ones, acs);
#pragma unroll
                for (int j = 0; j < 4; j++) { acc[0][j] = mfma(f[j], xa[ks], acc[0][j]); acc[1][j] = mfma(f[j], xb[ks], acc[1][j]); }
                if (ks + 1 < 16) {
                    SGB(0x100, 2);
#pragma unroll
                    for (int i = 0; i < 8; i++) { SGB(0x8, 1); SGB(0x100, 1); }
                    SGB(0x8, 1);
                }
                SCHED();
            });
            if (li == 0) *(LAS f4*)(lds + L_CS + 512 + rq * 64 + lg * 16) = acs;
            post(B_PB);
            wait_grp(1, B_PB);
            st128(geglu8(acc[0], lds + L_CS + 512 + lg * 16, st0), rs, l16, hoff0 + fb2 * 1024);
            st128(geglu8(acc[1], lds + L_CS + 512 + lg * 16, st1), rs, l16, hoff1 + fb2 * 1024);
        } else { WAITVM(0); flag_store(WS_FHA + ((rg * 8 + w) * 16 + cg) * 8); }
        STAMP(6);
        RELANE();
        wait_grp(0, A_FP);
        sfor<4>([&](auto I) { dma_wd(IC<0>{}, I, 1); });
        if (nblk == 3) { WAITVM(16); flag_store(WS_FHB + ((rg * 4 + rq) * 16 + fb2 - 32) * 8); }
        u4 hfr[16];
        flag_wait(li * 8, WS_FHA + (rg * 8 + w) * 128);
        post(B_D1);
#pragma unroll
        for (int i = 0; i < 16; i++) { hfr[i] = __builtin_amdgcn_raw_buffer_load_b128(rs, l16, hsrc + 2 * ((i + rot) & 15) * 1024, 16); SCHED(); }
        STAMP(9);
        wait_grp(1, B_D1);
        g2_mma(IC<16>{}, hfr, 0);
        post(B_G1);
        pin4(acc2);
        STAMP(10);
        wait_grp(1, B_G1);
        sfor<3>([&](auto I) { constexpr int i = I; dma4k_c<2 * PG + i * 16384, 0>(l16, wdp + (size_t)min(32 + 4 * i + rq, 42) * 4096, (u32)(rq * 4096)); });
        flag_wait(min(li, 10) * 8, WS_FHB + (rg * 4 + rq) * 128);
        post(B_D3);
#pragma unroll
        for (int i = 0; i < 11; i++) { hfr[i] = __builtin_amdgcn_raw_buffer_load_b128(rs, l16, hoff0 + (32 + i) * 1024, 16); SCHED(); }
        STAMP(13);
        wait_grp(1, B_D3);
        g2_mma(IC<11>{}, hfr, 2 * PG);
    }
    RELANE();
    STAMP(11);
    BAR();
    {
        LAS f4* xs = (LAS f4*)(lds + w * 2048) + lane;
        LAS f4* xr = (LAS f4*)(lds + (w ^ 4) * 2048) + lane;
        f4 me, mo;
        if (hf) { xs[0] = acc2[0]; xs[64] = acc2[1]; me = acc2[2]; mo = acc2[3]; } else { xs[0] = acc2[2]; xs[64] = acc2[3]; me = acc2[0]; mo = acc2[1]; }
        BAR();
        const int c1 = 4 * hf + lg;
        me += xr[0]; mo += xr[64];
        const u4 sv = *(LAS u4*)(lds + L_S + (16 * rq + li) * 144 + c1 * 16);
        const u4 xv = *(LAS u4*)(lds + ((rq >> 1) ? L_X1 : L_X0) + ((rq & 1) * 16 + li) * 128 + ((c1 ^ (li & 7)) * 16));
        u32 res[4];
#pragma unroll
        for (int e = 0; e < 4; e++) {
            const f2 ov = bfup(pkbf(f2{me[e], mo[e]}));
            const f2 z = ov * -1.44269504f;
            const f2 d = f2{__builtin_amdgcn_exp2f(z.x), __builtin_amdgcn_exp2f(z.y)} + f2{1.f, 1.f};
            const f2 g = bfup(pkbf(ov * f2{__builtin_amdgcn_rcpf(d.x), __builtin_amdgcn_rcpf(d.y)}));
            res[e] = pkbf(bfup(xv[e]) + bfup(pkbf(bfup(sv[e]) * g)));
        }
        *(u4*)(a.out + (size_t)(s2 + 32 * (16 * rq + li)) * 512 + 64 * c1 + 8 * t) = u4{res[0], res[1], res[2], res[3]};
    }
    STAMP(12);
}

extern "C" void kernel_launch(void* const* d_in, const int* in_sizes, int n_in,
                              void* d_out, int out_size, void* d_ws, size_t ws_size,
                              hipStream_t stream) {
    Args a;
    a.x = (const u16*)d_in[0]; a.wr = (const u16*)d_in[1]; a.wi = (const u16*)d_in[2];
    a.wg = (const u16*)d_in[4]; a.wu = (const u16*)d_in[5]; a.wd = (const u16*)d_in[6];
    a.out = (u16*)d_out; a.ws = (unsigned char*)d_ws;
    (void)hipFuncSetAttribute((const void*)fused, hipFuncAttributeMaxDynamicSharedMemorySize, L_END);
    fused<<<256, 512, L_END, stream>>>(a);
}
```

```cpp
#include <hip/hip_runtime.h>
#include <stdint.h>
#include <type_traits>

typedef unsigned short u16;
typedef unsigned u32;
typedef unsigned long long u64;
typedef float f2 __attribute__((ext_vector_type(2)));
typedef float f4 __attribute__((ext_vector_type(4)));
typedef u32 u4 __attribute__((ext_vector_type(4)));
typedef u32 u2 __attribute__((ext_vector_type(2)));
typedef short s4 __attribute__((ext_vector_type(4)));
typedef __bf16 bf8 __attribute__((ext_vector_type(8)));
typedef __bf16 bf2 __attribute__((ext_vector_type(2)));
#define LAS __attribute__((address_space(3)))
#define GAS __attribute__((address_space(1)))
#define DEV __device__ __forceinline__
template <int N> using IC = std::integral_constant<int, N>;

constexpr int NFB = 43;
constexpr int WS_FF1 = 4096;
constexpr int WS_FF2 = WS_FF1 + 8192;
constexpr int WS_FHA = WS_FF2 + 8192;
constexpr int WS_FHB = WS_FHA + 16384;
constexpr int WS_FP = WS_FHB + 16384;
constexpr int WS_FW = WS_FP + 8192;
constexpr int WS_F1 = 1 << 20, WS_F2 = WS_F1 + (4 << 20), WS_H = WS_F2 + (4 << 20);
constexpr int WS_WDP = WS_H + 32 * 4 * NFB * 1024;
constexpr int WS_WP = WS_WDP + 8 * NFB * 4 * 1024;
constexpr int WS_STAMP = WS_WP + (2 << 20);
constexpr int WS_BYTES = WS_STAMP + (1 << 20);
constexpr int PG = 8192;
constexpr int L_SET1 = 8 * PG;
constexpr int L_FFT = 16 * PG;
constexpr int L_SMALL = 19 * PG;
constexpr int L_PROG = L_SMALL;
constexpr int L_TAG = L_SMALL + 48;
constexpr int L_STAT = L_SMALL + 64;
constexpr int L_PST = L_SMALL + 576;
constexpr int L_CS = L_SMALL + 2048;
constexpr int L_X0 = L_FFT + 18688, L_X1 = L_SMALL + 3072;
constexpr int L_S = L_FFT;
constexpr int L_END = 20 * PG;
constexpr u32 SENT = 0xFFFFFFFFu;
constexpr int CODE_BYTES = 32 * 1024;
constexpr int SPIN_CAP = 1 << 16;

struct Args { const u16* x; const u16* wr; const u16* wi; const u16* wg; const u16* wu; const u16* wd; u16* out; unsigned char* ws; };

#define BAR() asm volatile("s_waitcnt lgkmcnt(0)\n\ts_barrier" ::: "memory")
#define WAITVM(n) asm volatile("s_waitcnt vmcnt(" #n ")" ::: "memory")
#define NOF(x) (x)
#define NOPL(x) (x)
#define FENCE() asm volatile("" ::: "memory")
#define SCHED() __builtin_amdgcn_sched_barrier(0)
#define SGB(mask, n) __builtin_amdgcn_sched_group_barrier(mask, n, 0)
#define STAMP(i)
#define CSTAMP(i)

DEV f2 pkfma(f2 a, f2 b, f2 c) { return __builtin_elementwise_fma(a, b, c); }
struct KC { f2 pm, rpm; };
DEV f2 addmi(f2 a, f2 b, const KC& k) { return pkfma(b.yx, k.pm, a); }
DEV f2 addpi(f2 a, f2 b, const KC& k) { return pkfma(b.yx, k.pm.yx, a); }
template <bool INV> DEV void dft4(f2& a0, f2& a1, f2& a2, f2& a3, const KC& k) {
    f2 t0 = a0 + a2, t1 = a0 - a2, t2 = a1 + a3, t3 = a1 - a3;
    a0 = t0 + t2; a2 = t0 - t2;
    if (INV) { a1 = addpi(t1, t3, k); a3 = addmi(t1, t3, k); } else { a1 = addmi(t1, t3, k); a3 = addpi(t1, t3, k); }
}
template <bool INV> DEV void dft8(f2 (&v)[8], const KC& k) {
    f2 a0 = v[0] + v[4], a4 = v[0] - v[4], a1 = v[1] + v[5], a5 = v[1] - v[5];
    f2 a2 = v[2] + v[6], a6 = v[2] - v[6], a3 = v[3] + v[7], a7 = v[3] - v[7];
    dft4<INV>(a0, a1, a2, a3, k);
    v[0] = a0; v[2] = a1; v[4] = a2; v[6] = a3;
    f2 b1, b3, t0, t1;
    if (INV) { b1 = addpi(a5, a5, k); b3 = addpi(-a7, a7, k); t0 = addpi(a4, a6, k); t1 = addmi(a4, a6, k); }
    else { b1 = addmi(a5, a5, k); b3 = addmi(-a7, a7, k); t0 = addmi(a4, a6, k); t1 = addpi(a4, a6, k); }
    f2 t2 = b1 + b3, t3 = b1 - b3;
    v[1] = pkfma(t2, k.rpm.xx, t0); v[5] = pkfma(t2, k.rpm.yy, t0);
    if (INV) { v[3] = pkfma(t3.yx, k.rpm.yx, t1); v[7] = pkfma(t3.yx, k.rpm, t1); }
    else { v[3] = pkfma(t3.yx, k.rpm, t1); v[7] = pkfma(t3.yx, k.rpm.yx, t1); }
}
template <bool CONJ> DEV f2 cmul(f2 a, f2 w) {
    f2 r;
    if (CONJ) asm("v_pk_mul_f32 %0, %1, %2 op_sel:[1,1] op_sel_hi:[0,1]\n\tv_pk_fma_f32 %0, %1, %2, %0 op_sel_hi:[1,0,1] neg_hi:[0,0,1]" : "=&v"(r) : "v"(a), "v"(w));
    else asm("v_pk_mul_f32 %0, %1, %2 op_sel:[1,1] op_sel_hi:[0,1]\n\tv_pk_fma_f32 %0, %1, %2, %0 op_sel_hi:[1,0,1] neg_lo:[0,0,1]" : "=&v"(r) : "v"(a), "v"(w));
    return r;
}
DEV f2 cfmac_conj(f2 t, f2 beta, f2 B) {
    asm("v_pk_fma_f32 %0, %2, %1, %0 op_sel_hi:[0,1,1]\n\tv_pk_fma_f32 %0, %2, %1, %0 op_sel:[1,1,0] op_sel_hi:[1,0,1] neg_hi:[0,1,0]" : "+v"(t) : "v"(beta), "v"(B));
    return t;
}
DEV f2 cis(float r) { return f2{__builtin_amdgcn_cosf(r), __builtin_amdgcn_sinf(r)}; }
DEV float bflo(u32 v) { return __uint_as_float(v << 16); }
DEV float bfhi(u32 v) { return __uint_as_float(v & 0xFFFF0000u); }
DEV f2 bfup(u32 v) { return f2{bflo(v), bfhi(v)}; }
DEV u32 pkbf(f2 v) { bf2 r; r.x = (__bf16)v.x; r.y = (__bf16)v.y; u32 o = __builtin_bit_cast(u32, r); asm("" : "+v"(o)); return o; }
DEV float rbf(float f) { return __uint_as_float(((u32)__builtin_bit_cast(u16, (__bf16)f)) << 16); }
DEV f4 mfma(u4 a, u4 b, f4 c) { return __builtin_amdgcn_mfma_f32_16x16x32_bf16(__builtin_bit_cast(bf8, a), __builtin_bit_cast(bf8, b), c, 0, 0, 0); }
DEV u4 sanitize(u4 v) { return u4{min(v.x, 0xFFFFFFFEu), min(v.y, 0xFFFFFFFEu), min(v.z, 0xFFFFFFFEu), min(v.w, 0xFFFFFFFEu)}; }
DEV u32 max4(u4 v) { return max(max(v.x, v.y), max(v.z, v.w)); }
DEV void st128(u4 v, __amdgpu_buffer_rsrc_t rs, u32 voff, int soff) {
    __builtin_amdgcn_raw_buffer_store_b128(v, rs, voff, soff, 16);
    asm volatile("s_nop 1" :: "v"(v) : "memory");
}
template <int OFF2> DEV u4 trfrag2(LAS char* p) {
    s4 r0 = __builtin_amdgcn_ds_read_tr16_b64_v4i16((LAS s4*)p);
    s4 r1 = __builtin_amdgcn_ds_read_tr16_b64_v4i16((LAS s4*)(p + OFF2));
    u2 x0 = __builtin_bit_cast(u2, r0), x1 = __builtin_bit_cast(u2, r1);
    return u4{x0.x, x0.y, x1.x, x1.y};
}
template <int LI, int GI> DEV void dma_add(u32 vb, const void* sbase, u32 sl) {
    const u32 va = vb + (u32)GI;
    asm volatile("s_add_u32 m0, %2, %3\n\ts_nop 0\n\tglobal_load_lds_dwordx4 %0, %1" :: "v"(va), "s"(sbase), "s"(sl), "i"(LI) : "memory", "scc");
}
template <int LI, int GI> DEV void dma4k_c(u32 vb, const void* sbase, u32 sl) {
    const u32 va = vb + (u32)GI;
    asm volatile("s_add_u32 m0, %2, %3\n\ts_nop 0\n\tglobal_load_lds_dwordx4 %0, %1 sc1\n\tglobal_load_lds_dwordx4 %0, %1 offset:1024 sc1\n\t"
                 "global_load_lds_dwordx4 %0, %1 offset:2048 sc1\n\tglobal_load_lds_dwordx4 %0, %1 offset:3072 sc1"
                 :: "v"(va), "s"(sbase), "s"(sl), "i"(LI) : "memory", "scc");
}
template <int OFF> DEV void ldg_u16(u32& d, u32 vo, const void* sb) { asm volatile("global_load_ushort %0, %1, %2 offset:%3" : "=v"(d) : "v"(vo), "s"(sb), "i"(OFF) : "memory"); }
template <int OFF> DEV void ldg_u32(u32& d, u32 vo, const void* sb) { asm volatile("global_load_dword %0, %1, %2 offset:%3" : "=v"(d) : "v"(vo), "s"(sb), "i"(OFF) : "memory"); }
DEV void lds_wait(u32 addr, u32 v) {
    u32 tmp, cnt;
    asm volatile("s_mov_b32 %1, 0x80000\n"
                 "1:\n\tds_read_b32 %0, %2\n\ts_sub_u32 %1, %1, 1\n\ts_waitcnt lgkmcnt(0)\n\ts_cbranch_scc1 2f\n\tv_cmp_gt_u32 vcc, %3, %0\n\ts_cbranch_vccnz 1b\n2:"
                 : "=&v"(tmp), "=&s"(cnt) : "v"(addr), "s"(v) : "memory", "vcc", "scc");
}
template <int N, typename F> DEV void sfor(F&& f) { if constexpr (N > 0) { sfor<N - 1>(f); f(IC<N - 1>{}); } }

DEV void pin9(f4 (&x)[4], f4 (&y)[4], f4& z) {
    asm volatile("" : "+v"(x[0]), "+v"(x[1]), "+v"(x[2]), "+v"(x[3]), "+v"(y[0]), "+v"(y[1]), "+v"(y[2]), "+v"(y[3]), "+v"(z));
}
DEV void pin4(f4 (&x)[4]) { asm volatile("" : "+v"(x[0]), "+v"(x[1]), "+v"(x[2]), "+v"(x[3])); }
DEV void dma_rt(u32 voff, const void* sbase, u32 sl) {
    asm volatile("s_mov_b32 m0, %2\n\ts_nop 0\n\tglobal_load_lds_dwordx4 %0, %1" :: "v"(voff), "s"(sbase), "s"(sl) : "memory");
}
DEV u4 geglu8(const f4 (&acc)[4], LAS char* cs, f2 st) {
    u32 hw[4];
    const f2 nm = f2{-st.x, -st.x}, ri = f2{st.y, st.y};
#pragma unroll
    for (int c = 0; c < 2; c++) {
        f4 cg_ = *(LAS f4*)(cs + c * 64), cu_ = *(LAS f4*)(cs + (2 + c) * 64);
#pragma unroll
        for (int e = 0; e < 2; e++) {
            f2 g = pkfma(e ? cg_.zw : cg_.xy, nm, e ? acc[c].zw : acc[c].xy) * ri;
            f2 u = pkfma(e ? cu_.zw : cu_.xy, nm, e ? acc[2 + c].zw : acc[2 + c].xy) * ri;
            f2 z = g * pkfma(g * g, f2{-0.10294324f, -0.10294324f}, f2{-2.3022082f, -2.3022082f});
            f2 d = f2{__builtin_amdgcn_exp2f(z.x), __builtin_amdgcn_exp2f(z.y)} + f2{1.f, 1.f};
            f2 r = f2{__builtin_amdgcn_rcpf(d.x), __builtin_amdgcn_rcpf(d.y)};
            hw[2 * c + e] = pkbf(g * r * u);
        }
    }
    return u4{hw[0], hw[1], hw[2], hw[3]};
}

enum { F_XC = 1, F_P1, F_C0, F_CS, F_FP, F_E1, F_E2, F_D1, F_D2, F_F3, F_F4, F_S };
enum { G_XL = 1, G_XC, G_W0, G_XD, G_W1, G_S0, G_CS, G_XW, G_EP, G_WB, G_PB, G_XB, G_EB, G_G1, G_G2, G_X };

__global__ void __launch_bounds__(768, 3) fused(Args a) {
    extern __shared__ __attribute__((aligned(16))) char smem[];
    LAS char* lds = (LAS char*)smem;
    const int tid = threadIdx.x, w = __builtin_amdgcn_readfirstlane(tid >> 6);
    int lane = tid & 63;
    const int b = blockIdx.x, xcd = b & 7, r = b >> 3, t = r & 7, par = xcd & 1, rg = (xcd >> 1) * 4 + (r >> 3);
    const int s2 = 2 * rg + par, cg = par * 8 + t, sig = xcd * 32 + r, pid = s2 * 8 + t;
    const int rq = w & 3, hf = w >> 3, j4 = r >> 3;
    u32 l16 = lane * 16;
    u64 pc = __builtin_amdgcn_s_getpc() & ~(u64)255;
    asm volatile("" : "+s"(pc));
    *(volatile LAS u32*)(lds + L_PROG + w * 4) = 0;
    BAR();
    STAMP(0);
    auto dma_x = [&](int tile) {
        const char* xs = (const char*)a.x + (size_t)((s2 ^ tile) + 512 * rq) * 1024;
#pragma unroll
        for (int i = 0; i < 16; i++) { const int row = (i + 2 * t) & 15; dma_rt(l16 ^ (u32)(row * (32768 + 16)), xs, (u32)(L_SET1 + rq * 16384 + row * 1024)); }
    };
    if (w >= 4 && w < 8) { __builtin_amdgcn_s_setprio(1); dma_x(0); }
    int li, lg, c2p, l3, qb; u32 vpg;
#define RELANE() do { asm volatile("v_mbcnt_lo_u32_b32 %0, -1, 0\n\tv_mbcnt_hi_u32_b32 %0, -1, %0" : "=v"(lane)); l16 = lane * 16; li = lane & 15; lg = lane >> 4; c2p = lane & 3; l3 = (lane >> 2) & 7; qb = lane >> 5; vpg = (u32)(L_PROG + (lane & 3) * 4); } while (0)
    const __amdgpu_buffer_rsrc_t rs = __builtin_amdgcn_make_buffer_rsrc(a.ws, 0, WS_BYTES, 0x00020000);
    KC kc; kc.pm = f2{1.f, -1.f}; kc.rpm = f2{0.70710678118654752f, -0.70710678118654752f};
    asm volatile("" : "+s"(kc.pm), "+s"(kc.rpm));
    const u4 ones = {0x3F803F80u, 0x3F803F80u, 0x3F803F80u, 0x3F803F80u};
    auto post = [&](u32 v) { asm volatile("s_waitcnt lgkmcnt(0)" ::: "memory"); *(volatile LAS u32*)(lds + L_PROG + w * 4) = v; FENCE(); };
    auto wait_grp = [&](int g, u32 v) { lds_wait(vpg + g * 16, v); };
    auto wait_one = [&](int ww, u32 v) { lds_wait((u32)(L_PROG + ww * 4), v); };
    auto wait_G = [&](u32 v) { lds_wait((u32)(L_PROG + 16 + (lane & 7) * 4), v); };
    u2 tag;
    auto get_tag = [&]() { u2 tg = *(LAS u2*)(lds + L_TAG); tag.x = __builtin_amdgcn_readfirstlane(tg.x); tag.y = __builtin_amdgcn_readfirstlane(tg.y); };
    auto flag_store = [&](int off) { __builtin_amdgcn_raw_buffer_store_b64(tag, rs, lane ? WS_BYTES : 0, off, 16); };
    auto flag_wait = [&](u32 voff, int soff) {
        _Pragma("unroll 1") for (int spins = 0; spins < SPIN_CAP; spins++) {
            u2 f = __builtin_amdgcn_raw_buffer_load_b64(rs, voff, soff, 16);
            if (__all(f.x == tag.x && f.y == tag.y)) break;
            __builtin_amdgcn_s_sleep(16);
            FENCE();
        }
        __builtin_amdgcn_fence(__ATOMIC_ACQUIRE, "wavefront");
        FENCE();
    };
    auto stats = [&](const u4 (&x)[16]) {
        f4 s1 = {0, 0, 0, 0}, sq = s1, s1b = s1, sqb = s1;
#pragma unroll
        for (int k = 0; k < 16; k += 2) { s1 = mfma(ones, x[k], s1); sq = mfma(x[k], x[k], sq); s1b = mfma(ones, x[k + 1], s1b); sqb = mfma(x[k + 1], x[k + 1], sqb); }
        sq += sqb;
        const int rr = li & 3;
        const float d = rr == 0 ? sq.x : rr == 1 ? sq.y : rr == 2 ? sq.z : sq.w;
        if (lg == (li >> 2)) {
            const float mean = (s1.x + s1b.x) * (1.0f / 512.0f);
            float var = d * (1.0f / 512.0f) - mean * mean;
            if (!(mean * mean <= 1024.0f * var)) {
                const u32* xrow = (const u32*)(a.x + (size_t)((s2 ^ hf) + 32 * (16 * rq + li)) * 512);
                float q = 0.f;
                _Pragma("unroll 1") for (int i = 0; i < 256; i++) { u32 v = xrow[i]; float d0 = bflo(v) - mean, d1 = bfhi(v) - mean; q += d0 * d0; q += d1 * d1; }
                var = q * (1.0f / 512.0f);
            }
            const float den = rbf(__builtin_amdgcn_sqrtf(rbf(rbf(var) + 1e-5f)));
            *(LAS f2*)(lds + L_STAT + (hf * 64 + 16 * rq + li) * 8) = f2{rbf(mean), __builtin_amdgcn_rcpf(den)};
        }
        FENCE();
    };
    const int nblk = (t < 6 - par) ? 3 : 2, fb2 = 32 + par * 6 + t;
    const char* wdp = (const char*)a.ws + WS_WDP + (size_t)(t * NFB) * 4096;

    f2 tw64[8], twT[8];
    int x3a[4];
    auto fft13_consts = [&](float scale) {
        int lane_ = lane; asm volatile("" : "+v"(lane_));
        const int c2p = lane_ & 3, l3 = (lane_ >> 2) & 7, qb = lane_ >> 5;
        const int wq = rq, q1 = qb ? (wq == 0 ? 4 : 8 - wq) : wq, c2 = 4 * t + c2p;
        const float f64 = (float)l3 * (1.0f / 64.0f);
#pragma unroll
        for (int k = 1; k < 8; k++) tw64[k] = cis(f64 * (float)k);
        const float fT = (float)(8 * c2 * q1 + s2 * l3) * (1.0f / 2048.0f), dT = (float)s2 * (1.0f / 256.0f);
#pragma unroll
        for (int k = 0; k < 8; k++) twT[k] = cis(fT + dT * (float)k) * scale;
#pragma unroll
        for (int k = 0; k < 4; k++) {
            const int ua = (k >> 1) * 4 + (k & 1), u1 = l3 + 8 * ua;
            const int cls = (u1 < 32) ? u1 : ((64 - u1) & 31), hi = u1 >> 5;
            int mbit, sub;
            if (wq == 0) { mbit = qb; sub = hi; } else if (cls != 0) { sub = hi; mbit = qb ^ hi; } else { mbit = hi; sub = qb ^ hi; }
            x3a[k] = L_FFT + wq * 4608 + (cls * 18 + mbit * 8 + sub * 4 + c2p) * 8 - (k == 3 ? 2304 : 0);
        }
        asm volatile("" : "+v"(tw64[1]), "+v"(tw64[2]), "+v"(tw64[3]), "+v"(tw64[4]), "+v"(tw64[5]), "+v"(tw64[6]), "+v"(tw64[7]),
                     "+v"(twT[0]), "+v"(twT[1]), "+v"(twT[2]), "+v"(twT[3]), "+v"(twT[4]), "+v"(twT[5]), "+v"(twT[6]), "+v"(twT[7]),
                     "+v"(x3a[0]), "+v"(x3a[1]), "+v"(x3a[2]), "+v"(x3a[3]));
    };

    RELANE();
    const int rot = (2 * t + 4 * j4) & 15;
    auto dma_wd = [&](auto LI, auto I, int p0) { dma4k_c<LI + I * 16384, 0>(l16, wdp + (size_t)(p0 + ((rq + 4 * I + rot) & 15)) * 4096, (u32)(rq * 4096)); };
    if (w < 4) {
        RELANE();
        const int f1st = WS_F1 + ((lane >> 3) * 8 + (((lane >> 2) & 1) ? (rq == 0 ? 4 : 8 - rq) : rq)) * 16384 + pid * 64 + c2p * 16;
        u32 cpf = 0;
        if (w == 3) {
            const u32 off = min((u32)((t * 64 + lane) * 128), (u32)(CODE_BYTES - 128));
            asm volatile("global_load_dword %0, %1, %2" : "=v"(cpf) : "v"(off), "s"(pc) : "memory");
        }
        u2 ev;
        { u32 zero = 0; asm volatile("global_load_dwordx2 %0, %1, %2 sc1" : "=v"(ev) : "v"(zero), "s"(a.ws) : "memory"); }
        fft13_consts(1.0f);
        __builtin_amdgcn_s_setprio(2);
        u32 raw[8];
        const int rw = lane >> 2;
        wait_one(4 + rq, G_XL);
        STAMP(1);
        {
            u4 xf[16];
            LAS char* p = lds + L_SET1 + (16 * rq + li) * 1024;
#pragma unroll
            for (int k = 0; k < 16; k++) xf[k] = *(LAS u4*)(p + (((4 * k + lg) ^ li) * 16));
#pragma unroll
            for (int c1 = 0; c1 < 8; c1++) raw[c1] = *(LAS u32*)(lds + L_SET1 + (16 * rq + rw) * 1024 + (((8 * c1 + t) ^ rw) * 16) + c2p * 4);
            post(F_XC);
            stats(xf);
        }
        STAMP(2);
        f2 v[8];
        {
            const f2 st = *(LAS f2*)(lds + L_STAT + (16 * rq + rw) * 8);
#pragma unroll
            for (int c1 = 0; c1 < 8; c1++) v[c1] = bfup(pkbf(bfup(pkbf(bfup(raw[c1]) - st.xx)) * st.yy));
            dft8<false>(v, kc);
            LAS f2* xb1 = (LAS f2*)(lds + L_FFT) + w * 64 + lane;
#pragma unroll
            for (int q1 = 0; q1 < 8; q1++) { const int reg = (q1 < 4) ? q1 * 2 : (((8 - q1) & 3) * 2 + 1); xb1[reg * 288] = v[q1]; }
        }
        asm volatile("s_waitcnt vmcnt(0)" : "+v"(ev), "+v"(cpf) :: "memory");
        {
            const u64 epoch = ((u64)(u32)__builtin_amdgcn_readfirstlane(ev.y) << 32) | (u32)__builtin_amdgcn_readfirstlane(ev.x);
            u64 tg = epoch + 0x9E3779B97F4A7C15ull; tg = (tg ^ (tg >> 30)) * 0xBF58476D1CE4E5B9ull; tg = (tg ^ (tg >> 27)) * 0x94D049BB133111EBull; tg ^= tg >> 31;
            tag = u2{(u32)tg, (u32)(tg >> 32)};
            *(LAS u4*)(lds + L_TAG) = u4{(u32)tg, (u32)(tg >> 32), (u32)(epoch + 1), (u32)((epoch + 1) >> 32)};
        }
        post(F_P1);
#pragma unroll
        for (int c1 = 0; c1 < 8; c1++) *(LAS u32*)(lds + ((rq >> 1) ? L_X1 : L_X0) + ((rq & 1) * 16 + rw) * 128 + ((c1 ^ (rw & 7)) * 16) + c2p * 4) = raw[c1];
        u32 pk[8], fr[16];
        const int un = sig < 4 * NFB ? sig : 0, fb = un >> 2, jj = (un >> 1) & 1, tcol = (un & 1) * 4 + rq;
        {
            const u32 vd = (u32)(((fb * 32 + 4 * lg) * 512 + 64 * (4 * jj + (li >> 2)) + 8 * tcol + 2 * (li & 3)) * 2);
            sfor<4>([&](auto I) {
                ldg_u32<I * 1024>(pk[I], vd, a.wd);
                ldg_u32<I * 1024>(pk[4 + I], vd, (const char*)a.wd + 16384);
            });
            const u32 vo = (u32)(((8 * sig + 2 * rq) * 257 + lane) * 2);
            sfor<8>([&](auto I) { constexpr int rr = I >> 2, k = I & 3; ldg_u16<rr * 514 + k * 128>(fr[I], vo, a.wr); ldg_u16<rr * 514 + k * 128>(fr[8 + I], vo, a.wi); });
        }
        wait_grp(0, F_P1);
        {
            LAS f2* reg = (LAS f2*)(lds + L_FFT) + (w * 2 + qb) * 288;
#pragma unroll
            for (int k = 0; k < 8; k++) v[k] = reg[(lane & 31) + 32 * k];
            dft8<false>(v, kc);
#pragma unroll
            for (int k = 1; k < 8; k++) v[k] = cmul<true>(v[k], tw64[k]);
#pragma unroll
            for (int k = 0; k < 8; k++) reg[36 * k + (lane & 31)] = v[k];
            FENCE();
#pragma unroll
            for (int k = 0; k < 8; k++) v[k] = reg[36 * l3 + 4 * k + c2p];
            dft8<false>(v, kc);
#pragma unroll
            for (int k = 0; k < 8; k++) v[k] = cmul<true>(v[k], twT[k]);
            FENCE();
            *(LAS f2*)(lds + x3a[0]) = v[0];
            *(LAS f2*)(lds + x3a[1]) = v[1]; *(LAS f2*)(lds + x3a[1] + 1152) = v[2]; *(LAS f2*)(lds + x3a[1] + 2304) = v[3];
            *(LAS f2*)(lds + x3a[2]) = v[4];
            *(LAS f2*)(lds + x3a[3] + 2304) = v[5]; *(LAS f2*)(lds + x3a[3] + 1152) = v[6]; *(LAS f2*)(lds + x3a[3]) = v[7];
            FENCE();
#pragma unroll
            for (int i = 0; i < 4; i++) {
                u4 o = *(LAS u4*)(lds + L_FFT + w * 4608 + (i * 8 + (lane >> 3)) * 144 + (lane & 7) * 16);
                st128(o, rs, NOF(f1st), i * (64 * 16384));
            }
        }
        STAMP(3);
        __builtin_amdgcn_s_setprio(0);
        asm volatile("s_waitcnt vmcnt(4)" : "+v"(pk[0]), "+v"(pk[1]), "+v"(pk[2]), "+v"(pk[3]), "+v"(pk[4]), "+v"(pk[5]), "+v"(pk[6]), "+v"(pk[7]),
                     "+v"(fr[0]), "+v"(fr[1]), "+v"(fr[2]), "+v"(fr[3]), "+v"(fr[4]), "+v"(fr[5]), "+v"(fr[6]), "+v"(fr[7]),
                     "+v"(fr[8]), "+v"(fr[9]), "+v"(fr[10]), "+v"(fr[11]), "+v"(fr[12]), "+v"(fr[13]), "+v"(fr[14]), "+v"(fr[15]) :: "memory");
        {
            const int so = sig < 4 * NFB ? WS_WDP + ((tcol * NFB + fb) * 4 + 2 * jj) * 1024 : WS_BYTES;
            u4 oe = {__builtin_amdgcn_perm(pk[1], pk[0], 0x05040100), __builtin_amdgcn_perm(pk[3], pk[2], 0x05040100), __builtin_amdgcn_perm(pk[5], pk[4], 0x05040100), __builtin_amdgcn_perm(pk[7], pk[6], 0x05040100)};
            u4 oo = {__builtin_amdgcn_perm(pk[1], pk[0], 0x07060302), __builtin_amdgcn_perm(pk[3], pk[2], 0x07060302), __builtin_amdgcn_perm(pk[5], pk[4], 0x07060302), __builtin_amdgcn_perm(pk[7], pk[6], 0x07060302)};
            st128(oe, rs, l16, so);
            st128(oo, rs, l16, so + 1024);
        }
        {
            const int q1 = lane >> 3, vt = lane & 7, srcl = (q1 + 8 * vt) * 4;
#pragma unroll
            for (int rr = 0; rr < 2; rr++) {
                const int u = 8 * sig + 2 * rq + rr, u1 = u & 63, u2_ = u >> 6;
                int cls, m, sub;
                if ((u1 & 31) != 0) { sub = u1 >> 5; cls = sub ? 64 - u1 : u1; m = sub ? ((8 - q1) & 7) : q1; }
                else { cls = 0; const int hi = u1 >> 5; if ((q1 & 3) == 0) { m = q1; sub = hi; } else { sub = (q1 >> 2) ^ hi; m = sub ? 8 - q1 : q1; } }
                u4 o;
                o.x = __builtin_amdgcn_ds_bpermute(srcl, fr[rr * 4 + 0] | (fr[8 + rr * 4 + 0] << 16)); o.y = __builtin_amdgcn_ds_bpermute(srcl, fr[rr * 4 + 1] | (fr[8 + rr * 4 + 1] << 16));
                o.z = __builtin_amdgcn_ds_bpermute(srcl, fr[rr * 4 + 2] | (fr[8 + rr * 4 + 2] << 16)); o.w = __builtin_amdgcn_ds_bpermute(srcl, fr[rr * 4 + 3] | (fr[8 + rr * 4 + 3] << 16));
                st128(o, rs, NOF(WS_WP + (cls * 8 + m) * 8192 + sub * 4096 + u2_ * 128 + vt * 16), 0);
            }
        }
        WAITVM(4);
        flag_store(WS_FF1 + ((rq * 4 + ((s2 & 7) >> 1)) * 64 + (s2 >> 3) * 16 + (s2 & 1) * 8 + t) * 8);
        WAITVM(1);
        flag_store(sig < 4 * NFB ? WS_FP + (((sig & 1) * 4 + rq) * 128 + (sig >> 1)) * 8 : WS_BYTES);
        flag_store(WS_FW + (((sig & 7) * 4 + rq) * 32 + (sig >> 3)) * 8);
        STAMP(4);
        RELANE();
        {
            const u32 fz = (u32)((li >> 3) | ((lg & 1) << 1));
            const u32 tb = (u32)((8 * lg + (li >> 2)) * 128 + 8 * (li & 3)) + (rq >> 1) * 4096;
            u32 pc0 = tb + (((rq & 1) ^ fz) << 5), pc1 = tb + (((2 + (rq & 1)) ^ fz) << 5);
            f4 c0 = {0, 0, 0, 0}, c1 = c0;
            wait_grp(2, G_W0);
#pragma unroll
            for (int ks = 0; ks < 8; ks++) { c0 = mfma(trfrag2<512>(lds + pc0 + ks * PG), ones, c0); c1 = mfma(trfrag2<512>(lds + pc1 + ks * PG), ones, c1); }
            post(F_C0);
            pc0 += 65536; pc1 += 65536; asm volatile("" : "+v"(pc0), "+v"(pc1));
            wait_G(G_W1);
#pragma unroll
            for (int ks = 0; ks < 8; ks++) { c0 = mfma(trfrag2<512>(lds + pc0 + ks * PG), ones, c0); c1 = mfma(trfrag2<512>(lds + pc1 + ks * PG), ones, c1); }
            if (li == 0) { *(LAS f4*)(lds + L_CS + rq * 64 + lg * 16) = c0; *(LAS f4*)(lds + L_CS + 256 + rq * 64 + lg * 16) = c1; }
            post(F_CS);
        }
        STAMP(15);
        __builtin_amdgcn_s_setprio(2);
        RELANE();
        f2 tw0[4], tw1[8], al[8], be[8];
        const int rb = qb, r3 = 2 * rq + rb;
        const int cls2 = sig >> 3, m2 = sig & 7;
        const int wq2 = (m2 < 4) ? m2 : ((8 - m2) & 3), pos1 = wq2 * 64 + cls2 * 2 + (m2 >> 2);
        const bool special = (cls2 == 0) && ((m2 & 3) == 0);
        int u1a, q1a, u1b, q1b;
        if (cls2 != 0) { u1a = cls2; q1a = m2; u1b = 64 - cls2; q1b = (8 - m2) & 7; }
        else if (special) { u1a = 0; u1b = 32; q1a = q1b = m2; }
        else { u1a = u1b = (m2 < 4) ? 0 : 32; q1a = m2; q1b = 8 - m2; }
        int sA, vH0, sB, vH1;
        if (!special) { sA = 0; vH0 = rq; sB = 1; vH1 = (u1a == 0) ? ((4 - rq) & 3) : 3 - rq; }
        else { sA = sB = rq >> 1; vH0 = rq & 1; vH1 = (rq == 0) ? 2 : (rq == 1) ? 3 : (rq == 2) ? 3 : 2; }
        const bool q0 = (m2 == 0);
        const int dy = ((sA ? u1b : u1a) == 0 && vH0 == 0) ? 8 : 7, dq = q0 ? 8 : 7;
        int y1 = lane >> 3, t1 = lane & 7, y2 = (dy - y1) & 7, t2 = (dq - t1) & 7, hA = vH0, hB = vH1;
        bool self = false;
        if (special && rq == 0) {
            const int k = lane & 31;
            if (qb) { hA = hB = 2; y1 = k >> 3; t1 = k & 7; y2 = 7 - y1; t2 = (dq - t1) & 7; }
            else {
                hA = hB = 0; const int vx = k & 3, y = k >> 2;
                if (!q0 || vx) { y1 = y; t1 = vx; y2 = (8 - y) & 7; t2 = dq - vx; }
                else { t1 = t2 = (y >= 4) ? 4 : 0; const int yy = y & 3; if (yy) { y1 = yy; y2 = 8 - yy; } else { y1 = 0; y2 = 4; self = true; } }
            }
        }
        const int uA = (sA ? u1b : u1a) + 64 * (hA + 4 * y1), uB = (sB ? u1b : u1a) + 64 * (hB + 4 * y2);
        const int qA = (sA ? q1b : q1a) + 8 * t1, qB = (sB ? q1b : q1a) + 8 * t2;
        const int rowB = L_FFT + ((qb ? (vH1 * 2 + sB) : (vH0 * 2 + sA)) * 36 + (lane & 31)) * 8;
        const bool rot = q0 && (t1 == 0);
        const int p3a1 = L_FFT + (y1 * 290 + (hA * 2 + sA) * 36 + t1 * 4) * 8;
        const int p3a2 = L_FFT + (y2 * 290 + (hB * 2 + sB) * 36 + t2 * 4) * 8;
        const float thA = (float)qA * (1.0f / 512.0f), thB = (float)qB * (1.0f / 512.0f);
#pragma unroll
        for (int k = 1; k < 4; k++) tw0[k] = cis((float)r3 * ((float)k / 32.0f));
#pragma unroll
        for (int k = 1; k < 8; k++) tw1[k] = cis((float)c2p * ((float)k / 32.0f));
        f2 csq[8];
#pragma unroll
        for (int e = 0; e < 8; e++) csq[e] = cis(((e >> 2) ? thB : thA) + 0.125f * (float)(e & 3));
        asm volatile("" : "+v"(csq[0]), "+v"(csq[1]), "+v"(csq[2]), "+v"(csq[3]), "+v"(csq[4]), "+v"(csq[5]), "+v"(csq[6]), "+v"(csq[7]),
                     "+v"(tw0[1]), "+v"(tw0[2]), "+v"(tw0[3]), "+v"(tw1[1]), "+v"(tw1[2]), "+v"(tw1[3]), "+v"(tw1[4]), "+v"(tw1[5]), "+v"(tw1[6]), "+v"(tw1[7]));
        {
            const u32 o1 = min(rq * 22 + lane, 85) * 8, o2 = (((qb ? u1b : u1a) >> 1) * 32 + (lane & 31)) * 8, o3 = lane * 8;
            _Pragma("unroll 1") for (int spins = 0; spins < SPIN_CAP; spins++) {
                u2 f1 = __builtin_amdgcn_raw_buffer_load_b64(rs, o1, WS_FP + t * 1024, 16);
                u2 f2_ = __builtin_amdgcn_raw_buffer_load_b64(rs, o2, WS_FW, 16);
                u2 f3 = __builtin_amdgcn_raw_buffer_load_b64(rs, o3, WS_FF1 + (wq2 * 4 + rq) * 512, 16);
                if (__all(f1.x == tag.x && f1.y == tag.y && f2_.x == tag.x && f2_.y == tag.y && f3.x == tag.x && f3.y == tag.y)) break;
                __builtin_amdgcn_s_sleep(16);
                FENCE();
            }
            __builtin_amdgcn_fence(__ATOMIC_ACQUIRE, "wavefront");
            FENCE();
        }
        post(F_FP);
        wait_grp(0, F_FP);
        STAMP(14);
        const int f1off = WS_F1 + sig * 16384 + 2 * rq * 512;
        u4 d[4];
        const u4 wa4 = __builtin_amdgcn_raw_buffer_load_b128(rs, (hA + 4 * y1) * 128 + t1 * 16, WS_WP + sig * 8192 + sA * 4096, 16);
        const u4 wb4 = __builtin_amdgcn_raw_buffer_load_b128(rs, (hB + 4 * y2) * 128 + t2 * 16, WS_WP + sig * 8192 + sB * 4096, 16);
        SCHED();
#pragma unroll
        for (int H = 0; H < 4; H++) d[H] = __builtin_amdgcn_raw_buffer_load_b128(rs, NOF(l16), f1off + H * 4096, 16);
        SCHED();
        f2 wS[2] = {f2{0.f, 0.f}, f2{0.f, 0.f}};
        if (q0) {
            wS[0] = f2{bflo(a.wr[uA * 257 + 256]), bflo(a.wi[uA * 257 + 256])};
            wS[1] = f2{bflo(a.wr[uB * 257 + 256]), bflo(a.wi[uB * 257 + 256])};
        }
        {
            f2 W[8], WB[8];
#pragma unroll
            for (int e = 0; e < 4; e++) { W[e] = bfup(wa4[e]); W[4 + e] = bfup(wb4[e]); }
#pragma unroll
            for (int k = 0; k < 4; k++) { WB[k] = W[4 + 3 - k]; WB[4 + k] = W[3 - k]; }
            if (q0) {
#pragma unroll
                for (int k = 0; k < 4; k++) {
                    f2 n1 = rot ? W[(4 - k) & 3] : W[3 - k], n2 = rot ? W[4 + ((4 - k) & 3)] : W[4 + 3 - k];
                    WB[k] = self ? n1 : n2; WB[4 + k] = self ? n2 : n1;
                }
#pragma unroll
                for (int e = 0; e < 8; e += 4) {
                    f2 own = wS[e >> 2], oth = self ? own : wS[1 - (e >> 2)];
                    f2 S0 = pkfma(WB[e], kc.pm, W[e]) * 0.5f, S1c = pkfma(own, kc.pm, oth) * 0.5f;
                    WB[e] = rot ? S1c : WB[e];
                    al[e] = rot ? S0 : W[e];
                }
                W[0] = al[0]; W[4] = al[4];
            }
#pragma unroll
            for (int e = 0; e < 8; e++) {
                const f2 cs = csq[e];
                f2 P = pkfma(WB[e], kc.pm, W[e]), M = pkfma(WB[e], kc.pm.yx, W[e]);
                asm("v_pk_fma_f32 %0, %1, %2, %3 op_sel:[1,0,0] op_sel_hi:[1,1,1] neg_lo:[1,0,0] neg_hi:[1,0,0]" : "=v"(al[e]) : "v"(cs), "v"(M), "v"(P));
                asm("v_pk_mul_f32 %0, %1, %2 op_sel:[1,0] op_sel_hi:[0,0] neg_lo:[1,0]" : "=v"(be[e]) : "v"(M), "v"(cs));
            }
            asm volatile("" : "+v"(al[0]), "+v"(al[1]), "+v"(al[2]), "+v"(al[3]), "+v"(al[4]), "+v"(al[5]), "+v"(al[6]), "+v"(al[7]),
                         "+v"(be[0]), "+v"(be[1]), "+v"(be[2]), "+v"(be[3]), "+v"(be[4]), "+v"(be[5]), "+v"(be[6]), "+v"(be[7]),
                         "+v"(tw0[1]), "+v"(tw0[2]), "+v"(tw0[3]), "+v"(tw1[1]), "+v"(tw1[2]), "+v"(tw1[3]), "+v"(tw1[4]), "+v"(tw1[5]), "+v"(tw1[6]), "+v"(tw1[7]));
        }
        STAMP(5);
        __builtin_amdgcn_s_setprio(2);
        f2 e0[4], e1[4];
#pragma unroll
        for (int H = 0; H < 4; H++) { e0[H] = f2{__uint_as_float(d[H].x), __uint_as_float(d[H].y)}; e1[H] = f2{__uint_as_float(d[H].z), __uint_as_float(d[H].w)}; }
        dft4<false>(e0[0], e0[1], e0[2], e0[3], kc); dft4<false>(e1[0], e1[1], e1[2], e1[3], kc);
        STAMP(6);
        LAS char* area = lds + L_FFT + rq * 4640;
#pragma unroll
        for (int k = 0; k < 4; k++) {
            if (k) { e0[k] = cmul<true>(e0[k], tw0[k]); e1[k] = cmul<true>(e1[k], tw0[k]); }
            *(LAS f4*)(area + (l3 * 72 + 2 * c2p + 8 * k + 32 * rb) * 8) = f4{e0[k].x, e0[k].y, e1[k].x, e1[k].y};
        }
        FENCE();
#pragma unroll
        for (int k = 0; k < 8; k++) v[k] = *(LAS f2*)(area + (k * 72 + lane) * 8);
        dft8<false>(v, kc);
#pragma unroll
        for (int k = 1; k < 8; k++) v[k] = cmul<true>(v[k], tw1[k]);
        FENCE();
        LAS char* pb = lds + L_FFT + (r3 * 290 + l3 * 36 + c2p) * 8;
#pragma unroll
        for (int k = 0; k < 8; k++) *(LAS f2*)(pb + k * 32) = v[k];
        post(F_E1);
        wait_grp(0, F_E1);
#pragma unroll
        for (int k = 0; k < 8; k++) v[k] = *(LAS f2*)(lds + rowB + k * 2320);
        dft8<false>(v, kc);
#pragma unroll
        for (int k = 0; k < 8; k++) *(LAS f2*)(lds + rowB + k * 2320) = v[k];
        FENCE();
        f4 a0 = *(LAS f4*)(lds + p3a1), a1 = *(LAS f4*)(lds + p3a1 + 16), b0 = *(LAS f4*)(lds + p3a2), b1 = *(LAS f4*)(lds + p3a2 + 16);
        f2 X[8] = {a0.xy, a0.zw, a1.xy, a1.zw, b0.xy, b0.zw, b1.xy, b1.zw};
        dft4<false>(X[0], X[1], X[2], X[3], kc); dft4<false>(X[4], X[5], X[6], X[7], kc);
        f2 Y[8];
#pragma unroll
        for (int k = 0; k < 4; k++) { Y[k] = X[4 + 3 - k]; Y[4 + k] = X[3 - k]; }
        if (q0) {
#pragma unroll
            for (int k = 0; k < 4; k++) {
                f2 n1 = rot ? X[(4 - k) & 3] : X[3 - k], n2 = rot ? X[4 + ((4 - k) & 3)] : X[4 + 3 - k];
                Y[k] = self ? n1 : n2; Y[4 + k] = self ? n2 : n1;
            }
        }
        f2 O[8];
#pragma unroll
        for (int e = 0; e < 8; e++) O[e] = cfmac_conj(cmul<false>(X[e], al[e]), be[e], Y[e]);
        dft4<true>(O[0], O[1], O[2], O[3], kc); dft4<true>(O[4], O[5], O[6], O[7], kc);
        *(LAS f4*)(lds + p3a1) = f4{O[0].x, O[0].y, O[1].x, O[1].y}; *(LAS f4*)(lds + p3a1 + 16) = f4{O[2].x, O[2].y, O[3].x, O[3].y};
        *(LAS f4*)(lds + p3a2) = f4{O[4].x, O[4].y, O[5].x, O[5].y}; *(LAS f4*)(lds + p3a2 + 16) = f4{O[6].x, O[6].y, O[7].x, O[7].y};
        FENCE();
#pragma unroll
        for (int k = 0; k < 8; k++) v[k] = *(LAS f2*)(lds + rowB + k * 2320);
        dft8<true>(v, kc);
#pragma unroll
        for (int k = 0; k < 8; k++) *(LAS f2*)(lds + rowB + k * 2320) = v[k];
        post(F_E2);
        wait_grp(0, F_E2);
#pragma unroll
        for (int k = 0; k < 8; k++) v[k] = *(LAS f2*)(pb + k * 32);
#pragma unroll
        for (int k = 1; k < 8; k++) v[k] = cmul<false>(v[k], tw1[k]);
        dft8<true>(v, kc);
        FENCE();
#pragma unroll
        for (int k = 0; k < 8; k++) *(LAS f2*)(area + (k * 72 + lane) * 8) = v[k];
        FENCE();
#pragma unroll
        for (int k = 0; k < 4; k++) {
            f4 q = *(LAS f4*)(area + (l3 * 72 + 2 * c2p + 8 * k + 32 * rb) * 8);
            e0[k] = q.xy; e1[k] = q.zw;
            if (k) { e0[k] = cmul<false>(e0[k], tw0[k]); e1[k] = cmul<false>(e1[k], tw0[k]); }
        }
        dft4<true>(e0[0], e0[1], e0[2], e0[3], kc); dft4<true>(e1[0], e1[1], e1[2], e1[3], kc);
        const int f2st = WS_F2 + (r3 * 8 + l3) * 16384 + pos1 * 64 + c2p * 16;
#pragma unroll
        for (int H = 0; H < 4; H++) {
            u4 o = {__float_as_uint(e0[H].x), __float_as_uint(e0[H].y), __float_as_uint(e1[H].x), __float_as_uint(e1[H].y)};
            st128(o, rs, NOF(f2st), H * (64 * 16384));
        }
        STAMP(7);
        WAITVM(0);
        flag_store(WS_FF2 + (rq * 256 + pos1) * 8);
        STAMP(8);
        RELANE();
        __builtin_amdgcn_s_setprio(0);
        wait_G(G_EP);
        sfor<4>([&](auto I) { dma_wd(IC<L_SET1>{}, I, 0); });
        wait_G(G_EB);
        sfor<4>([&](auto I) { dma_wd(IC<0>{}, I, 16); });
        WAITVM(16); post(F_D1);
        WAITVM(0); post(F_D2);
        wait_grp(0, F_D1);
        fft13_consts(0.5f / 524288.0f);
        flag_wait(lane * 8, WS_FF2 + (((s2 & 7) >> 1) * 256 + rq * 64) * 8);
        const int off = WS_F2 + pid * 16384 + rq * 4096;
#pragma unroll
        for (int i = 0; i < 4; i++) d[i] = __builtin_amdgcn_raw_buffer_load_b128(rs, NOF(l16), off + i * 1024, 16);
        STAMP(13);
        __builtin_amdgcn_s_setprio(2);
#pragma unroll
        for (int i = 0; i < 4; i++) *(LAS u4*)(lds + L_FFT + rq * 4608 + (i * 8 + (lane >> 3)) * 144 + (lane & 7) * 16) = d[i];
        FENCE();
        v[0] = *(LAS f2*)(lds + x3a[0]);
        v[1] = *(LAS f2*)(lds + x3a[1]); v[2] = *(LAS f2*)(lds + x3a[1] + 1152); v[3] = *(LAS f2*)(lds + x3a[1] + 2304);
        v[4] = *(LAS f2*)(lds + x3a[2]);
        v[5] = *(LAS f2*)(lds + x3a[3] + 2304); v[6] = *(LAS f2*)(lds + x3a[3] + 1152); v[7] = *(LAS f2*)(lds + x3a[3]);
#pragma unroll
        for (int k = 0; k < 8; k++) v[k] = cmul<false>(v[k], twT[k]);
        dft8<true>(v, kc);
#pragma unroll
        for (int k = 1; k < 8; k++) v[k] = cmul<false>(v[k], tw64[k]);
        FENCE();
        LAS f2* reg = (LAS f2*)(lds + L_FFT) + (rq * 2 + qb) * 288;
#pragma unroll
        for (int k = 0; k < 8; k++) reg[36 * l3 + 4 * k + c2p] = v[k];
        FENCE();
#pragma unroll
        for (int k = 0; k < 8; k++) v[k] = reg[36 * k + (lane & 31)];
        dft8<true>(v, kc);
        FENCE();
#pragma unroll
        for (int k = 0; k < 8; k++) reg[(lane & 31) + 32 * k] = v[k];
        post(F_F3);
        wait_grp(0, F_F3);
        if (b == 0 && rq == 0) __builtin_amdgcn_raw_buffer_store_b64(*(LAS u2*)(lds + L_TAG + 8), rs, lane ? WS_BYTES : 0, 0, 16);
        LAS f2* xb1 = (LAS f2*)(lds + L_FFT) + rq * 64 + lane;
#pragma unroll
        for (int q1 = 0; q1 < 8; q1++) { const int rg_ = (q1 < 4) ? q1 * 2 : (((8 - q1) & 3) * 2 + 1); v[q1] = xb1[rg_ * 288]; }
        post(F_F4);
        dft8<true>(v, kc);
        wait_grp(0, F_F4);
#pragma unroll
        for (int c1 = 0; c1 < 8; c1++) *(LAS u32*)(lds + L_S + (16 * rq + (lane >> 2)) * 144 + c1 * 16 + c2p * 4) = pkbf(v[c1]);
        post(F_S);
        STAMP(10);
    } else {
        RELANE();
        const int g = w - 4, wp = 4 + (g ^ 4);
        const int hoff = WS_H + (((s2 ^ hf) * 4 + rq) * NFB) * 1024;
        const char* wgs = (const char*)a.wg + (size_t)(32 * rq) * 2752 + cg * 128;
        const char* wus = (const char*)a.wu + (size_t)(32 * rq) * 2752 + cg * 128;
        auto dma_w = [&](int cls) {
            const u32 wvE = (u32)((lane >> 3) * 2752 + (((lane & 7) ^ (((lane >> 4) & 1) << 1)) * 16)), wvO = (u32)((lane >> 3) * 2752 + (((lane & 7) ^ (((lane >> 4) & 1) << 1) ^ 4) * 16));
            sfor<8>([&](auto I) { constexpr int gg = I >> 1, mat = I & 1;
                dma_add<mat * 4096 + gg * 1024, gg * 8 * 2752>((gg & 1) ? wvO : wvE, (mat ? wus : wgs) + ((cls + j4) & 3) * (128 * 2752), (u32)((4 * cls + rq) * PG)); });
        };
        auto read_half = [&](u4 (&x)[8]) {
            LAS char* p = lds + L_SET1 + (16 * rq + li) * 1024;
#pragma unroll
            for (int k = 0; k < 8; k++) x[k] = *(LAS u4*)(p + (((4 * (k & 3) + lg) ^ li) * 16) + ((2 * (k >> 2) + hf + j4) & 3) * 256);
        };
        u4 x0[8], x1[8];
        if (hf == 0) { WAITVM(0); post(G_XL); }
        else { __builtin_amdgcn_s_sleep(2); dma_w(0); wait_one(wp, G_XL); }
        read_half(x0);
        post(G_XC);
        STAMP(1);
        if (hf == 0) { wait_one(wp, G_XC); wait_one(rq, F_XC); dma_x(1); __builtin_amdgcn_s_setprio(0); } else dma_w(1);
        WAITVM(0);
        post(G_W0);
        wait_one(4 + rq, G_W0);
        read_half(x1);
        post(G_XD);
        {
            f4 s1 = {0, 0, 0, 0}, sq = s1;
#pragma unroll
            for (int k = 0; k < 8; k++) { s1 = mfma(ones, x1[k], s1); sq = mfma(x1[k], x1[k], sq); }
            const int rr = li & 3;
            const float d = rr == 0 ? sq.x : rr == 1 ? sq.y : rr == 2 ? sq.z : sq.w;
            if (lg == (li >> 2)) *(LAS f2*)(lds + L_PST + (hf * 64 + 16 * rq + li) * 8) = f2{s1.x, d};
        }
        SCHED();
        wait_G(G_XD);
        dma_w(2 + hf);
        STAMP(2);
        f2 stP;
        {
            const u32 fz = (u32)((li >> 3) | ((lg & 1) << 1));
            const u32 tb = (u32)((8 * lg + (li >> 2)) * 128 + 8 * (li & 3)) + hf * 4 * PG;
            u32 pt[4];
#pragma unroll
            for (int c = 0; c < 4; c++) pt[c] = tb + ((c ^ fz) << 5);
            f4 acc[2][2][4];
#pragma unroll
            for (int k = 0; k < 2; k++) {
#pragma unroll
                for (int j = 0; j < 4; j++) { acc[k][0][j] = f4{0, 0, 0, 0}; acc[k][1][j] = f4{0, 0, 0, 0}; }
            }
            u4 F[2][2];
            auto ldf = [&](int ks, int k, int m, u4 (&f)[2]) {
                const int off = (ks & 3) * PG + m * 4096;
                f[0] = trfrag2<512>(lds + pt[2 * k] + off); f[1] = trfrag2<512>(lds + pt[2 * k + 1] + off);
            };
            STAMP(3);
            ldf(0, 0, 0, F[0]);
            sfor<32>([&](auto S) {
                constexpr int qs = S, ks = qs >> 2, k = (qs >> 1) & 1, m = qs & 1, qn = qs + 1;
                if (qs == 15) {
                    WAITVM(0); post(G_W1); wait_grp(1 + hf, G_W1); STAMP(14);
#pragma unroll
                    for (int c = 0; c < 4; c++) pt[c] += 65536;
                    asm volatile("" : "+v"(pt[0]), "+v"(pt[1]), "+v"(pt[2]), "+v"(pt[3]));
                }
                if (qs == 16) post(G_S0);
                SCHED();
                if (qs + 1 < 32) ldf(qn >> 2, (qn >> 1) & 1, qn & 1, F[qn & 1]);
                u4 (&f)[2] = F[qs & 1];
#pragma unroll
                for (int j = 0; j < 2; j++) { acc[k][0][2 * m + j] = mfma(f[j], x0[ks], acc[k][0][2 * m + j]); acc[k][1][2 * m + j] = mfma(f[j], x1[ks], acc[k][1][2 * m + j]); }
                if (qs + 1 < 32) {
#pragma unroll
                    for (int i = 0; i < 4; i++) { SGB(0x100, 1); SGB(0x8, 1); }
                }
                SCHED();
            });
            RELANE();
            post(G_CS);
            STAMP(4);
            if (nblk == 3) {
                const u32 wvB = (u32)((lane >> 2) * 2752 + (((lane & 3) ^ (((lane >> 5) & 1) << 1)) * 16));
                const char* wbs = (const char*)((rq >> 1) ? a.wu : a.wg) + (size_t)((rq & 1) * 16) * 2752 + fb2 * 64;
                wait_grp(0, F_C0); wait_G(G_S0);
                sfor<8>([&](auto K) { constexpr int k = K; dma_add<(k >> 2) * 32768 + (k & 3) * 4096, (k & 3) * 32 * 2752>(wvB, wbs + ((2 * (k >> 2) + hf + j4) & 3) * (128 * 2752), (u32)(hf * 16384 + rq * 1024)); });
            }
            wait_G(G_CS); wait_grp(0, F_CS);
            LAS f4* xs = (LAS f4*)(lds + L_SET1 + g * PG) + lane;
            LAS f4* xr = (LAS f4*)(lds + L_SET1 + (g ^ 4) * PG) + lane;
            f4 mine[2][4];
            if (hf) {
#pragma unroll
                for (int k = 0; k < 2; k++)
#pragma unroll
                    for (int j = 0; j < 4; j++) { xs[(k * 4 + j) * 64] = acc[k][0][j]; mine[k][j] = acc[k][1][j]; }
                const f2 p0 = *(LAS f2*)(lds + L_PST + (16 * rq + li) * 8), p1 = *(LAS f2*)(lds + L_PST + (64 + 16 * rq + li) * 8);
                const float mean = (p0.x + p1.x) * (1.0f / 512.0f);
                float var = (p0.y + p1.y) * (1.0f / 512.0f) - mean * mean;
                if (!(mean * mean <= 1024.0f * var)) {
                    const u32* xrow = (const u32*)(a.x + (size_t)((s2 ^ 1) + 32 * (16 * rq + li)) * 512);
                    float q = 0.f;
                    _Pragma("unroll 1") for (int i = 0; i < 256; i++) { u32 v = xrow[i]; float d0 = bflo(v) - mean, d1 = bfhi(v) - mean; q += d0 * d0; q += d1 * d1; }
                    var = q * (1.0f / 512.0f);
                }
                stP = f2{rbf(mean), __builtin_amdgcn_rcpf(rbf(__builtin_amdgcn_sqrtf(rbf(rbf(var) + 1e-5f))))};
            } else {
#pragma unroll
                for (int k = 0; k < 2; k++)
#pragma unroll
                    for (int j = 0; j < 4; j++) { xs[(k * 4 + j) * 64] = acc[k][1][j]; mine[k][j] = acc[k][0][j]; }
                stP = *(LAS f2*)(lds + L_STAT + (16 * rq + li) * 8);
            }
            post(G_XW);
            get_tag();
            wait_one(wp, G_XW);
#pragma unroll
            for (int k = 0; k < 2; k++)
#pragma unroll
                for (int j = 0; j < 4; j++) mine[k][j] += xr[(k * 4 + j) * 64];
            post(G_EP);
            st128(geglu8(mine[0], lds + L_CS + lg * 16, stP), rs, l16, hoff + (2 * cg) * 1024);
            st128(geglu8(mine[1], lds + L_CS + 256 + lg * 16, stP), rs, l16, hoff + (2 * cg + 1) * 1024);
        }
        STAMP(5);
        if (nblk == 3) {
            WAITVM(2); post(G_WB);
            wait_grp(1 + hf, G_WB);
            const u32 tb = (u32)((8 * lg + (li >> 2)) * 64 + (lg & 1) * 32 + 8 * (li & 3)) + hf * 16384;
            LAS char* p0 = lds + tb; LAS char* p1 = lds + (tb ^ 32);
            LAS char* pc = lds + (tb ^ ((rq & 1) * 32)) + (rq >> 1) * 2048;
            f4 acc[2][4], acs = {0, 0, 0, 0};
#pragma unroll
            for (int j = 0; j < 4; j++) { acc[0][j] = f4{0, 0, 0, 0}; acc[1][j] = f4{0, 0, 0, 0}; }
            u4 F[2][5];
            auto ldf = [&](int ks, u4 (&f)[5]) {
                const int off = (ks >> 2) * 32768 + (ks & 3) * 4096;
                f[4] = trfrag2<256>(pc + off);
                f[0] = trfrag2<256>(p0 + off); f[1] = trfrag2<256>(p1 + off); f[2] = trfrag2<256>(p0 + off + 2048); f[3] = trfrag2<256>(p1 + off + 2048);
            };
            ldf(0, F[0]);
            sfor<8>([&](auto S) {
                constexpr int ks = S;
                if (ks == 3) { WAITVM(0); flag_store(WS_FHA + (((s2 ^ hf) * 4 + rq) * 16 + cg) * 8); }
                SCHED();
                if (ks + 1 < 8) ldf(ks + 1, F[(ks + 1) & 1]);
                u4 (&f)[5] = F[ks & 1];
                acs = mfma(f[4], ones, acs);
#pragma unroll
                for (int j = 0; j < 4; j++) { acc[0][j] = mfma(f[j], x0[ks], acc[0][j]); acc[1][j] = mfma(f[j], x1[ks], acc[1][j]); }
                if (ks + 1 < 8) {
                    SGB(0x100, 2);
#pragma unroll
                    for (int i = 0; i < 8; i++) { SGB(0x8, 1); SGB(0x100, 1); }
                    SGB(0x8, 1);
                }
                SCHED();
            });
            RELANE();
            if (li == 0) *(LAS f4*)(lds + L_CS + 512 + g * 64 + lg * 16) = acs;
            post(G_PB);
            wait_G(G_PB);
            LAS f4* xs = (LAS f4*)(lds + g * 4096) + lane;
            LAS f4* xr = (LAS f4*)(lds + (g ^ 4) * 4096) + lane;
            f4 mine[4];
            if (hf) {
#pragma unroll
                for (int j = 0; j < 4; j++) { xs[j * 64] = acc[0][j]; mine[j] = acc[1][j]; }
            } else {
#pragma unroll
                for (int j = 0; j < 4; j++) { xs[j * 64] = acc[1][j]; mine[j] = acc[0][j]; }
            }
            if (lane < 16) { LAS f4* c = (LAS f4*)(lds + L_CS + 512) + lane; *(LAS f4*)(lds + L_CS + lane * 16) = c[0] + c[16]; }
            post(G_XB);
            wait_one(wp, G_XB);
#pragma unroll
            for (int j = 0; j < 4; j++) mine[j] += xr[j * 64];
            post(G_EB);
            st128(geglu8(mine, lds + L_CS + lg * 16, stP), rs, l16, hoff + fb2 * 1024);
        } else { post(G_EB); WAITVM(0); flag_store(WS_FHA + (((s2 ^ hf) * 4 + rq) * 16 + cg) * 8); }
        STAMP(6);
        f4 acc2[4];
#pragma unroll
        for (int j = 0; j < 4; j++) acc2[j] = f4{0, 0, 0, 0};
        const int hsrc = WS_H + ((s2 * 4 + rq) * NFB + hf) * 1024;
        auto g2_mma = [&](auto NC, const u4* hfr, int ldsbase) {
            constexpr int N = decltype(NC)::value;
            u32 pa = (u32)(ldsbase + hf * 4096) + l16; asm volatile("" : "+v"(pa));
            LAS u4* p = (LAS u4*)(lds + pa);
            u4 F[2][4];
#pragma unroll
            for (int j = 0; j < 4; j++) F[0][j] = p[j * 64];
            sfor<N>([&](auto I) {
                constexpr int i = I;
                SCHED();
                if (i + 1 < N) {
#pragma unroll
                    for (int j = 0; j < 4; j++) F[(i + 1) & 1][j] = p[(i + 1) * 512 + j * 64];
                }
#pragma unroll
                for (int j = 0; j < 4; j++) acc2[j] = mfma(F[i & 1][j], hfr[i], acc2[j]);
                if (i + 1 < N) {
#pragma unroll
                    for (int j = 0; j < 4; j++) { SGB(0x100, 1); SGB(0x8, 1); }
                }
                SCHED();
            });
        };
        u4 hfr[16];
        {
            _Pragma("unroll 1") for (int spins = 0; spins < SPIN_CAP; spins++) {
                u2 f = __builtin_amdgcn_raw_buffer_load_b64(rs, li * 8, WS_FHA + (s2 * 4 + rq) * 128, 16);
                const bool all = __all(f.x == tag.x && f.y == tag.y);
                if (spins == 0) { WAITVM(0); flag_store(nblk == 3 ? WS_FHB + (((s2 ^ hf) * 4 + rq) * 16 + fb2 - 32) * 8 : WS_BYTES); }
                if (all) break;
                __builtin_amdgcn_s_sleep(16);
                FENCE();
            }
            __builtin_amdgcn_fence(__ATOMIC_ACQUIRE, "wavefront");
            FENCE();
        }
#pragma unroll
        for (int i = 0; i < 16; i++) { hfr[i] = __builtin_amdgcn_raw_buffer_load_b128(rs, l16, hsrc + ((i >> 3) * 16 + ((2 * i + rot) & 15)) * 1024, 16); SCHED(); }
        STAMP(9);
        wait_grp(0, F_D1);
        g2_mma(IC<8>{}, hfr, L_SET1);
        post(G_G1);
        pin4(acc2);
        wait_G(G_G1);
        if (g < 6) {
            dma4k_c<L_SET1, 0>(l16, wdp + (size_t)(32 + 2 * g) * 4096, (u32)(g * PG));
            dma4k_c<L_SET1 + 4096, 0>(l16, wdp + (size_t)min(33 + 2 * g, 42) * 4096, (u32)(g * PG));
        }
        wait_grp(0, F_D2);
        g2_mma(IC<8>{}, hfr + 8, 0);
        pin4(acc2);
        STAMP(10);
        flag_wait(min(li, 10) * 8, WS_FHB + (s2 * 4 + rq) * 128);
        post(G_G2);
#pragma unroll
        for (int i = 0; i < 6; i++) { hfr[i] = __builtin_amdgcn_raw_buffer_load_b128(rs, l16, (i == 5 && hf) ? WS_BYTES : hsrc + (32 + 2 * i) * 1024, 16); SCHED(); }
        STAMP(13);
        wait_G(G_G2);
        g2_mma(IC<6>{}, hfr, L_SET1);
        STAMP(11);
        RELANE();
        LAS f4* xs = (LAS f4*)(lds + g * 2048) + lane;
        LAS f4* xr = (LAS f4*)(lds + (g ^ 4) * 2048) + lane;
        f4 me, mo;
        if (hf) { xs[0] = acc2[0]; xs[64] = acc2[1]; me = acc2[2]; mo = acc2[3]; } else { xs[0] = acc2[2]; xs[64] = acc2[3]; me = acc2[0]; mo = acc2[1]; }
        post(G_X);
        wait_one(4 + (g ^ 4), G_X);
        const int c1 = 4 * hf + lg;
        me += xr[0]; mo += xr[64];
        f2 gv[4];
#pragma unroll
        for (int e = 0; e < 4; e++) {
            const f2 ov = bfup(pkbf(f2{me[e], mo[e]}));
            const f2 z = ov * -1.44269504f;
            const f2 d = f2{__builtin_amdgcn_exp2f(z.x), __builtin_amdgcn_exp2f(z.y)} + f2{1.f, 1.f};
            gv[e] = bfup(pkbf(ov * f2{__builtin_amdgcn_rcpf(d.x), __builtin_amdgcn_rcpf(d.y)}));
        }
        asm volatile("" : "+v"(gv[0]), "+v"(gv[1]), "+v"(gv[2]), "+v"(gv[3]));
        wait_one(rq, F_S);
        const u4 sv = *(LAS u4*)(lds + L_S + (16 * rq + li) * 144 + c1 * 16);
        const u4 xv = *(LAS u4*)(lds + ((rq >> 1) ? L_X1 : L_X0) + ((rq & 1) * 16 + li) * 128 + ((c1 ^ (li & 7)) * 16));
        u32 res[4];
#pragma unroll
        for (int e = 0; e < 4; e++) res[e] = pkbf(bfup(xv[e]) + bfup(pkbf(bfup(sv[e]) * gv[e])));
        *(u4*)(a.out + (size_t)(s2 + 32 * (16 * rq + li)) * 512 + 64 * c1 + 8 * t) = u4{res[0], res[1], res[2], res[3]};
        STAMP(12);
    }
}

extern "C" void kernel_launch(void* const* d_in, const int* in_sizes, int n_in,
                              void* d_out, int out_size, void* d_ws, size_t ws_size,
                              hipStream_t stream) {
    Args a;
    a.x = (const u16*)d_in[0]; a.wr = (const u16*)d_in[1]; a.wi = (const u16*)d_in[2];
    a.wg = (const u16*)d_in[4]; a.wu = (const u16*)d_in[5]; a.wd = (const u16*)d_in[6];
    a.out = (u16*)d_out; a.ws = (unsigned char*)d_ws;
    (void)hipFuncSetAttribute((const void*)fused, hipFuncAttributeMaxDynamicSharedMemorySize, L_END);
    fused<<<256, 768, L_END, stream>>>(a);
}
```

```cpp
#include <hip/hip_runtime.h>
#include <stdint.h>
#include <type_traits>

typedef unsigned short u16;
typedef unsigned u32;
typedef unsigned long long u64;
typedef float f2 __attribute__((ext_vector_type(2)));
typedef float f4 __attribute__((ext_vector_type(4)));
typedef u32 u4 __attribute__((ext_vector_type(4)));
typedef u32 u2 __attribute__((ext_vector_type(2)));
typedef short s4 __attribute__((ext_vector_type(4)));
typedef __bf16 bf8 __attribute__((ext_vector_type(8)));
typedef __bf16 bf2 __attribute__((ext_vector_type(2)));
#define LAS __attribute__((address_space(3)))
#define DEV __device__ __forceinline__
template <int N> using IC = std::integral_constant<int, N>;

constexpr int NFB = 43;
constexpr int WS_FF1 = 4096;
constexpr int WS_FF2 = WS_FF1 + 8192;
constexpr int WS_FHA = WS_FF2 + 8192;
constexpr int WS_FHB = WS_FHA + 16384;
constexpr int WS_FP = WS_FHB + 16384;
constexpr int WS_F1 = 1 << 20, WS_F2 = WS_F1 + (4 << 20), WS_H = WS_F2 + (4 << 20);
constexpr int WS_WDP = WS_H + 32 * 4 * NFB * 1024;
constexpr int WS_DUMMY = WS_WDP + 8 * NFB * 4 * 1024;
constexpr int WS_BYTES = WS_DUMMY + (2 << 20);
constexpr int PG = 8192;
constexpr int L_SET1 = 8 * PG;
constexpr int L_FFT = 16 * PG;
constexpr int L_SMALL = 19 * PG;
constexpr int L_PROG = L_SMALL;
constexpr int L_TAG = L_SMALL + 32;
constexpr int L_STAT = L_SMALL + 64;
constexpr int L_CS = L_SMALL + 2048;
constexpr int L_X0 = L_FFT + 18688, L_X1 = L_SMALL + 3072;
constexpr int L_S = 4 * PG;
constexpr int L_END = 20 * PG;
constexpr int CODE_BYTES = 40 * 1024;
constexpr int SPIN_CAP = 1 << 16;

struct Args { const u16* x; const u16* wr; const u16* wi; const u16* wg; const u16* wu; const u16* wd; u16* out; unsigned char* ws; };

#define BAR() asm volatile("s_waitcnt lgkmcnt(0)\n\ts_barrier" ::: "memory")
#define WAITVM(n) asm volatile("s_waitcnt vmcnt(" #n ")" ::: "memory")
#define FENCE() asm volatile("" ::: "memory")
#define SCHED() __builtin_amdgcn_sched_barrier(0)
#define SGB(mask, n) __builtin_amdgcn_sched_group_barrier(mask, n, 0)

DEV f2 pkfma(f2 a, f2 b, f2 c) { return __builtin_elementwise_fma(a, b, c); }
struct KC { f2 pm, rpm; };
DEV f2 addmi(f2 a, f2 b, const KC& k) { return pkfma(b.yx, k.pm, a); }
DEV f2 addpi(f2 a, f2 b, const KC& k) { return pkfma(b.yx, k.pm.yx, a); }
template <bool INV> DEV void dft4(f2& a0, f2& a1, f2& a2, f2& a3, const KC& k) {
    f2 t0 = a0 + a2, t1 = a0 - a2, t2 = a1 + a3, t3 = a1 - a3;
    a0 = t0 + t2; a2 = t0 - t2;
    if (INV) { a1 = addpi(t1, t3, k); a3 = addmi(t1, t3, k); } else { a1 = addmi(t1, t3, k); a3 = addpi(t1, t3, k); }
}
template <bool INV> DEV void dft8(f2 (&v)[8], const KC& k) {
    f2 a0 = v[0] + v[4], a4 = v[0] - v[4], a1 = v[1] + v[5], a5 = v[1] - v[5];
    f2 a2 = v[2] + v[6], a6 = v[2] - v[6], a3 = v[3] + v[7], a7 = v[3] - v[7];
    dft4<INV>(a0, a1, a2, a3, k);
    v[0] = a0; v[2] = a1; v[4] = a2; v[6] = a3;
    f2 b1, b3, t0, t1;
    if (INV) { b1 = addpi(a5, a5, k); b3 = addpi(-a7, a7, k); t0 = addpi(a4, a6, k); t1 = addmi(a4, a6, k); }
    else { b1 = addmi(a5, a5, k); b3 = addmi(-a7, a7, k); t0 = addmi(a4, a6, k); t1 = addpi(a4, a6, k); }
    f2 t2 = b1 + b3, t3 = b1 - b3;
    v[1] = pkfma(t2, k.rpm.xx, t0); v[5] = pkfma(t2, k.rpm.yy, t0);
    if (INV) { v[3] = pkfma(t3.yx, k.rpm.yx, t1); v[7] = pkfma(t3.yx, k.rpm, t1); }
    else { v[3] = pkfma(t3.yx, k.rpm, t1); v[7] = pkfma(t3.yx, k.rpm.yx, t1); }
}
template <bool CONJ> DEV f2 cmul(f2 a, f2 w) {
    f2 r;
    if (CONJ) asm("v_pk_mul_f32 %0, %1, %2 op_sel:[1,1] op_sel_hi:[0,1]\n\tv_pk_fma_f32 %0, %1, %2, %0 op_sel_hi:[1,0,1] neg_hi:[0,0,1]" : "=&v"(r) : "v"(a), "v"(w));
    else asm("v_pk_mul_f32 %0, %1, %2 op_sel:[1,1] op_sel_hi:[0,1]\n\tv_pk_fma_f32 %0, %1, %2, %0 op_sel_hi:[1,0,1] neg_lo:[0,0,1]" : "=&v"(r) : "v"(a), "v"(w));
    return r;
}
DEV f2 cfmac_conj(f2 t, f2 beta, f2 B) {
    asm("v_pk_fma_f32 %0, %2, %1, %0 op_sel_hi:[0,1,1]\n\tv_pk_fma_f32 %0, %2, %1, %0 op_sel:[1,1,0] op_sel_hi:[1,0,1] neg_hi:[0,1,0]" : "+v"(t) : "v"(beta), "v"(B));
    return t;
}
DEV f2 cis(float r) { return f2{__builtin_amdgcn_cosf(r), __builtin_amdgcn_sinf(r)}; }
DEV float bflo(u32 v) { return __uint_as_float(v << 16); }
DEV float bfhi(u32 v) { return __uint_as_float(v & 0xFFFF0000u); }
DEV f2 bfup(u32 v) { return f2{bflo(v), bfhi(v)}; }
DEV u32 pkbf(f2 v) { bf2 r; r.x = (__bf16)v.x; r.y = (__bf16)v.y; u32 o = __builtin_bit_cast(u32, r); asm("" : "+v"(o)); return o; }
DEV float rbf(float f) { return __uint_as_float(((u32)__builtin_bit_cast(u16, (__bf16)f)) << 16); }
DEV f4 mfma(u4 a, u4 b, f4 c) { return __builtin_amdgcn_mfma_f32_16x16x32_bf16(__builtin_bit_cast(bf8, a), __builtin_bit_cast(bf8, b), c, 0, 0, 0); }
DEV void st128(u4 v, __amdgpu_buffer_rsrc_t rs, u32 voff, int soff) {
    __builtin_amdgcn_raw_buffer_store_b128(v, rs, voff, soff, 16);
    asm volatile("s_nop 1" :: "v"(v) : "memory");
}
template <int OFF2> DEV u4 trfrag2(LAS char* p) {
    s4 r0 = __builtin_amdgcn_ds_read_tr16_b64_v4i16((LAS s4*)p);
    s4 r1 = __builtin_amdgcn_ds_read_tr16_b64_v4i16((LAS s4*)(p + OFF2));
    u2 x0 = __builtin_bit_cast(u2, r0), x1 = __builtin_bit_cast(u2, r1);
    return u4{x0.x, x0.y, x1.x, x1.y};
}
template <int LI, int GI> DEV void dma_add(u32 vb, const void* sbase, u32 sl) {
    const u32 va = vb + (u32)GI;
    asm volatile("s_add_u32 m0, %2, %3\n\ts_nop 0\n\tglobal_load_lds_dwordx4 %0, %1" :: "v"(va), "s"(sbase), "s"(sl), "i"(LI) : "memory", "scc");
}
template <int LI, int GI> DEV void dma4k_c(u32 vb, const void* sbase, u32 sl) {
    const u32 va = vb + (u32)GI;
    asm volatile("s_add_u32 m0, %2, %3\n\ts_nop 0\n\tglobal_load_lds_dwordx4 %0, %1 sc1\n\tglobal_load_lds_dwordx4 %0, %1 offset:1024 sc1\n\t"
                 "global_load_lds_dwordx4 %0, %1 offset:2048 sc1\n\tglobal_load_lds_dwordx4 %0, %1 offset:3072 sc1"
                 :: "v"(va), "s"(sbase), "s"(sl), "i"(LI) : "memory", "scc");
}
template <int OFF> DEV void ldg_u16(u32& d, u32 vo, const void* sb) { asm volatile("global_load_ushort %0, %1, %2 offset:%3" : "=v"(d) : "v"(vo), "s"(sb), "i"(OFF) : "memory"); }
template <int OFF> DEV void ldg_u32(u32& d, u32 vo, const void* sb) { asm volatile("global_load_dword %0, %1, %2 offset:%3" : "=v"(d) : "v"(vo), "s"(sb), "i"(OFF) : "memory"); }
DEV void lds_wait(u32 addr, u32 v) {
    u32 tmp, cnt;
    asm volatile("s_mov_b32 %1, 0x80000\n"
                 "1:\n\tds_read_b32 %0, %2\n\ts_sub_u32 %1, %1, 1\n\ts_waitcnt lgkmcnt(0)\n\ts_cbranch_scc1 2f\n\tv_cmp_gt_u32 vcc, %3, %0\n\ts_cbranch_vccnz 1b\n2:"
                 : "=&v"(tmp), "=&s"(cnt) : "v"(addr), "s"(v) : "memory", "vcc", "scc");
}
template <int N, typename F> DEV void sfor(F&& f) { if constexpr (N > 0) { sfor<N - 1>(f); f(IC<N - 1>{}); } }

DEV void pin9(f4 (&x)[4], f4 (&y)[4], f4& z) {
    asm volatile("" : "+v"(x[0]), "+v"(x[1]), "+v"(x[2]), "+v"(x[3]), "+v"(y[0]), "+v"(y[1]), "+v"(y[2]), "+v"(y[3]), "+v"(z));
}
DEV void pin4(f4 (&x)[4]) { asm volatile("" : "+v"(x[0]), "+v"(x[1]), "+v"(x[2]), "+v"(x[3])); }
DEV void dma_rt(u32 voff, const void* sbase, u32 sl) {
    asm volatile("s_mov_b32 m0, %2\n\ts_nop 0\n\tglobal_load_lds_dwordx4 %0, %1" :: "v"(voff), "s"(sbase), "s"(sl) : "memory");
}
DEV u4 geglu8(const f4 (&acc)[4], LAS char* cs, f2 st) {
    u32 hw[4];
    const f2 nm = f2{-st.x, -st.x}, ri = f2{st.y, st.y};
#pragma unroll
    for (int c = 0; c < 2; c++) {
        f4 cg_ = *(LAS f4*)(cs + c * 64), cu_ = *(LAS f4*)(cs + (2 + c) * 64);
#pragma unroll
        for (int e = 0; e < 2; e++) {
            f2 g = pkfma(e ? cg_.zw : cg_.xy, nm, e ? acc[c].zw : acc[c].xy) * ri;
            f2 u = pkfma(e ? cu_.zw : cu_.xy, nm, e ? acc[2 + c].zw : acc[2 + c].xy) * ri;
            f2 z = g * pkfma(g * g, f2{-0.10294324f, -0.10294324f}, f2{-2.3022082f, -2.3022082f});
            f2 d = f2{__builtin_amdgcn_exp2f(z.x), __builtin_amdgcn_exp2f(z.y)} + f2{1.f, 1.f};
            f2 r = f2{__builtin_amdgcn_rcpf(d.x), __builtin_amdgcn_rcpf(d.y)};
            hw[2 * c + e] = pkbf(g * r * u);
        }
    }
    return u4{hw[0], hw[1], hw[2], hw[3]};
}

enum { A_XL = 1, A_XC, A_P1, A_XB, A_S0, A_CS, A_WB, A_PB, A_D1, A_G1, A_D3 };
enum { B_XL = 1, B_W0, B_W1, B_W2, B_S0, B_W3, B_CS, B_FP, B_E1, B_E2, B_G1, B_F3 };

__global__ void __launch_bounds__(512, 2) fused(Args a) {
    extern __shared__ __attribute__((aligned(16))) char smem[];
    LAS char* lds = (LAS char*)smem;
    const int tid = threadIdx.x, w = __builtin_amdgcn_readfirstlane(tid >> 6);
    int lane = tid & 63;
    const int b = blockIdx.x, xcd = b & 7, r = b >> 3, t = r & 7, par = xcd & 1, rg = (xcd >> 1) * 4 + (r >> 3);
    const int s2 = 2 * rg + par, cg = par * 8 + t, sig = xcd * 32 + r, pid = s2 * 8 + t;
    const int rq = w & 3, hf = w >> 2, j4 = r >> 3;
#define BE(l) ((((l) >> 3) << 1) | ((l) & 1))
#define IX(l) (((l) >> 1) & 3)
    u32 l16 = lane * 16;
    u32 cpf = 0;
    if (w == 7) {
        const u64 pc = __builtin_amdgcn_s_getpc() & ~(u64)255;
        const u32 off = min((u32)((t * 64 + lane) * 128), (u32)(CODE_BYTES - 128));
        asm volatile("global_load_dword %0, %1, %2" : "=v"(cpf) : "v"(off), "s"(pc) : "memory");
    }
    *(volatile LAS u32*)(lds + L_PROG + w * 4) = 0;
    BAR();
    auto dma_x = [&]() {
        const char* xs = (const char*)a.x + (size_t)((s2 ^ hf) + 512 * rq) * 1024;
#pragma unroll
        for (int i = 0; i < 16; i++) { const int row = (i + 2 * t) & 15; dma_rt(l16 ^ (u32)(row * (32768 + 16)), xs, (u32)(L_SET1 + rq * 16384 + row * 1024)); }
    };
    if (hf == 0) dma_x();
    int li, lg, c2p, l3, qb; u32 vpg;
#define RELANE() do { asm volatile("" : "+v"(lane)); l16 = lane * 16; li = lane & 15; lg = lane >> 4; c2p = lane & 3; l3 = (lane >> 2) & 7; qb = lane >> 5; vpg = (u32)(L_PROG + (lane & 3) * 4); } while (0)
    const __amdgpu_buffer_rsrc_t rs = __builtin_amdgcn_make_buffer_rsrc(a.ws, 0, WS_BYTES, 0x00020000);
    KC kc; kc.pm = f2{1.f, -1.f}; kc.rpm = f2{0.70710678118654752f, -0.70710678118654752f};
    asm volatile("" : "+s"(kc.pm), "+s"(kc.rpm));
    const u4 ones = {0x3F803F80u, 0x3F803F80u, 0x3F803F80u, 0x3F803F80u};
    auto post = [&](u32 v) { asm volatile("s_waitcnt lgkmcnt(0)" ::: "memory"); *(volatile LAS u32*)(lds + L_PROG + w * 4) = v; FENCE(); };
    auto wait_grp = [&](int g, u32 v) { lds_wait(vpg + g * 16, v); };
    auto wait_pair = [&](u32 v) { lds_wait((u32)(L_PROG + (w ^ 4) * 4), v); };
    u2 tag;
    auto get_tag = [&]() { u2 tg = *(LAS u2*)(lds + L_TAG); tag.x = __builtin_amdgcn_readfirstlane(tg.x); tag.y = __builtin_amdgcn_readfirstlane(tg.y); };
    auto flag_store = [&](int off) { __builtin_amdgcn_raw_buffer_store_b64(tag, rs, lane ? WS_BYTES : 0, off, 16); };
    auto flag_wait = [&](u32 voff, int soff) {
        _Pragma("unroll 1") for (int spins = 0; spins < SPIN_CAP; spins++) {
            u2 f = __builtin_amdgcn_raw_buffer_load_b64(rs, voff, soff, 16);
            if (__all(f.x == tag.x && f.y == tag.y)) break;
            __builtin_amdgcn_s_sleep(16);
            FENCE();
        }
        __builtin_amdgcn_fence(__ATOMIC_ACQUIRE, "wavefront");
        FENCE();
    };
    auto flag_wait_post = [&](u32 voff, int soff, int post_off) {
        _Pragma("unroll 1") for (int spins = 0; spins < SPIN_CAP; spins++) {
            u2 f = __builtin_amdgcn_raw_buffer_load_b64(rs, voff, soff, 16);
            const bool all = __all(f.x == tag.x && f.y == tag.y);
            if (spins == 0) { WAITVM(0); flag_store(post_off); }
            if (all) break;
            __builtin_amdgcn_s_sleep(16);
            FENCE();
        }
        __builtin_amdgcn_fence(__ATOMIC_ACQUIRE, "wavefront");
        FENCE();
    };
    auto read_tile = [&](u4 (&x)[16], int tile) {
        LAS char* p = lds + L_SET1 + (16 * rq + li) * 1024;
#pragma unroll
        for (int k = 0; k < 16; k++) x[k] = *(LAS u4*)(p + (((4 * IX(k) + lg) ^ li) * 16) + ((BE(k) + j4) & 3) * 256);
    };
    auto stats = [&](const u4 (&x)[16]) {
        f4 s1 = {0, 0, 0, 0}, sq = s1, s1b = s1, sqb = s1;
#pragma unroll
        for (int k = 0; k < 16; k += 2) { s1 = mfma(ones, x[k], s1); sq = mfma(x[k], x[k], sq); s1b = mfma(ones, x[k + 1], s1b); sqb = mfma(x[k + 1], x[k + 1], sqb); }
        sq += sqb;
        const int rr = li & 3;
        const float d = rr == 0 ? sq.x : rr == 1 ? sq.y : rr == 2 ? sq.z : sq.w;
        if (lg == (li >> 2)) {
            const float mean = (s1.x + s1b.x) * (1.0f / 512.0f);
            float var = d * (1.0f / 512.0f) - mean * mean;
            if (!(mean * mean <= 1024.0f * var)) {
                const u32* xrow = (const u32*)(a.x + (size_t)((s2 ^ hf) + 32 * (16 * rq + li)) * 512);
                float q = 0.f;
                _Pragma("unroll 1") for (int i = 0; i < 256; i++) { u32 v = xrow[i]; float d0 = bflo(v) - mean, d1 = bfhi(v) - mean; q += d0 * d0; q += d1 * d1; }
                var = q * (1.0f / 512.0f);
            }
            const float den = rbf(__builtin_amdgcn_sqrtf(rbf(rbf(var) + 1e-5f)));
            *(LAS f2*)(lds + L_STAT + (hf * 64 + 16 * rq + li) * 8) = f2{rbf(mean), __builtin_amdgcn_rcpf(den)};
        }
        FENCE();
    };
    const int nblk = (t < 6 - par) ? 3 : 2, fb2 = 32 + par * 6 + t;
    const char* wdp = (const char*)a.ws + WS_WDP + (size_t)(t * NFB) * 4096;

    f2 tw64[8], twT[8];
    int x3a[4];
    auto fft13_consts = [&](float scale) {
        int lane_ = lane; asm volatile("" : "+v"(lane_));
        const int c2p = lane_ & 3, l3 = (lane_ >> 2) & 7, qb = lane_ >> 5;
        const int wq = rq, q1 = qb ? (wq == 0 ? 4 : 8 - wq) : wq, c2 = 4 * t + c2p;
        const float f64 = (float)l3 * (1.0f / 64.0f);
#pragma unroll
        for (int k = 1; k < 8; k++) tw64[k] = cis(f64 * (float)k);
        const float fT = (float)(8 * c2 * q1 + s2 * l3) * (1.0f / 2048.0f), dT = (float)s2 * (1.0f / 256.0f);
#pragma unroll
        for (int k = 0; k < 8; k++) twT[k] = cis(fT + dT * (float)k) * scale;
#pragma unroll
        for (int k = 0; k < 4; k++) {
            const int ua = (k >> 1) * 4 + (k & 1), u1 = l3 + 8 * ua;
            const int cls = (u1 < 32) ? u1 : ((64 - u1) & 31), hi = u1 >> 5;
            int mbit, sub;
            if (wq == 0) { mbit = qb; sub = hi; } else if (cls != 0) { sub = hi; mbit = qb ^ hi; } else { mbit = hi; sub = qb ^ hi; }
            x3a[k] = L_FFT + wq * 4608 + (cls * 18 + mbit * 8 + sub * 4 + c2p) * 8 - (k == 3 ? 2304 : 0);
        }
        asm volatile("" : "+v"(tw64[1]), "+v"(tw64[2]), "+v"(tw64[3]), "+v"(tw64[4]), "+v"(tw64[5]), "+v"(tw64[6]), "+v"(tw64[7]),
                     "+v"(twT[0]), "+v"(twT[1]), "+v"(twT[2]), "+v"(twT[3]), "+v"(twT[4]), "+v"(twT[5]), "+v"(twT[6]), "+v"(twT[7]),
                     "+v"(x3a[0]), "+v"(x3a[1]), "+v"(x3a[2]), "+v"(x3a[3]));
    };


    u4 xa[16], xb[16];
    const int hoff0 = WS_H + ((s2 * 4 + rq) * NFB) * 1024, hoff1 = WS_H + (((s2 ^ 1) * 4 + rq) * NFB) * 1024;
    RELANE();
    const char* wgs = (const char*)a.wg + (size_t)(32 * rq) * 2752 + cg * 128;
    const char* wus = (const char*)a.wu + (size_t)(32 * rq) * 2752 + cg * 128;
    const u32 wvE = (u32)((lane >> 3) * 2752 + (((lane & 7) ^ (((lane >> 4) & 1) << 1)) * 16)), wvO = (u32)((lane >> 3) * 2752 + (((lane & 7) ^ (((lane >> 4) & 1) << 1) ^ 4) * 16));
    auto dma_w1 = [&](auto KK, auto I) {
        constexpr int kk = KK, g = I >> 1, mat = I & 1;
        dma_add<kk * PG + mat * 4096 + g * 1024, g * 8 * 2752>((g & 1) ? wvO : wvE, (mat ? wus : wgs) + ((BE(kk) + j4) & 3) * (128 * 2752), (u32)(rq * 2 * PG));
    };
    const int rot = (2 * t + 4 * j4) & 15;
    auto dma_wd = [&](auto LI, auto I, int p0) { dma4k_c<LI + I * 16384, 0>(l16, wdp + (size_t)(p0 + 2 * ((rq + 4 * I + rot) & 15)) * 4096, (u32)(rq * 4096)); };

    if (hf == 0) {
        __builtin_amdgcn_s_setprio(2);
        const int f1st = WS_F1 + ((lane >> 3) * 8 + (((lane >> 2) & 1) ? (rq == 0 ? 4 : 8 - rq) : rq)) * 16384 + pid * 64 + c2p * 16;
        u2 ev;
        { u32 zero = 0; asm volatile("global_load_dwordx2 %0, %1, %2 sc1" : "=v"(ev) : "v"(zero), "s"(a.ws) : "memory"); }
        fft13_consts(1.0f);
        WAITVM(1);
        post(A_XL);
        u32 raw[8];
        const int rw = lane >> 2;
        read_tile(xa, 0);
#pragma unroll
        for (int c1 = 0; c1 < 8; c1++) raw[c1] = *(LAS u32*)(lds + L_SET1 + (16 * rq + rw) * 1024 + (((8 * c1 + t) ^ rw) * 16) + c2p * 4);
        post(A_XC);
        stats(xa);
        f2 v[8];
        {
            const f2 st = *(LAS f2*)(lds + L_STAT + (16 * rq + rw) * 8);
#pragma unroll
            for (int c1 = 0; c1 < 8; c1++) v[c1] = bfup(pkbf(bfup(pkbf(bfup(raw[c1]) - st.xx)) * st.yy));
            dft8<false>(v, kc);
            LAS f2* xb1 = (LAS f2*)(lds + L_FFT) + w * 64 + lane;
#pragma unroll
            for (int q1 = 0; q1 < 8; q1++) { const int reg = (q1 < 4) ? q1 * 2 : (((8 - q1) & 3) * 2 + 1); xb1[reg * 288] = v[q1]; }
        }
        asm volatile("s_waitcnt vmcnt(0)" : "+v"(ev) :: "memory");
        {
            const u64 epoch = ((u64)(u32)__builtin_amdgcn_readfirstlane(ev.y) << 32) | (u32)__builtin_amdgcn_readfirstlane(ev.x);
            u64 tg = epoch + 0x9E3779B97F4A7C15ull; tg = (tg ^ (tg >> 30)) * 0xBF58476D1CE4E5B9ull; tg = (tg ^ (tg >> 27)) * 0x94D049BB133111EBull; tg ^= tg >> 31;
            tag = u2{(u32)tg, (u32)(tg >> 32)};
            *(LAS u4*)(lds + L_TAG) = u4{(u32)tg, (u32)(tg >> 32), (u32)(epoch + 1), (u32)((epoch + 1) >> 32)};
        }
        post(A_P1);
#pragma unroll
        for (int c1 = 0; c1 < 8; c1++) *(LAS u32*)(lds + ((rq >> 1) ? L_X1 : L_X0) + ((rq & 1) * 16 + rw) * 128 + ((c1 ^ (rw & 7)) * 16) + c2p * 4) = raw[c1];
        wait_pair(B_XL);
        read_tile(xb, 1);
        post(A_XB);
        wait_grp(0, A_P1);
        {
            LAS f2* reg = (LAS f2*)(lds + L_FFT) + (w * 2 + qb) * 288;
#pragma unroll
            for (int k = 0; k < 8; k++) v[k] = reg[(lane & 31) + 32 * k];
            dft8<false>(v, kc);
#pragma unroll
            for (int k = 1; k < 8; k++) v[k] = cmul<true>(v[k], tw64[k]);
#pragma unroll
            for (int k = 0; k < 8; k++) reg[36 * k + (lane & 31)] = v[k];
            FENCE();
#pragma unroll
            for (int k = 0; k < 8; k++) v[k] = reg[36 * l3 + 4 * k + c2p];
            dft8<false>(v, kc);
#pragma unroll
            for (int k = 0; k < 8; k++) v[k] = cmul<true>(v[k], twT[k]);
            FENCE();
            *(LAS f2*)(lds + x3a[0]) = v[0];
            *(LAS f2*)(lds + x3a[1]) = v[1]; *(LAS f2*)(lds + x3a[1] + 1152) = v[2]; *(LAS f2*)(lds + x3a[1] + 2304) = v[3];
            *(LAS f2*)(lds + x3a[2]) = v[4];
            *(LAS f2*)(lds + x3a[3] + 2304) = v[5]; *(LAS f2*)(lds + x3a[3] + 1152) = v[6]; *(LAS f2*)(lds + x3a[3]) = v[7];
            FENCE();
#pragma unroll
            for (int i = 0; i < 4; i++) {
                u4 o = *(LAS u4*)(lds + L_FFT + w * 4608 + (i * 8 + (lane >> 3)) * 144 + (lane & 7) * 16);
                st128(o, rs, f1st, i * (64 * 16384));
            }
        }
        __builtin_amdgcn_s_setprio(0);
    } else {
        u32 pk[8];
        const int un = sig < 4 * NFB ? sig : 0, fb = un >> 2, jj = (un >> 1) & 1, tcol = (un & 1) * 4 + rq;
        __builtin_amdgcn_s_sleep(2);
        {
            const u32 vd = (u32)(((fb * 32 + 4 * lg) * 512 + 64 * (4 * jj + (li >> 2)) + 8 * tcol + 2 * (li & 3)) * 2);
            sfor<4>([&](auto I) {
                ldg_u32<I * 1024>(pk[I], vd, a.wd);
                ldg_u32<I * 1024>(pk[4 + I], vd, (const char*)a.wd + 16384);
            });
        }
        sfor<8>([&](auto I) { dma_w1(IC<0>{}, I); });
        wait_pair(A_XL);
        read_tile(xa, 0);
        asm volatile("s_waitcnt lgkmcnt(0)" ::: "memory");
        wait_pair(A_XC);
        dma_x();
        asm volatile("s_waitcnt vmcnt(24)" : "+v"(cpf), "+v"(pk[0]), "+v"(pk[1]), "+v"(pk[2]), "+v"(pk[3]), "+v"(pk[4]), "+v"(pk[5]), "+v"(pk[6]), "+v"(pk[7]) :: "memory");
        sfor<8>([&](auto I) { dma_w1(IC<1>{}, I); });
        {
            const int so = sig < 4 * NFB ? WS_WDP + ((tcol * NFB + fb) * 4 + 2 * jj) * 1024 : WS_DUMMY + (sig * 4 + rq) * 2048;
            u4 oe = {__builtin_amdgcn_perm(pk[1], pk[0], 0x05040100), __builtin_amdgcn_perm(pk[3], pk[2], 0x05040100), __builtin_amdgcn_perm(pk[5], pk[4], 0x05040100), __builtin_amdgcn_perm(pk[7], pk[6], 0x05040100)};
            u4 oo = {__builtin_amdgcn_perm(pk[1], pk[0], 0x07060302), __builtin_amdgcn_perm(pk[3], pk[2], 0x07060302), __builtin_amdgcn_perm(pk[5], pk[4], 0x07060302), __builtin_amdgcn_perm(pk[7], pk[6], 0x07060302)};
            st128(oe, rs, l16, so);
            st128(oo, rs, l16, so + 1024);
        }
        WAITVM(10);
        post(B_W0);
        read_tile(xb, 1);
        __builtin_amdgcn_s_setprio(1);
    }
    f2 st0, st1;
    {
        const u32 fz = (u32)((li >> 3) | ((lg & 1) << 1));
        const u32 tb = (u32)((8 * lg + (li >> 2)) * 128 + 8 * (li & 3));
        u32 p0 = tb + (((2 * hf) ^ fz) << 5), p1 = tb + (((2 * hf + 1) ^ fz) << 5), pc = tb + (((2 * hf + (rq & 1)) ^ fz) << 5) + (rq >> 1) * 4096;
        const u32 wvB = (u32)((lane >> 2) * 2752 + (((lane & 3) ^ (((lane >> 5) & 1) << 1)) * 16));
        const char* wbs = (const char*)((rq >> 1) ? a.wu : a.wg) + (size_t)((rq & 1) * 16) * 2752 + fb2 * 64;
        f4 accA[4], accB[4], acs = {0, 0, 0, 0};
#pragma unroll
        for (int j = 0; j < 4; j++) { accA[j] = f4{0, 0, 0, 0}; accB[j] = f4{0, 0, 0, 0}; }
        u4 F[2][5];
        auto ldf = [&](int ks, u4 (&f)[5]) {
            const int off = (ks & 7) * PG;
            f[0] = trfrag2<512>(lds + p0 + off); f[1] = trfrag2<512>(lds + p1 + off); f[2] = trfrag2<512>(lds + p0 + off + 4096); f[3] = trfrag2<512>(lds + p1 + off + 4096); f[4] = trfrag2<512>(lds + pc + off);
        };
        wait_grp(1, B_W0);
        ldf(0, F[0]);
        sfor<16>([&](auto S) {
            constexpr int s = S, ks = (s & 8) | ((s & 3) << 1) | ((s >> 2) & 1), sn = s + 1, kn = (sn & 8) | ((sn & 3) << 1) | ((sn >> 2) & 1);
            if ((s >= 1 && s <= 4) || s == 7 || s >= 9) pin9(accA, accB, acs);
            if (s == 3) {
                if (hf) { WAITVM(10); post(B_W1); }
                else { WAITVM(0); flag_store(WS_FF1 + ((rq * 4 + ((s2 & 7) >> 1)) * 64 + (s2 >> 3) * 16 + (s2 & 1) * 8 + t) * 8); }
                wait_grp(1, B_W1);
            }
            if constexpr (s >= 1 && s <= 4) if (hf) {
                if (s == 1) { wait_pair(A_XB); get_tag(); }
                sfor<4>([&](auto I) { dma_w1(IC<8 + ((s - 1) >> 1)>{}, IC<((s - 1) & 1) * 4 + I>{}); });
            }
            if (s == 7) {
                if (hf) { WAITVM(8); post(B_W2); }
                wait_grp(1, B_W2);
                p0 += 65536; p1 += 65536; pc += 65536;
                asm volatile("" : "+v"(p0), "+v"(p1), "+v"(pc));
            }
            if (s == 8) post(hf ? B_S0 : A_S0);
            if (s == 11) {
                if (hf) { WAITVM(0); post(B_W3); flag_store(WS_FP + (((sig & 1) * 4 + rq) * 128 + (sig >> 1)) * 8); }
                wait_grp(1, B_W3);
            }
            if constexpr (s >= 9) if (hf == 0 && nblk == 3) {
                if (s == 9) { wait_grp(0, A_S0); wait_grp(1, B_S0); sfor<2>([&](auto I) { dma_add<I * 4096, IX(I) * 32 * 2752>(wvB, wbs + ((BE(I) + j4) & 3) * (128 * 2752), (u32)(rq * 1024)); }); }
                sfor<2>([&](auto I) { constexpr int k = (s - 8) * 2 + I; dma_add<k * 4096, IX(k) * 32 * 2752>(wvB, wbs + ((BE(k) + j4) & 3) * (128 * 2752), (u32)(rq * 1024)); });
            }
            SCHED();
            if (s + 1 < 16) ldf(kn, F[(s + 1) & 1]);
            u4 (&f)[5] = F[s & 1];
#pragma unroll
            for (int j = 0; j < 4; j++) { accA[j] = mfma(f[j], xa[ks], accA[j]); accB[j] = mfma(f[j], xb[ks], accB[j]); }
            acs = mfma(f[4], ones, acs);
            if (s + 1 < 16) {
                SGB(0x100, 2);
#pragma unroll
                for (int i = 0; i < 8; i++) { SGB(0x8, 1); SGB(0x100, 1); }
                SGB(0x8, 1);
            }
            SCHED();
        });
        RELANE();
        if (hf) stats(xb);
        if (li == 0) *(LAS f4*)(lds + L_CS + (hf * 4 + rq) * 64 + lg * 16) = acs;
        post(hf ? B_CS : A_CS);
        wait_grp(hf, hf ? B_CS : A_CS);
        if (hf == 0) wait_pair(B_CS);
        LAS char* cs = lds + L_CS + hf * 256 + lg * 16;
        st0 = *(LAS f2*)(lds + L_STAT + (16 * rq + li) * 8);
        st1 = *(LAS f2*)(lds + L_STAT + (64 + 16 * rq + li) * 8);
        st128(geglu8(accA, cs, st0), rs, l16, hoff0 + (2 * cg + hf) * 1024);
        st128(geglu8(accB, cs, st1), rs, l16, hoff1 + (2 * cg + hf) * 1024);
    }
    if (hf == 0) {
        WAITVM(2); post(A_WB);
        if (nblk == 3) {
            wait_grp(0, A_WB);
            const u32 tb = (u32)((8 * lg + (li >> 2)) * 64 + (lg & 1) * 32 + 8 * (li & 3));
            LAS char* p0 = lds + tb; LAS char* p1 = lds + (tb ^ 32);
            LAS char* pc = lds + (tb ^ ((rq & 1) * 32)) + (rq >> 1) * 2048;
            f4 acc[2][4], acs = {0, 0, 0, 0};
#pragma unroll
            for (int j = 0; j < 4; j++) { acc[0][j] = f4{0, 0, 0, 0}; acc[1][j] = f4{0, 0, 0, 0}; }
            u4 F[2][5];
            auto ldf = [&](int ks, u4 (&f)[5]) {
                const int off = ks * 4096;
                f[4] = trfrag2<256>(pc + off);
                f[0] = trfrag2<256>(p0 + off); f[1] = trfrag2<256>(p1 + off); f[2] = trfrag2<256>(p0 + off + 2048); f[3] = trfrag2<256>(p1 + off + 2048);
            };
            ldf(0, F[0]);
            sfor<16>([&](auto S) {
                constexpr int ks = S;
                if (ks == 4) { WAITVM(0); flag_store(WS_FHA + ((rg * 8 + w) * 16 + cg) * 8); wait_grp(1, B_FP); }
                if constexpr (ks >= 4 && ks % 3 == 1) dma_wd(IC<L_SET1>{}, IC<(ks - 4) / 3>{}, 0);
                SCHED();
                if (ks + 1 < 16) ldf(ks + 1, F[(ks + 1) & 1]);
                u4 (&f)[5] = F[ks & 1];
                acs = mfma(f[4], ones, acs);
#pragma unroll
                for (int j = 0; j < 4; j++) { acc[0][j] = mfma(f[j], xa[ks], acc[0][j]); acc[1][j] = mfma(f[j], xb[ks], acc[1][j]); }
                if (ks + 1 < 16) {
                    SGB(0x100, 2);
#pragma unroll
                    for (int i = 0; i < 8; i++) { SGB(0x8, 1); SGB(0x100, 1); }
                    SGB(0x8, 1);
                }
                SCHED();
            });
            if (li == 0) *(LAS f4*)(lds + L_CS + 512 + rq * 64 + lg * 16) = acs;
            post(A_PB);
            wait_grp(0, A_PB);
            st128(geglu8(acc[0], lds + L_CS + 512 + lg * 16, st0), rs, l16, hoff0 + fb2 * 1024);
            st128(geglu8(acc[1], lds + L_CS + 512 + lg * 16, st1), rs, l16, hoff1 + fb2 * 1024);
        } else {
            WAITVM(0); flag_store(WS_FHA + ((rg * 8 + w) * 16 + cg) * 8);
            wait_grp(1, B_FP);
            sfor<4>([&](auto I) { dma_wd(IC<L_SET1>{}, I, 0); });
        }
        sfor<4>([&](auto I) { dma_wd(IC<0>{}, I, 1); });
    } else {
        f2 v[8], tw0[4], tw1[8], al[8], be[8];
        const int rb = qb, r3 = 2 * rq + rb;
        const int cls2 = sig >> 3, m2 = sig & 7;
        const int wq2 = (m2 < 4) ? m2 : ((8 - m2) & 3), pos1 = wq2 * 64 + cls2 * 2 + (m2 >> 2);
        const bool special = (cls2 == 0) && ((m2 & 3) == 0);
        int u1a, q1a, u1b, q1b;
        if (cls2 != 0) { u1a = cls2; q1a = m2; u1b = 64 - cls2; q1b = (8 - m2) & 7; }
        else if (special) { u1a = 0; u1b = 32; q1a = q1b = m2; }
        else { u1a = u1b = (m2 < 4) ? 0 : 32; q1a = m2; q1b = 8 - m2; }
        int sA, vH0, sB, vH1;
        if (!special) { sA = 0; vH0 = rq; sB = 1; vH1 = (u1a == 0) ? ((4 - rq) & 3) : 3 - rq; }
        else { sA = sB = rq >> 1; vH0 = rq & 1; vH1 = (rq == 0) ? 2 : (rq == 1) ? 3 : (rq == 2) ? 3 : 2; }
        const bool q0 = (m2 == 0);
        const int dy = ((sA ? u1b : u1a) == 0 && vH0 == 0) ? 8 : 7, dq = q0 ? 8 : 7;
        int y1 = lane >> 3, t1 = lane & 7, y2 = (dy - y1) & 7, t2 = (dq - t1) & 7, hA = vH0, hB = vH1;
        bool self = false;
        if (special && rq == 0) {
            const int k = lane & 31;
            if (qb) { hA = hB = 2; y1 = k >> 3; t1 = k & 7; y2 = 7 - y1; t2 = (dq - t1) & 7; }
            else {
                hA = hB = 0; const int vx = k & 3, y = k >> 2;
                if (!q0 || vx) { y1 = y; t1 = vx; y2 = (8 - y) & 7; t2 = dq - vx; }
                else { t1 = t2 = (y >= 4) ? 4 : 0; const int yy = y & 3; if (yy) { y1 = yy; y2 = 8 - yy; } else { y1 = 0; y2 = 4; self = true; } }
            }
        }
        const int uA = (sA ? u1b : u1a) + 64 * (hA + 4 * y1), uB = (sB ? u1b : u1a) + 64 * (hB + 4 * y2);
        const int qA = (sA ? q1b : q1a) + 8 * t1, qB = (sB ? q1b : q1a) + 8 * t2;
        u32 wl[20];
        {
            const u32 vA = (u32)((uA * 257 + qA) * 2), vB = (u32)((uB * 257 + qB) * 2);
            sfor<4>([&](auto K) { ldg_u16<K * 128>(wl[K], vA, a.wr); ldg_u16<K * 128>(wl[8 + K], vA, a.wi); ldg_u16<K * 128>(wl[4 + K], vB, a.wr); ldg_u16<K * 128>(wl[12 + K], vB, a.wi); });
            const u32 vA2 = (u32)(uA * 514 + 512), vB2 = (u32)(uB * 514 + 512);
            ldg_u16<0>(wl[16], vA2, a.wr); ldg_u16<0>(wl[17], vA2, a.wi); ldg_u16<0>(wl[18], vB2, a.wr); ldg_u16<0>(wl[19], vB2, a.wi);
        }
        const int rowB = L_FFT + ((qb ? (vH1 * 2 + sB) : (vH0 * 2 + sA)) * 36 + (lane & 31)) * 8;
        const bool rot = q0 && (t1 == 0);
        const int p3a1 = L_FFT + (y1 * 290 + (hA * 2 + sA) * 36 + t1 * 4) * 8;
        const int p3a2 = L_FFT + (y2 * 290 + (hB * 2 + sB) * 36 + t2 * 4) * 8;
        const float thA = (float)qA * (1.0f / 512.0f), thB = (float)qB * (1.0f / 512.0f);
#pragma unroll
        for (int k = 1; k < 4; k++) tw0[k] = cis((float)r3 * ((float)k / 32.0f));
#pragma unroll
        for (int k = 1; k < 8; k++) tw1[k] = cis((float)c2p * ((float)k / 32.0f));
        f2 csq[8];
#pragma unroll
        for (int e = 0; e < 8; e++) csq[e] = cis(((e >> 2) ? thB : thA) + 0.125f * (float)(e & 3));
        asm volatile("" : "+v"(csq[0]), "+v"(csq[1]), "+v"(csq[2]), "+v"(csq[3]), "+v"(csq[4]), "+v"(csq[5]), "+v"(csq[6]), "+v"(csq[7]),
                     "+v"(tw0[1]), "+v"(tw0[2]), "+v"(tw0[3]), "+v"(tw1[1]), "+v"(tw1[2]), "+v"(tw1[3]), "+v"(tw1[4]), "+v"(tw1[5]), "+v"(tw1[6]), "+v"(tw1[7]));
        {
            const u32 o1 = min(rq * 22 + lane, 85) * 8, o3 = lane * 8;
            bool posted = false;
            _Pragma("unroll 1") for (int spins = 0; spins < SPIN_CAP; spins++) {
                u2 f1 = __builtin_amdgcn_raw_buffer_load_b64(rs, o1, WS_FP + t * 1024, 16);
                u2 f3 = __builtin_amdgcn_raw_buffer_load_b64(rs, o3, WS_FF1 + (wq2 * 4 + rq) * 512, 16);
                const bool ok = f1.x == tag.x && f1.y == tag.y && f3.x == tag.x && f3.y == tag.y;
                const bool all = __all(ok);
                if (!posted) { WAITVM(0); flag_store(WS_FHA + ((rg * 8 + w) * 16 + cg) * 8); posted = true; }
                if (all) break;
                __builtin_amdgcn_s_sleep(16);
                FENCE();
            }
            __builtin_amdgcn_fence(__ATOMIC_ACQUIRE, "wavefront");
            FENCE();
        }
        post(B_FP);
        const int f1off = WS_F1 + sig * 16384 + 2 * rq * 512;
        u4 d[4];
        SCHED();
#pragma unroll
        for (int H = 0; H < 4; H++) d[H] = __builtin_amdgcn_raw_buffer_load_b128(rs, l16, f1off + H * 4096, 16);
        SCHED();
        asm volatile("" : "+v"(wl[0]), "+v"(wl[1]), "+v"(wl[2]), "+v"(wl[3]), "+v"(wl[4]), "+v"(wl[5]), "+v"(wl[6]), "+v"(wl[7]), "+v"(wl[8]), "+v"(wl[9]),
                     "+v"(wl[10]), "+v"(wl[11]), "+v"(wl[12]), "+v"(wl[13]), "+v"(wl[14]), "+v"(wl[15]), "+v"(wl[16]), "+v"(wl[17]), "+v"(wl[18]), "+v"(wl[19]) :: "memory");
        const f2 wS[2] = {f2{bflo(wl[16]), bflo(wl[17])}, f2{bflo(wl[18]), bflo(wl[19])}};
        {
            f2 W[8], WB[8];
#pragma unroll
            for (int e = 0; e < 8; e++) W[e] = f2{bflo(wl[e]), bflo(wl[8 + e])};
#pragma unroll
            for (int k = 0; k < 4; k++) { WB[k] = W[4 + 3 - k]; WB[4 + k] = W[3 - k]; }
            if (q0) {
#pragma unroll
                for (int k = 0; k < 4; k++) {
                    f2 n1 = rot ? W[(4 - k) & 3] : W[3 - k], n2 = rot ? W[4 + ((4 - k) & 3)] : W[4 + 3 - k];
                    WB[k] = self ? n1 : n2; WB[4 + k] = self ? n2 : n1;
                }
#pragma unroll
                for (int e = 0; e < 8; e += 4) {
                    f2 own = wS[e >> 2], oth = self ? own : wS[1 - (e >> 2)];
                    f2 S0 = pkfma(WB[e], kc.pm, W[e]) * 0.5f, S1c = pkfma(own, kc.pm, oth) * 0.5f;
                    WB[e] = rot ? S1c : WB[e];
                    al[e] = rot ? S0 : W[e];
                }
                W[0] = al[0]; W[4] = al[4];
            }
#pragma unroll
            for (int e = 0; e < 8; e++) {
                const f2 cs = csq[e];
                f2 P = pkfma(WB[e], kc.pm, W[e]), M = pkfma(WB[e], kc.pm.yx, W[e]);
                asm("v_pk_fma_f32 %0, %1, %2, %3 op_sel:[1,0,0] op_sel_hi:[1,1,1] neg_lo:[1,0,0] neg_hi:[1,0,0]" : "=v"(al[e]) : "v"(cs), "v"(M), "v"(P));
                asm("v_pk_mul_f32 %0, %1, %2 op_sel:[1,0] op_sel_hi:[0,0] neg_lo:[1,0]" : "=v"(be[e]) : "v"(M), "v"(cs));
            }
            asm volatile("" : "+v"(al[0]), "+v"(al[1]), "+v"(al[2]), "+v"(al[3]), "+v"(al[4]), "+v"(al[5]), "+v"(al[6]), "+v"(al[7]),
                         "+v"(be[0]), "+v"(be[1]), "+v"(be[2]), "+v"(be[3]), "+v"(be[4]), "+v"(be[5]), "+v"(be[6]), "+v"(be[7]),
                         "+v"(tw0[1]), "+v"(tw0[2]), "+v"(tw0[3]), "+v"(tw1[1]), "+v"(tw1[2]), "+v"(tw1[3]), "+v"(tw1[4]), "+v"(tw1[5]), "+v"(tw1[6]), "+v"(tw1[7]));
        }
        wait_grp(0, A_S0);
        __builtin_amdgcn_s_setprio(2);
        f2 e0[4], e1[4];
#pragma unroll
        for (int H = 0; H < 4; H++) { e0[H] = f2{__uint_as_float(d[H].x), __uint_as_float(d[H].y)}; e1[H] = f2{__uint_as_float(d[H].z), __uint_as_float(d[H].w)}; }
        dft4<false>(e0[0], e0[1], e0[2], e0[3], kc); dft4<false>(e1[0], e1[1], e1[2], e1[3], kc);
        LAS char* area = lds + L_FFT + rq * 4640;
#pragma unroll
        for (int k = 0; k < 4; k++) {
            if (k) { e0[k] = cmul<true>(e0[k], tw0[k]); e1[k] = cmul<true>(e1[k], tw0[k]); }
            *(LAS f4*)(area + (l3 * 72 + 2 * c2p + 8 * k + 32 * rb) * 8) = f4{e0[k].x, e0[k].y, e1[k].x, e1[k].y};
        }
        FENCE();
#pragma unroll
        for (int k = 0; k < 8; k++) v[k] = *(LAS f2*)(area + (k * 72 + lane) * 8);
        dft8<false>(v, kc);
#pragma unroll
        for (int k = 1; k < 8; k++) v[k] = cmul<true>(v[k], tw1[k]);
        FENCE();
        LAS char* pb = lds + L_FFT + (r3 * 290 + l3 * 36 + c2p) * 8;
#pragma unroll
        for (int k = 0; k < 8; k++) *(LAS f2*)(pb + k * 32) = v[k];
        post(B_E1);
        wait_grp(1, B_E1);
#pragma unroll
        for (int k = 0; k < 8; k++) v[k] = *(LAS f2*)(lds + rowB + k * 2320);
        dft8<false>(v, kc);
#pragma unroll
        for (int k = 0; k < 8; k++) *(LAS f2*)(lds + rowB + k * 2320) = v[k];
        FENCE();
        f4 a0 = *(LAS f4*)(lds + p3a1), a1 = *(LAS f4*)(lds + p3a1 + 16), b0 = *(LAS f4*)(lds + p3a2), b1 = *(LAS f4*)(lds + p3a2 + 16);
        f2 X[8] = {a0.xy, a0.zw, a1.xy, a1.zw, b0.xy, b0.zw, b1.xy, b1.zw};
        dft4<false>(X[0], X[1], X[2], X[3], kc); dft4<false>(X[4], X[5], X[6], X[7], kc);
        f2 Y[8];
#pragma unroll
        for (int k = 0; k < 4; k++) { Y[k] = X[4 + 3 - k]; Y[4 + k] = X[3 - k]; }
        if (q0) {
#pragma unroll
            for (int k = 0; k < 4; k++) {
                f2 n1 = rot ? X[(4 - k) & 3] : X[3 - k], n2 = rot ? X[4 + ((4 - k) & 3)] : X[4 + 3 - k];
                Y[k] = self ? n1 : n2; Y[4 + k] = self ? n2 : n1;
            }
        }
        f2 O[8];
#pragma unroll
        for (int e = 0; e < 8; e++) O[e] = cfmac_conj(cmul<false>(X[e], al[e]), be[e], Y[e]);
        dft4<true>(O[0], O[1], O[2], O[3], kc); dft4<true>(O[4], O[5], O[6], O[7], kc);
        *(LAS f4*)(lds + p3a1) = f4{O[0].x, O[0].y, O[1].x, O[1].y}; *(LAS f4*)(lds + p3a1 + 16) = f4{O[2].x, O[2].y, O[3].x, O[3].y};
        *(LAS f4*)(lds + p3a2) = f4{O[4].x, O[4].y, O[5].x, O[5].y}; *(LAS f4*)(lds + p3a2 + 16) = f4{O[6].x, O[6].y, O[7].x, O[7].y};
        FENCE();
#pragma unroll
        for (int k = 0; k < 8; k++) v[k] = *(LAS f2*)(lds + rowB + k * 2320);
        dft8<true>(v, kc);
#pragma unroll
        for (int k = 0; k < 8; k++) *(LAS f2*)(lds + rowB + k * 2320) = v[k];
        post(B_E2);
        wait_grp(1, B_E2);
#pragma unroll
        for (int k = 0; k < 8; k++) v[k] = *(LAS f2*)(pb + k * 32);
#pragma unroll
        for (int k = 1; k < 8; k++) v[k] = cmul<false>(v[k], tw1[k]);
        dft8<true>(v, kc);
        FENCE();
#pragma unroll
        for (int k = 0; k < 8; k++) *(LAS f2*)(area + (k * 72 + lane) * 8) = v[k];
        FENCE();
#pragma unroll
        for (int k = 0; k < 4; k++) {
            f4 q = *(LAS f4*)(area + (l3 * 72 + 2 * c2p + 8 * k + 32 * rb) * 8);
            e0[k] = q.xy; e1[k] = q.zw;
            if (k) { e0[k] = cmul<false>(e0[k], tw0[k]); e1[k] = cmul<false>(e1[k], tw0[k]); }
        }
        dft4<true>(e0[0], e0[1], e0[2], e0[3], kc); dft4<true>(e1[0], e1[1], e1[2], e1[3], kc);
        const int f2st = WS_F2 + (r3 * 8 + l3) * 16384 + pos1 * 64 + c2p * 16;
#pragma unroll
        for (int H = 0; H < 4; H++) {
            u4 o = {__float_as_uint(e0[H].x), __float_as_uint(e0[H].y), __float_as_uint(e1[H].x), __float_as_uint(e1[H].y)};
            st128(o, rs, f2st, H * (64 * 16384));
        }
        __builtin_amdgcn_s_setprio(0);
        WAITVM(0);
        flag_store(WS_FF2 + (rq * 256 + pos1) * 8);
    }
    RELANE();
    f4 acc2[4];
#pragma unroll
    for (int j = 0; j < 4; j++) acc2[j] = f4{0, 0, 0, 0};
    const int hsrc = hoff0 + hf * 1024;
    auto g2_mma = [&](auto NC, const u4* hfr, int ldsbase) {
        constexpr int N = decltype(NC)::value;
        u32 pa = (u32)ldsbase + l16; asm volatile("" : "+v"(pa));
        LAS u4* p = (LAS u4*)(lds + pa);
        u4 F[2][4];
#pragma unroll
        for (int j = 0; j < 4; j++) F[0][j] = p[j * 64];
        sfor<N>([&](auto I) {
            constexpr int i = I;
            SCHED();
            if (i + 1 < N) {
#pragma unroll
                for (int j = 0; j < 4; j++) F[(i + 1) & 1][j] = p[(i + 1) * 256 + j * 64];
            }
#pragma unroll
            for (int j = 0; j < 4; j++) acc2[j] = mfma(F[i & 1][j], hfr[i], acc2[j]);
            if (i + 1 < N) {
#pragma unroll
                for (int j = 0; j < 4; j++) { SGB(0x100, 1); SGB(0x8, 1); }
            }
            SCHED();
        });
    };
    u4 hfr[16];
    flag_wait_post(li * 8, WS_FHA + (rg * 8 + w) * 128, (hf == 0 && nblk == 3) ? WS_FHB + ((rg * 4 + rq) * 16 + fb2 - 32) * 8 : WS_BYTES);
    if (hf == 0) post(A_D1);
#pragma unroll
    for (int i = 0; i < 16; i++) { hfr[i] = __builtin_amdgcn_raw_buffer_load_b128(rs, l16, hsrc + 2 * ((i + rot) & 15) * 1024, 16); SCHED(); }
    wait_grp(0, A_D1);
    g2_mma(IC<16>{}, hfr, hf ? 0 : L_SET1);
    post(hf ? B_G1 : A_G1);
    pin4(acc2);
    if (hf == 0) {
        wait_grp(0, A_G1);
        sfor<3>([&](auto I) { constexpr int i = I; if (i < 2 || rq < 3) dma4k_c<L_SET1 + i * 16384, (32 + 4 * i) * 4096>(l16, wdp + rq * 4096, (u32)(rq * 4096)); });
        flag_wait(min(li, 10) * 8, WS_FHB + (rg * 4 + rq) * 128);
        post(A_D3);
#pragma unroll
        for (int i = 0; i < 11; i++) { hfr[i] = __builtin_amdgcn_raw_buffer_load_b128(rs, l16, hoff0 + (32 + i) * 1024, 16); SCHED(); }
        wait_grp(0, A_D3);
        g2_mma(IC<11>{}, hfr, L_SET1);
    } else {
        wait_grp(1, B_G1);
        fft13_consts(0.5f / 524288.0f);
        flag_wait(lane * 8, WS_FF2 + (((s2 & 7) >> 1) * 256 + rq * 64) * 8);
        u4 d[4]; f2 v[8];
        const int off = WS_F2 + pid * 16384 + rq * 4096;
#pragma unroll
        for (int i = 0; i < 4; i++) d[i] = __builtin_amdgcn_raw_buffer_load_b128(rs, l16, off + i * 1024, 16);
        __builtin_amdgcn_s_setprio(2);
#pragma unroll
        for (int i = 0; i < 4; i++) *(LAS u4*)(lds + L_FFT + rq * 4608 + (i * 8 + (lane >> 3)) * 144 + (lane & 7) * 16) = d[i];
        FENCE();
        v[0] = *(LAS f2*)(lds + x3a[0]);
        v[1] = *(LAS f2*)(lds + x3a[1]); v[2] = *(LAS f2*)(lds + x3a[1] + 1152); v[3] = *(LAS f2*)(lds + x3a[1] + 2304);
        v[4] = *(LAS f2*)(lds + x3a[2]);
        v[5] = *(LAS f2*)(lds + x3a[3] + 2304); v[6] = *(LAS f2*)(lds + x3a[3] + 1152); v[7] = *(LAS f2*)(lds + x3a[3]);
#pragma unroll
        for (int k = 0; k < 8; k++) v[k] = cmul<false>(v[k], twT[k]);
        dft8<true>(v, kc);
#pragma unroll
        for (int k = 1; k < 8; k++) v[k] = cmul<false>(v[k], tw64[k]);
        FENCE();
        LAS f2* reg = (LAS f2*)(lds + L_FFT) + (rq * 2 + qb) * 288;
#pragma unroll
        for (int k = 0; k < 8; k++) reg[36 * l3 + 4 * k + c2p] = v[k];
        FENCE();
#pragma unroll
        for (int k = 0; k < 8; k++) v[k] = reg[36 * k + (lane & 31)];
        dft8<true>(v, kc);
        FENCE();
#pragma unroll
        for (int k = 0; k < 8; k++) reg[(lane & 31) + 32 * k] = v[k];
        post(B_F3);
        wait_grp(1, B_F3);
        if (b == 0 && rq == 0) __builtin_amdgcn_raw_buffer_store_b64(*(LAS u2*)(lds + L_TAG + 8), rs, lane ? WS_BYTES : 0, 0, 16);
        LAS f2* xb1 = (LAS f2*)(lds + L_FFT) + rq * 64 + lane;
#pragma unroll
        for (int q1 = 0; q1 < 8; q1++) { const int rg_ = (q1 < 4) ? q1 * 2 : (((8 - q1) & 3) * 2 + 1); v[q1] = xb1[rg_ * 288]; }
        dft8<true>(v, kc);
#pragma unroll
        for (int c1 = 0; c1 < 8; c1++) *(LAS u32*)(lds + L_S + (16 * rq + (lane >> 2)) * 144 + c1 * 16 + c2p * 4) = pkbf(v[c1]);
    }
    BAR();
    {
        LAS f4* xs = (LAS f4*)(lds + w * 2048) + lane;
        LAS f4* xr = (LAS f4*)(lds + (w ^ 4) * 2048) + lane;
        f4 me, mo;
        if (hf) { xs[0] = acc2[0]; xs[64] = acc2[1]; me = acc2[2]; mo = acc2[3]; } else { xs[0] = acc2[2]; xs[64] = acc2[3]; me = acc2[0]; mo = acc2[1]; }
        BAR();
        const int c1 = 4 * hf + lg;
        me += xr[0]; mo += xr[64];
        const u4 sv = *(LAS u4*)(lds + L_S + (16 * rq + li) * 144 + c1 * 16);
        const u4 xv = *(LAS u4*)(lds + ((rq >> 1) ? L_X1 : L_X0) + ((rq & 1) * 16 + li) * 128 + ((c1 ^ (li & 7)) * 16));
        u32 res[4];
#pragma unroll
        for (int e = 0; e < 4; e++) {
            const f2 ov = bfup(pkbf(f2{me[e], mo[e]}));
            const f2 z = ov * -1.44269504f;
            const f2 d = f2{__builtin_amdgcn_exp2f(z.x), __builtin_amdgcn_exp2f(z.y)} + f2{1.f, 1.f};
            const f2 g = bfup(pkbf(ov * f2{__builtin_amdgcn_rcpf(d.x), __builtin_amdgcn_rcpf(d.y)}));
            res[e] = pkbf(bfup(xv[e]) + bfup(pkbf(bfup(sv[e]) * g)));
        }
        *(u4*)(a.out + (size_t)(s2 + 32 * (16 * rq + li)) * 512 + 64 * c1 + 8 * t) = u4{res[0], res[1], res[2], res[3]};
    }
}

extern "C" void kernel_launch(void* const* d_in, const int* in_sizes, int n_in,
                              void* d_out, int out_size, void* d_ws, size_t ws_size,
                              hipStream_t stream) {
    Args a;
    a.x = (const u16*)d_in[0]; a.wr = (const u16*)d_in[1]; a.wi = (const u16*)d_in[2];
    a.wg = (const u16*)d_in[4]; a.wu = (const u16*)d_in[5]; a.wd = (const u16*)d_in[6];
    a.out = (u16*)d_out; a.ws = (unsigned char*)d_ws;
    (void)hipFuncSetAttribute((const void*)fused, hipFuncAttributeMaxDynamicSharedMemorySize, L_END);
    fused<<<256, 512, L_END, stream>>>(a);
}
```
